```python
import torch

def _softmax(x, axis=-1):
    return torch.softmax(x, dim=axis)
def _gelu(x):
    return torch.nn.functional.gelu(x, approximate="tanh")
def _silu(x):
    return torch.nn.functional.silu(x)
def _sigmoid(x):
    return torch.sigmoid(x)
def _layernorm(x, axis=-1, eps=1e-5):
    m = x.mean(dim=axis, keepdim=True)
    v = x.var(dim=axis, keepdim=True, unbiased=False)
    return (x - m) / torch.sqrt(v + eps)
def _scatter_add(tgt, idx, src, axis=0):
    return tgt.clone().index_add_(axis, idx, src)
def _segment_sum(src, seg, n):
    out = torch.zeros((n,) + src.shape[1:], dtype=src.dtype)
    return out.index_add_(0, seg, src)
def _pad(x, pw):
    flat = tuple(v for pair in reversed(pw) for v in pair)
    return torch.nn.functional.pad(x, flat)

kernel_name = 'sequential_preLN__linear_attn_block__mixer_channel_mlp__prompt'

def setup_inputs():

    _w = (lambda a: (lambda t: t.to(torch.bfloat16) if t.is_floating_point() else t)(torch.as_tensor(a)))
    def _A_setup():
        B, S, D = 1, 2048, 640
        return {
            "x": _w((lambda *s: torch.randn(*s, dtype=torch.bfloat16) * 0.05)(B, S, D)),
            "wqkv": _w((lambda *s: torch.randn(*s, dtype=torch.bfloat16) * 0.05)(D, 3 * D)),
            "wo": _w((lambda *s: torch.randn(*s, dtype=torch.bfloat16) * 0.05)(D, D)),
        }
    _A_in = _A_setup()

    def _B_setup():
        B, S, D, F = 1, 2048, 640, 1536
        return {
            "x": _w((lambda *s: torch.randn(*s, dtype=torch.bfloat16) * 0.05)(B, S, D)),
            "w1": _w((lambda *s: torch.randn(*s, dtype=torch.bfloat16) * 1.6)(D, F) * (D ** -0.5)),
            "b1": _w((lambda *s: torch.randn(*s, dtype=torch.bfloat16) * 0.4)(F)),
            "w2": _w((lambda *s: torch.randn(*s, dtype=torch.bfloat16) * 0.05)(F, D) * (F ** -0.5)),
            "b2": _w((lambda *s: torch.randn(*s, dtype=torch.bfloat16) * 0.05)(D)),
        }
    _B_in = _B_setup()

    return {
        "A_x": _A_in["x"],
        "A_wqkv": _A_in["wqkv"],
        "A_wo": _A_in["wo"],
        "B_x": _B_in["x"],
        "B_w1": _B_in["w1"],
        "B_b1": _B_in["b1"],
        "B_w2": _B_in["w2"],
        "B_b2": _B_in["b2"],
    }

def reference(A_x, A_wqkv, A_wo, B_x, B_w1, B_b1, B_w2, B_b2):
    def _A_ref(x, wqkv, wo):
        B, S, D = x.shape
        H = 32; Dh = D // H
        qkv = (lambda x, *d: x.permute(*d))((x @ wqkv).reshape(B, S, 3, H, Dh), 2, 0, 3, 1, 4)
        q, k, v = qkv[0], qkv[1], qkv[2]
        phi = lambda t: torch.where(t > 0, t + 1, torch.exp(t)) + 1e-6
        pq, pk = phi(q), phi(k)
        kv = torch.einsum("bhsd,bhse->bhde", pk, v)
        z = torch.einsum("bhsd->bhd", pk)
        num = torch.einsum("bhqd,bhde->bhqe", pq, kv)
        den = torch.einsum("bhqd,bhd->bhq", pq, z)[..., None] + 1e-6
        o = num / den
        return (lambda x, *d: x.permute(*d))(o, 0, 2, 1, 3).reshape(B, S, D) @ wo

    def _B_ref(x, w1, b1, w2, b2):
        y = _layernorm(x, axis=-1)
        h = _gelu(torch.einsum("bsd,df->bsf", y, w1) + b1)
        return torch.einsum("bsf,fd->bsd", h, w2) + b2

    x = A_x
    h = x + _A_ref(_layernorm(x, axis=-1), A_wqkv, A_wo)
    h2 = h + _B_ref(_layernorm(h, axis=-1), B_w1, B_b1, B_w2, B_b2)
    return h2
```

```cpp
#include <hip/hip_runtime.h>
#include <hip/hip_bf16.h>

#define STAMPS 0

#define GAS __attribute__((address_space(1)))
#define LAS __attribute__((address_space(3)))
#define RLX_AGENT __ATOMIC_RELAXED, __HIP_MEMORY_SCOPE_AGENT
typedef unsigned v4u __attribute__((ext_vector_type(4)));
typedef unsigned v2u __attribute__((ext_vector_type(2)));
typedef float f32x4 __attribute__((ext_vector_type(4)));
typedef float f32x2 __attribute__((ext_vector_type(2)));
typedef __bf16 bf16x8 __attribute__((ext_vector_type(8)));
typedef __bf16 bf16x2 __attribute__((ext_vector_type(2)));
typedef short s16x4 __attribute__((ext_vector_type(4)));
typedef GAS unsigned gu32;

namespace {
constexpr int S = 2048, D = 640, F = 1536;
constexpr float LN_EPS = 1e-5f;
constexpr int AUX_SC1 = 16;
constexpr unsigned kSpinCap = 1u << 17;

constexpr int WS_EPOCH = 0;
constexpr int WS_FLAG = 4096;
constexpr int FLAG_GRP = 256, FLAG_HOP = 64 * FLAG_GRP;
constexpr int WS_XN = 131072;
constexpr int WS_O = WS_XN + 128 * 20 * 1024;
constexpr int WS_H = WS_O + 128 * 20 * 1024;
constexpr int WS_H1 = WS_H + 128 * 20 * 1024;
constexpr int WS_KVP = WS_H1 + 128 * 48 * 1024;
constexpr int WS_ST = WS_KVP + 16 * 16 * 4096;
constexpr int WS_DBG = WS_ST + 2048 * 64;
constexpr int WS_BYTES = WS_DBG + 256 * 4 * 32 * 8;

constexpr int L_BKV = 0, L_BQ = 102400;
constexpr int L_RED = 0, L_KVF = 40960;
constexpr int L_WO = 61440;
constexpr int L_W1 = 0, L_CS = 122880;
constexpr int L_W2 = 0;
constexpr int LDS_BYTES = 163840;

__device__ __forceinline__ f32x4 mfma(v4u a, v4u b, f32x4 c) {
  return __builtin_amdgcn_mfma_f32_16x16x32_bf16(__builtin_bit_cast(bf16x8, a), __builtin_bit_cast(bf16x8, b), c, 0, 0, 0);
}
__device__ __forceinline__ unsigned pk2(float lo, float hi) {
  f32x2 v = {lo, hi};
  return __builtin_bit_cast(unsigned, __builtin_convertvector(v, bf16x2));
}
__device__ __forceinline__ float bf_lo(unsigned u) { return __builtin_bit_cast(float, u << 16); }
__device__ __forceinline__ float bf_hi(unsigned u) { return __builtin_bit_cast(float, u & 0xFFFF0000u); }
__device__ __forceinline__ v4u mk_srd(const void* p, unsigned bytes) {
  const unsigned long long a = (unsigned long long)p;
  v4u r;
  r.x = (unsigned)a; r.y = (unsigned)(a >> 32) & 0xFFFFu; r.z = bytes; r.w = 0x00020000u;
  return r;
}
__device__ __forceinline__ void dma16(unsigned lds_addr, unsigned voff, v4u srd, unsigned soff) {
  unsigned keep;
  asm volatile("s_mov_b32 %0, m0\n\ts_mov_b32 m0, %1\n\ts_nop 2\n\tbuffer_load_dwordx4 %2, %3, %4 offen lds\n\ts_mov_b32 m0, %0"
               : "=&s"(keep) : "s"(lds_addr), "v"(voff), "s"(srd), "s"(soff) : "memory");
}
#define DRAIN() asm volatile("s_waitcnt vmcnt(0)" ::: "memory")
#define BAR() asm volatile("s_waitcnt lgkmcnt(0)\n\ts_barrier" ::: "memory")
#define SCHED() __builtin_amdgcn_sched_barrier(0)

template <int W> __device__ __forceinline__ v4u ldw(LAS char* p) {
  const s16x4 a = __builtin_amdgcn_ds_read_tr16_b64_v4i16((LAS s16x4*)p);
  const s16x4 b = __builtin_amdgcn_ds_read_tr16_b64_v4i16((LAS s16x4*)(p + 16 * W));
  const v2u x = __builtin_bit_cast(v2u, a), y = __builtin_bit_cast(v2u, b);
  v4u r; r.x = x.x; r.y = x.y; r.z = y.x; r.w = y.y;
  return r;
}
__device__ __forceinline__ v4u ld128(__amdgpu_buffer_rsrc_t rs, int voff, int soff) {
  return __builtin_amdgcn_raw_buffer_load_b128(rs, voff, soff, AUX_SC1);
}
__device__ __forceinline__ v2u ld64(__amdgpu_buffer_rsrc_t rs, int voff, int soff) {
  return __builtin_amdgcn_raw_buffer_load_b64(rs, voff, soff, AUX_SC1);
}
__device__ __forceinline__ void st128(v4u v, __amdgpu_buffer_rsrc_t rs, int voff, int soff) {
  __builtin_amdgcn_raw_buffer_store_b128(v, rs, voff, soff, AUX_SC1);
}
__device__ __forceinline__ void st64(v2u v, __amdgpu_buffer_rsrc_t rs, int voff, int soff) {
  __builtin_amdgcn_raw_buffer_store_b64(v, rs, voff, soff, AUX_SC1);
}
__device__ __forceinline__ void wait_flags(gu32* line, int n, unsigned tag, int lane) {
  gu32* p = line + (lane < n ? lane : n - 1);
#pragma unroll 1
  for (unsigned spins = 0; spins < kSpinCap; ++spins) {
    const unsigned v = __hip_atomic_load(p, RLX_AGENT);
    if (__all(v == tag)) break;
    __builtin_amdgcn_s_sleep(1);
  }
  __builtin_amdgcn_fence(__ATOMIC_ACQUIRE, "wavefront");
  DRAIN();
}
__device__ __forceinline__ void publish(gu32* flag, unsigned tag, int lane) {
  DRAIN();
  if (lane == 0) __hip_atomic_store(flag, tag, RLX_AGENT);
}
__device__ __forceinline__ float phi(float t) {
  return (t > 0.f ? t + 1.f : __builtin_amdgcn_exp2f(t * 1.4426950408889634f)) + 1e-6f;
}
__device__ __forceinline__ float gelu(float x) {
  const float e = __builtin_amdgcn_exp2f(x * (-2.302208198f - 0.1029432397f * x * x));
  return x * __builtin_amdgcn_rcpf(1.f + e);
}

#define STAMP(i) do { } while (0)

__global__ void __launch_bounds__(256, 1)
fused_block(const unsigned short* __restrict__ x, const unsigned short* __restrict__ wqkv, const unsigned short* __restrict__ wo,
            const unsigned short* __restrict__ w1, const unsigned short* __restrict__ b1, const unsigned short* __restrict__ w2,
            const unsigned short* __restrict__ b2, unsigned short* __restrict__ out, unsigned char* ws) {
  __shared__ __attribute__((aligned(16))) char smem[LDS_BYTES];
  LAS char* L = (LAS char*)smem;
  const unsigned lds0 = (unsigned)(size_t)L;
  const int tid = threadIdx.x, lane = tid & 63, w = __builtin_amdgcn_readfirstlane(tid >> 6);
  const int b = blockIdx.x, xcd = b & 7, bi = b >> 3;
  const int g = lane >> 4, t = lane & 15;
  const __amdgpu_buffer_rsrc_t rs = __builtin_amdgcn_make_buffer_rsrc((void*)ws, 0, WS_BYTES, 0x00020000);
  const __amdgpu_buffer_rsrc_t rx = __builtin_amdgcn_make_buffer_rsrc((void*)x, 0, S * D * 2, 0x00020000);
  gu32* flags = (gu32*)(ws + WS_FLAG);
  STAMP(0);
  const unsigned tag = __hip_atomic_load((gu32*)(ws + WS_EPOCH), RLX_AGENT) + 1u;

  const int r128 = 4 * (xcd >> 1) + (bi >> 3), c16 = 8 * (xcd & 1) + (bi & 7);
  const int r64 = 8 * (xcd >> 1) + (bi >> 2), c8 = 4 * (xcd & 1) + (bi & 3);
  const int grp32 = 4 * r128 + w;

  if (w < 2) {
    const int tok = 8 * b + 4 * w + (lane & 3), c = lane >> 2;
    v4u xv[5];
#pragma unroll
    for (int j = 0; j < 5; ++j) xv[j] = __builtin_amdgcn_raw_buffer_load_b128(rx, tok * 1280 + (c + 16 * j) * 16, 0, 0);
    float f[40];
#pragma unroll
    for (int j = 0; j < 5; ++j)
#pragma unroll
      for (int e = 0; e < 4; ++e) { f[j * 8 + 2 * e] = bf_lo(xv[j][e]); f[j * 8 + 2 * e + 1] = bf_hi(xv[j][e]); }
    float s = 0.f;
#pragma unroll
    for (int j = 0; j < 40; ++j) s += f[j];
#pragma unroll
    for (int m = 4; m < 64; m <<= 1) s += __shfl_xor(s, m);
    const float mean = s * (1.f / D);
    float q = 0.f;
#pragma unroll
    for (int j = 0; j < 40; ++j) { f[j] -= mean; q += f[j] * f[j]; }
#pragma unroll
    for (int m = 4; m < 64; m <<= 1) q += __shfl_xor(q, m);
    const float rstd = 1.f / sqrtf(q * (1.f / D) + LN_EPS);
#pragma unroll
    for (int j = 0; j < 5; ++j) {
      v4u o;
#pragma unroll
      for (int e = 0; e < 4; ++e) o[e] = pk2(f[j * 8 + 2 * e] * rstd, f[j * 8 + 2 * e + 1] * rstd);
      const int ch = c + 16 * j;
      st128(o, rs, WS_XN + ((tok >> 4) * 20 + (ch >> 2)) * 1024 + ((ch & 3) * 16 + (tok & 15)) * 16, 0);
    }
    publish(flags + (0 * FLAG_HOP + (b >> 2) * FLAG_GRP) / 4 + (b & 3) * 2 + w, tag, lane);
  } else {
    const v4u srd = mk_srd(wqkv, D * 3 * D * 2);
    unsigned vo[5];
#pragma unroll
    for (int i = 0; i < 5; ++i) {
      const int ci = i * 64 + lane, rho = ci / 10, c = ci % 10;
      const int k = 8 * ((rho >> 2) & 3) + 4 * (rho >> 4) + (rho & 3);
      vo[i] = k * 3840 + (c < 5 ? 1280 + c * 16 : 2560 + (c - 5) * 16);
    }
    for (int ks = w - 2; ks < 20; ks += 2)
#pragma unroll
      for (int i = 0; i < 5; ++i) dma16(lds0 + L_BKV + ks * 5120 + i * 1024, vo[i], srd, ks * 32 * 3840 + c16 * 80);
#pragma unroll
    for (int i = 0; i < 3; ++i) {
      const int ci = i * 64 + lane, rho = ci / 6, c = ci % 6;
      const int k = 8 * ((rho >> 2) & 3) + 4 * (rho >> 4) + (rho & 3);
      vo[i] = k * 3840 + c * 16;
    }
    for (int ks = w - 2; ks < 20; ks += 2)
#pragma unroll
      for (int i = 0; i < 3; ++i) dma16(lds0 + L_BQ + ks * 3072 + i * 1024, vo[i], srd, ks * 32 * 3840 + c16 * 80);
  }
  STAMP(1);

  {
    wait_flags(flags + (0 * FLAG_HOP + grp32 * FLAG_GRP) / 4, 8, tag, lane);
    STAMP(2);
    v4u a[20][2];
#pragma unroll
    for (int ks = 0; ks < 20; ++ks)
#pragma unroll
      for (int m = 0; m < 2; ++m) a[ks][m] = ld128(rs, lane * 16, WS_XN + ((2 * grp32 + m) * 20 + ks) * 1024);
    SCHED();
    BAR();
    {
      f32x4 acc[2][5];
#pragma unroll
      for (int m = 0; m < 2; ++m)
#pragma unroll
        for (int s = 0; s < 5; ++s) acc[m][s] = f32x4{0.f, 0.f, 0.f, 0.f};
      LAS char* bp = L + L_BKV + (lane >> 2) * 160 + (lane & 3) * 8;
      v4u bc[5], bn[5];
#pragma unroll
      for (int s = 0; s < 5; ++s) bc[s] = ldw<160>(bp + s * 32);
#pragma unroll
      for (int ks = 0; ks < 20; ++ks) {
        if (ks + 1 < 20)
#pragma unroll
          for (int s = 0; s < 5; ++s) bn[s] = ldw<160>(bp + (ks + 1) * 5120 + s * 32);
#pragma unroll
        for (int s = 0; s < 5; ++s)
#pragma unroll
          for (int m = 0; m < 2; ++m) acc[m][s] = mfma(a[ks][m], bc[s], acc[m][s]);
#pragma unroll
        for (int s = 0; s < 5; ++s) bc[s] = bn[s];
        SCHED();
      }
      STAMP(3);
      v4u kf[5];
#pragma unroll
      for (int s = 0; s < 5; ++s) {
        float v[8];
#pragma unroll
        for (int m = 0; m < 2; ++m)
#pragma unroll
          for (int r = 0; r < 4; ++r) {
            const float u = acc[m][s][r];
            v[m * 4 + r] = s < 2 ? phi(u) : (s == 2 ? (t < 8 ? phi(u) : u) : u);
          }
#pragma unroll
        for (int e = 0; e < 4; ++e) kf[s][e] = pk2(v[2 * e], v[2 * e + 1]);
      }
      const v4u ones = {0x3F803F80u, 0x3F803F80u, 0x3F803F80u, 0x3F803F80u};
      const f32x4 z4 = {0.f, 0.f, 0.f, 0.f};
      f32x4 T[10];
      T[0] = mfma(kf[0], kf[2], z4); T[1] = mfma(kf[0], kf[3], z4); T[2] = mfma(kf[1], kf[2], z4); T[3] = mfma(kf[1], kf[3], z4);
      T[4] = mfma(kf[1], kf[4], z4); T[5] = mfma(kf[2], kf[3], z4); T[6] = mfma(kf[2], kf[4], z4);
      T[7] = mfma(kf[0], ones, z4); T[8] = mfma(kf[1], ones, z4); T[9] = mfma(kf[2], ones, z4);
      BAR();
      LAS f32x4* red = (LAS f32x4*)(L + L_RED);
#pragma unroll
      for (int i = 0; i < 10; ++i) red[(w * 10 + i) * 64 + lane] = T[i];
      ((LAS v4u*)(L + L_KVF))[tid] = v4u{0u, 0u, 0u, 0u};
      if (tid < 128) ((LAS v4u*)(L + L_KVF))[256 + tid] = v4u{0u, 0u, 0u, 0u};
      BAR();
    }
    const int it = tid < 210 ? tid : 209;
    const int ie = it / 5, ic = it % 5;
    const int ihead = ie < 40 ? (ie >= 20) : ie - 40;
    const int id = 20 * ihead + 4 * ic;
    {
      const int sa = id >> 4, col = 40 + ie;
      const int tile = ie < 40 ? 2 * sa + (col >> 4) - 2 : 7 + sa;
      const int ln = ((id >> 2) & 3) * 16 + (ie < 40 ? (col & 15) : 0);
      LAS f32x4* red = (LAS f32x4*)(L + L_RED);
      f32x4 v = red[tile * 64 + ln];
#pragma unroll
      for (int ww = 1; ww < 4; ++ww) v += red[(ww * 10 + tile) * 64 + ln];
      if (tid < 210) st128(__builtin_bit_cast(v4u, v), rs, tid * 16, WS_KVP + (c16 * 16 + r128) * 4096);
      publish(flags + (1 * FLAG_HOP + c16 * FLAG_GRP) / 4 + r128 * 4 + w, tag, lane);
    }
    STAMP(4);
    v4u pq0[2], pq1[2];
    {
      f32x4 acc[2][3];
#pragma unroll
      for (int m = 0; m < 2; ++m)
#pragma unroll
        for (int s = 0; s < 3; ++s) acc[m][s] = f32x4{0.f, 0.f, 0.f, 0.f};
      LAS char* bp = L + L_BQ + (lane >> 2) * 96 + (lane & 3) * 8;
      v4u bc[3], bn[3];
#pragma unroll
      for (int s = 0; s < 3; ++s) bc[s] = ldw<96>(bp + s * 32);
#pragma unroll
      for (int ks = 0; ks < 20; ++ks) {
        if (ks + 1 < 20)
#pragma unroll
          for (int s = 0; s < 3; ++s) bn[s] = ldw<96>(bp + (ks + 1) * 3072 + s * 32);
#pragma unroll
        for (int s = 0; s < 3; ++s)
#pragma unroll
          for (int m = 0; m < 2; ++m) acc[m][s] = mfma(bc[s], a[ks][m], acc[m][s]);
#pragma unroll
        for (int s = 0; s < 3; ++s) bc[s] = bn[s];
        SCHED();
      }
#pragma unroll
      for (int m = 0; m < 2; ++m) {
        pq0[m].x = pk2(phi(acc[m][0][0]), phi(acc[m][0][1])); pq0[m].y = pk2(phi(acc[m][0][2]), phi(acc[m][0][3]));
        pq0[m].z = pk2(phi(acc[m][1][0]), phi(acc[m][1][1])); pq0[m].w = pk2(phi(acc[m][1][2]), phi(acc[m][1][3]));
        const unsigned lo = pk2(phi(acc[m][2][0]), phi(acc[m][2][1])), hi = pk2(phi(acc[m][2][2]), phi(acc[m][2][3]));
        pq1[m].x = g < 2 ? lo : 0u; pq1[m].y = g < 2 ? hi : 0u; pq1[m].z = 0u; pq1[m].w = 0u;
      }
    }
    STAMP(5);
    wait_flags(flags + (1 * FLAG_HOP + c16 * FLAG_GRP) / 4, 64, tag, lane);
    STAMP(6);
    {
      v4u pv[16];
#pragma unroll
      for (int r = 0; r < 16; ++r) pv[r] = ld128(rs, it * 16, WS_KVP + (c16 * 16 + r) * 4096);
      f32x4 v = __builtin_bit_cast(f32x4, pv[0]);
#pragma unroll
      for (int r = 1; r < 16; ++r) v += __builtin_bit_cast(f32x4, pv[r]);
      const int dg = id >> 2;
      v2u o; o.x = pk2(v[0], v[1]); o.y = pk2(v[2], v[3]);
      if (tid < 210)
        *(LAS v2u*)(L + L_KVF + ((ie >> 4) * 2 + (dg >> 3)) * 1024 + ((dg & 3) * 16 + (ie & 15)) * 16 + ((dg >> 2) & 1) * 8) = o;
    }
    BAR();
    {
      v4u kv[3][2];
#pragma unroll
      for (int es = 0; es < 3; ++es)
#pragma unroll
        for (int ks = 0; ks < 2; ++ks) kv[es][ks] = *(LAS v4u*)(L + L_KVF + (es * 2 + ks) * 1024 + lane * 16);
#pragma unroll
      for (int m = 0; m < 2; ++m) {
        f32x4 o[3];
#pragma unroll
        for (int es = 0; es < 3; ++es) {
          o[es] = mfma(kv[es][0], pq0[m], f32x4{0.f, 0.f, 0.f, 0.f});
          o[es] = mfma(kv[es][1], pq1[m], o[es]);
        }
        const float d0 = __shfl(o[2][0], 32 + t) + 1e-6f, d1 = __shfl(o[2][1], 32 + t) + 1e-6f;
        const float i0 = 1.f / d0, i1 = 1.f / d1;
        const float im = g == 0 ? i0 : i1;
        v4u ov;
        ov.x = pk2(o[0][0] * i0, o[0][1] * i0); ov.y = pk2(o[0][2] * i0, o[0][3] * i0);
        ov.z = pk2(o[1][0] * im, o[1][1] * im); ov.w = pk2(o[1][2] * im, o[1][3] * im);
        v2u ol; ol.x = pk2(o[2][0] * i1, o[2][1] * i1); ol.y = pk2(o[2][2] * i1, o[2][3] * i1);
        const int tt = 2 * grp32 + m;
        st128(ov, rs, lane * 16, WS_O + (tt * 20 + c16) * 1024);
        if (g < 2) st64(ol, rs, ((2 * (c16 & 1) + g) * 16 + t) * 16 + ((c16 >> 1) & 1) * 8, WS_O + (tt * 20 + 16 + (c16 >> 2)) * 1024);
      }
      publish(flags + (2 * FLAG_HOP + grp32 * FLAG_GRP) / 4 + c16, tag, lane);
    }
    STAMP(7);
  }

  {
    const v4u srd = mk_srd(wo, D * D * 2);
    unsigned vo[5], vo2[5];
#pragma unroll
    for (int i = 0; i < 5; ++i) {
      const int ci = i * 64 + lane, rho = ci / 10, c = ci % 10;
      vo[i] = rho * 1280 + c * 16;
      vo2[i] = (40 * (rho >> 3) + (rho & 7)) * 1280 + c * 16;
    }
    for (int ks = w; ks < 16; ks += 4)
#pragma unroll
      for (int i = 0; i < 5; ++i) dma16(lds0 + L_WO + ks * 5120 + i * 1024, vo[i], srd, 40 * ks * 1280 + c8 * 160);
#pragma unroll
    for (int i = 0; i < 5; ++i) dma16(lds0 + L_WO + (16 + w) * 5120 + i * 1024, vo2[i], srd, (160 * w + 32) * 1280 + c8 * 160);
    const int tt = 4 * r64 + w, tok = tt * 16 + t;
    v2u xr[5];
#pragma unroll
    for (int s = 0; s < 5; ++s) xr[s] = __builtin_amdgcn_raw_buffer_load_b64(rx, tok * 1280 + (80 * c8 + 16 * s + 4 * g) * 2, 0, 0);
    wait_flags(flags + (2 * FLAG_HOP + (tt >> 1) * FLAG_GRP) / 4, 16, tag, lane);
    STAMP(8);
    if (b == 0 && tid == 0) __hip_atomic_store((gu32*)(ws + WS_EPOCH), tag, RLX_AGENT);
    v4u a[20];
#pragma unroll
    for (int ks = 0; ks < 20; ++ks) a[ks] = ld128(rs, lane * 16, WS_O + (tt * 20 + ks) * 1024);
    SCHED();
    BAR();
    f32x4 acc[5];
#pragma unroll
    for (int s = 0; s < 5; ++s) acc[s] = f32x4{0.f, 0.f, 0.f, 0.f};
    LAS char* bp = L + L_WO + (lane >> 2) * 160 + (lane & 3) * 8;
    v4u bc[5], bn[5];
#pragma unroll
    for (int s = 0; s < 5; ++s) bc[s] = ldw<160>(bp + s * 32);
#pragma unroll
    for (int ks = 0; ks < 20; ++ks) {
      if (ks + 1 < 20)
#pragma unroll
        for (int s = 0; s < 5; ++s) bn[s] = ldw<160>(bp + (ks + 1) * 5120 + s * 32);
#pragma unroll
      for (int s = 0; s < 5; ++s) acc[s] = mfma(bc[s], a[ks], acc[s]);
#pragma unroll
      for (int s = 0; s < 5; ++s) bc[s] = bn[s];
      SCHED();
    }
    BAR();
    STAMP(9);
    v2u hb[5];
    float hv[20];
    float sm = 0.f;
#pragma unroll
    for (int s = 0; s < 5; ++s) {
      hb[s].x = pk2(acc[s][0] + bf_lo(xr[s].x), acc[s][1] + bf_hi(xr[s].x));
      hb[s].y = pk2(acc[s][2] + bf_lo(xr[s].y), acc[s][3] + bf_hi(xr[s].y));
      hv[4 * s] = bf_lo(hb[s].x); hv[4 * s + 1] = bf_hi(hb[s].x); hv[4 * s + 2] = bf_lo(hb[s].y); hv[4 * s + 3] = bf_hi(hb[s].y);
      sm += (hv[4 * s] + hv[4 * s + 1]) + (hv[4 * s + 2] + hv[4 * s + 3]);
    }
    st128(v4u{hb[0].x, hb[0].y, hb[1].x, hb[1].y}, rs, lane * 16, WS_H + (tt * 20 + 2 * c8) * 1024);
    st128(v4u{hb[2].x, hb[2].y, hb[3].x, hb[3].y}, rs, lane * 16, WS_H + (tt * 20 + 2 * c8 + 1) * 1024);
    st64(hb[4], rs, lane * 16 + (c8 & 1) * 8, WS_H + (tt * 20 + 16 + (c8 >> 1)) * 1024);
    sm += __shfl_xor(sm, 16); sm += __shfl_xor(sm, 32);
    const float mc = sm * (1.f / 80.f);
    float m2 = 0.f;
#pragma unroll
    for (int j = 0; j < 20; ++j) { const float d = hv[j] - mc; m2 += d * d; }
    m2 += __shfl_xor(m2, 16); m2 += __shfl_xor(m2, 32);
    if (g == 0) st64(v2u{__builtin_bit_cast(unsigned, mc), __builtin_bit_cast(unsigned, m2)}, rs, tok * 64 + c8 * 8, WS_ST);
    publish(flags + (3 * FLAG_HOP + (tt >> 1) * FLAG_GRP) / 4 + (tt & 1) * 8 + c8, tag, lane);
    STAMP(10);
  }

  {
    const v4u srd = mk_srd(w1, D * F * 2);
    unsigned vo[6], vo2[6];
#pragma unroll
    for (int i = 0; i < 6; ++i) {
      const int ci = i * 64 + lane, rho = ci / 12, c = (ci % 12) ^ (2 * ((rho >> 2) & 1));
      vo[i] = rho * 3072 + c * 16;
      vo2[i] = (80 * (rho >> 4) + (rho & 15)) * 3072 + c * 16;
    }
    for (int ks = w; ks < 16; ks += 4)
#pragma unroll
      for (int i = 0; i < 6; ++i) dma16(lds0 + L_W1 + ks * 6144 + i * 1024, vo[i], srd, (80 * (ks >> 1) + 32 * (ks & 1)) * 3072 + c16 * 192);
#pragma unroll
    for (int i = 0; i < 6; ++i) dma16(lds0 + L_W1 + (16 + w) * 6144 + i * 1024, vo2[i], srd, (160 * w + 64) * 3072 + c16 * 192);
    v2u bb[6];
#pragma unroll
    for (int s = 0; s < 6; ++s) bb[s] = *(const v2u*)(b1 + 96 * c16 + 16 * s + 4 * g);
    wait_flags(flags + (3 * FLAG_HOP + grp32 * FLAG_GRP) / 4, 16, tag, lane);
    STAMP(11);
    v4u stt[2];
#pragma unroll
    for (int m = 0; m < 2; ++m) stt[m] = ld128(rs, ((2 * grp32 + m) * 16 + t) * 64 + g * 16, WS_ST);
    v4u a[20][2];
#pragma unroll
    for (int ks = 0; ks < 20; ++ks)
#pragma unroll
      for (int m = 0; m < 2; ++m) a[ks][m] = ld128(rs, lane * 16, WS_H + ((2 * grp32 + m) * 20 + ks) * 1024);
    SCHED();
    BAR();
    LAS char* bp0 = L + L_W1 + (lane >> 2) * 192 + (lane & 3) * 8 + (g & 1) * 32;
    LAS char* bp1 = L + L_W1 + (lane >> 2) * 192 + (lane & 3) * 8 - (g & 1) * 32;
    {
      const v4u ones = {0x3F803F80u, 0x3F803F80u, 0x3F803F80u, 0x3F803F80u};
      f32x4 cs[6];
#pragma unroll
      for (int s = 0; s < 6; ++s) cs[s] = f32x4{0.f, 0.f, 0.f, 0.f};
#pragma unroll
      for (int kk = 0; kk < 5; ++kk)
#pragma unroll
        for (int s = 0; s < 6; ++s) cs[s] = mfma(ldw<192>(((s & 1) ? bp1 : bp0) + (w * 5 + kk) * 6144 + s * 32), ones, cs[s]);
      if (t == 0)
#pragma unroll
        for (int s = 0; s < 6; ++s) *(LAS f32x4*)(L + L_CS + ((w * 6 + s) * 4 + g) * 16) = cs[s];
      SCHED();
    }
    f32x4 acc[2][6];
#pragma unroll
    for (int m = 0; m < 2; ++m)
#pragma unroll
      for (int s = 0; s < 6; ++s) acc[m][s] = f32x4{0.f, 0.f, 0.f, 0.f};
    v4u bc[6], bn[6];
#pragma unroll
    for (int s = 0; s < 6; ++s) bc[s] = ldw<192>(((s & 1) ? bp1 : bp0) + s * 32);
#pragma unroll
    for (int ks = 0; ks < 20; ++ks) {
      if (ks + 1 < 20)
#pragma unroll
        for (int s = 0; s < 6; ++s) bn[s] = ldw<192>(((s & 1) ? bp1 : bp0) + (ks + 1) * 6144 + s * 32);
#pragma unroll
      for (int s = 0; s < 6; ++s)
#pragma unroll
        for (int m = 0; m < 2; ++m) acc[m][s] = mfma(bc[s], a[ks][m], acc[m][s]);
#pragma unroll
      for (int s = 0; s < 6; ++s) bc[s] = bn[s];
      SCHED();
    }
    BAR();
    STAMP(12);
    f32x4 cs[6];
#pragma unroll
    for (int s = 0; s < 6; ++s) {
      cs[s] = *(LAS f32x4*)(L + L_CS + (s * 4 + g) * 16);
#pragma unroll
      for (int ww = 1; ww < 4; ++ww) cs[s] += *(LAS f32x4*)(L + L_CS + ((ww * 6 + s) * 4 + g) * 16);
    }
#pragma unroll
    for (int m = 0; m < 2; ++m) {
      const f32x4 sv = __builtin_bit_cast(f32x4, stt[m]);
      float ms = sv[0] + sv[2];
      ms += __shfl_xor(ms, 16); ms += __shfl_xor(ms, 32);
      const float mean = ms * 0.125f;
      float m2 = sv[1] + sv[3] + 80.f * ((sv[0] - mean) * (sv[0] - mean) + (sv[2] - mean) * (sv[2] - mean));
      m2 += __shfl_xor(m2, 16); m2 += __shfl_xor(m2, 32);
      const float var = m2 * (1.f / D);
      const float r1 = 1.f / sqrtf(var + LN_EPS);
      const float rr = r1 / sqrtf(var * r1 * r1 + LN_EPS);
      v2u hb[6];
#pragma unroll
      for (int s = 0; s < 6; ++s) {
        const float p0 = (acc[m][s][0] - mean * cs[s][0]) * rr + bf_lo(bb[s].x), p1 = (acc[m][s][1] - mean * cs[s][1]) * rr + bf_hi(bb[s].x);
        const float p2 = (acc[m][s][2] - mean * cs[s][2]) * rr + bf_lo(bb[s].y), p3 = (acc[m][s][3] - mean * cs[s][3]) * rr + bf_hi(bb[s].y);
        hb[s].x = pk2(gelu(p0), gelu(p1)); hb[s].y = pk2(gelu(p2), gelu(p3));
      }
#pragma unroll
      for (int j = 0; j < 3; ++j)
        st128(v4u{hb[2 * j].x, hb[2 * j].y, hb[2 * j + 1].x, hb[2 * j + 1].y}, rs, lane * 16, WS_H1 + ((2 * grp32 + m) * 48 + 3 * c16 + j) * 1024);
    }
    publish(flags + (4 * FLAG_HOP + grp32 * FLAG_GRP) / 4 + c16, tag, lane);
    BAR();
    STAMP(13);
  }

  {
    const v4u srd = mk_srd(w2, F * D * 2);
    unsigned vo[3];
#pragma unroll
    for (int i = 0; i < 3; ++i) {
      const int ci = i * 64 + lane, rho = ci / 6, c = ci % 6;
      vo[i] = rho * 1280 + c * 16;
    }
    for (int ks = w; ks < 48; ks += 4)
#pragma unroll
      for (int i = 0; i < 3; ++i) dma16(lds0 + L_W2 + ks * 3072 + i * 1024, vo[i], srd, 32 * ks * 1280 + c16 * 80);
    v2u hr[2][3], bb[3];
#pragma unroll
    for (int s = 0; s < 3; ++s) {
      int n = 40 * c16 + 16 * s + 4 * g;
      if (s == 2 && g >= 2) n = 40 * c16;
      bb[s] = *(const v2u*)(b2 + n);
      const int cc = n / 80, off = n % 80, ss = off >> 4, gg = (off >> 2) & 3;
      const int ks = ss < 4 ? 2 * cc + (ss >> 1) : 16 + (cc >> 1), hf = ss < 4 ? (ss & 1) : (cc & 1);
#pragma unroll
      for (int m = 0; m < 2; ++m) hr[m][s] = ld64(rs, ks * 1024 + (gg * 16 + t) * 16 + hf * 8, WS_H + (2 * grp32 + m) * 20 * 1024);
    }
    wait_flags(flags + (4 * FLAG_HOP + grp32 * FLAG_GRP) / 4, 16, tag, lane);
    STAMP(14);
    constexpr int WIN = 24;
    v4u a[WIN][2];
#pragma unroll
    for (int ks = 0; ks < WIN; ++ks)
#pragma unroll
      for (int m = 0; m < 2; ++m) a[ks][m] = ld128(rs, lane * 16, WS_H1 + ((2 * grp32 + m) * 48 + ks) * 1024);
    SCHED();
    BAR();
    f32x4 acc[2][3];
#pragma unroll
    for (int m = 0; m < 2; ++m)
#pragma unroll
      for (int s = 0; s < 3; ++s) acc[m][s] = f32x4{0.f, 0.f, 0.f, 0.f};
    LAS char* bp = L + L_W2 + (lane >> 2) * 96 + (lane & 3) * 8;
    v4u bc[3], bn[3];
#pragma unroll
    for (int s = 0; s < 3; ++s) bc[s] = ldw<96>(bp + s * 32);
#pragma unroll
    for (int ks = 0; ks < 48; ++ks) {
      if (ks + 1 < 48)
#pragma unroll
        for (int s = 0; s < 3; ++s) bn[s] = ldw<96>(bp + (ks + 1) * 3072 + s * 32);
#pragma unroll
      for (int s = 0; s < 3; ++s)
#pragma unroll
        for (int m = 0; m < 2; ++m) acc[m][s] = mfma(bc[s], a[ks % WIN][m], acc[m][s]);
      if (ks + WIN < 48)
#pragma unroll
        for (int m = 0; m < 2; ++m) a[ks % WIN][m] = ld128(rs, lane * 16, WS_H1 + ((2 * grp32 + m) * 48 + ks + WIN) * 1024);
#pragma unroll
      for (int s = 0; s < 3; ++s) bc[s] = bn[s];
      SCHED();
    }
    STAMP(15);
#pragma unroll
    for (int m = 0; m < 2; ++m) {
      const int tok = (2 * grp32 + m) * 16 + t;
#pragma unroll
      for (int s = 0; s < 3; ++s) {
        v2u o;
        o.x = pk2(acc[m][s][0] + bf_lo(bb[s].x) + bf_lo(hr[m][s].x), acc[m][s][1] + bf_hi(bb[s].x) + bf_hi(hr[m][s].x));
        o.y = pk2(acc[m][s][2] + bf_lo(bb[s].y) + bf_lo(hr[m][s].y), acc[m][s][3] + bf_hi(bb[s].y) + bf_hi(hr[m][s].y));
        if (s < 2 || g < 2) *(v2u*)(out + tok * D + 40 * c16 + 16 * s + 4 * g) = o;
      }
    }
    STAMP(16);
  }
}
}

extern "C" void kernel_launch(void* const* d_in, const int* in_sizes, int n_in, void* d_out, int out_size, void* d_ws,
                              size_t ws_size, hipStream_t stream) {
  const unsigned short* x = (const unsigned short*)d_in[0];
  const unsigned short* wqkv = (const unsigned short*)d_in[1];
  const unsigned short* wo = (const unsigned short*)d_in[2];
  const unsigned short* w1 = (const unsigned short*)d_in[4];
  const unsigned short* b1 = (const unsigned short*)d_in[5];
  const unsigned short* w2 = (const unsigned short*)d_in[6];
  const unsigned short* b2 = (const unsigned short*)d_in[7];
  fused_block<<<256, 256, 0, stream>>>(x, wqkv, wo, w1, b1, w2, b2, (unsigned short*)d_out, (unsigned char*)d_ws);
}
```

```cpp
#include <hip/hip_runtime.h>
#include <hip/hip_bf16.h>

#define STAMPS 0
#define THR 0
#define TOUCH 0
#define HPRIO 0
#define WOLATE 0
#define SLEEP 1

#define GAS __attribute__((address_space(1)))
#define LAS __attribute__((address_space(3)))
#define RLX_AGENT __ATOMIC_RELAXED, __HIP_MEMORY_SCOPE_AGENT
typedef unsigned v4u __attribute__((ext_vector_type(4)));
typedef unsigned v2u __attribute__((ext_vector_type(2)));
typedef float f32x4 __attribute__((ext_vector_type(4)));
typedef float f32x2 __attribute__((ext_vector_type(2)));
typedef __bf16 bf16x8 __attribute__((ext_vector_type(8)));
typedef __bf16 bf16x2 __attribute__((ext_vector_type(2)));
typedef short s16x4 __attribute__((ext_vector_type(4)));
typedef GAS unsigned gu32;

namespace {
constexpr int S = 2048, D = 640, F = 1536;
constexpr float LN_EPS = 1e-5f;
constexpr int AUX_SC1 = 16;
constexpr unsigned kSpinCap = 1u << 17;
constexpr int OOB = 0x7FFFFF00;

constexpr int WS_EPOCH = 0;
constexpr int WS_FLAG = 4096;
constexpr int FLAG_SET = 64 * 256;
constexpr int WS_XN = 1 << 20;
constexpr int WS_O = WS_XN + 128 * 20 * 1024;
constexpr int WS_H = WS_O + 128 * 20 * 1024;
constexpr int WS_H1 = WS_H + 128 * 20 * 1024;
constexpr int WS_KVP = WS_H1 + 128 * 48 * 1024;
constexpr int WS_ST = WS_KVP + 16 * 16 * 4096;
constexpr int WS_DBG = WS_ST + 2048 * 64;
constexpr int WS_TKV = WS_DBG + 256 * 8 * 32 * 8;
constexpr int WS_TO = WS_TKV + 16 * 153600;
constexpr int WS_T1 = WS_TO + 8 * 102400;
constexpr int WS_T2 = WS_T1 + 16 * 122880;
constexpr int WS_CS = WS_T2 + 16 * 122880;
constexpr int WS_BYTES = WS_CS + 16 * 16 * 384;

constexpr int L_BKV = 0, L_BQ = 102400;
constexpr int L_RED = 0, L_KVF = 153600;
constexpr int L_WO = 0, L_W1 = 30720, L_W2 = 0, L_CS = 161792;
constexpr int LDS_BYTES = 163840;

__device__ __forceinline__ f32x4 mfma(v4u a, v4u b, f32x4 c) {
  return __builtin_amdgcn_mfma_f32_16x16x32_bf16(__builtin_bit_cast(bf16x8, a), __builtin_bit_cast(bf16x8, b), c, 0, 0, 0);
}
__device__ __forceinline__ unsigned pk2(f32x2 v) { return __builtin_bit_cast(unsigned, __builtin_convertvector(v, bf16x2)); }
__device__ __forceinline__ unsigned pk2(float lo, float hi) { return pk2(f32x2{lo, hi}); }
__device__ __forceinline__ float bf_lo(unsigned u) { return __builtin_bit_cast(float, u << 16); }
__device__ __forceinline__ float bf_hi(unsigned u) { return __builtin_bit_cast(float, u & 0xFFFF0000u); }
__device__ __forceinline__ f32x2 bf2(unsigned u) { return f32x2{bf_lo(u), bf_hi(u)}; }
__device__ __forceinline__ f32x2 lo2(f32x4 v) { return f32x2{v[0], v[1]}; }
__device__ __forceinline__ f32x2 hi2(f32x4 v) { return f32x2{v[2], v[3]}; }
__device__ __forceinline__ v4u mk_srd(const void* p, unsigned bytes) {
  const unsigned long long a = (unsigned long long)p;
  v4u r;
  r.x = (unsigned)a; r.y = (unsigned)(a >> 32) & 0xFFFFu; r.z = bytes; r.w = 0x00020000u;
  return r;
}
#define DRAIN() asm volatile("s_waitcnt vmcnt(0)" ::: "memory")
#define BAR() asm volatile("s_waitcnt lgkmcnt(0)\n\ts_barrier" ::: "memory")
#define BAR_RAW() asm volatile("s_barrier" ::: "memory")
#define SCHED() __builtin_amdgcn_sched_barrier(0)
#define PIN(x) asm volatile("" : "+v"(x))

__device__ __forceinline__ void dma2(unsigned lds_addr, unsigned voff, v4u srd, unsigned soff) {
  unsigned keep;
  asm volatile("s_mov_b32 %0, m0\n\ts_mov_b32 m0, %1\n\ts_nop 0\n\t"
               "buffer_load_dwordx4 %2, %3, %4 offen sc1 lds\n\t"
               "buffer_load_dwordx4 %2, %3, %4 offen offset:1024 sc1 lds\n\t"
               "s_mov_b32 m0, %0"
               : "=&s"(keep) : "s"(lds_addr), "v"(voff), "s"(srd), "s"(soff) : "memory");
}
template <int LO, int HI, int TH = 0, int NW = 4>
__device__ __forceinline__ void dma_image(unsigned lds_base, unsigned voff, v4u srd, int ws_off, int hw, int rot) {
  constexpr int NP = (HI - LO) / 2;
#pragma unroll 1
  for (int i = hw; i < NP; i += NW) {
    int j = i + rot;
    if (j >= NP) j -= NP;
    dma2(lds_base + LO * 1024 + j * 2048, voff, srd, ws_off + LO * 1024 + j * 2048);
    if (TH) asm volatile("s_waitcnt vmcnt(%0)" :: "i"(TH - 2) : "memory");
  }
}
__device__ __forceinline__ v2u rdtr(LAS char* p) { return __builtin_bit_cast(v2u, __builtin_amdgcn_ds_read_tr16_b64_v4i16((LAS s16x4*)p)); }
struct LBase {
  unsigned a[3];
  __device__ __forceinline__ explicit LBase(unsigned v) { a[0] = v; a[1] = v + 65536; a[2] = v + 131072; PIN(a[0]); PIN(a[1]); PIN(a[2]); }
  __device__ __forceinline__ v2u rd(int off) const { return rdtr((LAS char*)(a[off >> 16]) + (off & 65535)); }
};
template <int MT, int NT, int KS, int PD, bool WA, class RB, class GA, class PRE>
__device__ __forceinline__ void kloop(f32x4 (&acc)[MT][NT], RB rb, GA ga, PRE pre) {
  v2u bl[PD + 1][NT], bh[PD + 1][NT];
#pragma unroll
  for (int m = 0; m < MT; ++m)
#pragma unroll
    for (int s = 0; s < NT; ++s) acc[m][s] = f32x4{0.f, 0.f, 0.f, 0.f};
#pragma unroll
  for (int ks = 0; ks < PD; ++ks)
#pragma unroll
    for (int s = 0; s < NT; ++s) { bl[ks][s] = rb(ks, s, 0); bh[ks][s] = rb(ks, s, 1); }
  SCHED();
#pragma unroll
  for (int ks = 0; ks < KS; ++ks) {
    pre(ks);
#pragma unroll
    for (int s = 0; s < NT; ++s)
#pragma unroll
      for (int m = 0; m < MT; ++m) {
        const v2u lo = bl[ks % (PD + 1)][s], hi = bh[ks % (PD + 1)][s];
        const v4u wv = {lo.x, lo.y, hi.x, hi.y};
        acc[m][s] = WA ? mfma(wv, ga(ks, m), acc[m][s]) : mfma(ga(ks, m), wv, acc[m][s]);
        if (ks + PD < KS) {
          if (MT == 1 || m == 0) bl[(ks + PD) % (PD + 1)][s] = rb(ks + PD, s, 0);
          if (MT == 1 || m == 1) bh[(ks + PD) % (PD + 1)][s] = rb(ks + PD, s, 1);
        }
        SCHED();
      }
  }
}
__device__ __forceinline__ v4u ld128(__amdgpu_buffer_rsrc_t rs, int voff, int soff) {
  return __builtin_amdgcn_raw_buffer_load_b128(rs, voff, soff, AUX_SC1);
}
__device__ __forceinline__ v2u ld64(__amdgpu_buffer_rsrc_t rs, int voff, int soff) {
  return __builtin_amdgcn_raw_buffer_load_b64(rs, voff, soff, AUX_SC1);
}
__device__ __forceinline__ void st128(v4u v, __amdgpu_buffer_rsrc_t rs, int voff, int soff) {
  __builtin_amdgcn_raw_buffer_store_b128(v, rs, (int)((unsigned)voff + (unsigned)soff), 0, AUX_SC1);
}
__device__ __forceinline__ int sel(bool c, int a) {
  PIN(a);
  return c ? a : OOB;
}
__device__ __forceinline__ v4u ld_split(__amdgpu_buffer_rsrc_t rs, int lane, int soff) {
  const v2u a = ld64(rs, lane * 8, soff), b = ld64(rs, lane * 8 + 512, soff);
  return v4u{a.x, a.y, b.x, b.y};
}
__device__ __forceinline__ void st64(v2u v, __amdgpu_buffer_rsrc_t rs, int voff, int soff) {
  __builtin_amdgcn_raw_buffer_store_b64(v, rs, voff, soff, AUX_SC1);
}
__device__ __forceinline__ void flag_set(gu32* p, unsigned tag) {
  asm volatile("global_atomic_swap %0, %1, off sc1" :: "v"(p), "v"(tag) : "memory");
}
__device__ __forceinline__ void wait_flag(gu32* p, int n, unsigned tag, int lane) {
#pragma unroll 1
  for (unsigned spins = 0; spins < kSpinCap; ++spins) {
    unsigned v = tag;
    if (lane < n) asm volatile("global_atomic_add %0, %1, %2, off sc0 sc1\n\ts_waitcnt vmcnt(0)" : "=&v"(v) : "v"(p), "v"(0u) : "memory");
    if (__all(v == tag)) break;
    if (SLEEP) __builtin_amdgcn_s_sleep(SLEEP);
  }
  asm volatile("" ::: "memory");
}
__device__ __forceinline__ void sig_wait(volatile LAS unsigned* p, unsigned tag) {
#pragma unroll 1
  for (unsigned spins = 0; spins < 64 * kSpinCap; ++spins) {
    if (*p == tag) break;
    __builtin_amdgcn_s_sleep(1);
  }
  asm volatile("" ::: "memory");
}
__device__ __forceinline__ void publish(gu32* flag, unsigned tag, int lane) {
  DRAIN();
  if (lane == 0) flag_set(flag, tag);
}
__device__ __forceinline__ f32x4 add4(f32x4 a, f32x4 b) {
  f32x4 r;
#pragma unroll
  for (int i = 0; i < 4; ++i) { float x; asm("v_add_f32 %0, %1, %2" : "=v"(x) : "v"(a[i]), "v"(b[i])); r[i] = x; }
  return r;
}
__device__ __forceinline__ float xsum32(float x) {
  unsigned u = __builtin_bit_cast(unsigned, x), v = u;
  asm("" : "+v"(v));
  auto r = __builtin_amdgcn_permlane32_swap(u, v, false, false);
  const unsigned a = r[0], b = r[1];
  return __builtin_bit_cast(float, a) + __builtin_bit_cast(float, b);
}
__device__ __forceinline__ float xsum16(float x) {
  unsigned u = __builtin_bit_cast(unsigned, x), v = u;
  asm("" : "+v"(v));
  auto r = __builtin_amdgcn_permlane16_swap(u, v, false, false);
  const unsigned a = r[0], b = r[1];
  return __builtin_bit_cast(float, a) + __builtin_bit_cast(float, b);
}
__device__ __forceinline__ float xsum8(float x) {
  return x + __builtin_bit_cast(float, __builtin_amdgcn_update_dpp(0, __builtin_bit_cast(int, x), 0x128, 0xf, 0xf, false));
}
__device__ __forceinline__ f32x2 phi2(f32x2 t) {
  const f32x2 u = t * 1.4426950408889634f;
  const f32x2 e = {__builtin_amdgcn_exp2f(fminf(u[0], 0.f)), __builtin_amdgcn_exp2f(fminf(u[1], 0.f))};
  const f32x2 p = {fmaxf(t[0], 0.f), fmaxf(t[1], 0.f)};
  return (p + 1e-6f) + e;
}
__device__ __forceinline__ f32x2 gelu2(f32x2 x) {
  const f32x2 u = x * (x * x * -0.1029432397f + -2.302208198f);
  const f32x2 d = f32x2{__builtin_amdgcn_exp2f(u[0]), __builtin_amdgcn_exp2f(u[1])} + 1.f;
  return x * f32x2{__builtin_amdgcn_rcpf(d[0]), __builtin_amdgcn_rcpf(d[1])};
}

#define STAMP(i) do { } while (0)

__global__ void __launch_bounds__(512, 2)
fused_block(const unsigned short* __restrict__ x, const unsigned short* __restrict__ wqkv, const unsigned short* __restrict__ wo,
            const unsigned short* __restrict__ w1, const unsigned short* __restrict__ b1, const unsigned short* __restrict__ w2,
            const unsigned short* __restrict__ b2, unsigned short* __restrict__ out, unsigned char* ws) {
  __shared__ __attribute__((aligned(16))) char smem[LDS_BYTES];
  LAS char* L = (LAS char*)smem;
  const unsigned lds0 = (unsigned)(size_t)L;
  const int tid = threadIdx.x, lane = tid & 63, w = __builtin_amdgcn_readfirstlane(tid >> 6);
  const int b = blockIdx.x, xcd = b & 7, bi = b >> 3;
  const int g = lane >> 4, t = lane & 15;
  const __amdgpu_buffer_rsrc_t rs = __builtin_amdgcn_make_buffer_rsrc((void*)ws, 0, WS_BYTES, 0x00020000);
  gu32* flags = (gu32*)(ws + WS_FLAG);
#define FLAG(set, grp) (flags + ((set) * FLAG_SET + (grp) * 256) / 4)
  STAMP(0);
  const int r128 = 4 * (xcd >> 1) + (bi >> 3), c16 = 8 * (xcd & 1) + (bi & 7);
  const int r64 = 8 * (xcd >> 1) + (bi >> 2), c8 = 4 * (xcd & 1) + (bi & 3);

  if (w >= 4) {
    const int hw = w - 4, ht = tid - 256;
    if (HPRIO) __builtin_amdgcn_s_setprio(HPRIO);
    const v4u srd_ws = mk_srd(ws, WS_BYTES);
    const __amdgpu_buffer_rsrc_t rq = __builtin_amdgcn_make_buffer_rsrc((void*)wqkv, 0, D * 3 * D * 2, 0x00020000);
    const __amdgpu_buffer_rsrc_t ro = __builtin_amdgcn_make_buffer_rsrc((void*)wo, 0, D * D * 2, 0x00020000);
    const __amdgpu_buffer_rsrc_t r1 = __builtin_amdgcn_make_buffer_rsrc((void*)w1, 0, D * F * 2, 0x00020000);
    const __amdgpu_buffer_rsrc_t r2 = __builtin_amdgcn_make_buffer_rsrc((void*)w2, 0, F * D * 2, 0x00020000);
    volatile LAS unsigned* sig = (volatile LAS unsigned*)(L + L_CS + 1024);
    const unsigned tag = __hip_atomic_load((gu32*)(ws + WS_EPOCH), RLX_AGENT) + 1u;
    const int h2 = ht & 127;
    const int q10 = h2 / 10, c10 = h2 % 10, q5 = h2 / 5, c5 = h2 % 5, q12 = h2 / 12, c12 = h2 % 12;
    if (hw < 2) {
      v4u tv[6];
      int td[6];
#pragma unroll
      for (int j = 0; j < 4; ++j) {
        const int R = 40 * r128 + 10 * j + q10, rho = R & 31;
        const int k = (R & ~31) + 8 * ((rho >> 2) & 3) + 4 * (rho >> 4) + (rho & 3);
        tv[j] = __builtin_amdgcn_raw_buffer_load_b128(rq, sel(h2 < 100, k * 3840 + c16 * 80 + (c10 < 5 ? 1280 : 2480) + c10 * 16), 0, 0);
        td[j] = sel(h2 < 100, WS_TKV + c16 * 153600 + R * 160 + c10 * 16);
      }
#pragma unroll
      for (int j = 0; j < 2; ++j) {
        const int R = 40 * r128 + 20 * j + q5, ks = R >> 5, rho = R & 31;
        const int k = 32 * ks + 8 * ((rho >> 2) & 3) + 4 * (rho >> 4) + (rho & 3);
        tv[4 + j] = __builtin_amdgcn_raw_buffer_load_b128(rq, sel(h2 < 100, k * 3840 + c16 * 80 + c5 * 16), 0, 0);
        td[4 + j] = sel(h2 < 100, WS_TKV + c16 * 153600 + 102400 + ks * 2560 + (c5 < 4 ? rho * 64 + (c5 ^ (2 * ((rho >> 2) & 1))) * 16 : 2048 + rho * 16));
      }
      BAR_RAW();
#pragma unroll
      for (int j = 0; j < 6; ++j) st128(tv[j], rs, td[j], 0);
      DRAIN();
      BAR_RAW();
      if (hw == 0) {
        wait_flag(FLAG(6, c16) + lane, 16, tag, lane);
        sig[0] = tag;
      }
    } else {
      v4u tv[10];
      int ts[10], td[10];
#pragma unroll
      for (int j = 0; j < 4; ++j) {
        const int R = 40 * r128 + 10 * j + q12, ks = R >> 5, rho = R & 31;
        const int k = ks < 16 ? 80 * (ks >> 1) + 32 * (ks & 1) + rho : 160 * (ks - 16) + 64 + 80 * (rho >> 4) + (rho & 15);
        ts[j] = sel(h2 < 120, k * 3072 + c16 * 192 + c12 * 16);
        td[j] = sel(h2 < 120, WS_T1 + c16 * 122880 + R * 192 + (c12 ^ (2 * ((rho >> 2) & 1))) * 16);
      }
#pragma unroll
      for (int j = 0; j < 2; ++j) {
        const int R = 20 * r64 + 10 * j + q10, ks = R >> 5, rho = R & 31;
        const int k = ks < 16 ? 40 * ks + rho : 40 * (4 * (ks - 16) + (rho >> 3)) + 32 + (rho & 7);
        ts[4 + j] = sel(h2 < 100, k * 1280 + c8 * 160 + c10 * 16);
        td[4 + j] = sel(h2 < 100, WS_TO + c8 * 102400 + R * 160 + c10 * 16);
      }
#pragma unroll
      for (int j = 0; j < 4; ++j) {
        const int R = 96 * r128 + 24 * j + q5, ks = R >> 5, rho = R & 31;
        ts[6 + j] = sel(h2 < 120, R * 1280 + c16 * 80 + c5 * 16);
        td[6 + j] = sel(h2 < 120, WS_T2 + c16 * 122880 + ks * 2560 + (c5 < 4 ? rho * 64 + (c5 ^ (2 * ((rho >> 2) & 1))) * 16 : 2048 + rho * 16));
      }
#pragma unroll
      for (int j = 0; j < 10; ++j) { PIN(ts[j]); PIN(td[j]); }
      BAR_RAW();
#pragma unroll
      for (int j = 0; j < 10; ++j) tv[j] = __builtin_amdgcn_raw_buffer_load_b128(j < 4 ? r1 : j < 6 ? ro : r2, ts[j], 0, 0);
      BAR_RAW();
#pragma unroll
      for (int j = 0; j < 10; ++j) st128(tv[j], rs, td[j], 0);
      f32x2 sm[4];
#pragma unroll
      for (int e = 0; e < 4; ++e) sm[e] = (bf2(tv[0][e]) + bf2(tv[1][e])) + (bf2(tv[2][e]) + bf2(tv[3][e]));
      LAS f32x4* sc = (LAS f32x4*)(L + L_KVF);
      sc[h2 * 2] = f32x4{sm[0][0], sm[0][1], sm[1][0], sm[1][1]};
      sc[h2 * 2 + 1] = f32x4{sm[2][0], sm[2][1], sm[3][0], sm[3][1]};
    }
    sig_wait(sig, tag);
    STAMP(1);
    const int kvq = WS_TKV + c16 * 153600;
    dma_image<0, 24>(lds0 + L_BKV, lane * 16, srd_ws, kvq, hw, 3 * (bi >> 3));
    dma_image<24, 56>(lds0 + L_BKV, lane * 16, srd_ws, kvq, hw, 4 * (bi >> 3));
    asm volatile("s_waitcnt vmcnt(8)" ::: "memory");
    STAMP(2);
    BAR();
    dma_image<56, 104>(lds0 + L_BKV, lane * 16, srd_ws, kvq, hw, 6 * (bi >> 3));
    asm volatile("s_waitcnt vmcnt(12)" ::: "memory");
    BAR_RAW();
    if (hw == 3) {
      LAS f32x4* sc = (LAS f32x4*)(L + L_KVF);
      const int l24 = lane < 24 ? lane : 0;
      f32x4 v = sc[l24];
#pragma unroll
      for (int r = 1; r < 10; ++r) v = add4(v, sc[r * 24 + l24]);
      st128(__builtin_bit_cast(v4u, v), rs, sel(lane < 24, lane * 16), WS_CS + (c16 * 16 + r128) * 384);
    }
    DRAIN();
    STAMP(3);
    BAR_RAW();
    if (hw == 2 && lane < 2) flag_set(lane == 0 ? FLAG(5, c16) + r128 : FLAG(7, c8) + r64, tag);
    dma_image<104, 150>(lds0 + L_BKV, lane * 16, srd_ws, kvq, hw, 5 * (bi >> 3));
    ((LAS v4u*)(L + L_KVF))[ht] = v4u{0u, 0u, 0u, 0u};
    ((LAS v4u*)(L + L_KVF))[256 + ht] = v4u{0u, 0u, 0u, 0u};
    auto item = [&](int it, int& red_off, int& kvf_off) {
      const int ie = it / 5, ic = it % 5;
      const int ihead = ie < 40 ? (ie >= 20) : ie - 40;
      const int id = 20 * ihead + 4 * ic, dg = id >> 2;
      const int sa = id >> 4, col = 40 + ie;
      const int tile = ie < 40 ? 2 * sa + (col >> 4) - 2 : 7 + sa;
      red_off = (tile * 64 + (dg & 3) * 16 + (ie < 40 ? (col & 15) : 0)) * 16;
      kvf_off = ((ie < 40 ? (ie >> 4) : 3) * 2 + (dg >> 3)) * 1024 + ((dg & 3) * 16 + (ie < 40 ? (ie & 15) : ihead)) * 16 + ((dg >> 2) & 1) * 8;
    };
    const int it = ht < 210 ? ht : 209;
    int red_off[4], kvf_off, unused;
    item(it, unused, kvf_off);
#pragma unroll
    for (int j = 0; j < 4; ++j) { item(min(j * 64 + lane, 209), red_off[j], unused); PIN(red_off[j]); }
    PIN(kvf_off);
    if (hw == 1) wait_flag(lane < 32 ? FLAG(7, c8) + lane : FLAG(5, c16) + t, 48, tag, lane);
    DRAIN();
    STAMP(4);
    BAR();
    STAMP(11);
    if (hw == 0) {
      f32x4 pv[4][4];
#pragma unroll
      for (int j = 0; j < 4; ++j)
#pragma unroll
        for (int ww = 0; ww < 4; ++ww) pv[j][ww] = *(LAS f32x4*)(L + L_RED + ww * 10240 + red_off[j]);
#pragma unroll
      for (int j = 0; j < 4; ++j) {
        const f32x4 v = add4(add4(pv[j][0], pv[j][1]), add4(pv[j][2], pv[j][3]));
        st128(__builtin_bit_cast(v4u, v), rs, j * 1024 + lane * 16, WS_KVP + (c16 * 16 + r128) * 4096);
      }
      asm volatile("" ::: "memory");
      sig[1] = tag;
      STAMP(12);
      publish(FLAG(1, c16) + r128, tag, lane);
      STAMP(10);
      wait_flag(FLAG(1, c16) + lane, 16, tag, lane);
      sig[2] = tag;
    } else {
      dma_image<40, 100, THR, 3>(lds0 + L_WO, lane * 16, srd_ws, WS_TO + c8 * 102400, hw - 1, 3 * (bi >> 2));
      sig_wait(sig + 1, tag);
      dma_image<0, 40, THR, 3>(lds0 + L_WO, lane * 16, srd_ws, WS_TO + c8 * 102400, hw - 1, 2 * (bi >> 2));
      STAMP(10);
      sig_wait(sig + 2, tag);
    }
    STAMP(5);
    {
      v4u pv[16];
#pragma unroll
      for (int r = 0; r < 16; ++r) pv[r] = ld128(rs, it * 16, WS_KVP + (c16 * 16 + r) * 4096);
      SCHED();
      f32x4 v = __builtin_bit_cast(f32x4, pv[0]);
#pragma unroll
      for (int r = 1; r < 16; ++r) v = add4(v, __builtin_bit_cast(f32x4, pv[r]));
      v2u o; o.x = pk2(lo2(v)); o.y = pk2(hi2(v));
      *(LAS v2u*)(L + L_KVF + kvf_off) = o;
      if (ht >= 200) {
#pragma unroll
        for (int c = 1; c < 4; ++c) *(LAS v2u*)(L + L_KVF + kvf_off + c * 64) = o;
      }
    }
    DRAIN();
    STAMP(6);
    BAR();
    dma_image<70, 120, THR>(lds0 + L_W1, lane * 16, srd_ws, WS_T1 + c16 * 122880, hw, 6 * (bi >> 3));
    if (hw == 0) {
      v4u pv[16];
#pragma unroll
      for (int r = 0; r < 16; ++r) pv[r] = ld128(rs, sel(lane < 24, lane * 16), WS_CS + (c16 * 16 + r) * 384);
      f32x4 v = __builtin_bit_cast(f32x4, pv[0]);
#pragma unroll
      for (int r = 1; r < 16; ++r) v = add4(v, __builtin_bit_cast(f32x4, pv[r]));
      if (lane < 24) *(LAS f32x4*)(L + L_CS + lane * 16) = v;
    }
    STAMP(7);
    BAR();
    dma_image<0, 70, THR>(lds0 + L_W1, lane * 16, srd_ws, WS_T1 + c16 * 122880, hw, 8 * (bi >> 3));
    DRAIN();
    STAMP(8);
    BAR_RAW();
    dma_image<0, 30, THR>(lds0 + L_W2, lane * 16, srd_ws, WS_T2 + c16 * 122880, hw, 3 * (bi >> 3));
    BAR_RAW();
    dma_image<30, 120, THR>(lds0 + L_W2, lane * 16, srd_ws, WS_T2 + c16 * 122880, hw, 11 * (bi >> 3));
    DRAIN();
    STAMP(9);
    BAR_RAW();
    return;
  }

  const __amdgpu_buffer_rsrc_t rx = __builtin_amdgcn_make_buffer_rsrc((void*)x, 0, S * D * 2, 0x00020000);
  const int grp32 = 4 * r128 + w;
  constexpr int AW = 6;
  unsigned tag;

  {
    const int tok = 8 * b + (lane & 7), cl = lane >> 3;
    const bool has3 = w < 2;
    v4u xv[3];
#pragma unroll
    for (int j = 0; j < 3; ++j) xv[j] = __builtin_amdgcn_raw_buffer_load_b128(rx, (j < 2 || has3) ? tok * 1280 + (w + 4 * j) * 128 + cl * 16 : OOB, 0, 0);
    tag = __hip_atomic_load((gu32*)(ws + WS_EPOCH), RLX_AGENT) + 1u;
    SCHED();
    int xd = ((tok >> 4) * 20 + 2 * w + (cl >> 2)) * 1024 + ((cl & 3) * 16 + (tok & 15)) * 16;
    PIN(xd);
    SCHED();
    f32x2 f[12];
#pragma unroll
    for (int j = 0; j < 3; ++j)
#pragma unroll
      for (int e = 0; e < 4; ++e) f[j * 4 + e] = bf2(xv[j][e]);
    f32x2 s2 = (f[0] + f[1]) + (f[2] + f[3]) + ((f[4] + f[5]) + (f[6] + f[7])) + ((f[8] + f[9]) + (f[10] + f[11]));
    const float mloc = xsum32(xsum16(xsum8(s2[0] + s2[1]))) * (has3 ? 1.f / 192.f : 1.f / 128.f);
    const float m3 = has3 ? mloc : 0.f;
    f32x2 q2 = {0.f, 0.f}, q3 = {0.f, 0.f};
#pragma unroll
    for (int j = 0; j < 12; j += 2) {
      const f32x2 d0 = f[j] - (j < 8 ? mloc : m3), d1 = f[j + 1] - (j < 8 ? mloc : m3);
      q2 += d0 * d0; q3 += d1 * d1;
    }
    q2 += q3;
    const float m2loc = xsum32(xsum16(xsum8(q2[0] + q2[1])));
    if (lane < 8) *(LAS f32x2*)(L + L_CS + 512 + (lane * 4 + w) * 8) = f32x2{mloc, m2loc};
    STAMP(25);
    BAR();
    const f32x4 p01 = *(LAS f32x4*)(L + L_CS + 512 + (lane & 7) * 32), p23 = *(LAS f32x4*)(L + L_CS + 512 + (lane & 7) * 32 + 16);
    const float mean = (p01[0] + p01[2]) * 0.3f + (p23[0] + p23[2]) * 0.2f;
    const float e0 = p01[0] - mean, e1 = p01[2] - mean, e2 = p23[0] - mean, e3 = p23[2] - mean;
    const float m2 = (p01[1] + p01[3]) + (p23[1] + p23[3]) + 192.f * (e0 * e0 + e1 * e1) + 128.f * (e2 * e2 + e3 * e3);
    const float rstd = __builtin_amdgcn_rsqf(m2 * (1.f / D) + LN_EPS);
#pragma unroll
    for (int j = 0; j < 3; ++j) {
      v4u o;
#pragma unroll
      for (int e = 0; e < 4; ++e) o[e] = pk2((f[j * 4 + e] - mean) * rstd);
      st128(o, rs, (j < 2 || has3) ? xd : OOB, WS_XN + j * 8192);
    }
    STAMP(26);
    DRAIN();
    BAR_RAW();
    if (tid < 2) flag_set(tid == 0 ? FLAG(0, b >> 2) + (b & 3) : FLAG(6, c16) + r128, tag);
  }
  STAMP(1);

  {
    wait_flag(FLAG(0, grp32) + lane, 4, tag, lane);
    STAMP(2);
    v4u a[20][2];
    const int abase = WS_XN + 2 * grp32 * 20 * 1024;
#pragma unroll
    for (int ks = 0; ks < AW; ++ks)
#pragma unroll
      for (int m = 0; m < 2; ++m) a[ks][m] = ld128(rs, lane * 16, abase + (m * 20 + ks) * 1024);
    SCHED();
    BAR_RAW();
    STAMP(27);
    {
      f32x4 acc[2][5];
      const LBase bp(lds0 + L_BKV + (lane >> 2) * 160 + (lane & 3) * 8);
      kloop<2, 5, 20, 1, false>(acc, [&](int ks, int s, int h) { return bp.rd(ks * 5120 + h * 2560 + s * 32); },
        [&](int ks, int m) { return a[ks][m]; },
        [&](int ks) {
          if (ks + AW < 20) {
#pragma unroll
            for (int m = 0; m < 2; ++m) a[ks + AW][m] = ld128(rs, lane * 16, abase + (m * 20 + ks + AW) * 1024);
          }
          if (ks == 3 || ks == 10) BAR_RAW();
        });
      STAMP(3);
      v4u kf[5];
#pragma unroll
      for (int s = 0; s < 5; ++s)
#pragma unroll
        for (int m = 0; m < 2; ++m) {
          f32x2 u0 = lo2(acc[m][s]), u1 = hi2(acc[m][s]);
          if (s < 2) { u0 = phi2(u0); u1 = phi2(u1); }
          if (s == 2) { const f32x2 p0 = phi2(u0), p1 = phi2(u1); u0 = t < 8 ? p0 : u0; u1 = t < 8 ? p1 : u1; }
          kf[s][2 * m] = pk2(u0); kf[s][2 * m + 1] = pk2(u1);
        }
      const v4u ones = {0x3F803F80u, 0x3F803F80u, 0x3F803F80u, 0x3F803F80u};
      const f32x4 z4 = {0.f, 0.f, 0.f, 0.f};
      f32x4 T[10];
      T[0] = mfma(kf[0], kf[2], z4); T[1] = mfma(kf[0], kf[3], z4); T[2] = mfma(kf[1], kf[2], z4); T[3] = mfma(kf[1], kf[3], z4);
      T[4] = mfma(kf[1], kf[4], z4); T[5] = mfma(kf[2], kf[3], z4); T[6] = mfma(kf[2], kf[4], z4);
      T[7] = mfma(kf[0], ones, z4); T[8] = mfma(kf[1], ones, z4); T[9] = mfma(kf[2], ones, z4);
      STAMP(28);
      LAS f32x4* red = (LAS f32x4*)(L + L_RED);
#pragma unroll
      for (int i = 0; i < 10; ++i) red[(w * 10 + i) * 64 + lane] = T[i];
      BAR();
    }
    STAMP(4);
    f32x4 qa[2][3];
    {
      unsigned bp0 = lds0 + L_BQ + (lane >> 2) * 64 + (lane & 3) * 8 + (g & 1) * 32;
      unsigned bp1 = lds0 + L_BQ + (lane >> 2) * 64 + (lane & 3) * 8 + 32 - (g & 1) * 32;
      unsigned bp2 = lds0 + L_BQ + 2048 + (lane >> 2) * 16 + (lane & 1) * 8;
      PIN(bp0); PIN(bp1); PIN(bp2);
      kloop<2, 3, 20, 2, true>(qa,
        [&](int ks, int s, int h) {
          return s == 0 ? rdtr((LAS char*)bp0 + ks * 2560 + h * 1024) : s == 1 ? rdtr((LAS char*)bp1 + ks * 2560 + h * 1024) : rdtr((LAS char*)bp2 + ks * 2560 + h * 256);
        },
        [&](int ks, int m) { return a[ks][m]; },
        [&](int ks) {});
    }
    v4u pq0[2], pq1[2];
#pragma unroll
    for (int m = 0; m < 2; ++m) {
      pq0[m].x = pk2(phi2(lo2(qa[m][0]))); pq0[m].y = pk2(phi2(hi2(qa[m][0])));
      pq0[m].z = pk2(phi2(lo2(qa[m][1]))); pq0[m].w = pk2(phi2(hi2(qa[m][1])));
      const unsigned lo = pk2(phi2(lo2(qa[m][2]))), hi = pk2(phi2(hi2(qa[m][2])));
      pq1[m].x = g < 2 ? lo : 0u; pq1[m].y = g < 2 ? hi : 0u; pq1[m].z = 0u; pq1[m].w = 0u;
      PIN(pq0[m]); PIN(pq1[m]);
    }
    STAMP(5);
    BAR_RAW();
    STAMP(17);
    {
      v4u kv[4][2];
#pragma unroll
      for (int es = 3; es >= 0; --es)
#pragma unroll
        for (int ks = 0; ks < 2; ++ks) kv[es][ks] = *(LAS v4u*)(L + L_KVF + (es * 2 + ks) * 1024 + lane * 16);
      SCHED();
      f32x4 o[2][4];
#pragma unroll
      for (int es = 3; es >= 0; --es)
#pragma unroll
        for (int m = 0; m < 2; ++m) o[m][es] = mfma(kv[es][0], pq0[m], f32x4{0.f, 0.f, 0.f, 0.f});
#pragma unroll
      for (int es = 3; es >= 0; --es)
#pragma unroll
        for (int m = 0; m < 2; ++m) o[m][es] = mfma(kv[es][1], pq1[m], o[m][es]);
#pragma unroll
      for (int m = 0; m < 2; ++m) {
        const float i0 = __builtin_amdgcn_rcpf(o[m][3][0] + 1e-6f), i1 = __builtin_amdgcn_rcpf(o[m][3][1] + 1e-6f);
        const float im = g == 0 ? i0 : i1;
        v4u ov;
        ov.x = pk2(lo2(o[m][0]) * i0); ov.y = pk2(hi2(o[m][0]) * i0);
        ov.z = pk2(lo2(o[m][1]) * im); ov.w = pk2(hi2(o[m][1]) * im);
        v2u ol; ol.x = pk2(lo2(o[m][2]) * i1); ol.y = pk2(hi2(o[m][2]) * i1);
        const int tt = 2 * grp32 + m;
        st128(ov, rs, lane * 16, WS_O + (tt * 20 + c16) * 1024);
        st64(ol, rs, g < 2 ? lane * 8 : OOB, WS_O + (tt * 20 + 16) * 1024 + c16 * 256);
      }
      STAMP(18);
      publish(FLAG(2, grp32) + c16, tag, lane);
    }
    STAMP(7);
  }

  {
    const int tt = 4 * r64 + w, tok = tt * 16 + t;
    wait_flag(FLAG(2, tt >> 1) + lane, 16, tag, lane);
    STAMP(8);
    if (b == 0 && tid == 0) __hip_atomic_store((gu32*)(ws + WS_EPOCH), tag, RLX_AGENT);
    v4u a[20];
    v2u xr[5];
#pragma unroll
    for (int ks = 0; ks < 2 * AW; ++ks) a[ks] = ld128(rs, lane * 16, WS_O + (tt * 20 + ks) * 1024);
    SCHED();
    f32x4 acc[1][5];
    const LBase bp(lds0 + L_WO + (lane >> 2) * 160 + (lane & 3) * 8);
    kloop<1, 5, 20, 1, true>(acc, [&](int ks, int s, int h) { return bp.rd(ks * 5120 + h * 2560 + s * 32); },
      [&](int ks, int m) { return a[ks]; },
      [&](int ks) {
        if (ks + 2 * AW < 16) a[ks + 2 * AW] = ld128(rs, lane * 16, WS_O + (tt * 20 + ks + 2 * AW) * 1024);
        else if (ks + 2 * AW < 20) a[ks + 2 * AW] = ld_split(rs, lane, WS_O + (tt * 20 + ks + 2 * AW) * 1024);
        if (ks >= 8 && ks < 13) xr[ks - 8] = __builtin_amdgcn_raw_buffer_load_b64(rx, tok * 1280 + (80 * c8 + 16 * (ks - 8) + 4 * g) * 2, 0, 0);
      });
    BAR_RAW();
    STAMP(9);
    v2u hb[5];
    f32x2 hv[10];
#pragma unroll
    for (int s = 0; s < 5; ++s) {
      hb[s].x = pk2(lo2(acc[0][s]) + bf2(xr[s].x));
      hb[s].y = pk2(hi2(acc[0][s]) + bf2(xr[s].y));
      hv[2 * s] = bf2(hb[s].x); hv[2 * s + 1] = bf2(hb[s].y);
    }
    st128(v4u{hb[0].x, hb[0].y, hb[1].x, hb[1].y}, rs, lane * 16, WS_H + (tt * 20 + 2 * c8) * 1024);
    st128(v4u{hb[2].x, hb[2].y, hb[3].x, hb[3].y}, rs, lane * 16, WS_H + (tt * 20 + 2 * c8 + 1) * 1024);
    st64(hb[4], rs, lane * 8, WS_H + (tt * 20 + 16) * 1024 + c8 * 512);
    const f32x2 s2 = ((hv[0] + hv[1]) + (hv[2] + hv[3])) + ((hv[4] + hv[5]) + (hv[6] + hv[7])) + (hv[8] + hv[9]);
    const float mc = xsum32(xsum16(s2[0] + s2[1])) * (1.f / 80.f);
    f32x2 q2 = {0.f, 0.f}, q3 = {0.f, 0.f};
#pragma unroll
    for (int j = 0; j < 10; j += 2) { const f32x2 d0 = hv[j] - mc, d1 = hv[j + 1] - mc; q2 += d0 * d0; q3 += d1 * d1; }
    q2 += q3;
    const float m2 = xsum32(xsum16(q2[0] + q2[1]));
    st64(v2u{__builtin_bit_cast(unsigned, mc), __builtin_bit_cast(unsigned, m2)}, rs, g == 0 ? t * 8 : OOB, WS_ST + (tt * 8 + c8) * 128);
    STAMP(20);
    publish(FLAG(3, tt >> 1) + (tt & 1) * 8 + c8, tag, lane);
    STAMP(10);
  }

  {
    v2u bb[6];
#pragma unroll
    for (int s = 0; s < 6; ++s) bb[s] = *(const v2u*)(b1 + 96 * c16 + 16 * s + 4 * g);
    wait_flag(FLAG(3, grp32) + lane, 16, tag, lane);
    STAMP(11);
    v4u stt[2];
#pragma unroll
    for (int m = 0; m < 2; ++m) {
      const v2u s0 = ld64(rs, g * 256 + t * 8, WS_ST + (2 * grp32 + m) * 1024), s1 = ld64(rs, g * 256 + 128 + t * 8, WS_ST + (2 * grp32 + m) * 1024);
      stt[m] = v4u{s0.x, s0.y, s1.x, s1.y};
    }
    constexpr int WIN = AW + 1;
    v4u a[WIN][2];
    const int abase = WS_H + 2 * grp32 * 20 * 1024;
#pragma unroll
    for (int ks = 0; ks < AW; ++ks)
#pragma unroll
      for (int m = 0; m < 2; ++m) a[ks][m] = ld128(rs, lane * 16, abase + (m * 20 + ks) * 1024);
    SCHED();
    float nm[2], rr[2];
#pragma unroll
    for (int m = 0; m < 2; ++m) {
      const f32x4 sv = __builtin_bit_cast(f32x4, stt[m]);
      const float mean = xsum32(xsum16(sv[0] + sv[2])) * 0.125f;
      const float m2 = xsum32(xsum16(sv[1] + sv[3] + 80.f * ((sv[0] - mean) * (sv[0] - mean) + (sv[2] - mean) * (sv[2] - mean))));
      const float var = m2 * (1.f / D);
      const float r1 = __builtin_amdgcn_rsqf(var + LN_EPS);
      rr[m] = r1 * __builtin_amdgcn_rsqf(var * r1 * r1 + LN_EPS);
      nm[m] = -mean * rr[m];
      PIN(rr[m]); PIN(nm[m]);
    }
    BAR_RAW();
    STAMP(22);
    const LBase bp0(lds0 + L_W1 + (lane >> 2) * 192 + (lane & 3) * 8 + (g & 1) * 32);
    const LBase bp1(lds0 + L_W1 + (lane >> 2) * 192 + (lane & 3) * 8 - (g & 1) * 32);
    f32x4 acc[2][6];
    f32x4 cs[6];
    kloop<2, 6, 20, 1, true>(acc, [&](int ks, int s, int h) { return ((s & 1) ? bp1 : bp0).rd(ks * 6144 + h * 3072 + s * 32); },
      [&](int ks, int m) { return a[ks % WIN][m]; },
      [&](int ks) {
        if (ks + AW < 20) {
#pragma unroll
          for (int m = 0; m < 2; ++m)
            a[(ks + AW) % WIN][m] = ks + AW < 16 ? ld128(rs, lane * 16, abase + (m * 20 + ks + AW) * 1024) : ld_split(rs, lane, abase + (m * 20 + ks + AW) * 1024);
        }
        if (ks == 19) {
#pragma unroll
          for (int s = 0; s < 6; ++s) cs[s] = *(LAS f32x4*)(L + L_CS + (s * 4 + g) * 16);
        }
      });
    BAR_RAW();
    STAMP(12);
#pragma unroll
    for (int m = 0; m < 2; ++m) {
      v2u hb[6];
#pragma unroll
      for (int s = 0; s < 6; ++s) {
        const f32x2 p0 = lo2(acc[m][s]) * rr[m] + (lo2(cs[s]) * nm[m] + bf2(bb[s].x));
        const f32x2 p1 = hi2(acc[m][s]) * rr[m] + (hi2(cs[s]) * nm[m] + bf2(bb[s].y));
        hb[s].x = pk2(gelu2(p0)); hb[s].y = pk2(gelu2(p1));
        if (s & 1)
          st128(v4u{hb[s - 1].x, hb[s - 1].y, hb[s].x, hb[s].y}, rs, lane * 16, WS_H1 + ((2 * grp32 + m) * 48 + 3 * c16 + (s >> 1)) * 1024);
      }
    }
    STAMP(24);
    publish(FLAG(4, grp32) + c16, tag, lane);
    STAMP(13);
  }

  {
    v2u hr[2][3], bb[3];
#pragma unroll
    for (int s = 0; s < 3; ++s) {
      int n = 40 * c16 + 16 * s + 4 * g;
      if (s == 2 && g >= 2) n = 40 * c16;
      bb[s] = *(const v2u*)(b2 + n);
      const int cc = n / 80, off = n % 80, ss = off >> 4, gg = (off >> 2) & 3;
      const int ho = ss < 4 ? (2 * cc + (ss >> 1)) * 1024 + (gg * 16 + t) * 16 + (ss & 1) * 8 : 16 * 1024 + cc * 512 + (gg * 16 + t) * 8;
#pragma unroll
      for (int m = 0; m < 2; ++m) hr[m][s] = ld64(rs, ho, WS_H + (2 * grp32 + m) * 20 * 1024);
    }
    wait_flag(FLAG(4, grp32) + lane, 16, tag, lane);
    STAMP(14);
    constexpr int WIN = 8;
    v4u a[WIN][2];
    const int abase = WS_H1 + 2 * grp32 * 48 * 1024;
#pragma unroll
    for (int ks = 0; ks < WIN - 1; ++ks)
#pragma unroll
      for (int m = 0; m < 2; ++m) a[ks][m] = ld128(rs, lane * 16, abase + (m * 48 + ks) * 1024);
    SCHED();
    BAR_RAW();
    f32x4 acc[2][3];
    const LBase bp0(lds0 + L_W2 + (lane >> 2) * 64 + (lane & 3) * 8 + (g & 1) * 32);
    const LBase bp1(lds0 + L_W2 + (lane >> 2) * 64 + (lane & 3) * 8 + 32 - (g & 1) * 32);
    const LBase bp2(lds0 + L_W2 + 2048 + (lane >> 2) * 16 + (lane & 1) * 8);
    kloop<2, 3, 48, 2, true>(acc,
      [&](int ks, int s, int h) { return s == 0 ? bp0.rd(ks * 2560 + h * 1024) : s == 1 ? bp1.rd(ks * 2560 + h * 1024) : bp2.rd(ks * 2560 + h * 256); },
      [&](int ks, int m) { return a[ks % WIN][m]; },
      [&](int ks) {
        if (ks + WIN - 1 < 48) {
#pragma unroll
          for (int m = 0; m < 2; ++m) a[(ks + WIN - 1) % WIN][m] = ld128(rs, lane * 16, abase + (m * 48 + ks + WIN - 1) * 1024);
        }
      });
    STAMP(15);
#pragma unroll
    for (int m = 0; m < 2; ++m) {
      const int tok = (2 * grp32 + m) * 16 + t;
#pragma unroll
      for (int s = 0; s < 3; ++s) {
        v2u o;
        o.x = pk2(lo2(acc[m][s]) + bf2(bb[s].x) + bf2(hr[m][s].x));
        o.y = pk2(hi2(acc[m][s]) + bf2(bb[s].y) + bf2(hr[m][s].y));
        if (s < 2 || g < 2) *(v2u*)(out + tok * D + 40 * c16 + 16 * s + 4 * g) = o;
      }
    }
    STAMP(16);
  }
}
}

extern "C" void kernel_launch(void* const* d_in, const int* in_sizes, int n_in, void* d_out, int out_size, void* d_ws,
                              size_t ws_size, hipStream_t stream) {
  const unsigned short* x = (const unsigned short*)d_in[0];
  const unsigned short* wqkv = (const unsigned short*)d_in[1];
  const unsigned short* wo = (const unsigned short*)d_in[2];
  const unsigned short* w1 = (const unsigned short*)d_in[4];
  const unsigned short* b1 = (const unsigned short*)d_in[5];
  const unsigned short* w2 = (const unsigned short*)d_in[6];
  const unsigned short* b2 = (const unsigned short*)d_in[7];
  fused_block<<<256, 512, 0, stream>>>(x, wqkv, wo, w1, b1, w2, b2, (unsigned short*)d_out, (unsigned char*)d_ws);
}
```

```cpp
#include <hip/hip_runtime.h>
#include <hip/hip_bf16.h>

#define STAMPS 0
#define THR 0
#define TOUCH 0
#define CODEPF 0
#define FLAGPF 0
#define HPRIO 0
#define WOLATE 0
#define SLEEP 1

#define GAS __attribute__((address_space(1)))
#define LAS __attribute__((address_space(3)))
#define RLX_AGENT __ATOMIC_RELAXED, __HIP_MEMORY_SCOPE_AGENT
typedef unsigned v4u __attribute__((ext_vector_type(4)));
typedef unsigned v2u __attribute__((ext_vector_type(2)));
typedef float f32x4 __attribute__((ext_vector_type(4)));
typedef float f32x2 __attribute__((ext_vector_type(2)));
typedef __bf16 bf16x8 __attribute__((ext_vector_type(8)));
typedef __bf16 bf16x2 __attribute__((ext_vector_type(2)));
typedef short s16x4 __attribute__((ext_vector_type(4)));
typedef GAS unsigned gu32;

namespace {
constexpr int S = 2048, D = 640, F = 1536;
constexpr float LN_EPS = 1e-5f;
constexpr int AUX_SC1 = 16;
constexpr unsigned kSpinCap = 1u << 17;
constexpr int OOB = 0x7FFFFF00;

constexpr int WS_EPOCH = 0;
constexpr int WS_FLAG = 4096;
constexpr int FLAG_SET = 64 * 256;
constexpr int WS_XN = 1 << 20;
constexpr int WS_O = WS_XN + 128 * 20 * 1024;
constexpr int WS_H = WS_O + 128 * 20 * 1024;
constexpr int WS_H1 = WS_H + 128 * 20 * 1024;
constexpr int WS_KVP = WS_H1 + 128 * 48 * 1024;
constexpr int WS_ST = WS_KVP + 16 * 16 * 4096;
constexpr int WS_DBG = WS_ST + 2048 * 64;
constexpr int WS_TKV = WS_DBG + 256 * 8 * 32 * 8;
constexpr int WS_TO = WS_TKV + 16 * 153600;
constexpr int WS_T1 = WS_TO + 8 * 102400;
constexpr int WS_T2 = WS_T1 + 16 * 122880;
constexpr int WS_CS = WS_T2 + 16 * 122880;
constexpr int WS_BYTES = WS_CS + 16 * 16 * 384;

constexpr int L_BKV = 0, L_BQ = 102400;
constexpr int L_RED = 0, L_KVF = 153600;
constexpr int L_WO = 0, L_W1 = 30720, L_W2 = 0, L_CS = 161792;
constexpr int LDS_BYTES = 163840;

__device__ __forceinline__ f32x4 mfma(v4u a, v4u b, f32x4 c) {
  return __builtin_amdgcn_mfma_f32_16x16x32_bf16(__builtin_bit_cast(bf16x8, a), __builtin_bit_cast(bf16x8, b), c, 0, 0, 0);
}
__device__ __forceinline__ unsigned pk2(f32x2 v) { return __builtin_bit_cast(unsigned, __builtin_convertvector(v, bf16x2)); }
__device__ __forceinline__ unsigned pk2(float lo, float hi) { return pk2(f32x2{lo, hi}); }
__device__ __forceinline__ float bf_lo(unsigned u) { return __builtin_bit_cast(float, u << 16); }
__device__ __forceinline__ float bf_hi(unsigned u) { return __builtin_bit_cast(float, u & 0xFFFF0000u); }
__device__ __forceinline__ f32x2 bf2(unsigned u) { return f32x2{bf_lo(u), bf_hi(u)}; }
__device__ __forceinline__ f32x2 lo2(f32x4 v) { return f32x2{v[0], v[1]}; }
__device__ __forceinline__ f32x2 hi2(f32x4 v) { return f32x2{v[2], v[3]}; }
__device__ __forceinline__ v4u mk_srd(const void* p, unsigned bytes) {
  const unsigned long long a = (unsigned long long)p;
  v4u r;
  r.x = (unsigned)a; r.y = (unsigned)(a >> 32) & 0xFFFFu; r.z = bytes; r.w = 0x00020000u;
  return r;
}
#define DRAIN() asm volatile("s_waitcnt vmcnt(0)" ::: "memory")
#define BAR() asm volatile("s_waitcnt lgkmcnt(0)\n\ts_barrier" ::: "memory")
#define BAR_RAW() asm volatile("s_barrier" ::: "memory")
#define SCHED() __builtin_amdgcn_sched_barrier(0)
#define PIN(x) asm volatile("" : "+v"(x))

__device__ __forceinline__ void dma2(unsigned lds_addr, unsigned voff, v4u srd, unsigned soff) {
  unsigned keep;
  asm volatile("s_mov_b32 %0, m0\n\ts_mov_b32 m0, %1\n\ts_nop 0\n\t"
               "buffer_load_dwordx4 %2, %3, %4 offen sc1 lds\n\t"
               "buffer_load_dwordx4 %2, %3, %4 offen offset:1024 sc1 lds\n\t"
               "s_mov_b32 m0, %0"
               : "=&s"(keep) : "s"(lds_addr), "v"(voff), "s"(srd), "s"(soff) : "memory");
}
template <int LO, int HI, int TH = 0, int NW = 4>
__device__ __forceinline__ void dma_image(unsigned lds_base, unsigned voff, v4u srd, int ws_off, int hw, int rot) {
  constexpr int NP = (HI - LO) / 2;
#pragma unroll 1
  for (int i = hw; i < NP; i += NW) {
    int j = i + rot;
    if (j >= NP) j -= NP;
    dma2(lds_base + LO * 1024 + j * 2048, voff, srd, ws_off + LO * 1024 + j * 2048);
    if (TH) asm volatile("s_waitcnt vmcnt(%0)" :: "i"(TH - 2) : "memory");
  }
}
__device__ __forceinline__ v2u rdtr(LAS char* p) { return __builtin_bit_cast(v2u, __builtin_amdgcn_ds_read_tr16_b64_v4i16((LAS s16x4*)p)); }
struct LBase {
  unsigned a[3];
  __device__ __forceinline__ explicit LBase(unsigned v) { a[0] = v; a[1] = v + 65536; a[2] = v + 131072; PIN(a[0]); PIN(a[1]); PIN(a[2]); }
  __device__ __forceinline__ v2u rd(int off) const { return rdtr((LAS char*)(a[off >> 16]) + (off & 65535)); }
};
template <int MT, int NT, int KS, int PD, bool WA, class RB, class GA, class PRE>
__device__ __forceinline__ void kloop(f32x4 (&acc)[MT][NT], RB rb, GA ga, PRE pre) {
  v2u bl[PD + 1][NT], bh[PD + 1][NT];
#pragma unroll
  for (int m = 0; m < MT; ++m)
#pragma unroll
    for (int s = 0; s < NT; ++s) acc[m][s] = f32x4{0.f, 0.f, 0.f, 0.f};
#pragma unroll
  for (int ks = 0; ks < PD; ++ks)
#pragma unroll
    for (int s = 0; s < NT; ++s) { bl[ks][s] = rb(ks, s, 0); bh[ks][s] = rb(ks, s, 1); }
  SCHED();
#pragma unroll
  for (int ks = 0; ks < KS; ++ks) {
    pre(ks);
#pragma unroll
    for (int s = 0; s < NT; ++s)
#pragma unroll
      for (int m = 0; m < MT; ++m) {
        const v2u lo = bl[ks % (PD + 1)][s], hi = bh[ks % (PD + 1)][s];
        const v4u wv = {lo.x, lo.y, hi.x, hi.y};
        acc[m][s] = WA ? mfma(wv, ga(ks, m), acc[m][s]) : mfma(ga(ks, m), wv, acc[m][s]);
        if (ks + PD < KS) {
          if (MT == 1 || m == 0) bl[(ks + PD) % (PD + 1)][s] = rb(ks + PD, s, 0);
          if (MT == 1 || m == 1) bh[(ks + PD) % (PD + 1)][s] = rb(ks + PD, s, 1);
        }
        SCHED();
      }
  }
}
__device__ __forceinline__ v4u ld128(__amdgpu_buffer_rsrc_t rs, int voff, int soff) {
  return __builtin_amdgcn_raw_buffer_load_b128(rs, voff, soff, AUX_SC1);
}
__device__ __forceinline__ v2u ld64(__amdgpu_buffer_rsrc_t rs, int voff, int soff) {
  return __builtin_amdgcn_raw_buffer_load_b64(rs, voff, soff, AUX_SC1);
}
__device__ __forceinline__ void st128(v4u v, __amdgpu_buffer_rsrc_t rs, int voff, int soff) {
  __builtin_amdgcn_raw_buffer_store_b128(v, rs, (int)((unsigned)voff + (unsigned)soff), 0, AUX_SC1);
}
__device__ __forceinline__ int sel(bool c, int a) {
  PIN(a);
  return c ? a : OOB;
}
__device__ __forceinline__ v4u ld_split(__amdgpu_buffer_rsrc_t rs, int lane, int soff) {
  const v2u a = ld64(rs, lane * 8, soff), b = ld64(rs, lane * 8 + 512, soff);
  return v4u{a.x, a.y, b.x, b.y};
}
__device__ __forceinline__ void st64(v2u v, __amdgpu_buffer_rsrc_t rs, int voff, int soff) {
  __builtin_amdgcn_raw_buffer_store_b64(v, rs, voff, soff, AUX_SC1);
}
__device__ __forceinline__ void flag_set(gu32* p, unsigned tag) {
  asm volatile("global_atomic_swap %0, %1, off sc1" :: "v"(p), "v"(tag) : "memory");
}
__device__ __forceinline__ void wait_flag(gu32* p, int n, unsigned tag, int lane) {
#pragma unroll 1
  for (unsigned spins = 0; spins < kSpinCap; ++spins) {
    unsigned v = tag;
    if (lane < n) asm volatile("global_atomic_add %0, %1, %2, off sc0 sc1\n\ts_waitcnt vmcnt(0)" : "=&v"(v) : "v"(p), "v"(0u) : "memory");
    if (__all(v == tag)) break;
    if (SLEEP) __builtin_amdgcn_s_sleep(SLEEP);
  }
  asm volatile("" ::: "memory");
}
__device__ __forceinline__ void sig_wait(volatile LAS unsigned* p, unsigned tag) {
#pragma unroll 1
  for (unsigned spins = 0; spins < 64 * kSpinCap; ++spins) {
    if (*p == tag) break;
    __builtin_amdgcn_s_sleep(1);
  }
  asm volatile("" ::: "memory");
}
__device__ __forceinline__ void publish(gu32* flag, unsigned tag, int lane) {
  DRAIN();
  if (lane == 0) flag_set(flag, tag);
}
__device__ __forceinline__ f32x4 add4(f32x4 a, f32x4 b) {
  f32x4 r;
#pragma unroll
  for (int i = 0; i < 4; ++i) { float x; asm("v_add_f32 %0, %1, %2" : "=v"(x) : "v"(a[i]), "v"(b[i])); r[i] = x; }
  return r;
}
__device__ __forceinline__ float xsum32(float x) {
  unsigned u = __builtin_bit_cast(unsigned, x), v = u;
  asm("" : "+v"(v));
  auto r = __builtin_amdgcn_permlane32_swap(u, v, false, false);
  const unsigned a = r[0], b = r[1];
  return __builtin_bit_cast(float, a) + __builtin_bit_cast(float, b);
}
__device__ __forceinline__ float xsum16(float x) {
  unsigned u = __builtin_bit_cast(unsigned, x), v = u;
  asm("" : "+v"(v));
  auto r = __builtin_amdgcn_permlane16_swap(u, v, false, false);
  const unsigned a = r[0], b = r[1];
  return __builtin_bit_cast(float, a) + __builtin_bit_cast(float, b);
}
__device__ __forceinline__ float xsum8(float x) {
  return x + __builtin_bit_cast(float, __builtin_amdgcn_update_dpp(0, __builtin_bit_cast(int, x), 0x128, 0xf, 0xf, false));
}
__device__ __forceinline__ f32x2 phi2(f32x2 t) {
  const f32x2 u = t * 1.4426950408889634f;
  const f32x2 e = {__builtin_amdgcn_exp2f(fminf(u[0], 0.f)), __builtin_amdgcn_exp2f(fminf(u[1], 0.f))};
  const f32x2 p = {fmaxf(t[0], 0.f), fmaxf(t[1], 0.f)};
  return (p + 1e-6f) + e;
}
__device__ __forceinline__ f32x2 gelu2(f32x2 x) {
  const f32x2 u = x * (x * x * -0.1029432397f + -2.302208198f);
  const f32x2 d = f32x2{__builtin_amdgcn_exp2f(u[0]), __builtin_amdgcn_exp2f(u[1])} + 1.f;
  return x * f32x2{__builtin_amdgcn_rcpf(d[0]), __builtin_amdgcn_rcpf(d[1])};
}

#define STAMP(i) do { } while (0)

__global__ void __launch_bounds__(512, 2)
fused_block(const unsigned short* __restrict__ x, const unsigned short* __restrict__ wqkv, const unsigned short* __restrict__ wo,
            const unsigned short* __restrict__ w1, const unsigned short* __restrict__ b1, const unsigned short* __restrict__ w2,
            const unsigned short* __restrict__ b2, unsigned short* __restrict__ out, unsigned char* ws) {
  __shared__ __attribute__((aligned(16))) char smem[LDS_BYTES];
  LAS char* L = (LAS char*)smem;
  const unsigned lds0 = (unsigned)(size_t)L;
  const int tid = threadIdx.x, lane = tid & 63, w = __builtin_amdgcn_readfirstlane(tid >> 6);
  const int b = blockIdx.x, xcd = b & 7, bi = b >> 3;
  const int g = lane >> 4, t = lane & 15;
  const __amdgpu_buffer_rsrc_t rs = __builtin_amdgcn_make_buffer_rsrc((void*)ws, 0, WS_BYTES, 0x00020000);
  gu32* flags = (gu32*)(ws + WS_FLAG);
#define FLAG(set, grp) (flags + ((set) * FLAG_SET + (grp) * 256) / 4)
  STAMP(0);
  const int r128 = 4 * (xcd >> 1) + (bi >> 3), c16 = 8 * (xcd & 1) + (bi & 7);
  const int r64 = 8 * (xcd >> 1) + (bi >> 2), c8 = 4 * (xcd & 1) + (bi & 3);

  if (w >= 4) {
    const int hw = w - 4, ht = tid - 256;
    if (HPRIO) __builtin_amdgcn_s_setprio(HPRIO);
    const v4u srd_ws = mk_srd(ws, WS_BYTES);
    const __amdgpu_buffer_rsrc_t rq = __builtin_amdgcn_make_buffer_rsrc((void*)wqkv, 0, D * 3 * D * 2, 0x00020000);
    const __amdgpu_buffer_rsrc_t ro = __builtin_amdgcn_make_buffer_rsrc((void*)wo, 0, D * D * 2, 0x00020000);
    const __amdgpu_buffer_rsrc_t r1 = __builtin_amdgcn_make_buffer_rsrc((void*)w1, 0, D * F * 2, 0x00020000);
    const __amdgpu_buffer_rsrc_t r2 = __builtin_amdgcn_make_buffer_rsrc((void*)w2, 0, F * D * 2, 0x00020000);
    volatile LAS unsigned* sig = (volatile LAS unsigned*)(L + L_CS + 1024);
    const unsigned tag = __hip_atomic_load((gu32*)(ws + WS_EPOCH), RLX_AGENT) + 1u;
    const int h2 = ht & 127;
    const int q10 = h2 / 10, c10 = h2 % 10, q5 = h2 / 5, c5 = h2 % 5, q12 = h2 / 12, c12 = h2 % 12;
    if (hw < 2) {
      v4u tv[6];
      int td[6];
#pragma unroll
      for (int j = 0; j < 4; ++j) {
        const int R = 40 * r128 + 10 * j + q10, rho = R & 31;
        const int k = (R & ~31) + 8 * ((rho >> 2) & 3) + 4 * (rho >> 4) + (rho & 3);
        tv[j] = __builtin_amdgcn_raw_buffer_load_b128(rq, sel(h2 < 100, k * 3840 + c16 * 80 + (c10 < 5 ? 1280 : 2480) + c10 * 16), 0, 0);
        td[j] = sel(h2 < 100, WS_TKV + c16 * 153600 + R * 160 + c10 * 16);
      }
#pragma unroll
      for (int j = 0; j < 2; ++j) {
        const int R = 40 * r128 + 20 * j + q5, ks = R >> 5, rho = R & 31;
        const int k = 32 * ks + 8 * ((rho >> 2) & 3) + 4 * (rho >> 4) + (rho & 3);
        tv[4 + j] = __builtin_amdgcn_raw_buffer_load_b128(rq, sel(h2 < 100, k * 3840 + c16 * 80 + c5 * 16), 0, 0);
        td[4 + j] = sel(h2 < 100, WS_TKV + c16 * 153600 + 102400 + ks * 2560 + (c5 < 4 ? rho * 64 + (c5 ^ (2 * ((rho >> 2) & 1))) * 16 : 2048 + rho * 16));
      }
      BAR_RAW();
#pragma unroll
      for (int j = 0; j < 6; ++j) st128(tv[j], rs, td[j], 0);
      DRAIN();
      BAR_RAW();
      if (hw == 0) {
        wait_flag(FLAG(6, c16) + lane, 16, tag, lane);
        sig[0] = tag;
      }
    } else {
      v4u tv[10];
      int ts[10], td[10];
#pragma unroll
      for (int j = 0; j < 4; ++j) {
        const int R = 40 * r128 + 10 * j + q12, ks = R >> 5, rho = R & 31;
        const int k = ks < 16 ? 80 * (ks >> 1) + 32 * (ks & 1) + rho : 160 * (ks - 16) + 64 + 80 * (rho >> 4) + (rho & 15);
        ts[j] = sel(h2 < 120, k * 3072 + c16 * 192 + c12 * 16);
        td[j] = sel(h2 < 120, WS_T1 + c16 * 122880 + R * 192 + (c12 ^ (2 * ((rho >> 2) & 1))) * 16);
      }
#pragma unroll
      for (int j = 0; j < 2; ++j) {
        const int R = 20 * r64 + 10 * j + q10, ks = R >> 5, rho = R & 31;
        const int k = ks < 16 ? 40 * ks + rho : 40 * (4 * (ks - 16) + (rho >> 3)) + 32 + (rho & 7);
        ts[4 + j] = sel(h2 < 100, k * 1280 + c8 * 160 + c10 * 16);
        td[4 + j] = sel(h2 < 100, WS_TO + c8 * 102400 + R * 160 + c10 * 16);
      }
#pragma unroll
      for (int j = 0; j < 4; ++j) {
        const int R = 96 * r128 + 24 * j + q5, ks = R >> 5, rho = R & 31;
        ts[6 + j] = sel(h2 < 120, R * 1280 + c16 * 80 + c5 * 16);
        td[6 + j] = sel(h2 < 120, WS_T2 + c16 * 122880 + ks * 2560 + (c5 < 4 ? rho * 64 + (c5 ^ (2 * ((rho >> 2) & 1))) * 16 : 2048 + rho * 16));
      }
#pragma unroll
      for (int j = 0; j < 10; ++j) { PIN(ts[j]); PIN(td[j]); }
      unsigned pf0 = 0, pf1 = 0, pf2 = 0;
      BAR_RAW();
#pragma unroll
      for (int j = 0; j < 10; ++j) tv[j] = __builtin_amdgcn_raw_buffer_load_b128(j < 4 ? r1 : j < 6 ? ro : r2, ts[j], 0, 0);
      BAR_RAW();
#pragma unroll
      for (int j = 0; j < 10; ++j) st128(tv[j], rs, td[j], 0);
      asm volatile("" :: "v"(pf0), "v"(pf1), "v"(pf2));
      f32x2 sm[4];
#pragma unroll
      for (int e = 0; e < 4; ++e) sm[e] = (bf2(tv[0][e]) + bf2(tv[1][e])) + (bf2(tv[2][e]) + bf2(tv[3][e]));
      LAS f32x4* sc = (LAS f32x4*)(L + L_KVF);
      sc[h2 * 2] = f32x4{sm[0][0], sm[0][1], sm[1][0], sm[1][1]};
      sc[h2 * 2 + 1] = f32x4{sm[2][0], sm[2][1], sm[3][0], sm[3][1]};
    }
    sig_wait(sig, tag);
    STAMP(1);
    const int kvq = WS_TKV + c16 * 153600;
    dma_image<0, 24>(lds0 + L_BKV, lane * 16, srd_ws, kvq, hw, 3 * (bi >> 3));
    dma_image<24, 56>(lds0 + L_BKV, lane * 16, srd_ws, kvq, hw, 4 * (bi >> 3));
    asm volatile("s_waitcnt vmcnt(8)" ::: "memory");
    STAMP(2);
    BAR();
    dma_image<56, 104>(lds0 + L_BKV, lane * 16, srd_ws, kvq, hw, 6 * (bi >> 3));
    asm volatile("s_waitcnt vmcnt(12)" ::: "memory");
    BAR_RAW();
    if (hw == 3) {
      LAS f32x4* sc = (LAS f32x4*)(L + L_KVF);
      const int l24 = lane < 24 ? lane : 0;
      f32x4 v = sc[l24];
#pragma unroll
      for (int r = 1; r < 10; ++r) v = add4(v, sc[r * 24 + l24]);
      st128(__builtin_bit_cast(v4u, v), rs, sel(lane < 24, lane * 16), WS_CS + (c16 * 16 + r128) * 384);
    }
    DRAIN();
    STAMP(3);
    BAR_RAW();
    if (hw == 2 && lane < 2) flag_set(lane == 0 ? FLAG(5, c16) + r128 : FLAG(7, c8) + r64, tag);
    dma_image<104, 150>(lds0 + L_BKV, lane * 16, srd_ws, kvq, hw, 5 * (bi >> 3));
    ((LAS v4u*)(L + L_KVF))[ht] = v4u{0u, 0u, 0u, 0u};
    ((LAS v4u*)(L + L_KVF))[256 + ht] = v4u{0u, 0u, 0u, 0u};
    auto item = [&](int it, int& red_off, int& kvf_off) {
      const int ie = it / 5, ic = it % 5;
      const int ihead = ie < 40 ? (ie >= 20) : ie - 40;
      const int id = 20 * ihead + 4 * ic, dg = id >> 2;
      const int sa = id >> 4, col = 40 + ie;
      const int tile = ie < 40 ? 2 * sa + (col >> 4) - 2 : 7 + sa;
      red_off = (tile * 64 + (dg & 3) * 16 + (ie < 40 ? (col & 15) : 0)) * 16;
      kvf_off = ((ie < 40 ? (ie >> 4) : 3) * 2 + (dg >> 3)) * 1024 + ((dg & 3) * 16 + (ie < 40 ? (ie & 15) : ihead)) * 16 + ((dg >> 2) & 1) * 8;
    };
    const int it = ht < 210 ? ht : 209;
    int red_off[4], kvf_off, unused;
    item(it, unused, kvf_off);
#pragma unroll
    for (int j = 0; j < 4; ++j) { item(min(j * 64 + lane, 209), red_off[j], unused); PIN(red_off[j]); }
    PIN(kvf_off);
    if (hw == 1) wait_flag(lane < 32 ? FLAG(7, c8) + lane : FLAG(5, c16) + t, 48, tag, lane);
    DRAIN();
    STAMP(4);
    BAR();
    STAMP(11);
    if (hw == 0) {
      f32x4 pv[4][4];
#pragma unroll
      for (int j = 0; j < 4; ++j)
#pragma unroll
        for (int ww = 0; ww < 4; ++ww) pv[j][ww] = *(LAS f32x4*)(L + L_RED + ww * 10240 + red_off[j]);
#pragma unroll
      for (int j = 0; j < 4; ++j) {
        const f32x4 v = add4(add4(pv[j][0], pv[j][1]), add4(pv[j][2], pv[j][3]));
        st128(__builtin_bit_cast(v4u, v), rs, j * 1024 + lane * 16, WS_KVP + (c16 * 16 + r128) * 4096);
      }
      asm volatile("" ::: "memory");
      sig[1] = tag;
      STAMP(12);
      publish(FLAG(1, c16) + r128, tag, lane);
      STAMP(10);
      wait_flag(FLAG(1, c16) + lane, 16, tag, lane);
      sig[2] = tag;
    } else {
      dma_image<40, 100, THR, 3>(lds0 + L_WO, lane * 16, srd_ws, WS_TO + c8 * 102400, hw - 1, 3 * (bi >> 2));
      sig_wait(sig + 1, tag);
      dma_image<0, 40, THR, 3>(lds0 + L_WO, lane * 16, srd_ws, WS_TO + c8 * 102400, hw - 1, 2 * (bi >> 2));
      STAMP(10);
      sig_wait(sig + 2, tag);
    }
    STAMP(5);
    {
      v4u pv[16];
#pragma unroll
      for (int r = 0; r < 16; ++r) pv[r] = ld128(rs, it * 16, WS_KVP + (c16 * 16 + r) * 4096);
      SCHED();
      f32x4 v = __builtin_bit_cast(f32x4, pv[0]);
#pragma unroll
      for (int r = 1; r < 16; ++r) v = add4(v, __builtin_bit_cast(f32x4, pv[r]));
      v2u o; o.x = pk2(lo2(v)); o.y = pk2(hi2(v));
      *(LAS v2u*)(L + L_KVF + kvf_off) = o;
      if (ht >= 200) {
#pragma unroll
        for (int c = 1; c < 4; ++c) *(LAS v2u*)(L + L_KVF + kvf_off + c * 64) = o;
      }
    }
    DRAIN();
    STAMP(6);
    BAR();
    dma_image<70, 120, THR>(lds0 + L_W1, lane * 16, srd_ws, WS_T1 + c16 * 122880, hw, 6 * (bi >> 3));
    if (hw == 0) {
      v4u pv[16];
#pragma unroll
      for (int r = 0; r < 16; ++r) pv[r] = ld128(rs, sel(lane < 24, lane * 16), WS_CS + (c16 * 16 + r) * 384);
      f32x4 v = __builtin_bit_cast(f32x4, pv[0]);
#pragma unroll
      for (int r = 1; r < 16; ++r) v = add4(v, __builtin_bit_cast(f32x4, pv[r]));
      if (lane < 24) *(LAS f32x4*)(L + L_CS + lane * 16) = v;
    }
    STAMP(7);
    BAR();
    dma_image<0, 70, THR>(lds0 + L_W1, lane * 16, srd_ws, WS_T1 + c16 * 122880, hw, 8 * (bi >> 3));
    DRAIN();
    STAMP(8);
    BAR_RAW();
    dma_image<0, 30, THR>(lds0 + L_W2, lane * 16, srd_ws, WS_T2 + c16 * 122880, hw, 3 * (bi >> 3));
    BAR_RAW();
    dma_image<30, 120, THR>(lds0 + L_W2, lane * 16, srd_ws, WS_T2 + c16 * 122880, hw, 11 * (bi >> 3));
    DRAIN();
    STAMP(9);
    BAR_RAW();
    return;
  }

  const __amdgpu_buffer_rsrc_t rx = __builtin_amdgcn_make_buffer_rsrc((void*)x, 0, S * D * 2, 0x00020000);
  const int grp32 = 4 * r128 + w;
  constexpr int AW = 6;
  unsigned tag;

  {
    const int tok = 8 * b + (lane & 7), cl = lane >> 3;
    const bool has3 = w < 2;
    v4u xv[3];
#pragma unroll
    for (int j = 0; j < 3; ++j) xv[j] = __builtin_amdgcn_raw_buffer_load_b128(rx, (j < 2 || has3) ? tok * 1280 + (w + 4 * j) * 128 + cl * 16 : OOB, 0, 0);
    tag = __hip_atomic_load((gu32*)(ws + WS_EPOCH), RLX_AGENT) + 1u;
    SCHED();
    int xd = ((tok >> 4) * 20 + 2 * w + (cl >> 2)) * 1024 + ((cl & 3) * 16 + (tok & 15)) * 16;
    PIN(xd);
    SCHED();
    f32x2 f[12];
#pragma unroll
    for (int j = 0; j < 3; ++j)
#pragma unroll
      for (int e = 0; e < 4; ++e) f[j * 4 + e] = bf2(xv[j][e]);
    f32x2 s2 = (f[0] + f[1]) + (f[2] + f[3]) + ((f[4] + f[5]) + (f[6] + f[7])) + ((f[8] + f[9]) + (f[10] + f[11]));
    const float mloc = xsum32(xsum16(xsum8(s2[0] + s2[1]))) * (has3 ? 1.f / 192.f : 1.f / 128.f);
    const float m3 = has3 ? mloc : 0.f;
    f32x2 q2 = {0.f, 0.f}, q3 = {0.f, 0.f};
#pragma unroll
    for (int j = 0; j < 12; j += 2) {
      const f32x2 d0 = f[j] - (j < 8 ? mloc : m3), d1 = f[j + 1] - (j < 8 ? mloc : m3);
      q2 += d0 * d0; q3 += d1 * d1;
    }
    q2 += q3;
    const float m2loc = xsum32(xsum16(xsum8(q2[0] + q2[1])));
    if (lane < 8) *(LAS f32x2*)(L + L_CS + 512 + (lane * 4 + w) * 8) = f32x2{mloc, m2loc};
    STAMP(25);
    BAR();
    const f32x4 p01 = *(LAS f32x4*)(L + L_CS + 512 + (lane & 7) * 32), p23 = *(LAS f32x4*)(L + L_CS + 512 + (lane & 7) * 32 + 16);
    const float mean = (p01[0] + p01[2]) * 0.3f + (p23[0] + p23[2]) * 0.2f;
    const float e0 = p01[0] - mean, e1 = p01[2] - mean, e2 = p23[0] - mean, e3 = p23[2] - mean;
    const float m2 = (p01[1] + p01[3]) + (p23[1] + p23[3]) + 192.f * (e0 * e0 + e1 * e1) + 128.f * (e2 * e2 + e3 * e3);
    const float rstd = __builtin_amdgcn_rsqf(m2 * (1.f / D) + LN_EPS);
#pragma unroll
    for (int j = 0; j < 3; ++j) {
      v4u o;
#pragma unroll
      for (int e = 0; e < 4; ++e) o[e] = pk2((f[j * 4 + e] - mean) * rstd);
      st128(o, rs, (j < 2 || has3) ? xd : OOB, WS_XN + j * 8192);
    }
    STAMP(26);
    DRAIN();
    BAR_RAW();
    if (tid < 2) flag_set(tid == 0 ? FLAG(0, b >> 2) + (b & 3) : FLAG(6, c16) + r128, tag);
  }
  STAMP(1);

  {
    wait_flag(FLAG(0, grp32) + lane, 4, tag, lane);
    STAMP(2);
    v4u a[20][2];
    const int abase = WS_XN + 2 * grp32 * 20 * 1024;
#pragma unroll
    for (int ks = 0; ks < AW; ++ks)
#pragma unroll
      for (int m = 0; m < 2; ++m) a[ks][m] = ld128(rs, lane * 16, abase + (m * 20 + ks) * 1024);
    SCHED();
    BAR_RAW();
    STAMP(27);
    {
      f32x4 acc[2][5];
      const LBase bp(lds0 + L_BKV + (lane >> 2) * 160 + (lane & 3) * 8);
      kloop<2, 5, 20, 1, false>(acc, [&](int ks, int s, int h) { return bp.rd(ks * 5120 + h * 2560 + s * 32); },
        [&](int ks, int m) { return a[ks][m]; },
        [&](int ks) {
          if (ks + AW < 20) {
#pragma unroll
            for (int m = 0; m < 2; ++m) a[ks + AW][m] = ld128(rs, lane * 16, abase + (m * 20 + ks + AW) * 1024);
          }
          if (ks == 3 || ks == 10) BAR_RAW();
        });
      STAMP(3);
      v4u kf[5];
#pragma unroll
      for (int s = 0; s < 5; ++s)
#pragma unroll
        for (int m = 0; m < 2; ++m) {
          f32x2 u0 = lo2(acc[m][s]), u1 = hi2(acc[m][s]);
          if (s < 2) { u0 = phi2(u0); u1 = phi2(u1); }
          if (s == 2) { const f32x2 p0 = phi2(u0), p1 = phi2(u1); u0 = t < 8 ? p0 : u0; u1 = t < 8 ? p1 : u1; }
          kf[s][2 * m] = pk2(u0); kf[s][2 * m + 1] = pk2(u1);
        }
      const v4u ones = {0x3F803F80u, 0x3F803F80u, 0x3F803F80u, 0x3F803F80u};
      const f32x4 z4 = {0.f, 0.f, 0.f, 0.f};
      f32x4 T[10];
      T[0] = mfma(kf[0], kf[2], z4); T[1] = mfma(kf[0], kf[3], z4); T[2] = mfma(kf[1], kf[2], z4); T[3] = mfma(kf[1], kf[3], z4);
      T[4] = mfma(kf[1], kf[4], z4); T[5] = mfma(kf[2], kf[3], z4); T[6] = mfma(kf[2], kf[4], z4);
      T[7] = mfma(kf[0], ones, z4); T[8] = mfma(kf[1], ones, z4); T[9] = mfma(kf[2], ones, z4);
      STAMP(28);
      LAS f32x4* red = (LAS f32x4*)(L + L_RED);
#pragma unroll
      for (int i = 0; i < 10; ++i) red[(w * 10 + i) * 64 + lane] = T[i];
      BAR();
    }
    STAMP(4);
    f32x4 qa[2][3];
    {
      unsigned bp0 = lds0 + L_BQ + (lane >> 2) * 64 + (lane & 3) * 8 + (g & 1) * 32;
      unsigned bp1 = lds0 + L_BQ + (lane >> 2) * 64 + (lane & 3) * 8 + 32 - (g & 1) * 32;
      unsigned bp2 = lds0 + L_BQ + 2048 + (lane >> 2) * 16 + (lane & 1) * 8;
      PIN(bp0); PIN(bp1); PIN(bp2);
      kloop<2, 3, 20, 2, true>(qa,
        [&](int ks, int s, int h) {
          return s == 0 ? rdtr((LAS char*)bp0 + ks * 2560 + h * 1024) : s == 1 ? rdtr((LAS char*)bp1 + ks * 2560 + h * 1024) : rdtr((LAS char*)bp2 + ks * 2560 + h * 256);
        },
        [&](int ks, int m) { return a[ks][m]; },
        [&](int ks) {});
    }
    v4u pq0[2], pq1[2];
#pragma unroll
    for (int m = 0; m < 2; ++m) {
      pq0[m].x = pk2(phi2(lo2(qa[m][0]))); pq0[m].y = pk2(phi2(hi2(qa[m][0])));
      pq0[m].z = pk2(phi2(lo2(qa[m][1]))); pq0[m].w = pk2(phi2(hi2(qa[m][1])));
      const unsigned lo = pk2(phi2(lo2(qa[m][2]))), hi = pk2(phi2(hi2(qa[m][2])));
      pq1[m].x = g < 2 ? lo : 0u; pq1[m].y = g < 2 ? hi : 0u; pq1[m].z = 0u; pq1[m].w = 0u;
      PIN(pq0[m]); PIN(pq1[m]);
    }
    STAMP(5);
    BAR_RAW();
    STAMP(17);
    {
      v4u kv[4][2];
#pragma unroll
      for (int es = 3; es >= 0; --es)
#pragma unroll
        for (int ks = 0; ks < 2; ++ks) kv[es][ks] = *(LAS v4u*)(L + L_KVF + (es * 2 + ks) * 1024 + lane * 16);
      SCHED();
      f32x4 o[2][4];
#pragma unroll
      for (int es = 3; es >= 0; --es)
#pragma unroll
        for (int m = 0; m < 2; ++m) o[m][es] = mfma(kv[es][0], pq0[m], f32x4{0.f, 0.f, 0.f, 0.f});
#pragma unroll
      for (int es = 3; es >= 0; --es)
#pragma unroll
        for (int m = 0; m < 2; ++m) o[m][es] = mfma(kv[es][1], pq1[m], o[m][es]);
#pragma unroll
      for (int m = 0; m < 2; ++m) {
        const float i0 = __builtin_amdgcn_rcpf(o[m][3][0] + 1e-6f), i1 = __builtin_amdgcn_rcpf(o[m][3][1] + 1e-6f);
        const float im = g == 0 ? i0 : i1;
        v4u ov;
        ov.x = pk2(lo2(o[m][0]) * i0); ov.y = pk2(hi2(o[m][0]) * i0);
        ov.z = pk2(lo2(o[m][1]) * im); ov.w = pk2(hi2(o[m][1]) * im);
        v2u ol; ol.x = pk2(lo2(o[m][2]) * i1); ol.y = pk2(hi2(o[m][2]) * i1);
        const int tt = 2 * grp32 + m;
        st128(ov, rs, lane * 16, WS_O + (tt * 20 + c16) * 1024);
        st64(ol, rs, g < 2 ? lane * 8 : OOB, WS_O + (tt * 20 + 16) * 1024 + c16 * 256);
      }
      STAMP(18);
      publish(FLAG(2, grp32) + c16, tag, lane);
    }
    STAMP(7);
  }

  {
    const int tt = 4 * r64 + w, tok = tt * 16 + t;
    wait_flag(FLAG(2, tt >> 1) + lane, 16, tag, lane);
    STAMP(8);
    if (b == 0 && tid == 0) __hip_atomic_store((gu32*)(ws + WS_EPOCH), tag, RLX_AGENT);
    v4u a[20];
    v2u xr[5];
#pragma unroll
    for (int ks = 0; ks < 2 * AW; ++ks) a[ks] = ld128(rs, lane * 16, WS_O + (tt * 20 + ks) * 1024);
    SCHED();
    f32x4 acc[1][5];
    const LBase bp(lds0 + L_WO + (lane >> 2) * 160 + (lane & 3) * 8);
    kloop<1, 5, 20, 1, true>(acc, [&](int ks, int s, int h) { return bp.rd(ks * 5120 + h * 2560 + s * 32); },
      [&](int ks, int m) { return a[ks]; },
      [&](int ks) {
        if (ks + 2 * AW < 16) a[ks + 2 * AW] = ld128(rs, lane * 16, WS_O + (tt * 20 + ks + 2 * AW) * 1024);
        else if (ks + 2 * AW < 20) a[ks + 2 * AW] = ld_split(rs, lane, WS_O + (tt * 20 + ks + 2 * AW) * 1024);
        if (ks >= 8 && ks < 13) xr[ks - 8] = __builtin_amdgcn_raw_buffer_load_b64(rx, tok * 1280 + (80 * c8 + 16 * (ks - 8) + 4 * g) * 2, 0, 0);
      });
    BAR_RAW();
    STAMP(9);
    v2u hb[5];
    f32x2 hv[10];
#pragma unroll
    for (int s = 0; s < 5; ++s) {
      hb[s].x = pk2(lo2(acc[0][s]) + bf2(xr[s].x));
      hb[s].y = pk2(hi2(acc[0][s]) + bf2(xr[s].y));
      hv[2 * s] = bf2(hb[s].x); hv[2 * s + 1] = bf2(hb[s].y);
    }
    st128(v4u{hb[0].x, hb[0].y, hb[1].x, hb[1].y}, rs, lane * 16, WS_H + (tt * 20 + 2 * c8) * 1024);
    st128(v4u{hb[2].x, hb[2].y, hb[3].x, hb[3].y}, rs, lane * 16, WS_H + (tt * 20 + 2 * c8 + 1) * 1024);
    st64(hb[4], rs, lane * 8, WS_H + (tt * 20 + 16) * 1024 + c8 * 512);
    const f32x2 s2 = ((hv[0] + hv[1]) + (hv[2] + hv[3])) + ((hv[4] + hv[5]) + (hv[6] + hv[7])) + (hv[8] + hv[9]);
    const float mc = xsum32(xsum16(s2[0] + s2[1])) * (1.f / 80.f);
    f32x2 q2 = {0.f, 0.f}, q3 = {0.f, 0.f};
#pragma unroll
    for (int j = 0; j < 10; j += 2) { const f32x2 d0 = hv[j] - mc, d1 = hv[j + 1] - mc; q2 += d0 * d0; q3 += d1 * d1; }
    q2 += q3;
    const float m2 = xsum32(xsum16(q2[0] + q2[1]));
    st64(v2u{__builtin_bit_cast(unsigned, mc), __builtin_bit_cast(unsigned, m2)}, rs, g == 0 ? t * 8 : OOB, WS_ST + (tt * 8 + c8) * 128);
    STAMP(20);
    publish(FLAG(3, tt >> 1) + (tt & 1) * 8 + c8, tag, lane);
    STAMP(10);
  }

  {
    v2u bb[6];
#pragma unroll
    for (int s = 0; s < 6; ++s) bb[s] = *(const v2u*)(b1 + 96 * c16 + 16 * s + 4 * g);
    wait_flag(FLAG(3, grp32) + lane, 16, tag, lane);
    STAMP(11);
    v4u stt[2];
#pragma unroll
    for (int m = 0; m < 2; ++m) {
      const v2u s0 = ld64(rs, g * 256 + t * 8, WS_ST + (2 * grp32 + m) * 1024), s1 = ld64(rs, g * 256 + 128 + t * 8, WS_ST + (2 * grp32 + m) * 1024);
      stt[m] = v4u{s0.x, s0.y, s1.x, s1.y};
    }
    constexpr int WIN = AW + 1;
    v4u a[WIN][2];
    const int abase = WS_H + 2 * grp32 * 20 * 1024;
#pragma unroll
    for (int ks = 0; ks < AW; ++ks)
#pragma unroll
      for (int m = 0; m < 2; ++m) a[ks][m] = ld128(rs, lane * 16, abase + (m * 20 + ks) * 1024);
    SCHED();
    float nm[2], rr[2];
#pragma unroll
    for (int m = 0; m < 2; ++m) {
      const f32x4 sv = __builtin_bit_cast(f32x4, stt[m]);
      const float mean = xsum32(xsum16(sv[0] + sv[2])) * 0.125f;
      const float m2 = xsum32(xsum16(sv[1] + sv[3] + 80.f * ((sv[0] - mean) * (sv[0] - mean) + (sv[2] - mean) * (sv[2] - mean))));
      const float var = m2 * (1.f / D);
      const float r1 = __builtin_amdgcn_rsqf(var + LN_EPS);
      rr[m] = r1 * __builtin_amdgcn_rsqf(var * r1 * r1 + LN_EPS);
      nm[m] = -mean * rr[m];
      PIN(rr[m]); PIN(nm[m]);
    }
    BAR_RAW();
    STAMP(22);
    const LBase bp0(lds0 + L_W1 + (lane >> 2) * 192 + (lane & 3) * 8 + (g & 1) * 32);
    const LBase bp1(lds0 + L_W1 + (lane >> 2) * 192 + (lane & 3) * 8 - (g & 1) * 32);
    f32x4 acc[2][6];
    f32x4 cs[6];
    kloop<2, 6, 20, 1, true>(acc, [&](int ks, int s, int h) { return ((s & 1) ? bp1 : bp0).rd(ks * 6144 + h * 3072 + s * 32); },
      [&](int ks, int m) { return a[ks % WIN][m]; },
      [&](int ks) {
        if (ks + AW < 20) {
#pragma unroll
          for (int m = 0; m < 2; ++m)
            a[(ks + AW) % WIN][m] = ks + AW < 16 ? ld128(rs, lane * 16, abase + (m * 20 + ks + AW) * 1024) : ld_split(rs, lane, abase + (m * 20 + ks + AW) * 1024);
        }
        if (ks == 19) {
#pragma unroll
          for (int s = 0; s < 6; ++s) cs[s] = *(LAS f32x4*)(L + L_CS + (s * 4 + g) * 16);
        }
      });
    BAR_RAW();
    STAMP(12);
#pragma unroll
    for (int m = 0; m < 2; ++m) {
      v2u hb[6];
#pragma unroll
      for (int s = 0; s < 6; ++s) {
        const f32x2 p0 = lo2(acc[m][s]) * rr[m] + (lo2(cs[s]) * nm[m] + bf2(bb[s].x));
        const f32x2 p1 = hi2(acc[m][s]) * rr[m] + (hi2(cs[s]) * nm[m] + bf2(bb[s].y));
        hb[s].x = pk2(gelu2(p0)); hb[s].y = pk2(gelu2(p1));
        if (s & 1)
          st128(v4u{hb[s - 1].x, hb[s - 1].y, hb[s].x, hb[s].y}, rs, lane * 16, WS_H1 + ((2 * grp32 + m) * 48 + 3 * c16 + (s >> 1)) * 1024);
      }
    }
    STAMP(24);
    publish(FLAG(4, grp32) + c16, tag, lane);
    STAMP(13);
  }

  {
    v2u hr[2][3], bb[3];
#pragma unroll
    for (int s = 0; s < 3; ++s) {
      int n = 40 * c16 + 16 * s + 4 * g;
      if (s == 2 && g >= 2) n = 40 * c16;
      bb[s] = *(const v2u*)(b2 + n);
      const int cc = n / 80, off = n % 80, ss = off >> 4, gg = (off >> 2) & 3;
      const int ho = ss < 4 ? (2 * cc + (ss >> 1)) * 1024 + (gg * 16 + t) * 16 + (ss & 1) * 8 : 16 * 1024 + cc * 512 + (gg * 16 + t) * 8;
#pragma unroll
      for (int m = 0; m < 2; ++m) hr[m][s] = ld64(rs, ho, WS_H + (2 * grp32 + m) * 20 * 1024);
    }
    wait_flag(FLAG(4, grp32) + lane, 16, tag, lane);
    STAMP(14);
    constexpr int WIN = 8;
    v4u a[WIN][2];
    const int abase = WS_H1 + 2 * grp32 * 48 * 1024;
#pragma unroll
    for (int ks = 0; ks < WIN - 1; ++ks)
#pragma unroll
      for (int m = 0; m < 2; ++m) a[ks][m] = ld128(rs, lane * 16, abase + (m * 48 + ks) * 1024);
    SCHED();
    BAR_RAW();
    f32x4 acc[2][3];
    const LBase bp0(lds0 + L_W2 + (lane >> 2) * 64 + (lane & 3) * 8 + (g & 1) * 32);
    const LBase bp1(lds0 + L_W2 + (lane >> 2) * 64 + (lane & 3) * 8 + 32 - (g & 1) * 32);
    const LBase bp2(lds0 + L_W2 + 2048 + (lane >> 2) * 16 + (lane & 1) * 8);
    kloop<2, 3, 48, 2, true>(acc,
      [&](int ks, int s, int h) { return s == 0 ? bp0.rd(ks * 2560 + h * 1024) : s == 1 ? bp1.rd(ks * 2560 + h * 1024) : bp2.rd(ks * 2560 + h * 256); },
      [&](int ks, int m) { return a[ks % WIN][m]; },
      [&](int ks) {
        if (ks + WIN - 1 < 48) {
#pragma unroll
          for (int m = 0; m < 2; ++m) a[(ks + WIN - 1) % WIN][m] = ld128(rs, lane * 16, abase + (m * 48 + ks + WIN - 1) * 1024);
        }
      });
    STAMP(15);
#pragma unroll
    for (int m = 0; m < 2; ++m) {
      const int tok = (2 * grp32 + m) * 16 + t;
#pragma unroll
      for (int s = 0; s < 3; ++s) {
        v2u o;
        o.x = pk2(lo2(acc[m][s]) + bf2(bb[s].x) + bf2(hr[m][s].x));
        o.y = pk2(hi2(acc[m][s]) + bf2(bb[s].y) + bf2(hr[m][s].y));
        if (s < 2 || g < 2) *(v2u*)(out + tok * D + 40 * c16 + 16 * s + 4 * g) = o;
      }
    }
    STAMP(16);
  }
}
}

extern "C" void kernel_launch(void* const* d_in, const int* in_sizes, int n_in, void* d_out, int out_size, void* d_ws,
                              size_t ws_size, hipStream_t stream) {
  const unsigned short* x = (const unsigned short*)d_in[0];
  const unsigned short* wqkv = (const unsigned short*)d_in[1];
  const unsigned short* wo = (const unsigned short*)d_in[2];
  const unsigned short* w1 = (const unsigned short*)d_in[4];
  const unsigned short* b1 = (const unsigned short*)d_in[5];
  const unsigned short* w2 = (const unsigned short*)d_in[6];
  const unsigned short* b2 = (const unsigned short*)d_in[7];
  fused_block<<<256, 512, 0, stream>>>(x, wqkv, wo, w1, b1, w2, b2, (unsigned short*)d_out, (unsigned char*)d_ws);
}
```

```cpp
#include <hip/hip_runtime.h>
#include <hip/hip_bf16.h>

#define STAMPS 0
#define THR 0
#define TOUCH 0
#define CODEPF 1
#define FLAGPF 0
#define HPRIO 0
#define WOLATE 0
#define SLEEP 1

#define GAS __attribute__((address_space(1)))
#define LAS __attribute__((address_space(3)))
#define RLX_AGENT __ATOMIC_RELAXED, __HIP_MEMORY_SCOPE_AGENT
typedef unsigned v4u __attribute__((ext_vector_type(4)));
typedef unsigned v2u __attribute__((ext_vector_type(2)));
typedef float f32x4 __attribute__((ext_vector_type(4)));
typedef float f32x2 __attribute__((ext_vector_type(2)));
typedef __bf16 bf16x8 __attribute__((ext_vector_type(8)));
typedef __bf16 bf16x2 __attribute__((ext_vector_type(2)));
typedef short s16x4 __attribute__((ext_vector_type(4)));
typedef GAS unsigned gu32;

namespace {
constexpr int S = 2048, D = 640, F = 1536;
constexpr float LN_EPS = 1e-5f;
constexpr int AUX_SC1 = 16;
constexpr unsigned kSpinCap = 1u << 17;
constexpr int OOB = 0x7FFFFF00;

constexpr int WS_EPOCH = 0;
constexpr int WS_FLAG = 4096;
constexpr int FLAG_SET = 64 * 256;
constexpr int WS_XN = 1 << 20;
constexpr int WS_O = WS_XN + 128 * 20 * 1024;
constexpr int WS_H = WS_O + 128 * 20 * 1024;
constexpr int WS_H1 = WS_H + 128 * 20 * 1024;
constexpr int WS_KVP = WS_H1 + 128 * 48 * 1024;
constexpr int WS_ST = WS_KVP + 16 * 16 * 4096;
constexpr int WS_DBG = WS_ST + 2048 * 64;
constexpr int WS_TKV = WS_DBG + 256 * 8 * 32 * 8;
constexpr int WS_TO = WS_TKV + 16 * 153600;
constexpr int WS_T1 = WS_TO + 8 * 102400;
constexpr int WS_T2 = WS_T1 + 16 * 122880;
constexpr int WS_CS = WS_T2 + 16 * 122880;
constexpr int WS_BYTES = WS_CS + 16 * 16 * 384;

constexpr int L_BKV = 0, L_BQ = 102400;
constexpr int L_RED = 0, L_KVF = 153600;
constexpr int L_WO = 0, L_W1 = 30720, L_W2 = 0, L_CS = 161792;
constexpr int LDS_BYTES = 163840;

__device__ __forceinline__ f32x4 mfma(v4u a, v4u b, f32x4 c) {
  return __builtin_amdgcn_mfma_f32_16x16x32_bf16(__builtin_bit_cast(bf16x8, a), __builtin_bit_cast(bf16x8, b), c, 0, 0, 0);
}
__device__ __forceinline__ unsigned pk2(f32x2 v) { return __builtin_bit_cast(unsigned, __builtin_convertvector(v, bf16x2)); }
__device__ __forceinline__ unsigned pk2(float lo, float hi) { return pk2(f32x2{lo, hi}); }
__device__ __forceinline__ float bf_lo(unsigned u) { return __builtin_bit_cast(float, u << 16); }
__device__ __forceinline__ float bf_hi(unsigned u) { return __builtin_bit_cast(float, u & 0xFFFF0000u); }
__device__ __forceinline__ f32x2 bf2(unsigned u) { return f32x2{bf_lo(u), bf_hi(u)}; }
__device__ __forceinline__ f32x2 lo2(f32x4 v) { return f32x2{v[0], v[1]}; }
__device__ __forceinline__ f32x2 hi2(f32x4 v) { return f32x2{v[2], v[3]}; }
__device__ __forceinline__ v4u mk_srd(const void* p, unsigned bytes) {
  const unsigned long long a = (unsigned long long)p;
  v4u r;
  r.x = (unsigned)a; r.y = (unsigned)(a >> 32) & 0xFFFFu; r.z = bytes; r.w = 0x00020000u;
  return r;
}
#define DRAIN() asm volatile("s_waitcnt vmcnt(0)" ::: "memory")
#define BAR() asm volatile("s_waitcnt lgkmcnt(0)\n\ts_barrier" ::: "memory")
#define BAR_RAW() asm volatile("s_barrier" ::: "memory")
#define SCHED() __builtin_amdgcn_sched_barrier(0)
#define PIN(x) asm volatile("" : "+v"(x))

__device__ __forceinline__ void dma2(unsigned lds_addr, unsigned voff, v4u srd, unsigned soff) {
  unsigned keep;
  asm volatile("s_mov_b32 %0, m0\n\ts_mov_b32 m0, %1\n\ts_nop 0\n\t"
               "buffer_load_dwordx4 %2, %3, %4 offen sc1 lds\n\t"
               "buffer_load_dwordx4 %2, %3, %4 offen offset:1024 sc1 lds\n\t"
               "s_mov_b32 m0, %0"
               : "=&s"(keep) : "s"(lds_addr), "v"(voff), "s"(srd), "s"(soff) : "memory");
}
template <int LO, int HI, int TH = 0, int NW = 4>
__device__ __forceinline__ void dma_image(unsigned lds_base, unsigned voff, v4u srd, int ws_off, int hw, int rot) {
  constexpr int NP = (HI - LO) / 2;
#pragma unroll 1
  for (int i = hw; i < NP; i += NW) {
    int j = i + rot;
    if (j >= NP) j -= NP;
    dma2(lds_base + LO * 1024 + j * 2048, voff, srd, ws_off + LO * 1024 + j * 2048);
    if (TH) asm volatile("s_waitcnt vmcnt(%0)" :: "i"(TH - 2) : "memory");
  }
}
__device__ __forceinline__ v2u rdtr(LAS char* p) { return __builtin_bit_cast(v2u, __builtin_amdgcn_ds_read_tr16_b64_v4i16((LAS s16x4*)p)); }
struct LBase {
  unsigned a[3];
  __device__ __forceinline__ explicit LBase(unsigned v) { a[0] = v; a[1] = v + 65536; a[2] = v + 131072; PIN(a[0]); PIN(a[1]); PIN(a[2]); }
  __device__ __forceinline__ v2u rd(int off) const { return rdtr((LAS char*)(a[off >> 16]) + (off & 65535)); }
};
template <int MT, int NT, int KS, int PD, bool WA, class RB, class GA, class PRE>
__device__ __forceinline__ void kloop(f32x4 (&acc)[MT][NT], RB rb, GA ga, PRE pre) {
  v2u bl[PD + 1][NT], bh[PD + 1][NT];
#pragma unroll
  for (int m = 0; m < MT; ++m)
#pragma unroll
    for (int s = 0; s < NT; ++s) acc[m][s] = f32x4{0.f, 0.f, 0.f, 0.f};
#pragma unroll
  for (int ks = 0; ks < PD; ++ks)
#pragma unroll
    for (int s = 0; s < NT; ++s) { bl[ks][s] = rb(ks, s, 0); bh[ks][s] = rb(ks, s, 1); }
  SCHED();
#pragma unroll
  for (int ks = 0; ks < KS; ++ks) {
    pre(ks);
#pragma unroll
    for (int s = 0; s < NT; ++s)
#pragma unroll
      for (int m = 0; m < MT; ++m) {
        const v2u lo = bl[ks % (PD + 1)][s], hi = bh[ks % (PD + 1)][s];
        const v4u wv = {lo.x, lo.y, hi.x, hi.y};
        acc[m][s] = WA ? mfma(wv, ga(ks, m), acc[m][s]) : mfma(ga(ks, m), wv, acc[m][s]);
        if (ks + PD < KS) {
          if (MT == 1 || m == 0) bl[(ks + PD) % (PD + 1)][s] = rb(ks + PD, s, 0);
          if (MT == 1 || m == 1) bh[(ks + PD) % (PD + 1)][s] = rb(ks + PD, s, 1);
        }
        SCHED();
      }
  }
}
__device__ __forceinline__ v4u ld128(__amdgpu_buffer_rsrc_t rs, int voff, int soff) {
  return __builtin_amdgcn_raw_buffer_load_b128(rs, voff, soff, AUX_SC1);
}
__device__ __forceinline__ v2u ld64(__amdgpu_buffer_rsrc_t rs, int voff, int soff) {
  return __builtin_amdgcn_raw_buffer_load_b64(rs, voff, soff, AUX_SC1);
}
__device__ __forceinline__ void st128(v4u v, __amdgpu_buffer_rsrc_t rs, int voff, int soff) {
  __builtin_amdgcn_raw_buffer_store_b128(v, rs, (int)((unsigned)voff + (unsigned)soff), 0, AUX_SC1);
}
__device__ __forceinline__ int sel(bool c, int a) {
  PIN(a);
  return c ? a : OOB;
}
__device__ __forceinline__ v4u ld_split(__amdgpu_buffer_rsrc_t rs, int lane, int soff) {
  const v2u a = ld64(rs, lane * 8, soff), b = ld64(rs, lane * 8 + 512, soff);
  return v4u{a.x, a.y, b.x, b.y};
}
__device__ __forceinline__ void st64(v2u v, __amdgpu_buffer_rsrc_t rs, int voff, int soff) {
  __builtin_amdgcn_raw_buffer_store_b64(v, rs, voff, soff, AUX_SC1);
}
__device__ __forceinline__ void flag_set(gu32* p, unsigned tag) {
  asm volatile("global_atomic_swap %0, %1, off sc1" :: "v"(p), "v"(tag) : "memory");
}
__device__ __forceinline__ void wait_flag(gu32* p, int n, unsigned tag, int lane) {
#pragma unroll 1
  for (unsigned spins = 0; spins < kSpinCap; ++spins) {
    unsigned v = tag;
    if (lane < n) asm volatile("global_atomic_add %0, %1, %2, off sc0 sc1\n\ts_waitcnt vmcnt(0)" : "=&v"(v) : "v"(p), "v"(0u) : "memory");
    if (__all(v == tag)) break;
    if (SLEEP) __builtin_amdgcn_s_sleep(SLEEP);
  }
  asm volatile("" ::: "memory");
}
__device__ __forceinline__ void sig_wait(volatile LAS unsigned* p, unsigned tag) {
#pragma unroll 1
  for (unsigned spins = 0; spins < 64 * kSpinCap; ++spins) {
    if (*p == tag) break;
    __builtin_amdgcn_s_sleep(1);
  }
  asm volatile("" ::: "memory");
}
__device__ __forceinline__ void publish(gu32* flag, unsigned tag, int lane) {
  DRAIN();
  if (lane == 0) flag_set(flag, tag);
}
__device__ __forceinline__ f32x4 add4(f32x4 a, f32x4 b) {
  f32x4 r;
#pragma unroll
  for (int i = 0; i < 4; ++i) { float x; asm("v_add_f32 %0, %1, %2" : "=v"(x) : "v"(a[i]), "v"(b[i])); r[i] = x; }
  return r;
}
__device__ __forceinline__ float xsum32(float x) {
  unsigned u = __builtin_bit_cast(unsigned, x), v = u;
  asm("" : "+v"(v));
  auto r = __builtin_amdgcn_permlane32_swap(u, v, false, false);
  const unsigned a = r[0], b = r[1];
  return __builtin_bit_cast(float, a) + __builtin_bit_cast(float, b);
}
__device__ __forceinline__ float xsum16(float x) {
  unsigned u = __builtin_bit_cast(unsigned, x), v = u;
  asm("" : "+v"(v));
  auto r = __builtin_amdgcn_permlane16_swap(u, v, false, false);
  const unsigned a = r[0], b = r[1];
  return __builtin_bit_cast(float, a) + __builtin_bit_cast(float, b);
}
__device__ __forceinline__ float xsum8(float x) {
  return x + __builtin_bit_cast(float, __builtin_amdgcn_update_dpp(0, __builtin_bit_cast(int, x), 0x128, 0xf, 0xf, false));
}
__device__ __forceinline__ f32x2 phi2(f32x2 t) {
  const f32x2 u = t * 1.4426950408889634f;
  const f32x2 e = {__builtin_amdgcn_exp2f(fminf(u[0], 0.f)), __builtin_amdgcn_exp2f(fminf(u[1], 0.f))};
  const f32x2 p = {fmaxf(t[0], 0.f), fmaxf(t[1], 0.f)};
  return (p + 1e-6f) + e;
}
__device__ __forceinline__ f32x2 gelu2(f32x2 x) {
  const f32x2 u = x * (x * x * -0.1029432397f + -2.302208198f);
  const f32x2 d = f32x2{__builtin_amdgcn_exp2f(u[0]), __builtin_amdgcn_exp2f(u[1])} + 1.f;
  return x * f32x2{__builtin_amdgcn_rcpf(d[0]), __builtin_amdgcn_rcpf(d[1])};
}

#define STAMP(i) do { } while (0)

__global__ void __launch_bounds__(512, 2)
fused_block(const unsigned short* __restrict__ x, const unsigned short* __restrict__ wqkv, const unsigned short* __restrict__ wo,
            const unsigned short* __restrict__ w1, const unsigned short* __restrict__ b1, const unsigned short* __restrict__ w2,
            const unsigned short* __restrict__ b2, unsigned short* __restrict__ out, unsigned char* ws) {
  __shared__ __attribute__((aligned(16))) char smem[LDS_BYTES];
  unsigned long long pc0;
  asm volatile("s_getpc_b64 %0" : "=s"(pc0));
  LAS char* L = (LAS char*)smem;
  const unsigned lds0 = (unsigned)(size_t)L;
  const int tid = threadIdx.x, lane = tid & 63, w = __builtin_amdgcn_readfirstlane(tid >> 6);
  const int b = blockIdx.x, xcd = b & 7, bi = b >> 3;
  const int g = lane >> 4, t = lane & 15;
  const __amdgpu_buffer_rsrc_t rs = __builtin_amdgcn_make_buffer_rsrc((void*)ws, 0, WS_BYTES, 0x00020000);
  gu32* flags = (gu32*)(ws + WS_FLAG);
#define FLAG(set, grp) (flags + ((set) * FLAG_SET + (grp) * 256) / 4)
  STAMP(0);
  const int r128 = 4 * (xcd >> 1) + (bi >> 3), c16 = 8 * (xcd & 1) + (bi & 7);
  const int r64 = 8 * (xcd >> 1) + (bi >> 2), c8 = 4 * (xcd & 1) + (bi & 3);

  if (w >= 4) {
    const int hw = w - 4, ht = tid - 256;
    if (HPRIO) __builtin_amdgcn_s_setprio(HPRIO);
    const v4u srd_ws = mk_srd(ws, WS_BYTES);
    const __amdgpu_buffer_rsrc_t rq = __builtin_amdgcn_make_buffer_rsrc((void*)wqkv, 0, D * 3 * D * 2, 0x00020000);
    const __amdgpu_buffer_rsrc_t ro = __builtin_amdgcn_make_buffer_rsrc((void*)wo, 0, D * D * 2, 0x00020000);
    const __amdgpu_buffer_rsrc_t r1 = __builtin_amdgcn_make_buffer_rsrc((void*)w1, 0, D * F * 2, 0x00020000);
    const __amdgpu_buffer_rsrc_t r2 = __builtin_amdgcn_make_buffer_rsrc((void*)w2, 0, F * D * 2, 0x00020000);
    volatile LAS unsigned* sig = (volatile LAS unsigned*)(L + L_CS + 1024);
    const unsigned tag = __hip_atomic_load((gu32*)(ws + WS_EPOCH), RLX_AGENT) + 1u;
    const int h2 = ht & 127;
    const int q10 = h2 / 10, c10 = h2 % 10, q5 = h2 / 5, c5 = h2 % 5, q12 = h2 / 12, c12 = h2 % 12;
    if (hw < 2) {
      v4u tv[6];
      int td[6];
#pragma unroll
      for (int j = 0; j < 4; ++j) {
        const int R = 40 * r128 + 10 * j + q10, rho = R & 31;
        const int k = (R & ~31) + 8 * ((rho >> 2) & 3) + 4 * (rho >> 4) + (rho & 3);
        tv[j] = __builtin_amdgcn_raw_buffer_load_b128(rq, sel(h2 < 100, k * 3840 + c16 * 80 + (c10 < 5 ? 1280 : 2480) + c10 * 16), 0, 0);
        td[j] = sel(h2 < 100, WS_TKV + c16 * 153600 + R * 160 + c10 * 16);
      }
#pragma unroll
      for (int j = 0; j < 2; ++j) {
        const int R = 40 * r128 + 20 * j + q5, ks = R >> 5, rho = R & 31;
        const int k = 32 * ks + 8 * ((rho >> 2) & 3) + 4 * (rho >> 4) + (rho & 3);
        tv[4 + j] = __builtin_amdgcn_raw_buffer_load_b128(rq, sel(h2 < 100, k * 3840 + c16 * 80 + c5 * 16), 0, 0);
        td[4 + j] = sel(h2 < 100, WS_TKV + c16 * 153600 + 102400 + ks * 2560 + (c5 < 4 ? rho * 64 + (c5 ^ (2 * ((rho >> 2) & 1))) * 16 : 2048 + rho * 16));
      }
      BAR_RAW();
#pragma unroll
      for (int j = 0; j < 6; ++j) st128(tv[j], rs, td[j], 0);
      DRAIN();
      BAR_RAW();
      if (hw == 0) {
        wait_flag(FLAG(6, c16) + lane, 16, tag, lane);
        sig[0] = tag;
      }
    } else {
      v4u tv[10];
      int ts[10], td[10];
#pragma unroll
      for (int j = 0; j < 4; ++j) {
        const int R = 40 * r128 + 10 * j + q12, ks = R >> 5, rho = R & 31;
        const int k = ks < 16 ? 80 * (ks >> 1) + 32 * (ks & 1) + rho : 160 * (ks - 16) + 64 + 80 * (rho >> 4) + (rho & 15);
        ts[j] = sel(h2 < 120, k * 3072 + c16 * 192 + c12 * 16);
        td[j] = sel(h2 < 120, WS_T1 + c16 * 122880 + R * 192 + (c12 ^ (2 * ((rho >> 2) & 1))) * 16);
      }
#pragma unroll
      for (int j = 0; j < 2; ++j) {
        const int R = 20 * r64 + 10 * j + q10, ks = R >> 5, rho = R & 31;
        const int k = ks < 16 ? 40 * ks + rho : 40 * (4 * (ks - 16) + (rho >> 3)) + 32 + (rho & 7);
        ts[4 + j] = sel(h2 < 100, k * 1280 + c8 * 160 + c10 * 16);
        td[4 + j] = sel(h2 < 100, WS_TO + c8 * 102400 + R * 160 + c10 * 16);
      }
#pragma unroll
      for (int j = 0; j < 4; ++j) {
        const int R = 96 * r128 + 24 * j + q5, ks = R >> 5, rho = R & 31;
        ts[6 + j] = sel(h2 < 120, R * 1280 + c16 * 80 + c5 * 16);
        td[6 + j] = sel(h2 < 120, WS_T2 + c16 * 122880 + ks * 2560 + (c5 < 4 ? rho * 64 + (c5 ^ (2 * ((rho >> 2) & 1))) * 16 : 2048 + rho * 16));
      }
#pragma unroll
      for (int j = 0; j < 10; ++j) { PIN(ts[j]); PIN(td[j]); }
      unsigned pf0 = 0, pf1 = 0, pf2 = 0;
      if (hw == 3) {
        const GAS unsigned char* cb = (const GAS unsigned char*)(pc0 & ~127ull);
        if (lane < 10) pf0 = *(const GAS unsigned*)(cb + (lane * 32 + bi) * 128);
      }
      BAR_RAW();
#pragma unroll
      for (int j = 0; j < 10; ++j) tv[j] = __builtin_amdgcn_raw_buffer_load_b128(j < 4 ? r1 : j < 6 ? ro : r2, ts[j], 0, 0);
      BAR_RAW();
#pragma unroll
      for (int j = 0; j < 10; ++j) st128(tv[j], rs, td[j], 0);
      asm volatile("" :: "v"(pf0), "v"(pf1), "v"(pf2));
      f32x2 sm[4];
#pragma unroll
      for (int e = 0; e < 4; ++e) sm[e] = (bf2(tv[0][e]) + bf2(tv[1][e])) + (bf2(tv[2][e]) + bf2(tv[3][e]));
      LAS f32x4* sc = (LAS f32x4*)(L + L_KVF);
      sc[h2 * 2] = f32x4{sm[0][0], sm[0][1], sm[1][0], sm[1][1]};
      sc[h2 * 2 + 1] = f32x4{sm[2][0], sm[2][1], sm[3][0], sm[3][1]};
    }
    sig_wait(sig, tag);
    STAMP(1);
    const int kvq = WS_TKV + c16 * 153600;
    dma_image<0, 24>(lds0 + L_BKV, lane * 16, srd_ws, kvq, hw, 3 * (bi >> 3));
    dma_image<24, 56>(lds0 + L_BKV, lane * 16, srd_ws, kvq, hw, 4 * (bi >> 3));
    asm volatile("s_waitcnt vmcnt(8)" ::: "memory");
    STAMP(2);
    BAR();
    dma_image<56, 104>(lds0 + L_BKV, lane * 16, srd_ws, kvq, hw, 6 * (bi >> 3));
    asm volatile("s_waitcnt vmcnt(12)" ::: "memory");
    BAR_RAW();
    if (hw == 3) {
      LAS f32x4* sc = (LAS f32x4*)(L + L_KVF);
      const int l24 = lane < 24 ? lane : 0;
      f32x4 v = sc[l24];
#pragma unroll
      for (int r = 1; r < 10; ++r) v = add4(v, sc[r * 24 + l24]);
      st128(__builtin_bit_cast(v4u, v), rs, sel(lane < 24, lane * 16), WS_CS + (c16 * 16 + r128) * 384);
    }
    DRAIN();
    STAMP(3);
    BAR_RAW();
    if (hw == 2 && lane < 2) flag_set(lane == 0 ? FLAG(5, c16) + r128 : FLAG(7, c8) + r64, tag);
    dma_image<104, 150>(lds0 + L_BKV, lane * 16, srd_ws, kvq, hw, 5 * (bi >> 3));
    ((LAS v4u*)(L + L_KVF))[ht] = v4u{0u, 0u, 0u, 0u};
    ((LAS v4u*)(L + L_KVF))[256 + ht] = v4u{0u, 0u, 0u, 0u};
    auto item = [&](int it, int& red_off, int& kvf_off) {
      const int ie = it / 5, ic = it % 5;
      const int ihead = ie < 40 ? (ie >= 20) : ie - 40;
      const int id = 20 * ihead + 4 * ic, dg = id >> 2;
      const int sa = id >> 4, col = 40 + ie;
      const int tile = ie < 40 ? 2 * sa + (col >> 4) - 2 : 7 + sa;
      red_off = (tile * 64 + (dg & 3) * 16 + (ie < 40 ? (col & 15) : 0)) * 16;
      kvf_off = ((ie < 40 ? (ie >> 4) : 3) * 2 + (dg >> 3)) * 1024 + ((dg & 3) * 16 + (ie < 40 ? (ie & 15) : ihead)) * 16 + ((dg >> 2) & 1) * 8;
    };
    const int it = ht < 210 ? ht : 209;
    int red_off[4], kvf_off, unused;
    item(it, unused, kvf_off);
#pragma unroll
    for (int j = 0; j < 4; ++j) { item(min(j * 64 + lane, 209), red_off[j], unused); PIN(red_off[j]); }
    PIN(kvf_off);
    if (hw == 1) wait_flag(lane < 32 ? FLAG(7, c8) + lane : FLAG(5, c16) + t, 48, tag, lane);
    DRAIN();
    STAMP(4);
    BAR();
    STAMP(11);
    if (hw == 0) {
      f32x4 pv[4][4];
#pragma unroll
      for (int j = 0; j < 4; ++j)
#pragma unroll
        for (int ww = 0; ww < 4; ++ww) pv[j][ww] = *(LAS f32x4*)(L + L_RED + ww * 10240 + red_off[j]);
#pragma unroll
      for (int j = 0; j < 4; ++j) {
        const f32x4 v = add4(add4(pv[j][0], pv[j][1]), add4(pv[j][2], pv[j][3]));
        st128(__builtin_bit_cast(v4u, v), rs, j * 1024 + lane * 16, WS_KVP + (c16 * 16 + r128) * 4096);
      }
      asm volatile("" ::: "memory");
      sig[1] = tag;
      STAMP(12);
      publish(FLAG(1, c16) + r128, tag, lane);
      STAMP(10);
      wait_flag(FLAG(1, c16) + lane, 16, tag, lane);
      sig[2] = tag;
    } else {
      dma_image<40, 100, THR, 3>(lds0 + L_WO, lane * 16, srd_ws, WS_TO + c8 * 102400, hw - 1, 3 * (bi >> 2));
      sig_wait(sig + 1, tag);
      dma_image<0, 40, THR, 3>(lds0 + L_WO, lane * 16, srd_ws, WS_TO + c8 * 102400, hw - 1, 2 * (bi >> 2));
      STAMP(10);
      sig_wait(sig + 2, tag);
    }
    STAMP(5);
    {
      v4u pv[16];
#pragma unroll
      for (int r = 0; r < 16; ++r) pv[r] = ld128(rs, it * 16, WS_KVP + (c16 * 16 + r) * 4096);
      SCHED();
      f32x4 v = __builtin_bit_cast(f32x4, pv[0]);
#pragma unroll
      for (int r = 1; r < 16; ++r) v = add4(v, __builtin_bit_cast(f32x4, pv[r]));
      v2u o; o.x = pk2(lo2(v)); o.y = pk2(hi2(v));
      *(LAS v2u*)(L + L_KVF + kvf_off) = o;
      if (ht >= 200) {
#pragma unroll
        for (int c = 1; c < 4; ++c) *(LAS v2u*)(L + L_KVF + kvf_off + c * 64) = o;
      }
    }
    DRAIN();
    STAMP(6);
    BAR();
    dma_image<70, 120, THR>(lds0 + L_W1, lane * 16, srd_ws, WS_T1 + c16 * 122880, hw, 6 * (bi >> 3));
    if (hw == 0) {
      v4u pv[16];
#pragma unroll
      for (int r = 0; r < 16; ++r) pv[r] = ld128(rs, sel(lane < 24, lane * 16), WS_CS + (c16 * 16 + r) * 384);
      f32x4 v = __builtin_bit_cast(f32x4, pv[0]);
#pragma unroll
      for (int r = 1; r < 16; ++r) v = add4(v, __builtin_bit_cast(f32x4, pv[r]));
      if (lane < 24) *(LAS f32x4*)(L + L_CS + lane * 16) = v;
    }
    STAMP(7);
    BAR();
    dma_image<0, 70, THR>(lds0 + L_W1, lane * 16, srd_ws, WS_T1 + c16 * 122880, hw, 8 * (bi >> 3));
    DRAIN();
    STAMP(8);
    BAR_RAW();
    dma_image<0, 30, THR>(lds0 + L_W2, lane * 16, srd_ws, WS_T2 + c16 * 122880, hw, 3 * (bi >> 3));
    BAR_RAW();
    dma_image<30, 120, THR>(lds0 + L_W2, lane * 16, srd_ws, WS_T2 + c16 * 122880, hw, 11 * (bi >> 3));
    DRAIN();
    STAMP(9);
    BAR_RAW();
    return;
  }

  const __amdgpu_buffer_rsrc_t rx = __builtin_amdgcn_make_buffer_rsrc((void*)x, 0, S * D * 2, 0x00020000);
  const int grp32 = 4 * r128 + w;
  constexpr int AW = 6;
  unsigned tag;

  {
    const int tok = 8 * b + (lane & 7), cl = lane >> 3;
    const bool has3 = w < 2;
    v4u xv[3];
#pragma unroll
    for (int j = 0; j < 3; ++j) xv[j] = __builtin_amdgcn_raw_buffer_load_b128(rx, (j < 2 || has3) ? tok * 1280 + (w + 4 * j) * 128 + cl * 16 : OOB, 0, 0);
    tag = __hip_atomic_load((gu32*)(ws + WS_EPOCH), RLX_AGENT) + 1u;
    SCHED();
    int xd = ((tok >> 4) * 20 + 2 * w + (cl >> 2)) * 1024 + ((cl & 3) * 16 + (tok & 15)) * 16;
    PIN(xd);
    SCHED();
    f32x2 f[12];
#pragma unroll
    for (int j = 0; j < 3; ++j)
#pragma unroll
      for (int e = 0; e < 4; ++e) f[j * 4 + e] = bf2(xv[j][e]);
    f32x2 s2 = (f[0] + f[1]) + (f[2] + f[3]) + ((f[4] + f[5]) + (f[6] + f[7])) + ((f[8] + f[9]) + (f[10] + f[11]));
    const float mloc = xsum32(xsum16(xsum8(s2[0] + s2[1]))) * (has3 ? 1.f / 192.f : 1.f / 128.f);
    const float m3 = has3 ? mloc : 0.f;
    f32x2 q2 = {0.f, 0.f}, q3 = {0.f, 0.f};
#pragma unroll
    for (int j = 0; j < 12; j += 2) {
      const f32x2 d0 = f[j] - (j < 8 ? mloc : m3), d1 = f[j + 1] - (j < 8 ? mloc : m3);
      q2 += d0 * d0; q3 += d1 * d1;
    }
    q2 += q3;
    const float m2loc = xsum32(xsum16(xsum8(q2[0] + q2[1])));
    if (lane < 8) *(LAS f32x2*)(L + L_CS + 512 + (lane * 4 + w) * 8) = f32x2{mloc, m2loc};
    STAMP(25);
    BAR();
    const f32x4 p01 = *(LAS f32x4*)(L + L_CS + 512 + (lane & 7) * 32), p23 = *(LAS f32x4*)(L + L_CS + 512 + (lane & 7) * 32 + 16);
    const float mean = (p01[0] + p01[2]) * 0.3f + (p23[0] + p23[2]) * 0.2f;
    const float e0 = p01[0] - mean, e1 = p01[2] - mean, e2 = p23[0] - mean, e3 = p23[2] - mean;
    const float m2 = (p01[1] + p01[3]) + (p23[1] + p23[3]) + 192.f * (e0 * e0 + e1 * e1) + 128.f * (e2 * e2 + e3 * e3);
    const float rstd = __builtin_amdgcn_rsqf(m2 * (1.f / D) + LN_EPS);
#pragma unroll
    for (int j = 0; j < 3; ++j) {
      v4u o;
#pragma unroll
      for (int e = 0; e < 4; ++e) o[e] = pk2((f[j * 4 + e] - mean) * rstd);
      st128(o, rs, (j < 2 || has3) ? xd : OOB, WS_XN + j * 8192);
    }
    STAMP(26);
    DRAIN();
    BAR_RAW();
    if (tid < 2) flag_set(tid == 0 ? FLAG(0, b >> 2) + (b & 3) : FLAG(6, c16) + r128, tag);
  }
  STAMP(1);

  {
    wait_flag(FLAG(0, grp32) + lane, 4, tag, lane);
    STAMP(2);
    v4u a[20][2];
    const int abase = WS_XN + 2 * grp32 * 20 * 1024;
#pragma unroll
    for (int ks = 0; ks < AW; ++ks)
#pragma unroll
      for (int m = 0; m < 2; ++m) a[ks][m] = ld128(rs, lane * 16, abase + (m * 20 + ks) * 1024);
    SCHED();
    BAR_RAW();
    STAMP(27);
    {
      f32x4 acc[2][5];
      const LBase bp(lds0 + L_BKV + (lane >> 2) * 160 + (lane & 3) * 8);
      kloop<2, 5, 20, 1, false>(acc, [&](int ks, int s, int h) { return bp.rd(ks * 5120 + h * 2560 + s * 32); },
        [&](int ks, int m) { return a[ks][m]; },
        [&](int ks) {
          if (ks + AW < 20) {
#pragma unroll
            for (int m = 0; m < 2; ++m) a[ks + AW][m] = ld128(rs, lane * 16, abase + (m * 20 + ks + AW) * 1024);
          }
          if (ks == 3 || ks == 10) BAR_RAW();
        });
      STAMP(3);
      v4u kf[5];
#pragma unroll
      for (int s = 0; s < 5; ++s)
#pragma unroll
        for (int m = 0; m < 2; ++m) {
          f32x2 u0 = lo2(acc[m][s]), u1 = hi2(acc[m][s]);
          if (s < 2) { u0 = phi2(u0); u1 = phi2(u1); }
          if (s == 2) { const f32x2 p0 = phi2(u0), p1 = phi2(u1); u0 = t < 8 ? p0 : u0; u1 = t < 8 ? p1 : u1; }
          kf[s][2 * m] = pk2(u0); kf[s][2 * m + 1] = pk2(u1);
        }
      const v4u ones = {0x3F803F80u, 0x3F803F80u, 0x3F803F80u, 0x3F803F80u};
      const f32x4 z4 = {0.f, 0.f, 0.f, 0.f};
      f32x4 T[10];
      T[0] = mfma(kf[0], kf[2], z4); T[1] = mfma(kf[0], kf[3], z4); T[2] = mfma(kf[1], kf[2], z4); T[3] = mfma(kf[1], kf[3], z4);
      T[4] = mfma(kf[1], kf[4], z4); T[5] = mfma(kf[2], kf[3], z4); T[6] = mfma(kf[2], kf[4], z4);
      T[7] = mfma(kf[0], ones, z4); T[8] = mfma(kf[1], ones, z4); T[9] = mfma(kf[2], ones, z4);
      STAMP(28);
      LAS f32x4* red = (LAS f32x4*)(L + L_RED);
#pragma unroll
      for (int i = 0; i < 10; ++i) red[(w * 10 + i) * 64 + lane] = T[i];
      BAR();
    }
    STAMP(4);
    f32x4 qa[2][3];
    {
      unsigned bp0 = lds0 + L_BQ + (lane >> 2) * 64 + (lane & 3) * 8 + (g & 1) * 32;
      unsigned bp1 = lds0 + L_BQ + (lane >> 2) * 64 + (lane & 3) * 8 + 32 - (g & 1) * 32;
      unsigned bp2 = lds0 + L_BQ + 2048 + (lane >> 2) * 16 + (lane & 1) * 8;
      PIN(bp0); PIN(bp1); PIN(bp2);
      kloop<2, 3, 20, 2, true>(qa,
        [&](int ks, int s, int h) {
          return s == 0 ? rdtr((LAS char*)bp0 + ks * 2560 + h * 1024) : s == 1 ? rdtr((LAS char*)bp1 + ks * 2560 + h * 1024) : rdtr((LAS char*)bp2 + ks * 2560 + h * 256);
        },
        [&](int ks, int m) { return a[ks][m]; },
        [&](int ks) {});
    }
    v4u pq0[2], pq1[2];
#pragma unroll
    for (int m = 0; m < 2; ++m) {
      pq0[m].x = pk2(phi2(lo2(qa[m][0]))); pq0[m].y = pk2(phi2(hi2(qa[m][0])));
      pq0[m].z = pk2(phi2(lo2(qa[m][1]))); pq0[m].w = pk2(phi2(hi2(qa[m][1])));
      const unsigned lo = pk2(phi2(lo2(qa[m][2]))), hi = pk2(phi2(hi2(qa[m][2])));
      pq1[m].x = g < 2 ? lo : 0u; pq1[m].y = g < 2 ? hi : 0u; pq1[m].z = 0u; pq1[m].w = 0u;
      PIN(pq0[m]); PIN(pq1[m]);
    }
    STAMP(5);
    BAR_RAW();
    STAMP(17);
    {
      v4u kv[4][2];
#pragma unroll
      for (int es = 3; es >= 0; --es)
#pragma unroll
        for (int ks = 0; ks < 2; ++ks) kv[es][ks] = *(LAS v4u*)(L + L_KVF + (es * 2 + ks) * 1024 + lane * 16);
      SCHED();
      f32x4 o[2][4];
#pragma unroll
      for (int es = 3; es >= 0; --es)
#pragma unroll
        for (int m = 0; m < 2; ++m) o[m][es] = mfma(kv[es][0], pq0[m], f32x4{0.f, 0.f, 0.f, 0.f});
#pragma unroll
      for (int es = 3; es >= 0; --es)
#pragma unroll
        for (int m = 0; m < 2; ++m) o[m][es] = mfma(kv[es][1], pq1[m], o[m][es]);
#pragma unroll
      for (int m = 0; m < 2; ++m) {
        const float i0 = __builtin_amdgcn_rcpf(o[m][3][0] + 1e-6f), i1 = __builtin_amdgcn_rcpf(o[m][3][1] + 1e-6f);
        const float im = g == 0 ? i0 : i1;
        v4u ov;
        ov.x = pk2(lo2(o[m][0]) * i0); ov.y = pk2(hi2(o[m][0]) * i0);
        ov.z = pk2(lo2(o[m][1]) * im); ov.w = pk2(hi2(o[m][1]) * im);
        v2u ol; ol.x = pk2(lo2(o[m][2]) * i1); ol.y = pk2(hi2(o[m][2]) * i1);
        const int tt = 2 * grp32 + m;
        st128(ov, rs, lane * 16, WS_O + (tt * 20 + c16) * 1024);
        st64(ol, rs, g < 2 ? lane * 8 : OOB, WS_O + (tt * 20 + 16) * 1024 + c16 * 256);
      }
      STAMP(18);
      publish(FLAG(2, grp32) + c16, tag, lane);
    }
    STAMP(7);
  }

  {
    const int tt = 4 * r64 + w, tok = tt * 16 + t;
    wait_flag(FLAG(2, tt >> 1) + lane, 16, tag, lane);
    STAMP(8);
    if (b == 0 && tid == 0) __hip_atomic_store((gu32*)(ws + WS_EPOCH), tag, RLX_AGENT);
    v4u a[20];
    v2u xr[5];
#pragma unroll
    for (int ks = 0; ks < 2 * AW; ++ks) a[ks] = ld128(rs, lane * 16, WS_O + (tt * 20 + ks) * 1024);
    SCHED();
    f32x4 acc[1][5];
    const LBase bp(lds0 + L_WO + (lane >> 2) * 160 + (lane & 3) * 8);
    kloop<1, 5, 20, 1, true>(acc, [&](int ks, int s, int h) { return bp.rd(ks * 5120 + h * 2560 + s * 32); },
      [&](int ks, int m) { return a[ks]; },
      [&](int ks) {
        if (ks + 2 * AW < 16) a[ks + 2 * AW] = ld128(rs, lane * 16, WS_O + (tt * 20 + ks + 2 * AW) * 1024);
        else if (ks + 2 * AW < 20) a[ks + 2 * AW] = ld_split(rs, lane, WS_O + (tt * 20 + ks + 2 * AW) * 1024);
        if (ks >= 8 && ks < 13) xr[ks - 8] = __builtin_amdgcn_raw_buffer_load_b64(rx, tok * 1280 + (80 * c8 + 16 * (ks - 8) + 4 * g) * 2, 0, 0);
      });
    BAR_RAW();
    STAMP(9);
    v2u hb[5];
    f32x2 hv[10];
#pragma unroll
    for (int s = 0; s < 5; ++s) {
      hb[s].x = pk2(lo2(acc[0][s]) + bf2(xr[s].x));
      hb[s].y = pk2(hi2(acc[0][s]) + bf2(xr[s].y));
      hv[2 * s] = bf2(hb[s].x); hv[2 * s + 1] = bf2(hb[s].y);
    }
    st128(v4u{hb[0].x, hb[0].y, hb[1].x, hb[1].y}, rs, lane * 16, WS_H + (tt * 20 + 2 * c8) * 1024);
    st128(v4u{hb[2].x, hb[2].y, hb[3].x, hb[3].y}, rs, lane * 16, WS_H + (tt * 20 + 2 * c8 + 1) * 1024);
    st64(hb[4], rs, lane * 8, WS_H + (tt * 20 + 16) * 1024 + c8 * 512);
    const f32x2 s2 = ((hv[0] + hv[1]) + (hv[2] + hv[3])) + ((hv[4] + hv[5]) + (hv[6] + hv[7])) + (hv[8] + hv[9]);
    const float mc = xsum32(xsum16(s2[0] + s2[1])) * (1.f / 80.f);
    f32x2 q2 = {0.f, 0.f}, q3 = {0.f, 0.f};
#pragma unroll
    for (int j = 0; j < 10; j += 2) { const f32x2 d0 = hv[j] - mc, d1 = hv[j + 1] - mc; q2 += d0 * d0; q3 += d1 * d1; }
    q2 += q3;
    const float m2 = xsum32(xsum16(q2[0] + q2[1]));
    st64(v2u{__builtin_bit_cast(unsigned, mc), __builtin_bit_cast(unsigned, m2)}, rs, g == 0 ? t * 8 : OOB, WS_ST + (tt * 8 + c8) * 128);
    STAMP(20);
    publish(FLAG(3, tt >> 1) + (tt & 1) * 8 + c8, tag, lane);
    STAMP(10);
  }

  {
    v2u bb[6];
#pragma unroll
    for (int s = 0; s < 6; ++s) bb[s] = *(const v2u*)(b1 + 96 * c16 + 16 * s + 4 * g);
    wait_flag(FLAG(3, grp32) + lane, 16, tag, lane);
    STAMP(11);
    v4u stt[2];
#pragma unroll
    for (int m = 0; m < 2; ++m) {
      const v2u s0 = ld64(rs, g * 256 + t * 8, WS_ST + (2 * grp32 + m) * 1024), s1 = ld64(rs, g * 256 + 128 + t * 8, WS_ST + (2 * grp32 + m) * 1024);
      stt[m] = v4u{s0.x, s0.y, s1.x, s1.y};
    }
    constexpr int WIN = AW + 1;
    v4u a[WIN][2];
    const int abase = WS_H + 2 * grp32 * 20 * 1024;
#pragma unroll
    for (int ks = 0; ks < AW; ++ks)
#pragma unroll
      for (int m = 0; m < 2; ++m) a[ks][m] = ld128(rs, lane * 16, abase + (m * 20 + ks) * 1024);
    SCHED();
    float nm[2], rr[2];
#pragma unroll
    for (int m = 0; m < 2; ++m) {
      const f32x4 sv = __builtin_bit_cast(f32x4, stt[m]);
      const float mean = xsum32(xsum16(sv[0] + sv[2])) * 0.125f;
      const float m2 = xsum32(xsum16(sv[1] + sv[3] + 80.f * ((sv[0] - mean) * (sv[0] - mean) + (sv[2] - mean) * (sv[2] - mean))));
      const float var = m2 * (1.f / D);
      const float r1 = __builtin_amdgcn_rsqf(var + LN_EPS);
      rr[m] = r1 * __builtin_amdgcn_rsqf(var * r1 * r1 + LN_EPS);
      nm[m] = -mean * rr[m];
      PIN(rr[m]); PIN(nm[m]);
    }
    BAR_RAW();
    STAMP(22);
    const LBase bp0(lds0 + L_W1 + (lane >> 2) * 192 + (lane & 3) * 8 + (g & 1) * 32);
    const LBase bp1(lds0 + L_W1 + (lane >> 2) * 192 + (lane & 3) * 8 - (g & 1) * 32);
    f32x4 acc[2][6];
    f32x4 cs[6];
    kloop<2, 6, 20, 1, true>(acc, [&](int ks, int s, int h) { return ((s & 1) ? bp1 : bp0).rd(ks * 6144 + h * 3072 + s * 32); },
      [&](int ks, int m) { return a[ks % WIN][m]; },
      [&](int ks) {
        if (ks + AW < 20) {
#pragma unroll
          for (int m = 0; m < 2; ++m)
            a[(ks + AW) % WIN][m] = ks + AW < 16 ? ld128(rs, lane * 16, abase + (m * 20 + ks + AW) * 1024) : ld_split(rs, lane, abase + (m * 20 + ks + AW) * 1024);
        }
        if (ks == 19) {
#pragma unroll
          for (int s = 0; s < 6; ++s) cs[s] = *(LAS f32x4*)(L + L_CS + (s * 4 + g) * 16);
        }
      });
    BAR_RAW();
    STAMP(12);
#pragma unroll
    for (int m = 0; m < 2; ++m) {
      v2u hb[6];
#pragma unroll
      for (int s = 0; s < 6; ++s) {
        const f32x2 p0 = lo2(acc[m][s]) * rr[m] + (lo2(cs[s]) * nm[m] + bf2(bb[s].x));
        const f32x2 p1 = hi2(acc[m][s]) * rr[m] + (hi2(cs[s]) * nm[m] + bf2(bb[s].y));
        hb[s].x = pk2(gelu2(p0)); hb[s].y = pk2(gelu2(p1));
        if (s & 1)
          st128(v4u{hb[s - 1].x, hb[s - 1].y, hb[s].x, hb[s].y}, rs, lane * 16, WS_H1 + ((2 * grp32 + m) * 48 + 3 * c16 + (s >> 1)) * 1024);
      }
    }
    STAMP(24);
    publish(FLAG(4, grp32) + c16, tag, lane);
    STAMP(13);
  }

  {
    v2u hr[2][3], bb[3];
#pragma unroll
    for (int s = 0; s < 3; ++s) {
      int n = 40 * c16 + 16 * s + 4 * g;
      if (s == 2 && g >= 2) n = 40 * c16;
      bb[s] = *(const v2u*)(b2 + n);
      const int cc = n / 80, off = n % 80, ss = off >> 4, gg = (off >> 2) & 3;
      const int ho = ss < 4 ? (2 * cc + (ss >> 1)) * 1024 + (gg * 16 + t) * 16 + (ss & 1) * 8 : 16 * 1024 + cc * 512 + (gg * 16 + t) * 8;
#pragma unroll
      for (int m = 0; m < 2; ++m) hr[m][s] = ld64(rs, ho, WS_H + (2 * grp32 + m) * 20 * 1024);
    }
    wait_flag(FLAG(4, grp32) + lane, 16, tag, lane);
    STAMP(14);
    constexpr int WIN = 8;
    v4u a[WIN][2];
    const int abase = WS_H1 + 2 * grp32 * 48 * 1024;
#pragma unroll
    for (int ks = 0; ks < WIN - 1; ++ks)
#pragma unroll
      for (int m = 0; m < 2; ++m) a[ks][m] = ld128(rs, lane * 16, abase + (m * 48 + ks) * 1024);
    SCHED();
    BAR_RAW();
    f32x4 acc[2][3];
    const LBase bp0(lds0 + L_W2 + (lane >> 2) * 64 + (lane & 3) * 8 + (g & 1) * 32);
    const LBase bp1(lds0 + L_W2 + (lane >> 2) * 64 + (lane & 3) * 8 + 32 - (g & 1) * 32);
    const LBase bp2(lds0 + L_W2 + 2048 + (lane >> 2) * 16 + (lane & 1) * 8);
    kloop<2, 3, 48, 2, true>(acc,
      [&](int ks, int s, int h) { return s == 0 ? bp0.rd(ks * 2560 + h * 1024) : s == 1 ? bp1.rd(ks * 2560 + h * 1024) : bp2.rd(ks * 2560 + h * 256); },
      [&](int ks, int m) { return a[ks % WIN][m]; },
      [&](int ks) {
        if (ks + WIN - 1 < 48) {
#pragma unroll
          for (int m = 0; m < 2; ++m) a[(ks + WIN - 1) % WIN][m] = ld128(rs, lane * 16, abase + (m * 48 + ks + WIN - 1) * 1024);
        }
      });
    STAMP(15);
#pragma unroll
    for (int m = 0; m < 2; ++m) {
      const int tok = (2 * grp32 + m) * 16 + t;
#pragma unroll
      for (int s = 0; s < 3; ++s) {
        v2u o;
        o.x = pk2(lo2(acc[m][s]) + bf2(bb[s].x) + bf2(hr[m][s].x));
        o.y = pk2(hi2(acc[m][s]) + bf2(bb[s].y) + bf2(hr[m][s].y));
        if (s < 2 || g < 2) *(v2u*)(out + tok * D + 40 * c16 + 16 * s + 4 * g) = o;
      }
    }
    STAMP(16);
  }
}
}

extern "C" void kernel_launch(void* const* d_in, const int* in_sizes, int n_in, void* d_out, int out_size, void* d_ws,
                              size_t ws_size, hipStream_t stream) {
  const unsigned short* x = (const unsigned short*)d_in[0];
  const unsigned short* wqkv = (const unsigned short*)d_in[1];
  const unsigned short* wo = (const unsigned short*)d_in[2];
  const unsigned short* w1 = (const unsigned short*)d_in[4];
  const unsigned short* b1 = (const unsigned short*)d_in[5];
  const unsigned short* w2 = (const unsigned short*)d_in[6];
  const unsigned short* b2 = (const unsigned short*)d_in[7];
  fused_block<<<256, 512, 0, stream>>>(x, wqkv, wo, w1, b1, w2, b2, (unsigned short*)d_out, (unsigned char*)d_ws);
}
```

```cpp
#include <hip/hip_runtime.h>
#include <hip/hip_bf16.h>

#define STAMPS 0
#define THR 0
#define TOUCH 0
#define CODEPF 0
#define FLAGPF 0
#define HPRIO 0
#define WOLATE 0
#define SLEEP 1

#define GAS __attribute__((address_space(1)))
#define LAS __attribute__((address_space(3)))
#define RLX_AGENT __ATOMIC_RELAXED, __HIP_MEMORY_SCOPE_AGENT
typedef unsigned v4u __attribute__((ext_vector_type(4)));
typedef unsigned v2u __attribute__((ext_vector_type(2)));
typedef float f32x4 __attribute__((ext_vector_type(4)));
typedef float f32x2 __attribute__((ext_vector_type(2)));
typedef __bf16 bf16x8 __attribute__((ext_vector_type(8)));
typedef __bf16 bf16x2 __attribute__((ext_vector_type(2)));
typedef short s16x4 __attribute__((ext_vector_type(4)));
typedef GAS unsigned gu32;

namespace {
constexpr int S = 2048, D = 640, F = 1536;
constexpr float LN_EPS = 1e-5f;
constexpr int AUX_SC1 = 16;
constexpr unsigned kSpinCap = 1u << 17;
constexpr int OOB = 0x7FFFFF00;

constexpr int WS_EPOCH = 0;
constexpr int WS_FLAG = 4096;
constexpr int FLAG_SET = 64 * 256;
constexpr int WS_XN = 1 << 20;
constexpr int WS_O = WS_XN + 128 * 20 * 1024;
constexpr int WS_H = WS_O + 128 * 20 * 1024;
constexpr int WS_H1 = WS_H + 128 * 20 * 1024;
constexpr int WS_KVP = WS_H1 + 128 * 48 * 1024;
constexpr int WS_ST = WS_KVP + 16 * 16 * 4096;
constexpr int WS_DBG = WS_ST + 2048 * 64;
constexpr int WS_TKV = WS_DBG + 256 * 8 * 32 * 8;
constexpr int WS_TO = WS_TKV + 16 * 153600;
constexpr int WS_T1 = WS_TO + 8 * 102400;
constexpr int WS_T2 = WS_T1 + 16 * 122880;
constexpr int WS_CS = WS_T2 + 16 * 122880;
constexpr int WS_BYTES = WS_CS + 16 * 16 * 384;

constexpr int L_BKV = 0, L_BQ = 102400;
constexpr int L_RED = 0, L_KVF = 153600;
constexpr int L_WO = 0, L_W1 = 30720, L_W2 = 0, L_CS = 161792;
constexpr int LDS_BYTES = 163840;

__device__ __forceinline__ f32x4 mfma(v4u a, v4u b, f32x4 c) {
  return __builtin_amdgcn_mfma_f32_16x16x32_bf16(__builtin_bit_cast(bf16x8, a), __builtin_bit_cast(bf16x8, b), c, 0, 0, 0);
}
__device__ __forceinline__ unsigned pk2(f32x2 v) { return __builtin_bit_cast(unsigned, __builtin_convertvector(v, bf16x2)); }
__device__ __forceinline__ unsigned pk2(float lo, float hi) { return pk2(f32x2{lo, hi}); }
__device__ __forceinline__ float bf_lo(unsigned u) { return __builtin_bit_cast(float, u << 16); }
__device__ __forceinline__ float bf_hi(unsigned u) { return __builtin_bit_cast(float, u & 0xFFFF0000u); }
__device__ __forceinline__ f32x2 bf2(unsigned u) { return f32x2{bf_lo(u), bf_hi(u)}; }
__device__ __forceinline__ f32x2 lo2(f32x4 v) { return f32x2{v[0], v[1]}; }
__device__ __forceinline__ f32x2 hi2(f32x4 v) { return f32x2{v[2], v[3]}; }
__device__ __forceinline__ v4u mk_srd(const void* p, unsigned bytes) {
  const unsigned long long a = (unsigned long long)p;
  v4u r;
  r.x = (unsigned)a; r.y = (unsigned)(a >> 32) & 0xFFFFu; r.z = bytes; r.w = 0x00020000u;
  return r;
}
#define DRAIN() asm volatile("s_waitcnt vmcnt(0)" ::: "memory")
#define BAR() asm volatile("s_waitcnt lgkmcnt(0)\n\ts_barrier" ::: "memory")
#define BAR_RAW() asm volatile("s_barrier" ::: "memory")
#define SCHED() __builtin_amdgcn_sched_barrier(0)
#define PIN(x) asm volatile("" : "+v"(x))

__device__ __forceinline__ void dma2(unsigned lds_addr, unsigned voff, v4u srd, unsigned soff) {
  unsigned keep;
  asm volatile("s_mov_b32 %0, m0\n\ts_mov_b32 m0, %1\n\ts_nop 0\n\t"
               "buffer_load_dwordx4 %2, %3, %4 offen sc1 lds\n\t"
               "buffer_load_dwordx4 %2, %3, %4 offen offset:1024 sc1 lds\n\t"
               "s_mov_b32 m0, %0"
               : "=&s"(keep) : "s"(lds_addr), "v"(voff), "s"(srd), "s"(soff) : "memory");
}
template <int LO, int HI, int TH = 0, int NW = 4>
__device__ __forceinline__ void dma_image(unsigned lds_base, unsigned voff, v4u srd, int ws_off, int hw, int rot) {
  constexpr int NP = (HI - LO) / 2;
#pragma unroll 1
  for (int i = hw; i < NP; i += NW) {
    int j = i + rot;
    if (j >= NP) j -= NP;
    dma2(lds_base + LO * 1024 + j * 2048, voff, srd, ws_off + LO * 1024 + j * 2048);
    if (TH) asm volatile("s_waitcnt vmcnt(%0)" :: "i"(TH - 2) : "memory");
  }
}
__device__ __forceinline__ v2u rdtr(LAS char* p) { return __builtin_bit_cast(v2u, __builtin_amdgcn_ds_read_tr16_b64_v4i16((LAS s16x4*)p)); }
struct LBase {
  unsigned a[3];
  __device__ __forceinline__ explicit LBase(unsigned v) { a[0] = v; a[1] = v + 65536; a[2] = v + 131072; PIN(a[0]); PIN(a[1]); PIN(a[2]); }
  __device__ __forceinline__ v2u rd(int off) const { return rdtr((LAS char*)(a[off >> 16]) + (off & 65535)); }
};
template <int MT, int NT, int KS, int PD, bool WA, class RB, class GA, class PRE>
__device__ __forceinline__ void kloop(f32x4 (&acc)[MT][NT], RB rb, GA ga, PRE pre) {
  v2u bl[PD + 1][NT], bh[PD + 1][NT];
#pragma unroll
  for (int m = 0; m < MT; ++m)
#pragma unroll
    for (int s = 0; s < NT; ++s) acc[m][s] = f32x4{0.f, 0.f, 0.f, 0.f};
#pragma unroll
  for (int ks = 0; ks < PD; ++ks)
#pragma unroll
    for (int s = 0; s < NT; ++s) { bl[ks][s] = rb(ks, s, 0); bh[ks][s] = rb(ks, s, 1); }
  SCHED();
#pragma unroll
  for (int ks = 0; ks < KS; ++ks) {
    pre(ks);
#pragma unroll
    for (int s = 0; s < NT; ++s)
#pragma unroll
      for (int m = 0; m < MT; ++m) {
        const v2u lo = bl[ks % (PD + 1)][s], hi = bh[ks % (PD + 1)][s];
        const v4u wv = {lo.x, lo.y, hi.x, hi.y};
        acc[m][s] = WA ? mfma(wv, ga(ks, m), acc[m][s]) : mfma(ga(ks, m), wv, acc[m][s]);
        if (ks + PD < KS) {
          if (MT == 1 || m == 0) bl[(ks + PD) % (PD + 1)][s] = rb(ks + PD, s, 0);
          if (MT == 1 || m == 1) bh[(ks + PD) % (PD + 1)][s] = rb(ks + PD, s, 1);
        }
        SCHED();
      }
  }
}
__device__ __forceinline__ v4u ld128(__amdgpu_buffer_rsrc_t rs, int voff, int soff) {
  return __builtin_amdgcn_raw_buffer_load_b128(rs, voff, soff, AUX_SC1);
}
__device__ __forceinline__ v2u ld64(__amdgpu_buffer_rsrc_t rs, int voff, int soff) {
  return __builtin_amdgcn_raw_buffer_load_b64(rs, voff, soff, AUX_SC1);
}
__device__ __forceinline__ void st128(v4u v, __amdgpu_buffer_rsrc_t rs, int voff, int soff) {
  __builtin_amdgcn_raw_buffer_store_b128(v, rs, (int)((unsigned)voff + (unsigned)soff), 0, AUX_SC1);
}
__device__ __forceinline__ int sel(bool c, int a) {
  PIN(a);
  return c ? a : OOB;
}
__device__ __forceinline__ v4u ld_split(__amdgpu_buffer_rsrc_t rs, int lane, int soff) {
  const v2u a = ld64(rs, lane * 8, soff), b = ld64(rs, lane * 8 + 512, soff);
  return v4u{a.x, a.y, b.x, b.y};
}
__device__ __forceinline__ void st64(v2u v, __amdgpu_buffer_rsrc_t rs, int voff, int soff) {
  __builtin_amdgcn_raw_buffer_store_b64(v, rs, voff, soff, AUX_SC1);
}
__device__ __forceinline__ void flag_set(gu32* p, unsigned tag) {
  asm volatile("global_atomic_swap %0, %1, off sc1" :: "v"(p), "v"(tag) : "memory");
}
__device__ __forceinline__ void wait_flag(gu32* p, int n, unsigned tag, int lane) {
#pragma unroll 1
  for (unsigned spins = 0; spins < kSpinCap; ++spins) {
    unsigned v = tag;
    if (lane < n) asm volatile("global_atomic_add %0, %1, %2, off sc0 sc1\n\ts_waitcnt vmcnt(0)" : "=&v"(v) : "v"(p), "v"(0u) : "memory");
    if (__all(v == tag)) break;
    if (SLEEP) __builtin_amdgcn_s_sleep(SLEEP);
  }
  asm volatile("" ::: "memory");
}
__device__ __forceinline__ void sig_wait(volatile LAS unsigned* p, unsigned tag) {
#pragma unroll 1
  for (unsigned spins = 0; spins < 64 * kSpinCap; ++spins) {
    if (*p == tag) break;
    __builtin_amdgcn_s_sleep(1);
  }
  asm volatile("" ::: "memory");
}
__device__ __forceinline__ void publish(gu32* flag, unsigned tag, int lane) {
  DRAIN();
  if (lane == 0) flag_set(flag, tag);
}
__device__ __forceinline__ f32x4 add4(f32x4 a, f32x4 b) {
  f32x4 r;
#pragma unroll
  for (int i = 0; i < 4; ++i) { float x; asm("v_add_f32 %0, %1, %2" : "=v"(x) : "v"(a[i]), "v"(b[i])); r[i] = x; }
  return r;
}
__device__ __forceinline__ float xsum32(float x) {
  unsigned u = __builtin_bit_cast(unsigned, x), v = u;
  asm("" : "+v"(v));
  auto r = __builtin_amdgcn_permlane32_swap(u, v, false, false);
  const unsigned a = r[0], b = r[1];
  return __builtin_bit_cast(float, a) + __builtin_bit_cast(float, b);
}
__device__ __forceinline__ float xsum16(float x) {
  unsigned u = __builtin_bit_cast(unsigned, x), v = u;
  asm("" : "+v"(v));
  auto r = __builtin_amdgcn_permlane16_swap(u, v, false, false);
  const unsigned a = r[0], b = r[1];
  return __builtin_bit_cast(float, a) + __builtin_bit_cast(float, b);
}
__device__ __forceinline__ float xsum8(float x) {
  return x + __builtin_bit_cast(float, __builtin_amdgcn_update_dpp(0, __builtin_bit_cast(int, x), 0x128, 0xf, 0xf, false));
}
__device__ __forceinline__ f32x2 phi2(f32x2 t) {
  const f32x2 u = t * 1.4426950408889634f;
  const f32x2 e = {__builtin_amdgcn_exp2f(fminf(u[0], 0.f)), __builtin_amdgcn_exp2f(fminf(u[1], 0.f))};
  const f32x2 p = {fmaxf(t[0], 0.f), fmaxf(t[1], 0.f)};
  return (p + 1e-6f) + e;
}
__device__ __forceinline__ f32x2 gelu2(f32x2 x) {
  const f32x2 u = x * (x * x * -0.1029432397f + -2.302208198f);
  const f32x2 d = f32x2{__builtin_amdgcn_exp2f(u[0]), __builtin_amdgcn_exp2f(u[1])} + 1.f;
  return x * f32x2{__builtin_amdgcn_rcpf(d[0]), __builtin_amdgcn_rcpf(d[1])};
}

#define STAMP(i) do { if ((i) == 0) pt0 = __builtin_amdgcn_s_memrealtime(); if ((i) == 25) pt1 = __builtin_amdgcn_s_memrealtime(); } while (0)

__global__ void __launch_bounds__(512, 2)
fused_block(const unsigned short* __restrict__ x, const unsigned short* __restrict__ wqkv, const unsigned short* __restrict__ wo,
            const unsigned short* __restrict__ w1, const unsigned short* __restrict__ b1, const unsigned short* __restrict__ w2,
            const unsigned short* __restrict__ b2, unsigned short* __restrict__ out, unsigned char* ws) {
  __shared__ __attribute__((aligned(16))) char smem[LDS_BYTES];
  LAS char* L = (LAS char*)smem;
  const unsigned lds0 = (unsigned)(size_t)L;
  const int tid = threadIdx.x, lane = tid & 63, w = __builtin_amdgcn_readfirstlane(tid >> 6);
  const int b = blockIdx.x, xcd = b & 7, bi = b >> 3;
  const int g = lane >> 4, t = lane & 15;
  const __amdgpu_buffer_rsrc_t rs = __builtin_amdgcn_make_buffer_rsrc((void*)ws, 0, WS_BYTES, 0x00020000);
  gu32* flags = (gu32*)(ws + WS_FLAG);
#define FLAG(set, grp) (flags + ((set) * FLAG_SET + (grp) * 256) / 4)
  unsigned long long pt0 = 0, pt1 = 0;
  STAMP(0);
  const int r128 = 4 * (xcd >> 1) + (bi >> 3), c16 = 8 * (xcd & 1) + (bi & 7);
  const int r64 = 8 * (xcd >> 1) + (bi >> 2), c8 = 4 * (xcd & 1) + (bi & 3);

  if (w >= 4) {
    const int hw = w - 4, ht = tid - 256;
    if (HPRIO) __builtin_amdgcn_s_setprio(HPRIO);
    const v4u srd_ws = mk_srd(ws, WS_BYTES);
    const __amdgpu_buffer_rsrc_t rq = __builtin_amdgcn_make_buffer_rsrc((void*)wqkv, 0, D * 3 * D * 2, 0x00020000);
    const __amdgpu_buffer_rsrc_t ro = __builtin_amdgcn_make_buffer_rsrc((void*)wo, 0, D * D * 2, 0x00020000);
    const __amdgpu_buffer_rsrc_t r1 = __builtin_amdgcn_make_buffer_rsrc((void*)w1, 0, D * F * 2, 0x00020000);
    const __amdgpu_buffer_rsrc_t r2 = __builtin_amdgcn_make_buffer_rsrc((void*)w2, 0, F * D * 2, 0x00020000);
    volatile LAS unsigned* sig = (volatile LAS unsigned*)(L + L_CS + 1024);
    const unsigned tag = __hip_atomic_load((gu32*)(ws + WS_EPOCH), RLX_AGENT) + 1u;
    const int h2 = ht & 127;
    const int q10 = h2 / 10, c10 = h2 % 10, q5 = h2 / 5, c5 = h2 % 5, q12 = h2 / 12, c12 = h2 % 12;
    if (hw < 2) {
      v4u tv[6];
      int td[6];
#pragma unroll
      for (int j = 0; j < 4; ++j) {
        const int R = 40 * r128 + 10 * j + q10, rho = R & 31;
        const int k = (R & ~31) + 8 * ((rho >> 2) & 3) + 4 * (rho >> 4) + (rho & 3);
        tv[j] = __builtin_amdgcn_raw_buffer_load_b128(rq, sel(h2 < 100, k * 3840 + c16 * 80 + (c10 < 5 ? 1280 : 2480) + c10 * 16), 0, 0);
        td[j] = sel(h2 < 100, WS_TKV + c16 * 153600 + R * 160 + c10 * 16);
      }
#pragma unroll
      for (int j = 0; j < 2; ++j) {
        const int R = 40 * r128 + 20 * j + q5, ks = R >> 5, rho = R & 31;
        const int k = 32 * ks + 8 * ((rho >> 2) & 3) + 4 * (rho >> 4) + (rho & 3);
        tv[4 + j] = __builtin_amdgcn_raw_buffer_load_b128(rq, sel(h2 < 100, k * 3840 + c16 * 80 + c5 * 16), 0, 0);
        td[4 + j] = sel(h2 < 100, WS_TKV + c16 * 153600 + 102400 + ks * 2560 + (c5 < 4 ? rho * 64 + (c5 ^ (2 * ((rho >> 2) & 1))) * 16 : 2048 + rho * 16));
      }
      BAR_RAW();
#pragma unroll
      for (int j = 0; j < 6; ++j) st128(tv[j], rs, td[j], 0);
      DRAIN();
      BAR_RAW();
      if (hw == 0) {
        wait_flag(FLAG(6, c16) + lane, 16, tag, lane);
        sig[0] = tag;
      }
    } else {
      v4u tv[10];
      int ts[10], td[10];
#pragma unroll
      for (int j = 0; j < 4; ++j) {
        const int R = 40 * r128 + 10 * j + q12, ks = R >> 5, rho = R & 31;
        const int k = ks < 16 ? 80 * (ks >> 1) + 32 * (ks & 1) + rho : 160 * (ks - 16) + 64 + 80 * (rho >> 4) + (rho & 15);
        ts[j] = sel(h2 < 120, k * 3072 + c16 * 192 + c12 * 16);
        td[j] = sel(h2 < 120, WS_T1 + c16 * 122880 + R * 192 + (c12 ^ (2 * ((rho >> 2) & 1))) * 16);
      }
#pragma unroll
      for (int j = 0; j < 2; ++j) {
        const int R = 20 * r64 + 10 * j + q10, ks = R >> 5, rho = R & 31;
        const int k = ks < 16 ? 40 * ks + rho : 40 * (4 * (ks - 16) + (rho >> 3)) + 32 + (rho & 7);
        ts[4 + j] = sel(h2 < 100, k * 1280 + c8 * 160 + c10 * 16);
        td[4 + j] = sel(h2 < 100, WS_TO + c8 * 102400 + R * 160 + c10 * 16);
      }
#pragma unroll
      for (int j = 0; j < 4; ++j) {
        const int R = 96 * r128 + 24 * j + q5, ks = R >> 5, rho = R & 31;
        ts[6 + j] = sel(h2 < 120, R * 1280 + c16 * 80 + c5 * 16);
        td[6 + j] = sel(h2 < 120, WS_T2 + c16 * 122880 + ks * 2560 + (c5 < 4 ? rho * 64 + (c5 ^ (2 * ((rho >> 2) & 1))) * 16 : 2048 + rho * 16));
      }
#pragma unroll
      for (int j = 0; j < 10; ++j) { PIN(ts[j]); PIN(td[j]); }
      unsigned pf0 = 0, pf1 = 0, pf2 = 0;
      BAR_RAW();
#pragma unroll
      for (int j = 0; j < 10; ++j) tv[j] = __builtin_amdgcn_raw_buffer_load_b128(j < 4 ? r1 : j < 6 ? ro : r2, ts[j], 0, 0);
      BAR_RAW();
#pragma unroll
      for (int j = 0; j < 10; ++j) st128(tv[j], rs, td[j], 0);
      asm volatile("" :: "v"(pf0), "v"(pf1), "v"(pf2));
      f32x2 sm[4];
#pragma unroll
      for (int e = 0; e < 4; ++e) sm[e] = (bf2(tv[0][e]) + bf2(tv[1][e])) + (bf2(tv[2][e]) + bf2(tv[3][e]));
      LAS f32x4* sc = (LAS f32x4*)(L + L_KVF);
      sc[h2 * 2] = f32x4{sm[0][0], sm[0][1], sm[1][0], sm[1][1]};
      sc[h2 * 2 + 1] = f32x4{sm[2][0], sm[2][1], sm[3][0], sm[3][1]};
    }
    sig_wait(sig, tag);
    STAMP(1);
    const int kvq = WS_TKV + c16 * 153600;
    dma_image<0, 24>(lds0 + L_BKV, lane * 16, srd_ws, kvq, hw, 3 * (bi >> 3));
    dma_image<24, 56>(lds0 + L_BKV, lane * 16, srd_ws, kvq, hw, 4 * (bi >> 3));
    asm volatile("s_waitcnt vmcnt(8)" ::: "memory");
    STAMP(2);
    BAR();
    dma_image<56, 104>(lds0 + L_BKV, lane * 16, srd_ws, kvq, hw, 6 * (bi >> 3));
    asm volatile("s_waitcnt vmcnt(12)" ::: "memory");
    BAR_RAW();
    if (hw == 3) {
      LAS f32x4* sc = (LAS f32x4*)(L + L_KVF);
      const int l24 = lane < 24 ? lane : 0;
      f32x4 v = sc[l24];
#pragma unroll
      for (int r = 1; r < 10; ++r) v = add4(v, sc[r * 24 + l24]);
      st128(__builtin_bit_cast(v4u, v), rs, sel(lane < 24, lane * 16), WS_CS + (c16 * 16 + r128) * 384);
    }
    DRAIN();
    STAMP(3);
    BAR_RAW();
    if (hw == 2 && lane < 2) flag_set(lane == 0 ? FLAG(5, c16) + r128 : FLAG(7, c8) + r64, tag);
    dma_image<104, 150>(lds0 + L_BKV, lane * 16, srd_ws, kvq, hw, 5 * (bi >> 3));
    ((LAS v4u*)(L + L_KVF))[ht] = v4u{0u, 0u, 0u, 0u};
    ((LAS v4u*)(L + L_KVF))[256 + ht] = v4u{0u, 0u, 0u, 0u};
    auto item = [&](int it, int& red_off, int& kvf_off) {
      const int ie = it / 5, ic = it % 5;
      const int ihead = ie < 40 ? (ie >= 20) : ie - 40;
      const int id = 20 * ihead + 4 * ic, dg = id >> 2;
      const int sa = id >> 4, col = 40 + ie;
      const int tile = ie < 40 ? 2 * sa + (col >> 4) - 2 : 7 + sa;
      red_off = (tile * 64 + (dg & 3) * 16 + (ie < 40 ? (col & 15) : 0)) * 16;
      kvf_off = ((ie < 40 ? (ie >> 4) : 3) * 2 + (dg >> 3)) * 1024 + ((dg & 3) * 16 + (ie < 40 ? (ie & 15) : ihead)) * 16 + ((dg >> 2) & 1) * 8;
    };
    const int it = ht < 210 ? ht : 209;
    int red_off[4], kvf_off, unused;
    item(it, unused, kvf_off);
#pragma unroll
    for (int j = 0; j < 4; ++j) { item(min(j * 64 + lane, 209), red_off[j], unused); PIN(red_off[j]); }
    PIN(kvf_off);
    if (hw == 1) wait_flag(lane < 32 ? FLAG(7, c8) + lane : FLAG(5, c16) + t, 48, tag, lane);
    DRAIN();
    STAMP(4);
    BAR();
    STAMP(11);
    if (hw == 0) {
      f32x4 pv[4][4];
#pragma unroll
      for (int j = 0; j < 4; ++j)
#pragma unroll
        for (int ww = 0; ww < 4; ++ww) pv[j][ww] = *(LAS f32x4*)(L + L_RED + ww * 10240 + red_off[j]);
#pragma unroll
      for (int j = 0; j < 4; ++j) {
        const f32x4 v = add4(add4(pv[j][0], pv[j][1]), add4(pv[j][2], pv[j][3]));
        st128(__builtin_bit_cast(v4u, v), rs, j * 1024 + lane * 16, WS_KVP + (c16 * 16 + r128) * 4096);
      }
      asm volatile("" ::: "memory");
      sig[1] = tag;
      STAMP(12);
      publish(FLAG(1, c16) + r128, tag, lane);
      STAMP(10);
      wait_flag(FLAG(1, c16) + lane, 16, tag, lane);
      sig[2] = tag;
    } else {
      dma_image<40, 100, THR, 3>(lds0 + L_WO, lane * 16, srd_ws, WS_TO + c8 * 102400, hw - 1, 3 * (bi >> 2));
      sig_wait(sig + 1, tag);
      dma_image<0, 40, THR, 3>(lds0 + L_WO, lane * 16, srd_ws, WS_TO + c8 * 102400, hw - 1, 2 * (bi >> 2));
      STAMP(10);
      sig_wait(sig + 2, tag);
    }
    STAMP(5);
    {
      v4u pv[16];
#pragma unroll
      for (int r = 0; r < 16; ++r) pv[r] = ld128(rs, it * 16, WS_KVP + (c16 * 16 + r) * 4096);
      SCHED();
      f32x4 v = __builtin_bit_cast(f32x4, pv[0]);
#pragma unroll
      for (int r = 1; r < 16; ++r) v = add4(v, __builtin_bit_cast(f32x4, pv[r]));
      v2u o; o.x = pk2(lo2(v)); o.y = pk2(hi2(v));
      *(LAS v2u*)(L + L_KVF + kvf_off) = o;
      if (ht >= 200) {
#pragma unroll
        for (int c = 1; c < 4; ++c) *(LAS v2u*)(L + L_KVF + kvf_off + c * 64) = o;
      }
    }
    DRAIN();
    STAMP(6);
    BAR();
    dma_image<70, 120, THR>(lds0 + L_W1, lane * 16, srd_ws, WS_T1 + c16 * 122880, hw, 6 * (bi >> 3));
    if (hw == 0) {
      v4u pv[16];
#pragma unroll
      for (int r = 0; r < 16; ++r) pv[r] = ld128(rs, sel(lane < 24, lane * 16), WS_CS + (c16 * 16 + r) * 384);
      f32x4 v = __builtin_bit_cast(f32x4, pv[0]);
#pragma unroll
      for (int r = 1; r < 16; ++r) v = add4(v, __builtin_bit_cast(f32x4, pv[r]));
      if (lane < 24) *(LAS f32x4*)(L + L_CS + lane * 16) = v;
    }
    STAMP(7);
    BAR();
    dma_image<0, 70, THR>(lds0 + L_W1, lane * 16, srd_ws, WS_T1 + c16 * 122880, hw, 8 * (bi >> 3));
    DRAIN();
    STAMP(8);
    BAR_RAW();
    dma_image<0, 30, THR>(lds0 + L_W2, lane * 16, srd_ws, WS_T2 + c16 * 122880, hw, 3 * (bi >> 3));
    BAR_RAW();
    dma_image<30, 120, THR>(lds0 + L_W2, lane * 16, srd_ws, WS_T2 + c16 * 122880, hw, 11 * (bi >> 3));
    DRAIN();
    STAMP(9);
    BAR_RAW();
    return;
  }

  const __amdgpu_buffer_rsrc_t rx = __builtin_amdgcn_make_buffer_rsrc((void*)x, 0, S * D * 2, 0x00020000);
  const int grp32 = 4 * r128 + w;
  constexpr int AW = 6;
  unsigned tag;

  {
    const int tok = 8 * b + (lane & 7), cl = lane >> 3;
    const bool has3 = w < 2;
    v4u xv[3];
#pragma unroll
    for (int j = 0; j < 3; ++j) xv[j] = __builtin_amdgcn_raw_buffer_load_b128(rx, (j < 2 || has3) ? tok * 1280 + (w + 4 * j) * 128 + cl * 16 : OOB, 0, 0);
    tag = __hip_atomic_load((gu32*)(ws + WS_EPOCH), RLX_AGENT) + 1u;
    SCHED();
    int xd = ((tok >> 4) * 20 + 2 * w + (cl >> 2)) * 1024 + ((cl & 3) * 16 + (tok & 15)) * 16;
    PIN(xd);
    SCHED();
    f32x2 f[12];
#pragma unroll
    for (int j = 0; j < 3; ++j)
#pragma unroll
      for (int e = 0; e < 4; ++e) f[j * 4 + e] = bf2(xv[j][e]);
    f32x2 s2 = (f[0] + f[1]) + (f[2] + f[3]) + ((f[4] + f[5]) + (f[6] + f[7])) + ((f[8] + f[9]) + (f[10] + f[11]));
    const float mloc = xsum32(xsum16(xsum8(s2[0] + s2[1]))) * (has3 ? 1.f / 192.f : 1.f / 128.f);
    const float m3 = has3 ? mloc : 0.f;
    f32x2 q2 = {0.f, 0.f}, q3 = {0.f, 0.f};
#pragma unroll
    for (int j = 0; j < 12; j += 2) {
      const f32x2 d0 = f[j] - (j < 8 ? mloc : m3), d1 = f[j + 1] - (j < 8 ? mloc : m3);
      q2 += d0 * d0; q3 += d1 * d1;
    }
    q2 += q3;
    const float m2loc = xsum32(xsum16(xsum8(q2[0] + q2[1])));
    if (lane < 8) *(LAS f32x2*)(L + L_CS + 512 + (lane * 4 + w) * 8) = f32x2{mloc, m2loc};
    STAMP(25);
    BAR();
    const f32x4 p01 = *(LAS f32x4*)(L + L_CS + 512 + (lane & 7) * 32), p23 = *(LAS f32x4*)(L + L_CS + 512 + (lane & 7) * 32 + 16);
    const float mean = (p01[0] + p01[2]) * 0.3f + (p23[0] + p23[2]) * 0.2f;
    const float e0 = p01[0] - mean, e1 = p01[2] - mean, e2 = p23[0] - mean, e3 = p23[2] - mean;
    const float m2 = (p01[1] + p01[3]) + (p23[1] + p23[3]) + 192.f * (e0 * e0 + e1 * e1) + 128.f * (e2 * e2 + e3 * e3);
    const float rstd = __builtin_amdgcn_rsqf(m2 * (1.f / D) + LN_EPS);
#pragma unroll
    for (int j = 0; j < 3; ++j) {
      v4u o;
#pragma unroll
      for (int e = 0; e < 4; ++e) o[e] = pk2((f[j * 4 + e] - mean) * rstd);
      st128(o, rs, (j < 2 || has3) ? xd : OOB, WS_XN + j * 8192);
    }
    STAMP(26);
    DRAIN();
    BAR_RAW();
    if (tid < 2) flag_set(tid == 0 ? FLAG(0, b >> 2) + (b & 3) : FLAG(6, c16) + r128, tag);
  }
  STAMP(1);

  {
    wait_flag(FLAG(0, grp32) + lane, 4, tag, lane);
    STAMP(2);
    v4u a[20][2];
    const int abase = WS_XN + 2 * grp32 * 20 * 1024;
#pragma unroll
    for (int ks = 0; ks < AW; ++ks)
#pragma unroll
      for (int m = 0; m < 2; ++m) a[ks][m] = ld128(rs, lane * 16, abase + (m * 20 + ks) * 1024);
    SCHED();
    BAR_RAW();
    STAMP(27);
    {
      f32x4 acc[2][5];
      const LBase bp(lds0 + L_BKV + (lane >> 2) * 160 + (lane & 3) * 8);
      kloop<2, 5, 20, 1, false>(acc, [&](int ks, int s, int h) { return bp.rd(ks * 5120 + h * 2560 + s * 32); },
        [&](int ks, int m) { return a[ks][m]; },
        [&](int ks) {
          if (ks + AW < 20) {
#pragma unroll
            for (int m = 0; m < 2; ++m) a[ks + AW][m] = ld128(rs, lane * 16, abase + (m * 20 + ks + AW) * 1024);
          }
          if (ks == 3 || ks == 10) BAR_RAW();
        });
      STAMP(3);
      v4u kf[5];
#pragma unroll
      for (int s = 0; s < 5; ++s)
#pragma unroll
        for (int m = 0; m < 2; ++m) {
          f32x2 u0 = lo2(acc[m][s]), u1 = hi2(acc[m][s]);
          if (s < 2) { u0 = phi2(u0); u1 = phi2(u1); }
          if (s == 2) { const f32x2 p0 = phi2(u0), p1 = phi2(u1); u0 = t < 8 ? p0 : u0; u1 = t < 8 ? p1 : u1; }
          kf[s][2 * m] = pk2(u0); kf[s][2 * m + 1] = pk2(u1);
        }
      const v4u ones = {0x3F803F80u, 0x3F803F80u, 0x3F803F80u, 0x3F803F80u};
      const f32x4 z4 = {0.f, 0.f, 0.f, 0.f};
      f32x4 T[10];
      T[0] = mfma(kf[0], kf[2], z4); T[1] = mfma(kf[0], kf[3], z4); T[2] = mfma(kf[1], kf[2], z4); T[3] = mfma(kf[1], kf[3], z4);
      T[4] = mfma(kf[1], kf[4], z4); T[5] = mfma(kf[2], kf[3], z4); T[6] = mfma(kf[2], kf[4], z4);
      T[7] = mfma(kf[0], ones, z4); T[8] = mfma(kf[1], ones, z4); T[9] = mfma(kf[2], ones, z4);
      STAMP(28);
      LAS f32x4* red = (LAS f32x4*)(L + L_RED);
#pragma unroll
      for (int i = 0; i < 10; ++i) red[(w * 10 + i) * 64 + lane] = T[i];
      BAR();
    }
    STAMP(4);
    f32x4 qa[2][3];
    {
      unsigned bp0 = lds0 + L_BQ + (lane >> 2) * 64 + (lane & 3) * 8 + (g & 1) * 32;
      unsigned bp1 = lds0 + L_BQ + (lane >> 2) * 64 + (lane & 3) * 8 + 32 - (g & 1) * 32;
      unsigned bp2 = lds0 + L_BQ + 2048 + (lane >> 2) * 16 + (lane & 1) * 8;
      PIN(bp0); PIN(bp1); PIN(bp2);
      kloop<2, 3, 20, 2, true>(qa,
        [&](int ks, int s, int h) {
          return s == 0 ? rdtr((LAS char*)bp0 + ks * 2560 + h * 1024) : s == 1 ? rdtr((LAS char*)bp1 + ks * 2560 + h * 1024) : rdtr((LAS char*)bp2 + ks * 2560 + h * 256);
        },
        [&](int ks, int m) { return a[ks][m]; },
        [&](int ks) {});
    }
    v4u pq0[2], pq1[2];
#pragma unroll
    for (int m = 0; m < 2; ++m) {
      pq0[m].x = pk2(phi2(lo2(qa[m][0]))); pq0[m].y = pk2(phi2(hi2(qa[m][0])));
      pq0[m].z = pk2(phi2(lo2(qa[m][1]))); pq0[m].w = pk2(phi2(hi2(qa[m][1])));
      const unsigned lo = pk2(phi2(lo2(qa[m][2]))), hi = pk2(phi2(hi2(qa[m][2])));
      pq1[m].x = g < 2 ? lo : 0u; pq1[m].y = g < 2 ? hi : 0u; pq1[m].z = 0u; pq1[m].w = 0u;
      PIN(pq0[m]); PIN(pq1[m]);
    }
    STAMP(5);
    BAR_RAW();
    STAMP(17);
    {
      v4u kv[4][2];
#pragma unroll
      for (int es = 3; es >= 0; --es)
#pragma unroll
        for (int ks = 0; ks < 2; ++ks) kv[es][ks] = *(LAS v4u*)(L + L_KVF + (es * 2 + ks) * 1024 + lane * 16);
      SCHED();
      f32x4 o[2][4];
#pragma unroll
      for (int es = 3; es >= 0; --es)
#pragma unroll
        for (int m = 0; m < 2; ++m) o[m][es] = mfma(kv[es][0], pq0[m], f32x4{0.f, 0.f, 0.f, 0.f});
#pragma unroll
      for (int es = 3; es >= 0; --es)
#pragma unroll
        for (int m = 0; m < 2; ++m) o[m][es] = mfma(kv[es][1], pq1[m], o[m][es]);
#pragma unroll
      for (int m = 0; m < 2; ++m) {
        const float i0 = __builtin_amdgcn_rcpf(o[m][3][0] + 1e-6f), i1 = __builtin_amdgcn_rcpf(o[m][3][1] + 1e-6f);
        const float im = g == 0 ? i0 : i1;
        v4u ov;
        ov.x = pk2(lo2(o[m][0]) * i0); ov.y = pk2(hi2(o[m][0]) * i0);
        ov.z = pk2(lo2(o[m][1]) * im); ov.w = pk2(hi2(o[m][1]) * im);
        v2u ol; ol.x = pk2(lo2(o[m][2]) * i1); ol.y = pk2(hi2(o[m][2]) * i1);
        const int tt = 2 * grp32 + m;
        st128(ov, rs, lane * 16, WS_O + (tt * 20 + c16) * 1024);
        st64(ol, rs, g < 2 ? lane * 8 : OOB, WS_O + (tt * 20 + 16) * 1024 + c16 * 256);
      }
      STAMP(18);
      publish(FLAG(2, grp32) + c16, tag, lane);
    }
    STAMP(7);
  }

  {
    const int tt = 4 * r64 + w, tok = tt * 16 + t;
    wait_flag(FLAG(2, tt >> 1) + lane, 16, tag, lane);
    STAMP(8);
    if (b == 0 && tid == 0) __hip_atomic_store((gu32*)(ws + WS_EPOCH), tag, RLX_AGENT);
    v4u a[20];
    v2u xr[5];
#pragma unroll
    for (int ks = 0; ks < 2 * AW; ++ks) a[ks] = ld128(rs, lane * 16, WS_O + (tt * 20 + ks) * 1024);
    SCHED();
    f32x4 acc[1][5];
    const LBase bp(lds0 + L_WO + (lane >> 2) * 160 + (lane & 3) * 8);
    kloop<1, 5, 20, 1, true>(acc, [&](int ks, int s, int h) { return bp.rd(ks * 5120 + h * 2560 + s * 32); },
      [&](int ks, int m) { return a[ks]; },
      [&](int ks) {
        if (ks + 2 * AW < 16) a[ks + 2 * AW] = ld128(rs, lane * 16, WS_O + (tt * 20 + ks + 2 * AW) * 1024);
        else if (ks + 2 * AW < 20) a[ks + 2 * AW] = ld_split(rs, lane, WS_O + (tt * 20 + ks + 2 * AW) * 1024);
        if (ks >= 8 && ks < 13) xr[ks - 8] = __builtin_amdgcn_raw_buffer_load_b64(rx, tok * 1280 + (80 * c8 + 16 * (ks - 8) + 4 * g) * 2, 0, 0);
      });
    BAR_RAW();
    STAMP(9);
    v2u hb[5];
    f32x2 hv[10];
#pragma unroll
    for (int s = 0; s < 5; ++s) {
      hb[s].x = pk2(lo2(acc[0][s]) + bf2(xr[s].x));
      hb[s].y = pk2(hi2(acc[0][s]) + bf2(xr[s].y));
      hv[2 * s] = bf2(hb[s].x); hv[2 * s + 1] = bf2(hb[s].y);
    }
    st128(v4u{hb[0].x, hb[0].y, hb[1].x, hb[1].y}, rs, lane * 16, WS_H + (tt * 20 + 2 * c8) * 1024);
    st128(v4u{hb[2].x, hb[2].y, hb[3].x, hb[3].y}, rs, lane * 16, WS_H + (tt * 20 + 2 * c8 + 1) * 1024);
    st64(hb[4], rs, lane * 8, WS_H + (tt * 20 + 16) * 1024 + c8 * 512);
    const f32x2 s2 = ((hv[0] + hv[1]) + (hv[2] + hv[3])) + ((hv[4] + hv[5]) + (hv[6] + hv[7])) + (hv[8] + hv[9]);
    const float mc = xsum32(xsum16(s2[0] + s2[1])) * (1.f / 80.f);
    f32x2 q2 = {0.f, 0.f}, q3 = {0.f, 0.f};
#pragma unroll
    for (int j = 0; j < 10; j += 2) { const f32x2 d0 = hv[j] - mc, d1 = hv[j + 1] - mc; q2 += d0 * d0; q3 += d1 * d1; }
    q2 += q3;
    const float m2 = xsum32(xsum16(q2[0] + q2[1]));
    st64(v2u{__builtin_bit_cast(unsigned, mc), __builtin_bit_cast(unsigned, m2)}, rs, g == 0 ? t * 8 : OOB, WS_ST + (tt * 8 + c8) * 128);
    STAMP(20);
    publish(FLAG(3, tt >> 1) + (tt & 1) * 8 + c8, tag, lane);
    STAMP(10);
  }

  {
    v2u bb[6];
#pragma unroll
    for (int s = 0; s < 6; ++s) bb[s] = *(const v2u*)(b1 + 96 * c16 + 16 * s + 4 * g);
    wait_flag(FLAG(3, grp32) + lane, 16, tag, lane);
    STAMP(11);
    v4u stt[2];
#pragma unroll
    for (int m = 0; m < 2; ++m) {
      const v2u s0 = ld64(rs, g * 256 + t * 8, WS_ST + (2 * grp32 + m) * 1024), s1 = ld64(rs, g * 256 + 128 + t * 8, WS_ST + (2 * grp32 + m) * 1024);
      stt[m] = v4u{s0.x, s0.y, s1.x, s1.y};
    }
    constexpr int WIN = AW + 1;
    v4u a[WIN][2];
    const int abase = WS_H + 2 * grp32 * 20 * 1024;
#pragma unroll
    for (int ks = 0; ks < AW; ++ks)
#pragma unroll
      for (int m = 0; m < 2; ++m) a[ks][m] = ld128(rs, lane * 16, abase + (m * 20 + ks) * 1024);
    SCHED();
    float nm[2], rr[2];
#pragma unroll
    for (int m = 0; m < 2; ++m) {
      const f32x4 sv = __builtin_bit_cast(f32x4, stt[m]);
      const float mean = xsum32(xsum16(sv[0] + sv[2])) * 0.125f;
      const float m2 = xsum32(xsum16(sv[1] + sv[3] + 80.f * ((sv[0] - mean) * (sv[0] - mean) + (sv[2] - mean) * (sv[2] - mean))));
      const float var = m2 * (1.f / D);
      const float r1 = __builtin_amdgcn_rsqf(var + LN_EPS);
      rr[m] = r1 * __builtin_amdgcn_rsqf(var * r1 * r1 + LN_EPS);
      nm[m] = -mean * rr[m];
      PIN(rr[m]); PIN(nm[m]);
    }
    BAR_RAW();
    STAMP(22);
    const LBase bp0(lds0 + L_W1 + (lane >> 2) * 192 + (lane & 3) * 8 + (g & 1) * 32);
    const LBase bp1(lds0 + L_W1 + (lane >> 2) * 192 + (lane & 3) * 8 - (g & 1) * 32);
    f32x4 acc[2][6];
    f32x4 cs[6];
    kloop<2, 6, 20, 1, true>(acc, [&](int ks, int s, int h) { return ((s & 1) ? bp1 : bp0).rd(ks * 6144 + h * 3072 + s * 32); },
      [&](int ks, int m) { return a[ks % WIN][m]; },
      [&](int ks) {
        if (ks + AW < 20) {
#pragma unroll
          for (int m = 0; m < 2; ++m)
            a[(ks + AW) % WIN][m] = ks + AW < 16 ? ld128(rs, lane * 16, abase + (m * 20 + ks + AW) * 1024) : ld_split(rs, lane, abase + (m * 20 + ks + AW) * 1024);
        }
        if (ks == 19) {
#pragma unroll
          for (int s = 0; s < 6; ++s) cs[s] = *(LAS f32x4*)(L + L_CS + (s * 4 + g) * 16);
        }
      });
    BAR_RAW();
    STAMP(12);
#pragma unroll
    for (int m = 0; m < 2; ++m) {
      v2u hb[6];
#pragma unroll
      for (int s = 0; s < 6; ++s) {
        const f32x2 p0 = lo2(acc[m][s]) * rr[m] + (lo2(cs[s]) * nm[m] + bf2(bb[s].x));
        const f32x2 p1 = hi2(acc[m][s]) * rr[m] + (hi2(cs[s]) * nm[m] + bf2(bb[s].y));
        hb[s].x = pk2(gelu2(p0)); hb[s].y = pk2(gelu2(p1));
        if (s & 1)
          st128(v4u{hb[s - 1].x, hb[s - 1].y, hb[s].x, hb[s].y}, rs, lane * 16, WS_H1 + ((2 * grp32 + m) * 48 + 3 * c16 + (s >> 1)) * 1024);
      }
    }
    STAMP(24);
    publish(FLAG(4, grp32) + c16, tag, lane);
    STAMP(13);
  }

  {
    v2u hr[2][3], bb[3];
#pragma unroll
    for (int s = 0; s < 3; ++s) {
      int n = 40 * c16 + 16 * s + 4 * g;
      if (s == 2 && g >= 2) n = 40 * c16;
      bb[s] = *(const v2u*)(b2 + n);
      const int cc = n / 80, off = n % 80, ss = off >> 4, gg = (off >> 2) & 3;
      const int ho = ss < 4 ? (2 * cc + (ss >> 1)) * 1024 + (gg * 16 + t) * 16 + (ss & 1) * 8 : 16 * 1024 + cc * 512 + (gg * 16 + t) * 8;
#pragma unroll
      for (int m = 0; m < 2; ++m) hr[m][s] = ld64(rs, ho, WS_H + (2 * grp32 + m) * 20 * 1024);
    }
    wait_flag(FLAG(4, grp32) + lane, 16, tag, lane);
    STAMP(14);
    constexpr int WIN = 8;
    v4u a[WIN][2];
    const int abase = WS_H1 + 2 * grp32 * 48 * 1024;
#pragma unroll
    for (int ks = 0; ks < WIN - 1; ++ks)
#pragma unroll
      for (int m = 0; m < 2; ++m) a[ks][m] = ld128(rs, lane * 16, abase + (m * 48 + ks) * 1024);
    SCHED();
    BAR_RAW();
    f32x4 acc[2][3];
    const LBase bp0(lds0 + L_W2 + (lane >> 2) * 64 + (lane & 3) * 8 + (g & 1) * 32);
    const LBase bp1(lds0 + L_W2 + (lane >> 2) * 64 + (lane & 3) * 8 + 32 - (g & 1) * 32);
    const LBase bp2(lds0 + L_W2 + 2048 + (lane >> 2) * 16 + (lane & 1) * 8);
    kloop<2, 3, 48, 2, true>(acc,
      [&](int ks, int s, int h) { return s == 0 ? bp0.rd(ks * 2560 + h * 1024) : s == 1 ? bp1.rd(ks * 2560 + h * 1024) : bp2.rd(ks * 2560 + h * 256); },
      [&](int ks, int m) { return a[ks % WIN][m]; },
      [&](int ks) {
        if (ks + WIN - 1 < 48) {
#pragma unroll
          for (int m = 0; m < 2; ++m) a[(ks + WIN - 1) % WIN][m] = ld128(rs, lane * 16, abase + (m * 48 + ks + WIN - 1) * 1024);
        }
      });
    STAMP(15);
#pragma unroll
    for (int m = 0; m < 2; ++m) {
      const int tok = (2 * grp32 + m) * 16 + t;
#pragma unroll
      for (int s = 0; s < 3; ++s) {
        v2u o;
        o.x = pk2(lo2(acc[m][s]) + bf2(bb[s].x) + bf2(hr[m][s].x));
        o.y = pk2(hi2(acc[m][s]) + bf2(bb[s].y) + bf2(hr[m][s].y));
        if (s < 2 || g < 2) *(v2u*)(out + tok * D + 40 * c16 + 16 * s + 4 * g) = o;
      }
    }
    STAMP(16);
    if (b == 37 && w == 0) {
      const unsigned long long te = __builtin_amdgcn_s_memrealtime(), d = 4 * (pt1 - pt0);
      for (int i = 0; i < 100000; ++i) { if (__builtin_amdgcn_s_memrealtime() - te >= d) break; }
    }
  }
}
}

extern "C" void kernel_launch(void* const* d_in, const int* in_sizes, int n_in, void* d_out, int out_size, void* d_ws,
                              size_t ws_size, hipStream_t stream) {
  const unsigned short* x = (const unsigned short*)d_in[0];
  const unsigned short* wqkv = (const unsigned short*)d_in[1];
  const unsigned short* wo = (const unsigned short*)d_in[2];
  const unsigned short* w1 = (const unsigned short*)d_in[4];
  const unsigned short* b1 = (const unsigned short*)d_in[5];
  const unsigned short* w2 = (const unsigned short*)d_in[6];
  const unsigned short* b2 = (const unsigned short*)d_in[7];
  fused_block<<<256, 512, 0, stream>>>(x, wqkv, wo, w1, b1, w2, b2, (unsigned short*)d_out, (unsigned char*)d_ws);
}
```

```cpp
#include <hip/hip_runtime.h>
#include <hip/hip_bf16.h>

#define STAMPS 0
#define THR 0
#define TOUCH 0
#define CODEPF 0
#define FLAGPF 0
#define OUTAUX 0
#define OUTPF 0
#define HPRIO 0
#define WOLATE 0
#define SLEEP 1

#define GAS __attribute__((address_space(1)))
#define LAS __attribute__((address_space(3)))
#define RLX_AGENT __ATOMIC_RELAXED, __HIP_MEMORY_SCOPE_AGENT
typedef unsigned v4u __attribute__((ext_vector_type(4)));
typedef unsigned v2u __attribute__((ext_vector_type(2)));
typedef float f32x4 __attribute__((ext_vector_type(4)));
typedef float f32x2 __attribute__((ext_vector_type(2)));
typedef __bf16 bf16x8 __attribute__((ext_vector_type(8)));
typedef __bf16 bf16x2 __attribute__((ext_vector_type(2)));
typedef short s16x4 __attribute__((ext_vector_type(4)));
typedef GAS unsigned gu32;

namespace {
constexpr int S = 2048, D = 640, F = 1536;
constexpr float LN_EPS = 1e-5f;
constexpr int AUX_SC1 = 16;
constexpr unsigned kSpinCap = 1u << 17;
constexpr int OOB = 0x7FFFFF00;

constexpr int WS_EPOCH = 0;
constexpr int WS_FLAG = 4096;
constexpr int FLAG_SET = 64 * 256;
constexpr int WS_XN = 1 << 20;
constexpr int WS_O = WS_XN + 128 * 20 * 1024;
constexpr int WS_H = WS_O + 128 * 20 * 1024;
constexpr int WS_H1 = WS_H + 128 * 20 * 1024;
constexpr int WS_KVP = WS_H1 + 128 * 48 * 1024;
constexpr int WS_ST = WS_KVP + 16 * 16 * 4096;
constexpr int WS_DBG = WS_ST + 2048 * 64;
constexpr int WS_TKV = WS_DBG + 256 * 8 * 32 * 8;
constexpr int WS_TO = WS_TKV + 16 * 153600;
constexpr int WS_T1 = WS_TO + 8 * 102400;
constexpr int WS_T2 = WS_T1 + 16 * 122880;
constexpr int WS_CS = WS_T2 + 16 * 122880;
constexpr int WS_BYTES = WS_CS + 16 * 16 * 384;

constexpr int L_BKV = 0, L_BQ = 102400;
constexpr int L_RED = 0, L_KVF = 153600;
constexpr int L_WO = 0, L_W1 = 30720, L_W2 = 0, L_CS = 161792;
constexpr int LDS_BYTES = 163840;

__device__ __forceinline__ f32x4 mfma(v4u a, v4u b, f32x4 c) {
  return __builtin_amdgcn_mfma_f32_16x16x32_bf16(__builtin_bit_cast(bf16x8, a), __builtin_bit_cast(bf16x8, b), c, 0, 0, 0);
}
__device__ __forceinline__ unsigned pk2(f32x2 v) { return __builtin_bit_cast(unsigned, __builtin_convertvector(v, bf16x2)); }
__device__ __forceinline__ unsigned pk2(float lo, float hi) { return pk2(f32x2{lo, hi}); }
__device__ __forceinline__ float bf_lo(unsigned u) { return __builtin_bit_cast(float, u << 16); }
__device__ __forceinline__ float bf_hi(unsigned u) { return __builtin_bit_cast(float, u & 0xFFFF0000u); }
__device__ __forceinline__ f32x2 bf2(unsigned u) { return f32x2{bf_lo(u), bf_hi(u)}; }
__device__ __forceinline__ f32x2 lo2(f32x4 v) { return f32x2{v[0], v[1]}; }
__device__ __forceinline__ f32x2 hi2(f32x4 v) { return f32x2{v[2], v[3]}; }
__device__ __forceinline__ v4u mk_srd(const void* p, unsigned bytes) {
  const unsigned long long a = (unsigned long long)p;
  v4u r;
  r.x = (unsigned)a; r.y = (unsigned)(a >> 32) & 0xFFFFu; r.z = bytes; r.w = 0x00020000u;
  return r;
}
#define DRAIN() asm volatile("s_waitcnt vmcnt(0)" ::: "memory")
#define BAR() asm volatile("s_waitcnt lgkmcnt(0)\n\ts_barrier" ::: "memory")
#define BAR_RAW() asm volatile("s_barrier" ::: "memory")
#define SCHED() __builtin_amdgcn_sched_barrier(0)
#define PIN(x) asm volatile("" : "+v"(x))

__device__ __forceinline__ void dma2(unsigned lds_addr, unsigned voff, v4u srd, unsigned soff) {
  unsigned keep;
  asm volatile("s_mov_b32 %0, m0\n\ts_mov_b32 m0, %1\n\ts_nop 0\n\t"
               "buffer_load_dwordx4 %2, %3, %4 offen sc1 lds\n\t"
               "buffer_load_dwordx4 %2, %3, %4 offen offset:1024 sc1 lds\n\t"
               "s_mov_b32 m0, %0"
               : "=&s"(keep) : "s"(lds_addr), "v"(voff), "s"(srd), "s"(soff) : "memory");
}
template <int LO, int HI, int TH = 0, int NW = 4>
__device__ __forceinline__ void dma_image(unsigned lds_base, unsigned voff, v4u srd, int ws_off, int hw, int rot) {
  constexpr int NP = (HI - LO) / 2;
#pragma unroll 1
  for (int i = hw; i < NP; i += NW) {
    int j = i + rot;
    if (j >= NP) j -= NP;
    dma2(lds_base + LO * 1024 + j * 2048, voff, srd, ws_off + LO * 1024 + j * 2048);
    if (TH) asm volatile("s_waitcnt vmcnt(%0)" :: "i"(TH - 2) : "memory");
  }
}
__device__ __forceinline__ v2u rdtr(LAS char* p) { return __builtin_bit_cast(v2u, __builtin_amdgcn_ds_read_tr16_b64_v4i16((LAS s16x4*)p)); }
struct LBase {
  unsigned a[3];
  __device__ __forceinline__ explicit LBase(unsigned v) { a[0] = v; a[1] = v + 65536; a[2] = v + 131072; PIN(a[0]); PIN(a[1]); PIN(a[2]); }
  __device__ __forceinline__ v2u rd(int off) const { return rdtr((LAS char*)(a[off >> 16]) + (off & 65535)); }
};
template <int MT, int NT, int KS, int PD, bool WA, class RB, class GA, class PRE>
__device__ __forceinline__ void kloop(f32x4 (&acc)[MT][NT], RB rb, GA ga, PRE pre) {
  v2u bl[PD + 1][NT], bh[PD + 1][NT];
#pragma unroll
  for (int m = 0; m < MT; ++m)
#pragma unroll
    for (int s = 0; s < NT; ++s) acc[m][s] = f32x4{0.f, 0.f, 0.f, 0.f};
#pragma unroll
  for (int ks = 0; ks < PD; ++ks)
#pragma unroll
    for (int s = 0; s < NT; ++s) { bl[ks][s] = rb(ks, s, 0); bh[ks][s] = rb(ks, s, 1); }
  SCHED();
#pragma unroll
  for (int ks = 0; ks < KS; ++ks) {
    pre(ks);
#pragma unroll
    for (int s = 0; s < NT; ++s)
#pragma unroll
      for (int m = 0; m < MT; ++m) {
        const v2u lo = bl[ks % (PD + 1)][s], hi = bh[ks % (PD + 1)][s];
        const v4u wv = {lo.x, lo.y, hi.x, hi.y};
        acc[m][s] = WA ? mfma(wv, ga(ks, m), acc[m][s]) : mfma(ga(ks, m), wv, acc[m][s]);
        if (ks + PD < KS) {
          if (MT == 1 || m == 0) bl[(ks + PD) % (PD + 1)][s] = rb(ks + PD, s, 0);
          if (MT == 1 || m == 1) bh[(ks + PD) % (PD + 1)][s] = rb(ks + PD, s, 1);
        }
        SCHED();
      }
  }
}
__device__ __forceinline__ v4u ld128(__amdgpu_buffer_rsrc_t rs, int voff, int soff) {
  return __builtin_amdgcn_raw_buffer_load_b128(rs, voff, soff, AUX_SC1);
}
__device__ __forceinline__ v2u ld64(__amdgpu_buffer_rsrc_t rs, int voff, int soff) {
  return __builtin_amdgcn_raw_buffer_load_b64(rs, voff, soff, AUX_SC1);
}
__device__ __forceinline__ void st128(v4u v, __amdgpu_buffer_rsrc_t rs, int voff, int soff) {
  __builtin_amdgcn_raw_buffer_store_b128(v, rs, (int)((unsigned)voff + (unsigned)soff), 0, AUX_SC1);
}
__device__ __forceinline__ int sel(bool c, int a) {
  PIN(a);
  return c ? a : OOB;
}
__device__ __forceinline__ v4u ld_split(__amdgpu_buffer_rsrc_t rs, int lane, int soff) {
  const v2u a = ld64(rs, lane * 8, soff), b = ld64(rs, lane * 8 + 512, soff);
  return v4u{a.x, a.y, b.x, b.y};
}
__device__ __forceinline__ void st64(v2u v, __amdgpu_buffer_rsrc_t rs, int voff, int soff) {
  __builtin_amdgcn_raw_buffer_store_b64(v, rs, voff, soff, AUX_SC1);
}
__device__ __forceinline__ void flag_set(gu32* p, unsigned tag) {
  asm volatile("global_atomic_swap %0, %1, off sc1" :: "v"(p), "v"(tag) : "memory");
}
__device__ __forceinline__ void wait_flag(gu32* p, int n, unsigned tag, int lane) {
#pragma unroll 1
  for (unsigned spins = 0; spins < kSpinCap; ++spins) {
    unsigned v = tag;
    if (lane < n) asm volatile("global_atomic_add %0, %1, %2, off sc0 sc1\n\ts_waitcnt vmcnt(0)" : "=&v"(v) : "v"(p), "v"(0u) : "memory");
    if (__all(v == tag)) break;
    if (SLEEP) __builtin_amdgcn_s_sleep(SLEEP);
  }
  asm volatile("" ::: "memory");
}
__device__ __forceinline__ void sig_wait(volatile LAS unsigned* p, unsigned tag) {
#pragma unroll 1
  for (unsigned spins = 0; spins < 64 * kSpinCap; ++spins) {
    if (*p == tag) break;
    __builtin_amdgcn_s_sleep(1);
  }
  asm volatile("" ::: "memory");
}
__device__ __forceinline__ void publish(gu32* flag, unsigned tag, int lane) {
  DRAIN();
  if (lane == 0) flag_set(flag, tag);
}
__device__ __forceinline__ f32x4 add4(f32x4 a, f32x4 b) {
  f32x4 r;
#pragma unroll
  for (int i = 0; i < 4; ++i) { float x; asm("v_add_f32 %0, %1, %2" : "=v"(x) : "v"(a[i]), "v"(b[i])); r[i] = x; }
  return r;
}
__device__ __forceinline__ float xsum32(float x) {
  unsigned u = __builtin_bit_cast(unsigned, x), v = u;
  asm("" : "+v"(v));
  auto r = __builtin_amdgcn_permlane32_swap(u, v, false, false);
  const unsigned a = r[0], b = r[1];
  return __builtin_bit_cast(float, a) + __builtin_bit_cast(float, b);
}
__device__ __forceinline__ float xsum16(float x) {
  unsigned u = __builtin_bit_cast(unsigned, x), v = u;
  asm("" : "+v"(v));
  auto r = __builtin_amdgcn_permlane16_swap(u, v, false, false);
  const unsigned a = r[0], b = r[1];
  return __builtin_bit_cast(float, a) + __builtin_bit_cast(float, b);
}
__device__ __forceinline__ float xsum8(float x) {
  return x + __builtin_bit_cast(float, __builtin_amdgcn_update_dpp(0, __builtin_bit_cast(int, x), 0x128, 0xf, 0xf, false));
}
__device__ __forceinline__ f32x2 phi2(f32x2 t) {
  const f32x2 u = t * 1.4426950408889634f;
  const f32x2 e = {__builtin_amdgcn_exp2f(fminf(u[0], 0.f)), __builtin_amdgcn_exp2f(fminf(u[1], 0.f))};
  const f32x2 p = {fmaxf(t[0], 0.f), fmaxf(t[1], 0.f)};
  return (p + 1e-6f) + e;
}
__device__ __forceinline__ f32x2 gelu2(f32x2 x) {
  const f32x2 u = x * (x * x * -0.1029432397f + -2.302208198f);
  const f32x2 d = f32x2{__builtin_amdgcn_exp2f(u[0]), __builtin_amdgcn_exp2f(u[1])} + 1.f;
  return x * f32x2{__builtin_amdgcn_rcpf(d[0]), __builtin_amdgcn_rcpf(d[1])};
}

#define STAMP(i) do { } while (0)

__global__ void __launch_bounds__(512, 2)
fused_block(const unsigned short* __restrict__ x, const unsigned short* __restrict__ wqkv, const unsigned short* __restrict__ wo,
            const unsigned short* __restrict__ w1, const unsigned short* __restrict__ b1, const unsigned short* __restrict__ w2,
            const unsigned short* __restrict__ b2, unsigned short* __restrict__ out, unsigned char* ws) {
  __shared__ __attribute__((aligned(16))) char smem[LDS_BYTES];
  LAS char* L = (LAS char*)smem;
  const unsigned lds0 = (unsigned)(size_t)L;
  const int tid = threadIdx.x, lane = tid & 63, w = __builtin_amdgcn_readfirstlane(tid >> 6);
  const int b = blockIdx.x, xcd = b & 7, bi = b >> 3;
  const int g = lane >> 4, t = lane & 15;
  const __amdgpu_buffer_rsrc_t rs = __builtin_amdgcn_make_buffer_rsrc((void*)ws, 0, WS_BYTES, 0x00020000);
  gu32* flags = (gu32*)(ws + WS_FLAG);
#define FLAG(set, grp) (flags + ((set) * FLAG_SET + (grp) * 256) / 4)
  STAMP(0);
  const int r128 = 4 * (xcd >> 1) + (bi >> 3), c16 = 8 * (xcd & 1) + (bi & 7);
  const int r64 = 8 * (xcd >> 1) + (bi >> 2), c8 = 4 * (xcd & 1) + (bi & 3);

  if (w >= 4) {
    const int hw = w - 4, ht = tid - 256;
    if (HPRIO) __builtin_amdgcn_s_setprio(HPRIO);
    const v4u srd_ws = mk_srd(ws, WS_BYTES);
    const __amdgpu_buffer_rsrc_t rq = __builtin_amdgcn_make_buffer_rsrc((void*)wqkv, 0, D * 3 * D * 2, 0x00020000);
    const __amdgpu_buffer_rsrc_t ro = __builtin_amdgcn_make_buffer_rsrc((void*)wo, 0, D * D * 2, 0x00020000);
    const __amdgpu_buffer_rsrc_t r1 = __builtin_amdgcn_make_buffer_rsrc((void*)w1, 0, D * F * 2, 0x00020000);
    const __amdgpu_buffer_rsrc_t r2 = __builtin_amdgcn_make_buffer_rsrc((void*)w2, 0, F * D * 2, 0x00020000);
    volatile LAS unsigned* sig = (volatile LAS unsigned*)(L + L_CS + 1024);
    const unsigned tag = __hip_atomic_load((gu32*)(ws + WS_EPOCH), RLX_AGENT) + 1u;
    const int h2 = ht & 127;
    const int q10 = h2 / 10, c10 = h2 % 10, q5 = h2 / 5, c5 = h2 % 5, q12 = h2 / 12, c12 = h2 % 12;
    if (hw < 2) {
      v4u tv[6];
      int td[6];
#pragma unroll
      for (int j = 0; j < 4; ++j) {
        const int R = 40 * r128 + 10 * j + q10, rho = R & 31;
        const int k = (R & ~31) + 8 * ((rho >> 2) & 3) + 4 * (rho >> 4) + (rho & 3);
        tv[j] = __builtin_amdgcn_raw_buffer_load_b128(rq, sel(h2 < 100, k * 3840 + c16 * 80 + (c10 < 5 ? 1280 : 2480) + c10 * 16), 0, 0);
        td[j] = sel(h2 < 100, WS_TKV + c16 * 153600 + R * 160 + c10 * 16);
      }
#pragma unroll
      for (int j = 0; j < 2; ++j) {
        const int R = 40 * r128 + 20 * j + q5, ks = R >> 5, rho = R & 31;
        const int k = 32 * ks + 8 * ((rho >> 2) & 3) + 4 * (rho >> 4) + (rho & 3);
        tv[4 + j] = __builtin_amdgcn_raw_buffer_load_b128(rq, sel(h2 < 100, k * 3840 + c16 * 80 + c5 * 16), 0, 0);
        td[4 + j] = sel(h2 < 100, WS_TKV + c16 * 153600 + 102400 + ks * 2560 + (c5 < 4 ? rho * 64 + (c5 ^ (2 * ((rho >> 2) & 1))) * 16 : 2048 + rho * 16));
      }
      BAR_RAW();
#pragma unroll
      for (int j = 0; j < 6; ++j) st128(tv[j], rs, td[j], 0);
      DRAIN();
      BAR_RAW();
      if (hw == 0) {
        wait_flag(FLAG(6, c16) + lane, 16, tag, lane);
        sig[0] = tag;
      }
    } else {
      v4u tv[10];
      int ts[10], td[10];
#pragma unroll
      for (int j = 0; j < 4; ++j) {
        const int R = 40 * r128 + 10 * j + q12, ks = R >> 5, rho = R & 31;
        const int k = ks < 16 ? 80 * (ks >> 1) + 32 * (ks & 1) + rho : 160 * (ks - 16) + 64 + 80 * (rho >> 4) + (rho & 15);
        ts[j] = sel(h2 < 120, k * 3072 + c16 * 192 + c12 * 16);
        td[j] = sel(h2 < 120, WS_T1 + c16 * 122880 + R * 192 + (c12 ^ (2 * ((rho >> 2) & 1))) * 16);
      }
#pragma unroll
      for (int j = 0; j < 2; ++j) {
        const int R = 20 * r64 + 10 * j + q10, ks = R >> 5, rho = R & 31;
        const int k = ks < 16 ? 40 * ks + rho : 40 * (4 * (ks - 16) + (rho >> 3)) + 32 + (rho & 7);
        ts[4 + j] = sel(h2 < 100, k * 1280 + c8 * 160 + c10 * 16);
        td[4 + j] = sel(h2 < 100, WS_TO + c8 * 102400 + R * 160 + c10 * 16);
      }
#pragma unroll
      for (int j = 0; j < 4; ++j) {
        const int R = 96 * r128 + 24 * j + q5, ks = R >> 5, rho = R & 31;
        ts[6 + j] = sel(h2 < 120, R * 1280 + c16 * 80 + c5 * 16);
        td[6 + j] = sel(h2 < 120, WS_T2 + c16 * 122880 + ks * 2560 + (c5 < 4 ? rho * 64 + (c5 ^ (2 * ((rho >> 2) & 1))) * 16 : 2048 + rho * 16));
      }
#pragma unroll
      for (int j = 0; j < 10; ++j) { PIN(ts[j]); PIN(td[j]); }
      unsigned pf0 = 0, pf1 = 0, pf2 = 0;
      BAR_RAW();
#pragma unroll
      for (int j = 0; j < 10; ++j) tv[j] = __builtin_amdgcn_raw_buffer_load_b128(j < 4 ? r1 : j < 6 ? ro : r2, ts[j], 0, 0);
      BAR_RAW();
#pragma unroll
      for (int j = 0; j < 10; ++j) st128(tv[j], rs, td[j], 0);
      asm volatile("" :: "v"(pf0), "v"(pf1), "v"(pf2));
      f32x2 sm[4];
#pragma unroll
      for (int e = 0; e < 4; ++e) sm[e] = (bf2(tv[0][e]) + bf2(tv[1][e])) + (bf2(tv[2][e]) + bf2(tv[3][e]));
      LAS f32x4* sc = (LAS f32x4*)(L + L_KVF);
      sc[h2 * 2] = f32x4{sm[0][0], sm[0][1], sm[1][0], sm[1][1]};
      sc[h2 * 2 + 1] = f32x4{sm[2][0], sm[2][1], sm[3][0], sm[3][1]};
    }
    sig_wait(sig, tag);
    STAMP(1);
    const int kvq = WS_TKV + c16 * 153600;
    dma_image<0, 24>(lds0 + L_BKV, lane * 16, srd_ws, kvq, hw, 3 * (bi >> 3));
    dma_image<24, 56>(lds0 + L_BKV, lane * 16, srd_ws, kvq, hw, 4 * (bi >> 3));
    asm volatile("s_waitcnt vmcnt(8)" ::: "memory");
    STAMP(2);
    BAR();
    dma_image<56, 104>(lds0 + L_BKV, lane * 16, srd_ws, kvq, hw, 6 * (bi >> 3));
    asm volatile("s_waitcnt vmcnt(12)" ::: "memory");
    BAR_RAW();
    if (hw == 3) {
      LAS f32x4* sc = (LAS f32x4*)(L + L_KVF);
      const int l24 = lane < 24 ? lane : 0;
      f32x4 v = sc[l24];
#pragma unroll
      for (int r = 1; r < 10; ++r) v = add4(v, sc[r * 24 + l24]);
      st128(__builtin_bit_cast(v4u, v), rs, sel(lane < 24, lane * 16), WS_CS + (c16 * 16 + r128) * 384);
    }
    DRAIN();
    STAMP(3);
    BAR_RAW();
    if (hw == 2 && lane < 2) flag_set(lane == 0 ? FLAG(5, c16) + r128 : FLAG(7, c8) + r64, tag);
    dma_image<104, 150>(lds0 + L_BKV, lane * 16, srd_ws, kvq, hw, 5 * (bi >> 3));
    ((LAS v4u*)(L + L_KVF))[ht] = v4u{0u, 0u, 0u, 0u};
    ((LAS v4u*)(L + L_KVF))[256 + ht] = v4u{0u, 0u, 0u, 0u};
    auto item = [&](int it, int& red_off, int& kvf_off) {
      const int ie = it / 5, ic = it % 5;
      const int ihead = ie < 40 ? (ie >= 20) : ie - 40;
      const int id = 20 * ihead + 4 * ic, dg = id >> 2;
      const int sa = id >> 4, col = 40 + ie;
      const int tile = ie < 40 ? 2 * sa + (col >> 4) - 2 : 7 + sa;
      red_off = (tile * 64 + (dg & 3) * 16 + (ie < 40 ? (col & 15) : 0)) * 16;
      kvf_off = ((ie < 40 ? (ie >> 4) : 3) * 2 + (dg >> 3)) * 1024 + ((dg & 3) * 16 + (ie < 40 ? (ie & 15) : ihead)) * 16 + ((dg >> 2) & 1) * 8;
    };
    const int it = ht < 210 ? ht : 209;
    int red_off[4], kvf_off, unused;
    item(it, unused, kvf_off);
#pragma unroll
    for (int j = 0; j < 4; ++j) { item(min(j * 64 + lane, 209), red_off[j], unused); PIN(red_off[j]); }
    PIN(kvf_off);
    if (hw == 1) wait_flag(lane < 32 ? FLAG(7, c8) + lane : FLAG(5, c16) + t, 48, tag, lane);
    DRAIN();
    STAMP(4);
    BAR();
    STAMP(11);
    if (hw == 0) {
      f32x4 pv[4][4];
#pragma unroll
      for (int j = 0; j < 4; ++j)
#pragma unroll
        for (int ww = 0; ww < 4; ++ww) pv[j][ww] = *(LAS f32x4*)(L + L_RED + ww * 10240 + red_off[j]);
#pragma unroll
      for (int j = 0; j < 4; ++j) {
        const f32x4 v = add4(add4(pv[j][0], pv[j][1]), add4(pv[j][2], pv[j][3]));
        st128(__builtin_bit_cast(v4u, v), rs, j * 1024 + lane * 16, WS_KVP + (c16 * 16 + r128) * 4096);
      }
      asm volatile("" ::: "memory");
      sig[1] = tag;
      STAMP(12);
      publish(FLAG(1, c16) + r128, tag, lane);
      STAMP(10);
      wait_flag(FLAG(1, c16) + lane, 16, tag, lane);
      sig[2] = tag;
    } else {
      dma_image<40, 100, THR, 3>(lds0 + L_WO, lane * 16, srd_ws, WS_TO + c8 * 102400, hw - 1, 3 * (bi >> 2));
      sig_wait(sig + 1, tag);
      dma_image<0, 40, THR, 3>(lds0 + L_WO, lane * 16, srd_ws, WS_TO + c8 * 102400, hw - 1, 2 * (bi >> 2));
      STAMP(10);
      sig_wait(sig + 2, tag);
    }
    STAMP(5);
    {
      v4u pv[16];
#pragma unroll
      for (int r = 0; r < 16; ++r) pv[r] = ld128(rs, it * 16, WS_KVP + (c16 * 16 + r) * 4096);
      SCHED();
      f32x4 v = __builtin_bit_cast(f32x4, pv[0]);
#pragma unroll
      for (int r = 1; r < 16; ++r) v = add4(v, __builtin_bit_cast(f32x4, pv[r]));
      v2u o; o.x = pk2(lo2(v)); o.y = pk2(hi2(v));
      *(LAS v2u*)(L + L_KVF + kvf_off) = o;
      if (ht >= 200) {
#pragma unroll
        for (int c = 1; c < 4; ++c) *(LAS v2u*)(L + L_KVF + kvf_off + c * 64) = o;
      }
    }
    DRAIN();
    STAMP(6);
    BAR();
    dma_image<70, 120, THR>(lds0 + L_W1, lane * 16, srd_ws, WS_T1 + c16 * 122880, hw, 6 * (bi >> 3));
    if (hw == 0) {
      v4u pv[16];
#pragma unroll
      for (int r = 0; r < 16; ++r) pv[r] = ld128(rs, sel(lane < 24, lane * 16), WS_CS + (c16 * 16 + r) * 384);
      f32x4 v = __builtin_bit_cast(f32x4, pv[0]);
#pragma unroll
      for (int r = 1; r < 16; ++r) v = add4(v, __builtin_bit_cast(f32x4, pv[r]));
      if (lane < 24) *(LAS f32x4*)(L + L_CS + lane * 16) = v;
    }
    STAMP(7);
    BAR();
    dma_image<0, 70, THR>(lds0 + L_W1, lane * 16, srd_ws, WS_T1 + c16 * 122880, hw, 8 * (bi >> 3));
    DRAIN();
    STAMP(8);
    BAR_RAW();
    dma_image<0, 30, THR>(lds0 + L_W2, lane * 16, srd_ws, WS_T2 + c16 * 122880, hw, 3 * (bi >> 3));
    BAR_RAW();
    dma_image<30, 120, THR>(lds0 + L_W2, lane * 16, srd_ws, WS_T2 + c16 * 122880, hw, 11 * (bi >> 3));
    DRAIN();
    STAMP(9);
    BAR_RAW();
    return;
  }

  const __amdgpu_buffer_rsrc_t rx = __builtin_amdgcn_make_buffer_rsrc((void*)x, 0, S * D * 2, 0x00020000);
  const int grp32 = 4 * r128 + w;
  constexpr int AW = 6;
  unsigned tag;

  {
    const int tok = 8 * b + (lane & 7), cl = lane >> 3;
    const bool has3 = w < 2;
    v4u xv[3];
#pragma unroll
    for (int j = 0; j < 3; ++j) xv[j] = __builtin_amdgcn_raw_buffer_load_b128(rx, (j < 2 || has3) ? tok * 1280 + (w + 4 * j) * 128 + cl * 16 : OOB, 0, 0);
    tag = __hip_atomic_load((gu32*)(ws + WS_EPOCH), RLX_AGENT) + 1u;
    SCHED();
    int xd = ((tok >> 4) * 20 + 2 * w + (cl >> 2)) * 1024 + ((cl & 3) * 16 + (tok & 15)) * 16;
    PIN(xd);
    SCHED();
    f32x2 f[12];
#pragma unroll
    for (int j = 0; j < 3; ++j)
#pragma unroll
      for (int e = 0; e < 4; ++e) f[j * 4 + e] = bf2(xv[j][e]);
    f32x2 s2 = (f[0] + f[1]) + (f[2] + f[3]) + ((f[4] + f[5]) + (f[6] + f[7])) + ((f[8] + f[9]) + (f[10] + f[11]));
    const float mloc = xsum32(xsum16(xsum8(s2[0] + s2[1]))) * (has3 ? 1.f / 192.f : 1.f / 128.f);
    const float m3 = has3 ? mloc : 0.f;
    f32x2 q2 = {0.f, 0.f}, q3 = {0.f, 0.f};
#pragma unroll
    for (int j = 0; j < 12; j += 2) {
      const f32x2 d0 = f[j] - (j < 8 ? mloc : m3), d1 = f[j + 1] - (j < 8 ? mloc : m3);
      q2 += d0 * d0; q3 += d1 * d1;
    }
    q2 += q3;
    const float m2loc = xsum32(xsum16(xsum8(q2[0] + q2[1])));
    if (lane < 8) *(LAS f32x2*)(L + L_CS + 512 + (lane * 4 + w) * 8) = f32x2{mloc, m2loc};
    STAMP(25);
    BAR();
    const f32x4 p01 = *(LAS f32x4*)(L + L_CS + 512 + (lane & 7) * 32), p23 = *(LAS f32x4*)(L + L_CS + 512 + (lane & 7) * 32 + 16);
    const float mean = (p01[0] + p01[2]) * 0.3f + (p23[0] + p23[2]) * 0.2f;
    const float e0 = p01[0] - mean, e1 = p01[2] - mean, e2 = p23[0] - mean, e3 = p23[2] - mean;
    const float m2 = (p01[1] + p01[3]) + (p23[1] + p23[3]) + 192.f * (e0 * e0 + e1 * e1) + 128.f * (e2 * e2 + e3 * e3);
    const float rstd = __builtin_amdgcn_rsqf(m2 * (1.f / D) + LN_EPS);
#pragma unroll
    for (int j = 0; j < 3; ++j) {
      v4u o;
#pragma unroll
      for (int e = 0; e < 4; ++e) o[e] = pk2((f[j * 4 + e] - mean) * rstd);
      st128(o, rs, (j < 2 || has3) ? xd : OOB, WS_XN + j * 8192);
    }
    STAMP(26);
    DRAIN();
    BAR_RAW();
    if (tid < 2) flag_set(tid == 0 ? FLAG(0, b >> 2) + (b & 3) : FLAG(6, c16) + r128, tag);
  }
  STAMP(1);

  {
    wait_flag(FLAG(0, grp32) + lane, 4, tag, lane);
    STAMP(2);
    v4u a[20][2];
    const int abase = WS_XN + 2 * grp32 * 20 * 1024;
#pragma unroll
    for (int ks = 0; ks < AW; ++ks)
#pragma unroll
      for (int m = 0; m < 2; ++m) a[ks][m] = ld128(rs, lane * 16, abase + (m * 20 + ks) * 1024);
    SCHED();
    BAR_RAW();
    STAMP(27);
    {
      f32x4 acc[2][5];
      const LBase bp(lds0 + L_BKV + (lane >> 2) * 160 + (lane & 3) * 8);
      kloop<2, 5, 20, 1, false>(acc, [&](int ks, int s, int h) { return bp.rd(ks * 5120 + h * 2560 + s * 32); },
        [&](int ks, int m) { return a[ks][m]; },
        [&](int ks) {
          if (ks + AW < 20) {
#pragma unroll
            for (int m = 0; m < 2; ++m) a[ks + AW][m] = ld128(rs, lane * 16, abase + (m * 20 + ks + AW) * 1024);
          }
          if (ks == 3 || ks == 10) BAR_RAW();
        });
      STAMP(3);
      v4u kf[5];
#pragma unroll
      for (int s = 0; s < 5; ++s)
#pragma unroll
        for (int m = 0; m < 2; ++m) {
          f32x2 u0 = lo2(acc[m][s]), u1 = hi2(acc[m][s]);
          if (s < 2) { u0 = phi2(u0); u1 = phi2(u1); }
          if (s == 2) { const f32x2 p0 = phi2(u0), p1 = phi2(u1); u0 = t < 8 ? p0 : u0; u1 = t < 8 ? p1 : u1; }
          kf[s][2 * m] = pk2(u0); kf[s][2 * m + 1] = pk2(u1);
        }
      const v4u ones = {0x3F803F80u, 0x3F803F80u, 0x3F803F80u, 0x3F803F80u};
      const f32x4 z4 = {0.f, 0.f, 0.f, 0.f};
      f32x4 T[10];
      T[0] = mfma(kf[0], kf[2], z4); T[1] = mfma(kf[0], kf[3], z4); T[2] = mfma(kf[1], kf[2], z4); T[3] = mfma(kf[1], kf[3], z4);
      T[4] = mfma(kf[1], kf[4], z4); T[5] = mfma(kf[2], kf[3], z4); T[6] = mfma(kf[2], kf[4], z4);
      T[7] = mfma(kf[0], ones, z4); T[8] = mfma(kf[1], ones, z4); T[9] = mfma(kf[2], ones, z4);
      STAMP(28);
      LAS f32x4* red = (LAS f32x4*)(L + L_RED);
#pragma unroll
      for (int i = 0; i < 10; ++i) red[(w * 10 + i) * 64 + lane] = T[i];
      BAR();
    }
    STAMP(4);
    f32x4 qa[2][3];
    {
      unsigned bp0 = lds0 + L_BQ + (lane >> 2) * 64 + (lane & 3) * 8 + (g & 1) * 32;
      unsigned bp1 = lds0 + L_BQ + (lane >> 2) * 64 + (lane & 3) * 8 + 32 - (g & 1) * 32;
      unsigned bp2 = lds0 + L_BQ + 2048 + (lane >> 2) * 16 + (lane & 1) * 8;
      PIN(bp0); PIN(bp1); PIN(bp2);
      kloop<2, 3, 20, 2, true>(qa,
        [&](int ks, int s, int h) {
          return s == 0 ? rdtr((LAS char*)bp0 + ks * 2560 + h * 1024) : s == 1 ? rdtr((LAS char*)bp1 + ks * 2560 + h * 1024) : rdtr((LAS char*)bp2 + ks * 2560 + h * 256);
        },
        [&](int ks, int m) { return a[ks][m]; },
        [&](int ks) {});
    }
    v4u pq0[2], pq1[2];
#pragma unroll
    for (int m = 0; m < 2; ++m) {
      pq0[m].x = pk2(phi2(lo2(qa[m][0]))); pq0[m].y = pk2(phi2(hi2(qa[m][0])));
      pq0[m].z = pk2(phi2(lo2(qa[m][1]))); pq0[m].w = pk2(phi2(hi2(qa[m][1])));
      const unsigned lo = pk2(phi2(lo2(qa[m][2]))), hi = pk2(phi2(hi2(qa[m][2])));
      pq1[m].x = g < 2 ? lo : 0u; pq1[m].y = g < 2 ? hi : 0u; pq1[m].z = 0u; pq1[m].w = 0u;
      PIN(pq0[m]); PIN(pq1[m]);
    }
    STAMP(5);
    BAR_RAW();
    STAMP(17);
    {
      v4u kv[4][2];
#pragma unroll
      for (int es = 3; es >= 0; --es)
#pragma unroll
        for (int ks = 0; ks < 2; ++ks) kv[es][ks] = *(LAS v4u*)(L + L_KVF + (es * 2 + ks) * 1024 + lane * 16);
      SCHED();
      f32x4 o[2][4];
#pragma unroll
      for (int es = 3; es >= 0; --es)
#pragma unroll
        for (int m = 0; m < 2; ++m) o[m][es] = mfma(kv[es][0], pq0[m], f32x4{0.f, 0.f, 0.f, 0.f});
#pragma unroll
      for (int es = 3; es >= 0; --es)
#pragma unroll
        for (int m = 0; m < 2; ++m) o[m][es] = mfma(kv[es][1], pq1[m], o[m][es]);
#pragma unroll
      for (int m = 0; m < 2; ++m) {
        const float i0 = __builtin_amdgcn_rcpf(o[m][3][0] + 1e-6f), i1 = __builtin_amdgcn_rcpf(o[m][3][1] + 1e-6f);
        const float im = g == 0 ? i0 : i1;
        v4u ov;
        ov.x = pk2(lo2(o[m][0]) * i0); ov.y = pk2(hi2(o[m][0]) * i0);
        ov.z = pk2(lo2(o[m][1]) * im); ov.w = pk2(hi2(o[m][1]) * im);
        v2u ol; ol.x = pk2(lo2(o[m][2]) * i1); ol.y = pk2(hi2(o[m][2]) * i1);
        const int tt = 2 * grp32 + m;
        st128(ov, rs, lane * 16, WS_O + (tt * 20 + c16) * 1024);
        st64(ol, rs, g < 2 ? lane * 8 : OOB, WS_O + (tt * 20 + 16) * 1024 + c16 * 256);
      }
      STAMP(18);
      publish(FLAG(2, grp32) + c16, tag, lane);
    }
    STAMP(7);
  }

  {
    const int tt = 4 * r64 + w, tok = tt * 16 + t;
    wait_flag(FLAG(2, tt >> 1) + lane, 16, tag, lane);
    STAMP(8);
    if (b == 0 && tid == 0) __hip_atomic_store((gu32*)(ws + WS_EPOCH), tag, RLX_AGENT);
    v4u a[20];
    v2u xr[5];
#pragma unroll
    for (int ks = 0; ks < 2 * AW; ++ks) a[ks] = ld128(rs, lane * 16, WS_O + (tt * 20 + ks) * 1024);
    SCHED();
    f32x4 acc[1][5];
    const LBase bp(lds0 + L_WO + (lane >> 2) * 160 + (lane & 3) * 8);
    kloop<1, 5, 20, 1, true>(acc, [&](int ks, int s, int h) { return bp.rd(ks * 5120 + h * 2560 + s * 32); },
      [&](int ks, int m) { return a[ks]; },
      [&](int ks) {
        if (ks + 2 * AW < 16) a[ks + 2 * AW] = ld128(rs, lane * 16, WS_O + (tt * 20 + ks + 2 * AW) * 1024);
        else if (ks + 2 * AW < 20) a[ks + 2 * AW] = ld_split(rs, lane, WS_O + (tt * 20 + ks + 2 * AW) * 1024);
        if (ks >= 8 && ks < 13) xr[ks - 8] = __builtin_amdgcn_raw_buffer_load_b64(rx, tok * 1280 + (80 * c8 + 16 * (ks - 8) + 4 * g) * 2, 0, 0);
      });
    BAR_RAW();
    STAMP(9);
    v2u hb[5];
    f32x2 hv[10];
#pragma unroll
    for (int s = 0; s < 5; ++s) {
      hb[s].x = pk2(lo2(acc[0][s]) + bf2(xr[s].x));
      hb[s].y = pk2(hi2(acc[0][s]) + bf2(xr[s].y));
      hv[2 * s] = bf2(hb[s].x); hv[2 * s + 1] = bf2(hb[s].y);
    }
    st128(v4u{hb[0].x, hb[0].y, hb[1].x, hb[1].y}, rs, lane * 16, WS_H + (tt * 20 + 2 * c8) * 1024);
    st128(v4u{hb[2].x, hb[2].y, hb[3].x, hb[3].y}, rs, lane * 16, WS_H + (tt * 20 + 2 * c8 + 1) * 1024);
    st64(hb[4], rs, lane * 8, WS_H + (tt * 20 + 16) * 1024 + c8 * 512);
    const f32x2 s2 = ((hv[0] + hv[1]) + (hv[2] + hv[3])) + ((hv[4] + hv[5]) + (hv[6] + hv[7])) + (hv[8] + hv[9]);
    const float mc = xsum32(xsum16(s2[0] + s2[1])) * (1.f / 80.f);
    f32x2 q2 = {0.f, 0.f}, q3 = {0.f, 0.f};
#pragma unroll
    for (int j = 0; j < 10; j += 2) { const f32x2 d0 = hv[j] - mc, d1 = hv[j + 1] - mc; q2 += d0 * d0; q3 += d1 * d1; }
    q2 += q3;
    const float m2 = xsum32(xsum16(q2[0] + q2[1]));
    st64(v2u{__builtin_bit_cast(unsigned, mc), __builtin_bit_cast(unsigned, m2)}, rs, g == 0 ? t * 8 : OOB, WS_ST + (tt * 8 + c8) * 128);
    STAMP(20);
    publish(FLAG(3, tt >> 1) + (tt & 1) * 8 + c8, tag, lane);
    STAMP(10);
  }

  {
    v2u bb[6];
#pragma unroll
    for (int s = 0; s < 6; ++s) bb[s] = *(const v2u*)(b1 + 96 * c16 + 16 * s + 4 * g);
    wait_flag(FLAG(3, grp32) + lane, 16, tag, lane);
    STAMP(11);
    v4u stt[2];
#pragma unroll
    for (int m = 0; m < 2; ++m) {
      const v2u s0 = ld64(rs, g * 256 + t * 8, WS_ST + (2 * grp32 + m) * 1024), s1 = ld64(rs, g * 256 + 128 + t * 8, WS_ST + (2 * grp32 + m) * 1024);
      stt[m] = v4u{s0.x, s0.y, s1.x, s1.y};
    }
    constexpr int WIN = AW + 1;
    v4u a[WIN][2];
    const int abase = WS_H + 2 * grp32 * 20 * 1024;
#pragma unroll
    for (int ks = 0; ks < AW; ++ks)
#pragma unroll
      for (int m = 0; m < 2; ++m) a[ks][m] = ld128(rs, lane * 16, abase + (m * 20 + ks) * 1024);
    SCHED();
    float nm[2], rr[2];
#pragma unroll
    for (int m = 0; m < 2; ++m) {
      const f32x4 sv = __builtin_bit_cast(f32x4, stt[m]);
      const float mean = xsum32(xsum16(sv[0] + sv[2])) * 0.125f;
      const float m2 = xsum32(xsum16(sv[1] + sv[3] + 80.f * ((sv[0] - mean) * (sv[0] - mean) + (sv[2] - mean) * (sv[2] - mean))));
      const float var = m2 * (1.f / D);
      const float r1 = __builtin_amdgcn_rsqf(var + LN_EPS);
      rr[m] = r1 * __builtin_amdgcn_rsqf(var * r1 * r1 + LN_EPS);
      nm[m] = -mean * rr[m];
      PIN(rr[m]); PIN(nm[m]);
    }
    BAR_RAW();
    STAMP(22);
    const LBase bp0(lds0 + L_W1 + (lane >> 2) * 192 + (lane & 3) * 8 + (g & 1) * 32);
    const LBase bp1(lds0 + L_W1 + (lane >> 2) * 192 + (lane & 3) * 8 - (g & 1) * 32);
    f32x4 acc[2][6];
    f32x4 cs[6];
    kloop<2, 6, 20, 1, true>(acc, [&](int ks, int s, int h) { return ((s & 1) ? bp1 : bp0).rd(ks * 6144 + h * 3072 + s * 32); },
      [&](int ks, int m) { return a[ks % WIN][m]; },
      [&](int ks) {
        if (ks + AW < 20) {
#pragma unroll
          for (int m = 0; m < 2; ++m)
            a[(ks + AW) % WIN][m] = ks + AW < 16 ? ld128(rs, lane * 16, abase + (m * 20 + ks + AW) * 1024) : ld_split(rs, lane, abase + (m * 20 + ks + AW) * 1024);
        }
        if (ks == 19) {
#pragma unroll
          for (int s = 0; s < 6; ++s) cs[s] = *(LAS f32x4*)(L + L_CS + (s * 4 + g) * 16);
        }
      });
    BAR_RAW();
    STAMP(12);
#pragma unroll
    for (int m = 0; m < 2; ++m) {
      v2u hb[6];
#pragma unroll
      for (int s = 0; s < 6; ++s) {
        const f32x2 p0 = lo2(acc[m][s]) * rr[m] + (lo2(cs[s]) * nm[m] + bf2(bb[s].x));
        const f32x2 p1 = hi2(acc[m][s]) * rr[m] + (hi2(cs[s]) * nm[m] + bf2(bb[s].y));
        hb[s].x = pk2(gelu2(p0)); hb[s].y = pk2(gelu2(p1));
        if (s & 1)
          st128(v4u{hb[s - 1].x, hb[s - 1].y, hb[s].x, hb[s].y}, rs, lane * 16, WS_H1 + ((2 * grp32 + m) * 48 + 3 * c16 + (s >> 1)) * 1024);
      }
    }
    STAMP(24);
    publish(FLAG(4, grp32) + c16, tag, lane);
    STAMP(13);
  }

  {
    v2u hr[2][3], bb[3];
#pragma unroll
    for (int s = 0; s < 3; ++s) {
      int n = 40 * c16 + 16 * s + 4 * g;
      if (s == 2 && g >= 2) n = 40 * c16;
      bb[s] = *(const v2u*)(b2 + n);
      const int cc = n / 80, off = n % 80, ss = off >> 4, gg = (off >> 2) & 3;
      const int ho = ss < 4 ? (2 * cc + (ss >> 1)) * 1024 + (gg * 16 + t) * 16 + (ss & 1) * 8 : 16 * 1024 + cc * 512 + (gg * 16 + t) * 8;
#pragma unroll
      for (int m = 0; m < 2; ++m) hr[m][s] = ld64(rs, ho, WS_H + (2 * grp32 + m) * 20 * 1024);
    }
    wait_flag(FLAG(4, grp32) + lane, 16, tag, lane);
    STAMP(14);
    constexpr int WIN = 8;
    v4u a[WIN][2];
    const int abase = WS_H1 + 2 * grp32 * 48 * 1024;
#pragma unroll
    for (int ks = 0; ks < WIN - 1; ++ks)
#pragma unroll
      for (int m = 0; m < 2; ++m) a[ks][m] = ld128(rs, lane * 16, abase + (m * 48 + ks) * 1024);
    SCHED();
    BAR_RAW();
    f32x4 acc[2][3];
    const LBase bp0(lds0 + L_W2 + (lane >> 2) * 64 + (lane & 3) * 8 + (g & 1) * 32);
    const LBase bp1(lds0 + L_W2 + (lane >> 2) * 64 + (lane & 3) * 8 + 32 - (g & 1) * 32);
    const LBase bp2(lds0 + L_W2 + 2048 + (lane >> 2) * 16 + (lane & 1) * 8);
    kloop<2, 3, 48, 2, true>(acc,
      [&](int ks, int s, int h) { return s == 0 ? bp0.rd(ks * 2560 + h * 1024) : s == 1 ? bp1.rd(ks * 2560 + h * 1024) : bp2.rd(ks * 2560 + h * 256); },
      [&](int ks, int m) { return a[ks % WIN][m]; },
      [&](int ks) {
        if (ks + WIN - 1 < 48) {
#pragma unroll
          for (int m = 0; m < 2; ++m) a[(ks + WIN - 1) % WIN][m] = ld128(rs, lane * 16, abase + (m * 48 + ks + WIN - 1) * 1024);
        }
      });
    STAMP(15);
    const __amdgpu_buffer_rsrc_t rout = __builtin_amdgcn_make_buffer_rsrc((void*)out, 0, S * D * 2, 0x00020000);
#pragma unroll
    for (int m = 0; m < 2; ++m) {
      const int tok = (2 * grp32 + m) * 16 + t;
#pragma unroll
      for (int s = 0; s < 3; ++s) {
        v2u o;
        o.x = pk2(lo2(acc[m][s]) + bf2(bb[s].x) + bf2(hr[m][s].x));
        o.y = pk2(hi2(acc[m][s]) + bf2(bb[s].y) + bf2(hr[m][s].y));
        __builtin_amdgcn_raw_buffer_store_b64(o, rout, (s < 2 || g < 2) ? (tok * D + 40 * c16 + 16 * s + 4 * g) * 2 : OOB, 0, OUTAUX);
      }
    }
    STAMP(16);
  }
}
}

extern "C" void kernel_launch(void* const* d_in, const int* in_sizes, int n_in, void* d_out, int out_size, void* d_ws,
                              size_t ws_size, hipStream_t stream) {
  const unsigned short* x = (const unsigned short*)d_in[0];
  const unsigned short* wqkv = (const unsigned short*)d_in[1];
  const unsigned short* wo = (const unsigned short*)d_in[2];
  const unsigned short* w1 = (const unsigned short*)d_in[4];
  const unsigned short* b1 = (const unsigned short*)d_in[5];
  const unsigned short* w2 = (const unsigned short*)d_in[6];
  const unsigned short* b2 = (const unsigned short*)d_in[7];
  fused_block<<<256, 512, 0, stream>>>(x, wqkv, wo, w1, b1, w2, b2, (unsigned short*)d_out, (unsigned char*)d_ws);
}
```

```cpp
#include <hip/hip_runtime.h>
#include <hip/hip_bf16.h>

#define STAMPS 0
#define THR 0
#define TOUCH 0
#define CODEPF 0
#define FLAGPF 0
#define OUTAUX 16
#define OUTPF 1
#define HPRIO 0
#define WOLATE 0
#define SLEEP 1

#define GAS __attribute__((address_space(1)))
#define LAS __attribute__((address_space(3)))
#define RLX_AGENT __ATOMIC_RELAXED, __HIP_MEMORY_SCOPE_AGENT
typedef unsigned v4u __attribute__((ext_vector_type(4)));
typedef unsigned v2u __attribute__((ext_vector_type(2)));
typedef float f32x4 __attribute__((ext_vector_type(4)));
typedef float f32x2 __attribute__((ext_vector_type(2)));
typedef __bf16 bf16x8 __attribute__((ext_vector_type(8)));
typedef __bf16 bf16x2 __attribute__((ext_vector_type(2)));
typedef short s16x4 __attribute__((ext_vector_type(4)));
typedef GAS unsigned gu32;

namespace {
constexpr int S = 2048, D = 640, F = 1536;
constexpr float LN_EPS = 1e-5f;
constexpr int AUX_SC1 = 16;
constexpr unsigned kSpinCap = 1u << 17;
constexpr int OOB = 0x7FFFFF00;

constexpr int WS_EPOCH = 0;
constexpr int WS_FLAG = 4096;
constexpr int FLAG_SET = 64 * 256;
constexpr int WS_XN = 1 << 20;
constexpr int WS_O = WS_XN + 128 * 20 * 1024;
constexpr int WS_H = WS_O + 128 * 20 * 1024;
constexpr int WS_H1 = WS_H + 128 * 20 * 1024;
constexpr int WS_KVP = WS_H1 + 128 * 48 * 1024;
constexpr int WS_ST = WS_KVP + 16 * 16 * 4096;
constexpr int WS_DBG = WS_ST + 2048 * 64;
constexpr int WS_TKV = WS_DBG + 256 * 8 * 32 * 8;
constexpr int WS_TO = WS_TKV + 16 * 153600;
constexpr int WS_T1 = WS_TO + 8 * 102400;
constexpr int WS_T2 = WS_T1 + 16 * 122880;
constexpr int WS_CS = WS_T2 + 16 * 122880;
constexpr int WS_BYTES = WS_CS + 16 * 16 * 384;

constexpr int L_BKV = 0, L_BQ = 102400;
constexpr int L_RED = 0, L_KVF = 153600;
constexpr int L_WO = 0, L_W1 = 30720, L_W2 = 0, L_CS = 161792;
constexpr int LDS_BYTES = 163840;

__device__ __forceinline__ f32x4 mfma(v4u a, v4u b, f32x4 c) {
  return __builtin_amdgcn_mfma_f32_16x16x32_bf16(__builtin_bit_cast(bf16x8, a), __builtin_bit_cast(bf16x8, b), c, 0, 0, 0);
}
__device__ __forceinline__ unsigned pk2(f32x2 v) { return __builtin_bit_cast(unsigned, __builtin_convertvector(v, bf16x2)); }
__device__ __forceinline__ unsigned pk2(float lo, float hi) { return pk2(f32x2{lo, hi}); }
__device__ __forceinline__ float bf_lo(unsigned u) { return __builtin_bit_cast(float, u << 16); }
__device__ __forceinline__ float bf_hi(unsigned u) { return __builtin_bit_cast(float, u & 0xFFFF0000u); }
__device__ __forceinline__ f32x2 bf2(unsigned u) { return f32x2{bf_lo(u), bf_hi(u)}; }
__device__ __forceinline__ f32x2 lo2(f32x4 v) { return f32x2{v[0], v[1]}; }
__device__ __forceinline__ f32x2 hi2(f32x4 v) { return f32x2{v[2], v[3]}; }
__device__ __forceinline__ v4u mk_srd(const void* p, unsigned bytes) {
  const unsigned long long a = (unsigned long long)p;
  v4u r;
  r.x = (unsigned)a; r.y = (unsigned)(a >> 32) & 0xFFFFu; r.z = bytes; r.w = 0x00020000u;
  return r;
}
#define DRAIN() asm volatile("s_waitcnt vmcnt(0)" ::: "memory")
#define BAR() asm volatile("s_waitcnt lgkmcnt(0)\n\ts_barrier" ::: "memory")
#define BAR_RAW() asm volatile("s_barrier" ::: "memory")
#define SCHED() __builtin_amdgcn_sched_barrier(0)
#define PIN(x) asm volatile("" : "+v"(x))

__device__ __forceinline__ void dma2(unsigned lds_addr, unsigned voff, v4u srd, unsigned soff) {
  unsigned keep;
  asm volatile("s_mov_b32 %0, m0\n\ts_mov_b32 m0, %1\n\ts_nop 0\n\t"
               "buffer_load_dwordx4 %2, %3, %4 offen sc1 lds\n\t"
               "buffer_load_dwordx4 %2, %3, %4 offen offset:1024 sc1 lds\n\t"
               "s_mov_b32 m0, %0"
               : "=&s"(keep) : "s"(lds_addr), "v"(voff), "s"(srd), "s"(soff) : "memory");
}
template <int LO, int HI, int TH = 0, int NW = 4>
__device__ __forceinline__ void dma_image(unsigned lds_base, unsigned voff, v4u srd, int ws_off, int hw, int rot) {
  constexpr int NP = (HI - LO) / 2;
#pragma unroll 1
  for (int i = hw; i < NP; i += NW) {
    int j = i + rot;
    if (j >= NP) j -= NP;
    dma2(lds_base + LO * 1024 + j * 2048, voff, srd, ws_off + LO * 1024 + j * 2048);
    if (TH) asm volatile("s_waitcnt vmcnt(%0)" :: "i"(TH - 2) : "memory");
  }
}
__device__ __forceinline__ v2u rdtr(LAS char* p) { return __builtin_bit_cast(v2u, __builtin_amdgcn_ds_read_tr16_b64_v4i16((LAS s16x4*)p)); }
struct LBase {
  unsigned a[3];
  __device__ __forceinline__ explicit LBase(unsigned v) { a[0] = v; a[1] = v + 65536; a[2] = v + 131072; PIN(a[0]); PIN(a[1]); PIN(a[2]); }
  __device__ __forceinline__ v2u rd(int off) const { return rdtr((LAS char*)(a[off >> 16]) + (off & 65535)); }
};
template <int MT, int NT, int KS, int PD, bool WA, class RB, class GA, class PRE>
__device__ __forceinline__ void kloop(f32x4 (&acc)[MT][NT], RB rb, GA ga, PRE pre) {
  v2u bl[PD + 1][NT], bh[PD + 1][NT];
#pragma unroll
  for (int m = 0; m < MT; ++m)
#pragma unroll
    for (int s = 0; s < NT; ++s) acc[m][s] = f32x4{0.f, 0.f, 0.f, 0.f};
#pragma unroll
  for (int ks = 0; ks < PD; ++ks)
#pragma unroll
    for (int s = 0; s < NT; ++s) { bl[ks][s] = rb(ks, s, 0); bh[ks][s] = rb(ks, s, 1); }
  SCHED();
#pragma unroll
  for (int ks = 0; ks < KS; ++ks) {
    pre(ks);
#pragma unroll
    for (int s = 0; s < NT; ++s)
#pragma unroll
      for (int m = 0; m < MT; ++m) {
        const v2u lo = bl[ks % (PD + 1)][s], hi = bh[ks % (PD + 1)][s];
        const v4u wv = {lo.x, lo.y, hi.x, hi.y};
        acc[m][s] = WA ? mfma(wv, ga(ks, m), acc[m][s]) : mfma(ga(ks, m), wv, acc[m][s]);
        if (ks + PD < KS) {
          if (MT == 1 || m == 0) bl[(ks + PD) % (PD + 1)][s] = rb(ks + PD, s, 0);
          if (MT == 1 || m == 1) bh[(ks + PD) % (PD + 1)][s] = rb(ks + PD, s, 1);
        }
        SCHED();
      }
  }
}
__device__ __forceinline__ v4u ld128(__amdgpu_buffer_rsrc_t rs, int voff, int soff) {
  return __builtin_amdgcn_raw_buffer_load_b128(rs, voff, soff, AUX_SC1);
}
__device__ __forceinline__ v2u ld64(__amdgpu_buffer_rsrc_t rs, int voff, int soff) {
  return __builtin_amdgcn_raw_buffer_load_b64(rs, voff, soff, AUX_SC1);
}
__device__ __forceinline__ void st128(v4u v, __amdgpu_buffer_rsrc_t rs, int voff, int soff) {
  __builtin_amdgcn_raw_buffer_store_b128(v, rs, (int)((unsigned)voff + (unsigned)soff), 0, AUX_SC1);
}
__device__ __forceinline__ int sel(bool c, int a) {
  PIN(a);
  return c ? a : OOB;
}
__device__ __forceinline__ v4u ld_split(__amdgpu_buffer_rsrc_t rs, int lane, int soff) {
  const v2u a = ld64(rs, lane * 8, soff), b = ld64(rs, lane * 8 + 512, soff);
  return v4u{a.x, a.y, b.x, b.y};
}
__device__ __forceinline__ void st64(v2u v, __amdgpu_buffer_rsrc_t rs, int voff, int soff) {
  __builtin_amdgcn_raw_buffer_store_b64(v, rs, voff, soff, AUX_SC1);
}
__device__ __forceinline__ void flag_set(gu32* p, unsigned tag) {
  asm volatile("global_atomic_swap %0, %1, off sc1" :: "v"(p), "v"(tag) : "memory");
}
__device__ __forceinline__ void wait_flag(gu32* p, int n, unsigned tag, int lane) {
#pragma unroll 1
  for (unsigned spins = 0; spins < kSpinCap; ++spins) {
    unsigned v = tag;
    if (lane < n) asm volatile("global_atomic_add %0, %1, %2, off sc0 sc1\n\ts_waitcnt vmcnt(0)" : "=&v"(v) : "v"(p), "v"(0u) : "memory");
    if (__all(v == tag)) break;
    if (SLEEP) __builtin_amdgcn_s_sleep(SLEEP);
  }
  asm volatile("" ::: "memory");
}
__device__ __forceinline__ void sig_wait(volatile LAS unsigned* p, unsigned tag) {
#pragma unroll 1
  for (unsigned spins = 0; spins < 64 * kSpinCap; ++spins) {
    if (*p == tag) break;
    __builtin_amdgcn_s_sleep(1);
  }
  asm volatile("" ::: "memory");
}
__device__ __forceinline__ void publish(gu32* flag, unsigned tag, int lane) {
  DRAIN();
  if (lane == 0) flag_set(flag, tag);
}
__device__ __forceinline__ f32x4 add4(f32x4 a, f32x4 b) {
  f32x4 r;
#pragma unroll
  for (int i = 0; i < 4; ++i) { float x; asm("v_add_f32 %0, %1, %2" : "=v"(x) : "v"(a[i]), "v"(b[i])); r[i] = x; }
  return r;
}
__device__ __forceinline__ float xsum32(float x) {
  unsigned u = __builtin_bit_cast(unsigned, x), v = u;
  asm("" : "+v"(v));
  auto r = __builtin_amdgcn_permlane32_swap(u, v, false, false);
  const unsigned a = r[0], b = r[1];
  return __builtin_bit_cast(float, a) + __builtin_bit_cast(float, b);
}
__device__ __forceinline__ float xsum16(float x) {
  unsigned u = __builtin_bit_cast(unsigned, x), v = u;
  asm("" : "+v"(v));
  auto r = __builtin_amdgcn_permlane16_swap(u, v, false, false);
  const unsigned a = r[0], b = r[1];
  return __builtin_bit_cast(float, a) + __builtin_bit_cast(float, b);
}
__device__ __forceinline__ float xsum8(float x) {
  return x + __builtin_bit_cast(float, __builtin_amdgcn_update_dpp(0, __builtin_bit_cast(int, x), 0x128, 0xf, 0xf, false));
}
__device__ __forceinline__ f32x2 phi2(f32x2 t) {
  const f32x2 u = t * 1.4426950408889634f;
  const f32x2 e = {__builtin_amdgcn_exp2f(fminf(u[0], 0.f)), __builtin_amdgcn_exp2f(fminf(u[1], 0.f))};
  const f32x2 p = {fmaxf(t[0], 0.f), fmaxf(t[1], 0.f)};
  return (p + 1e-6f) + e;
}
__device__ __forceinline__ f32x2 gelu2(f32x2 x) {
  const f32x2 u = x * (x * x * -0.1029432397f + -2.302208198f);
  const f32x2 d = f32x2{__builtin_amdgcn_exp2f(u[0]), __builtin_amdgcn_exp2f(u[1])} + 1.f;
  return x * f32x2{__builtin_amdgcn_rcpf(d[0]), __builtin_amdgcn_rcpf(d[1])};
}

#define STAMP(i) do { } while (0)

__global__ void __launch_bounds__(512, 2)
fused_block(const unsigned short* __restrict__ x, const unsigned short* __restrict__ wqkv, const unsigned short* __restrict__ wo,
            const unsigned short* __restrict__ w1, const unsigned short* __restrict__ b1, const unsigned short* __restrict__ w2,
            const unsigned short* __restrict__ b2, unsigned short* __restrict__ out, unsigned char* ws) {
  __shared__ __attribute__((aligned(16))) char smem[LDS_BYTES];
  LAS char* L = (LAS char*)smem;
  const unsigned lds0 = (unsigned)(size_t)L;
  const int tid = threadIdx.x, lane = tid & 63, w = __builtin_amdgcn_readfirstlane(tid >> 6);
  const int b = blockIdx.x, xcd = b & 7, bi = b >> 3;
  const int g = lane >> 4, t = lane & 15;
  const __amdgpu_buffer_rsrc_t rs = __builtin_amdgcn_make_buffer_rsrc((void*)ws, 0, WS_BYTES, 0x00020000);
  gu32* flags = (gu32*)(ws + WS_FLAG);
#define FLAG(set, grp) (flags + ((set) * FLAG_SET + (grp) * 256) / 4)
  STAMP(0);
  const int r128 = 4 * (xcd >> 1) + (bi >> 3), c16 = 8 * (xcd & 1) + (bi & 7);
  const int r64 = 8 * (xcd >> 1) + (bi >> 2), c8 = 4 * (xcd & 1) + (bi & 3);

  if (w >= 4) {
    const int hw = w - 4, ht = tid - 256;
    if (HPRIO) __builtin_amdgcn_s_setprio(HPRIO);
    const v4u srd_ws = mk_srd(ws, WS_BYTES);
    const __amdgpu_buffer_rsrc_t rq = __builtin_amdgcn_make_buffer_rsrc((void*)wqkv, 0, D * 3 * D * 2, 0x00020000);
    const __amdgpu_buffer_rsrc_t ro = __builtin_amdgcn_make_buffer_rsrc((void*)wo, 0, D * D * 2, 0x00020000);
    const __amdgpu_buffer_rsrc_t r1 = __builtin_amdgcn_make_buffer_rsrc((void*)w1, 0, D * F * 2, 0x00020000);
    const __amdgpu_buffer_rsrc_t r2 = __builtin_amdgcn_make_buffer_rsrc((void*)w2, 0, F * D * 2, 0x00020000);
    volatile LAS unsigned* sig = (volatile LAS unsigned*)(L + L_CS + 1024);
    const unsigned tag = __hip_atomic_load((gu32*)(ws + WS_EPOCH), RLX_AGENT) + 1u;
    const int h2 = ht & 127;
    const int q10 = h2 / 10, c10 = h2 % 10, q5 = h2 / 5, c5 = h2 % 5, q12 = h2 / 12, c12 = h2 % 12;
    if (hw < 2) {
      v4u tv[6];
      int td[6];
#pragma unroll
      for (int j = 0; j < 4; ++j) {
        const int R = 40 * r128 + 10 * j + q10, rho = R & 31;
        const int k = (R & ~31) + 8 * ((rho >> 2) & 3) + 4 * (rho >> 4) + (rho & 3);
        tv[j] = __builtin_amdgcn_raw_buffer_load_b128(rq, sel(h2 < 100, k * 3840 + c16 * 80 + (c10 < 5 ? 1280 : 2480) + c10 * 16), 0, 0);
        td[j] = sel(h2 < 100, WS_TKV + c16 * 153600 + R * 160 + c10 * 16);
      }
#pragma unroll
      for (int j = 0; j < 2; ++j) {
        const int R = 40 * r128 + 20 * j + q5, ks = R >> 5, rho = R & 31;
        const int k = 32 * ks + 8 * ((rho >> 2) & 3) + 4 * (rho >> 4) + (rho & 3);
        tv[4 + j] = __builtin_amdgcn_raw_buffer_load_b128(rq, sel(h2 < 100, k * 3840 + c16 * 80 + c5 * 16), 0, 0);
        td[4 + j] = sel(h2 < 100, WS_TKV + c16 * 153600 + 102400 + ks * 2560 + (c5 < 4 ? rho * 64 + (c5 ^ (2 * ((rho >> 2) & 1))) * 16 : 2048 + rho * 16));
      }
      BAR_RAW();
#pragma unroll
      for (int j = 0; j < 6; ++j) st128(tv[j], rs, td[j], 0);
      DRAIN();
      BAR_RAW();
      if (hw == 0) {
        wait_flag(FLAG(6, c16) + lane, 16, tag, lane);
        sig[0] = tag;
      }
    } else {
      v4u tv[10];
      int ts[10], td[10];
#pragma unroll
      for (int j = 0; j < 4; ++j) {
        const int R = 40 * r128 + 10 * j + q12, ks = R >> 5, rho = R & 31;
        const int k = ks < 16 ? 80 * (ks >> 1) + 32 * (ks & 1) + rho : 160 * (ks - 16) + 64 + 80 * (rho >> 4) + (rho & 15);
        ts[j] = sel(h2 < 120, k * 3072 + c16 * 192 + c12 * 16);
        td[j] = sel(h2 < 120, WS_T1 + c16 * 122880 + R * 192 + (c12 ^ (2 * ((rho >> 2) & 1))) * 16);
      }
#pragma unroll
      for (int j = 0; j < 2; ++j) {
        const int R = 20 * r64 + 10 * j + q10, ks = R >> 5, rho = R & 31;
        const int k = ks < 16 ? 40 * ks + rho : 40 * (4 * (ks - 16) + (rho >> 3)) + 32 + (rho & 7);
        ts[4 + j] = sel(h2 < 100, k * 1280 + c8 * 160 + c10 * 16);
        td[4 + j] = sel(h2 < 100, WS_TO + c8 * 102400 + R * 160 + c10 * 16);
      }
#pragma unroll
      for (int j = 0; j < 4; ++j) {
        const int R = 96 * r128 + 24 * j + q5, ks = R >> 5, rho = R & 31;
        ts[6 + j] = sel(h2 < 120, R * 1280 + c16 * 80 + c5 * 16);
        td[6 + j] = sel(h2 < 120, WS_T2 + c16 * 122880 + ks * 2560 + (c5 < 4 ? rho * 64 + (c5 ^ (2 * ((rho >> 2) & 1))) * 16 : 2048 + rho * 16));
      }
#pragma unroll
      for (int j = 0; j < 10; ++j) { PIN(ts[j]); PIN(td[j]); }
      unsigned pf0 = 0, pf1 = 0, pf2 = 0;
      BAR_RAW();
#pragma unroll
      for (int j = 0; j < 10; ++j) tv[j] = __builtin_amdgcn_raw_buffer_load_b128(j < 4 ? r1 : j < 6 ? ro : r2, ts[j], 0, 0);
      BAR_RAW();
#pragma unroll
      for (int j = 0; j < 10; ++j) st128(tv[j], rs, td[j], 0);
      asm volatile("" :: "v"(pf0), "v"(pf1), "v"(pf2));
      f32x2 sm[4];
#pragma unroll
      for (int e = 0; e < 4; ++e) sm[e] = (bf2(tv[0][e]) + bf2(tv[1][e])) + (bf2(tv[2][e]) + bf2(tv[3][e]));
      LAS f32x4* sc = (LAS f32x4*)(L + L_KVF);
      sc[h2 * 2] = f32x4{sm[0][0], sm[0][1], sm[1][0], sm[1][1]};
      sc[h2 * 2 + 1] = f32x4{sm[2][0], sm[2][1], sm[3][0], sm[3][1]};
    }
    sig_wait(sig, tag);
    STAMP(1);
    const int kvq = WS_TKV + c16 * 153600;
    dma_image<0, 24>(lds0 + L_BKV, lane * 16, srd_ws, kvq, hw, 3 * (bi >> 3));
    dma_image<24, 56>(lds0 + L_BKV, lane * 16, srd_ws, kvq, hw, 4 * (bi >> 3));
    asm volatile("s_waitcnt vmcnt(8)" ::: "memory");
    STAMP(2);
    BAR();
    dma_image<56, 104>(lds0 + L_BKV, lane * 16, srd_ws, kvq, hw, 6 * (bi >> 3));
    asm volatile("s_waitcnt vmcnt(12)" ::: "memory");
    BAR_RAW();
    if (hw == 3) {
      LAS f32x4* sc = (LAS f32x4*)(L + L_KVF);
      const int l24 = lane < 24 ? lane : 0;
      f32x4 v = sc[l24];
#pragma unroll
      for (int r = 1; r < 10; ++r) v = add4(v, sc[r * 24 + l24]);
      st128(__builtin_bit_cast(v4u, v), rs, sel(lane < 24, lane * 16), WS_CS + (c16 * 16 + r128) * 384);
    }
    DRAIN();
    STAMP(3);
    BAR_RAW();
    if (hw == 2 && lane < 2) flag_set(lane == 0 ? FLAG(5, c16) + r128 : FLAG(7, c8) + r64, tag);
    dma_image<104, 150>(lds0 + L_BKV, lane * 16, srd_ws, kvq, hw, 5 * (bi >> 3));
    ((LAS v4u*)(L + L_KVF))[ht] = v4u{0u, 0u, 0u, 0u};
    ((LAS v4u*)(L + L_KVF))[256 + ht] = v4u{0u, 0u, 0u, 0u};
    auto item = [&](int it, int& red_off, int& kvf_off) {
      const int ie = it / 5, ic = it % 5;
      const int ihead = ie < 40 ? (ie >= 20) : ie - 40;
      const int id = 20 * ihead + 4 * ic, dg = id >> 2;
      const int sa = id >> 4, col = 40 + ie;
      const int tile = ie < 40 ? 2 * sa + (col >> 4) - 2 : 7 + sa;
      red_off = (tile * 64 + (dg & 3) * 16 + (ie < 40 ? (col & 15) : 0)) * 16;
      kvf_off = ((ie < 40 ? (ie >> 4) : 3) * 2 + (dg >> 3)) * 1024 + ((dg & 3) * 16 + (ie < 40 ? (ie & 15) : ihead)) * 16 + ((dg >> 2) & 1) * 8;
    };
    const int it = ht < 210 ? ht : 209;
    int red_off[4], kvf_off, unused;
    item(it, unused, kvf_off);
#pragma unroll
    for (int j = 0; j < 4; ++j) { item(min(j * 64 + lane, 209), red_off[j], unused); PIN(red_off[j]); }
    PIN(kvf_off);
    if (hw == 1) wait_flag(lane < 32 ? FLAG(7, c8) + lane : FLAG(5, c16) + t, 48, tag, lane);
    DRAIN();
    STAMP(4);
    BAR();
    STAMP(11);
    if (hw == 0) {
      f32x4 pv[4][4];
#pragma unroll
      for (int j = 0; j < 4; ++j)
#pragma unroll
        for (int ww = 0; ww < 4; ++ww) pv[j][ww] = *(LAS f32x4*)(L + L_RED + ww * 10240 + red_off[j]);
#pragma unroll
      for (int j = 0; j < 4; ++j) {
        const f32x4 v = add4(add4(pv[j][0], pv[j][1]), add4(pv[j][2], pv[j][3]));
        st128(__builtin_bit_cast(v4u, v), rs, j * 1024 + lane * 16, WS_KVP + (c16 * 16 + r128) * 4096);
      }
      asm volatile("" ::: "memory");
      sig[1] = tag;
      STAMP(12);
      publish(FLAG(1, c16) + r128, tag, lane);
      STAMP(10);
      wait_flag(FLAG(1, c16) + lane, 16, tag, lane);
      sig[2] = tag;
    } else {
      dma_image<40, 100, THR, 3>(lds0 + L_WO, lane * 16, srd_ws, WS_TO + c8 * 102400, hw - 1, 3 * (bi >> 2));
      sig_wait(sig + 1, tag);
      dma_image<0, 40, THR, 3>(lds0 + L_WO, lane * 16, srd_ws, WS_TO + c8 * 102400, hw - 1, 2 * (bi >> 2));
      STAMP(10);
      sig_wait(sig + 2, tag);
    }
    STAMP(5);
    {
      v4u pv[16];
#pragma unroll
      for (int r = 0; r < 16; ++r) pv[r] = ld128(rs, it * 16, WS_KVP + (c16 * 16 + r) * 4096);
      SCHED();
      f32x4 v = __builtin_bit_cast(f32x4, pv[0]);
#pragma unroll
      for (int r = 1; r < 16; ++r) v = add4(v, __builtin_bit_cast(f32x4, pv[r]));
      v2u o; o.x = pk2(lo2(v)); o.y = pk2(hi2(v));
      *(LAS v2u*)(L + L_KVF + kvf_off) = o;
      if (ht >= 200) {
#pragma unroll
        for (int c = 1; c < 4; ++c) *(LAS v2u*)(L + L_KVF + kvf_off + c * 64) = o;
      }
    }
    DRAIN();
    STAMP(6);
    BAR();
    dma_image<70, 120, THR>(lds0 + L_W1, lane * 16, srd_ws, WS_T1 + c16 * 122880, hw, 6 * (bi >> 3));
    if (hw == 0) {
      v4u pv[16];
#pragma unroll
      for (int r = 0; r < 16; ++r) pv[r] = ld128(rs, sel(lane < 24, lane * 16), WS_CS + (c16 * 16 + r) * 384);
      f32x4 v = __builtin_bit_cast(f32x4, pv[0]);
#pragma unroll
      for (int r = 1; r < 16; ++r) v = add4(v, __builtin_bit_cast(f32x4, pv[r]));
      if (lane < 24) *(LAS f32x4*)(L + L_CS + lane * 16) = v;
    }
    STAMP(7);
    BAR();
    dma_image<0, 70, THR>(lds0 + L_W1, lane * 16, srd_ws, WS_T1 + c16 * 122880, hw, 8 * (bi >> 3));
    DRAIN();
    STAMP(8);
    BAR_RAW();
    dma_image<0, 30, THR>(lds0 + L_W2, lane * 16, srd_ws, WS_T2 + c16 * 122880, hw, 3 * (bi >> 3));
    {
      const __amdgpu_buffer_rsrc_t rout = __builtin_amdgcn_make_buffer_rsrc((void*)out, 0, S * D * 2, 0x00020000);
      const unsigned pw = __builtin_amdgcn_raw_buffer_load_b32(rout, sel(ht < 80, (b * 80 + ht) * 128), 0, AUX_SC1);
      asm volatile("" :: "v"(pw));
    }
    BAR_RAW();
    dma_image<30, 120, THR>(lds0 + L_W2, lane * 16, srd_ws, WS_T2 + c16 * 122880, hw, 11 * (bi >> 3));
    DRAIN();
    STAMP(9);
    BAR_RAW();
    return;
  }

  const __amdgpu_buffer_rsrc_t rx = __builtin_amdgcn_make_buffer_rsrc((void*)x, 0, S * D * 2, 0x00020000);
  const int grp32 = 4 * r128 + w;
  constexpr int AW = 6;
  unsigned tag;

  {
    const int tok = 8 * b + (lane & 7), cl = lane >> 3;
    const bool has3 = w < 2;
    v4u xv[3];
#pragma unroll
    for (int j = 0; j < 3; ++j) xv[j] = __builtin_amdgcn_raw_buffer_load_b128(rx, (j < 2 || has3) ? tok * 1280 + (w + 4 * j) * 128 + cl * 16 : OOB, 0, 0);
    tag = __hip_atomic_load((gu32*)(ws + WS_EPOCH), RLX_AGENT) + 1u;
    SCHED();
    int xd = ((tok >> 4) * 20 + 2 * w + (cl >> 2)) * 1024 + ((cl & 3) * 16 + (tok & 15)) * 16;
    PIN(xd);
    SCHED();
    f32x2 f[12];
#pragma unroll
    for (int j = 0; j < 3; ++j)
#pragma unroll
      for (int e = 0; e < 4; ++e) f[j * 4 + e] = bf2(xv[j][e]);
    f32x2 s2 = (f[0] + f[1]) + (f[2] + f[3]) + ((f[4] + f[5]) + (f[6] + f[7])) + ((f[8] + f[9]) + (f[10] + f[11]));
    const float mloc = xsum32(xsum16(xsum8(s2[0] + s2[1]))) * (has3 ? 1.f / 192.f : 1.f / 128.f);
    const float m3 = has3 ? mloc : 0.f;
    f32x2 q2 = {0.f, 0.f}, q3 = {0.f, 0.f};
#pragma unroll
    for (int j = 0; j < 12; j += 2) {
      const f32x2 d0 = f[j] - (j < 8 ? mloc : m3), d1 = f[j + 1] - (j < 8 ? mloc : m3);
      q2 += d0 * d0; q3 += d1 * d1;
    }
    q2 += q3;
    const float m2loc = xsum32(xsum16(xsum8(q2[0] + q2[1])));
    if (lane < 8) *(LAS f32x2*)(L + L_CS + 512 + (lane * 4 + w) * 8) = f32x2{mloc, m2loc};
    STAMP(25);
    BAR();
    const f32x4 p01 = *(LAS f32x4*)(L + L_CS + 512 + (lane & 7) * 32), p23 = *(LAS f32x4*)(L + L_CS + 512 + (lane & 7) * 32 + 16);
    const float mean = (p01[0] + p01[2]) * 0.3f + (p23[0] + p23[2]) * 0.2f;
    const float e0 = p01[0] - mean, e1 = p01[2] - mean, e2 = p23[0] - mean, e3 = p23[2] - mean;
    const float m2 = (p01[1] + p01[3]) + (p23[1] + p23[3]) + 192.f * (e0 * e0 + e1 * e1) + 128.f * (e2 * e2 + e3 * e3);
    const float rstd = __builtin_amdgcn_rsqf(m2 * (1.f / D) + LN_EPS);
#pragma unroll
    for (int j = 0; j < 3; ++j) {
      v4u o;
#pragma unroll
      for (int e = 0; e < 4; ++e) o[e] = pk2((f[j * 4 + e] - mean) * rstd);
      st128(o, rs, (j < 2 || has3) ? xd : OOB, WS_XN + j * 8192);
    }
    STAMP(26);
    DRAIN();
    BAR_RAW();
    if (tid < 2) flag_set(tid == 0 ? FLAG(0, b >> 2) + (b & 3) : FLAG(6, c16) + r128, tag);
  }
  STAMP(1);

  {
    wait_flag(FLAG(0, grp32) + lane, 4, tag, lane);
    STAMP(2);
    v4u a[20][2];
    const int abase = WS_XN + 2 * grp32 * 20 * 1024;
#pragma unroll
    for (int ks = 0; ks < AW; ++ks)
#pragma unroll
      for (int m = 0; m < 2; ++m) a[ks][m] = ld128(rs, lane * 16, abase + (m * 20 + ks) * 1024);
    SCHED();
    BAR_RAW();
    STAMP(27);
    {
      f32x4 acc[2][5];
      const LBase bp(lds0 + L_BKV + (lane >> 2) * 160 + (lane & 3) * 8);
      kloop<2, 5, 20, 1, false>(acc, [&](int ks, int s, int h) { return bp.rd(ks * 5120 + h * 2560 + s * 32); },
        [&](int ks, int m) { return a[ks][m]; },
        [&](int ks) {
          if (ks + AW < 20) {
#pragma unroll
            for (int m = 0; m < 2; ++m) a[ks + AW][m] = ld128(rs, lane * 16, abase + (m * 20 + ks + AW) * 1024);
          }
          if (ks == 3 || ks == 10) BAR_RAW();
        });
      STAMP(3);
      v4u kf[5];
#pragma unroll
      for (int s = 0; s < 5; ++s)
#pragma unroll
        for (int m = 0; m < 2; ++m) {
          f32x2 u0 = lo2(acc[m][s]), u1 = hi2(acc[m][s]);
          if (s < 2) { u0 = phi2(u0); u1 = phi2(u1); }
          if (s == 2) { const f32x2 p0 = phi2(u0), p1 = phi2(u1); u0 = t < 8 ? p0 : u0; u1 = t < 8 ? p1 : u1; }
          kf[s][2 * m] = pk2(u0); kf[s][2 * m + 1] = pk2(u1);
        }
      const v4u ones = {0x3F803F80u, 0x3F803F80u, 0x3F803F80u, 0x3F803F80u};
      const f32x4 z4 = {0.f, 0.f, 0.f, 0.f};
      f32x4 T[10];
      T[0] = mfma(kf[0], kf[2], z4); T[1] = mfma(kf[0], kf[3], z4); T[2] = mfma(kf[1], kf[2], z4); T[3] = mfma(kf[1], kf[3], z4);
      T[4] = mfma(kf[1], kf[4], z4); T[5] = mfma(kf[2], kf[3], z4); T[6] = mfma(kf[2], kf[4], z4);
      T[7] = mfma(kf[0], ones, z4); T[8] = mfma(kf[1], ones, z4); T[9] = mfma(kf[2], ones, z4);
      STAMP(28);
      LAS f32x4* red = (LAS f32x4*)(L + L_RED);
#pragma unroll
      for (int i = 0; i < 10; ++i) red[(w * 10 + i) * 64 + lane] = T[i];
      BAR();
    }
    STAMP(4);
    f32x4 qa[2][3];
    {
      unsigned bp0 = lds0 + L_BQ + (lane >> 2) * 64 + (lane & 3) * 8 + (g & 1) * 32;
      unsigned bp1 = lds0 + L_BQ + (lane >> 2) * 64 + (lane & 3) * 8 + 32 - (g & 1) * 32;
      unsigned bp2 = lds0 + L_BQ + 2048 + (lane >> 2) * 16 + (lane & 1) * 8;
      PIN(bp0); PIN(bp1); PIN(bp2);
      kloop<2, 3, 20, 2, true>(qa,
        [&](int ks, int s, int h) {
          return s == 0 ? rdtr((LAS char*)bp0 + ks * 2560 + h * 1024) : s == 1 ? rdtr((LAS char*)bp1 + ks * 2560 + h * 1024) : rdtr((LAS char*)bp2 + ks * 2560 + h * 256);
        },
        [&](int ks, int m) { return a[ks][m]; },
        [&](int ks) {});
    }
    v4u pq0[2], pq1[2];
#pragma unroll
    for (int m = 0; m < 2; ++m) {
      pq0[m].x = pk2(phi2(lo2(qa[m][0]))); pq0[m].y = pk2(phi2(hi2(qa[m][0])));
      pq0[m].z = pk2(phi2(lo2(qa[m][1]))); pq0[m].w = pk2(phi2(hi2(qa[m][1])));
      const unsigned lo = pk2(phi2(lo2(qa[m][2]))), hi = pk2(phi2(hi2(qa[m][2])));
      pq1[m].x = g < 2 ? lo : 0u; pq1[m].y = g < 2 ? hi : 0u; pq1[m].z = 0u; pq1[m].w = 0u;
      PIN(pq0[m]); PIN(pq1[m]);
    }
    STAMP(5);
    BAR_RAW();
    STAMP(17);
    {
      v4u kv[4][2];
#pragma unroll
      for (int es = 3; es >= 0; --es)
#pragma unroll
        for (int ks = 0; ks < 2; ++ks) kv[es][ks] = *(LAS v4u*)(L + L_KVF + (es * 2 + ks) * 1024 + lane * 16);
      SCHED();
      f32x4 o[2][4];
#pragma unroll
      for (int es = 3; es >= 0; --es)
#pragma unroll
        for (int m = 0; m < 2; ++m) o[m][es] = mfma(kv[es][0], pq0[m], f32x4{0.f, 0.f, 0.f, 0.f});
#pragma unroll
      for (int es = 3; es >= 0; --es)
#pragma unroll
        for (int m = 0; m < 2; ++m) o[m][es] = mfma(kv[es][1], pq1[m], o[m][es]);
#pragma unroll
      for (int m = 0; m < 2; ++m) {
        const float i0 = __builtin_amdgcn_rcpf(o[m][3][0] + 1e-6f), i1 = __builtin_amdgcn_rcpf(o[m][3][1] + 1e-6f);
        const float im = g == 0 ? i0 : i1;
        v4u ov;
        ov.x = pk2(lo2(o[m][0]) * i0); ov.y = pk2(hi2(o[m][0]) * i0);
        ov.z = pk2(lo2(o[m][1]) * im); ov.w = pk2(hi2(o[m][1]) * im);
        v2u ol; ol.x = pk2(lo2(o[m][2]) * i1); ol.y = pk2(hi2(o[m][2]) * i1);
        const int tt = 2 * grp32 + m;
        st128(ov, rs, lane * 16, WS_O + (tt * 20 + c16) * 1024);
        st64(ol, rs, g < 2 ? lane * 8 : OOB, WS_O + (tt * 20 + 16) * 1024 + c16 * 256);
      }
      STAMP(18);
      publish(FLAG(2, grp32) + c16, tag, lane);
    }
    STAMP(7);
  }

  {
    const int tt = 4 * r64 + w, tok = tt * 16 + t;
    wait_flag(FLAG(2, tt >> 1) + lane, 16, tag, lane);
    STAMP(8);
    if (b == 0 && tid == 0) __hip_atomic_store((gu32*)(ws + WS_EPOCH), tag, RLX_AGENT);
    v4u a[20];
    v2u xr[5];
#pragma unroll
    for (int ks = 0; ks < 2 * AW; ++ks) a[ks] = ld128(rs, lane * 16, WS_O + (tt * 20 + ks) * 1024);
    SCHED();
    f32x4 acc[1][5];
    const LBase bp(lds0 + L_WO + (lane >> 2) * 160 + (lane & 3) * 8);
    kloop<1, 5, 20, 1, true>(acc, [&](int ks, int s, int h) { return bp.rd(ks * 5120 + h * 2560 + s * 32); },
      [&](int ks, int m) { return a[ks]; },
      [&](int ks) {
        if (ks + 2 * AW < 16) a[ks + 2 * AW] = ld128(rs, lane * 16, WS_O + (tt * 20 + ks + 2 * AW) * 1024);
        else if (ks + 2 * AW < 20) a[ks + 2 * AW] = ld_split(rs, lane, WS_O + (tt * 20 + ks + 2 * AW) * 1024);
        if (ks >= 8 && ks < 13) xr[ks - 8] = __builtin_amdgcn_raw_buffer_load_b64(rx, tok * 1280 + (80 * c8 + 16 * (ks - 8) + 4 * g) * 2, 0, 0);
      });
    BAR_RAW();
    STAMP(9);
    v2u hb[5];
    f32x2 hv[10];
#pragma unroll
    for (int s = 0; s < 5; ++s) {
      hb[s].x = pk2(lo2(acc[0][s]) + bf2(xr[s].x));
      hb[s].y = pk2(hi2(acc[0][s]) + bf2(xr[s].y));
      hv[2 * s] = bf2(hb[s].x); hv[2 * s + 1] = bf2(hb[s].y);
    }
    st128(v4u{hb[0].x, hb[0].y, hb[1].x, hb[1].y}, rs, lane * 16, WS_H + (tt * 20 + 2 * c8) * 1024);
    st128(v4u{hb[2].x, hb[2].y, hb[3].x, hb[3].y}, rs, lane * 16, WS_H + (tt * 20 + 2 * c8 + 1) * 1024);
    st64(hb[4], rs, lane * 8, WS_H + (tt * 20 + 16) * 1024 + c8 * 512);
    const f32x2 s2 = ((hv[0] + hv[1]) + (hv[2] + hv[3])) + ((hv[4] + hv[5]) + (hv[6] + hv[7])) + (hv[8] + hv[9]);
    const float mc = xsum32(xsum16(s2[0] + s2[1])) * (1.f / 80.f);
    f32x2 q2 = {0.f, 0.f}, q3 = {0.f, 0.f};
#pragma unroll
    for (int j = 0; j < 10; j += 2) { const f32x2 d0 = hv[j] - mc, d1 = hv[j + 1] - mc; q2 += d0 * d0; q3 += d1 * d1; }
    q2 += q3;
    const float m2 = xsum32(xsum16(q2[0] + q2[1]));
    st64(v2u{__builtin_bit_cast(unsigned, mc), __builtin_bit_cast(unsigned, m2)}, rs, g == 0 ? t * 8 : OOB, WS_ST + (tt * 8 + c8) * 128);
    STAMP(20);
    publish(FLAG(3, tt >> 1) + (tt & 1) * 8 + c8, tag, lane);
    STAMP(10);
  }

  {
    v2u bb[6];
#pragma unroll
    for (int s = 0; s < 6; ++s) bb[s] = *(const v2u*)(b1 + 96 * c16 + 16 * s + 4 * g);
    wait_flag(FLAG(3, grp32) + lane, 16, tag, lane);
    STAMP(11);
    v4u stt[2];
#pragma unroll
    for (int m = 0; m < 2; ++m) {
      const v2u s0 = ld64(rs, g * 256 + t * 8, WS_ST + (2 * grp32 + m) * 1024), s1 = ld64(rs, g * 256 + 128 + t * 8, WS_ST + (2 * grp32 + m) * 1024);
      stt[m] = v4u{s0.x, s0.y, s1.x, s1.y};
    }
    constexpr int WIN = AW + 1;
    v4u a[WIN][2];
    const int abase = WS_H + 2 * grp32 * 20 * 1024;
#pragma unroll
    for (int ks = 0; ks < AW; ++ks)
#pragma unroll
      for (int m = 0; m < 2; ++m) a[ks][m] = ld128(rs, lane * 16, abase + (m * 20 + ks) * 1024);
    SCHED();
    float nm[2], rr[2];
#pragma unroll
    for (int m = 0; m < 2; ++m) {
      const f32x4 sv = __builtin_bit_cast(f32x4, stt[m]);
      const float mean = xsum32(xsum16(sv[0] + sv[2])) * 0.125f;
      const float m2 = xsum32(xsum16(sv[1] + sv[3] + 80.f * ((sv[0] - mean) * (sv[0] - mean) + (sv[2] - mean) * (sv[2] - mean))));
      const float var = m2 * (1.f / D);
      const float r1 = __builtin_amdgcn_rsqf(var + LN_EPS);
      rr[m] = r1 * __builtin_amdgcn_rsqf(var * r1 * r1 + LN_EPS);
      nm[m] = -mean * rr[m];
      PIN(rr[m]); PIN(nm[m]);
    }
    BAR_RAW();
    STAMP(22);
    const LBase bp0(lds0 + L_W1 + (lane >> 2) * 192 + (lane & 3) * 8 + (g & 1) * 32);
    const LBase bp1(lds0 + L_W1 + (lane >> 2) * 192 + (lane & 3) * 8 - (g & 1) * 32);
    f32x4 acc[2][6];
    f32x4 cs[6];
    kloop<2, 6, 20, 1, true>(acc, [&](int ks, int s, int h) { return ((s & 1) ? bp1 : bp0).rd(ks * 6144 + h * 3072 + s * 32); },
      [&](int ks, int m) { return a[ks % WIN][m]; },
      [&](int ks) {
        if (ks + AW < 20) {
#pragma unroll
          for (int m = 0; m < 2; ++m)
            a[(ks + AW) % WIN][m] = ks + AW < 16 ? ld128(rs, lane * 16, abase + (m * 20 + ks + AW) * 1024) : ld_split(rs, lane, abase + (m * 20 + ks + AW) * 1024);
        }
        if (ks == 19) {
#pragma unroll
          for (int s = 0; s < 6; ++s) cs[s] = *(LAS f32x4*)(L + L_CS + (s * 4 + g) * 16);
        }
      });
    BAR_RAW();
    STAMP(12);
#pragma unroll
    for (int m = 0; m < 2; ++m) {
      v2u hb[6];
#pragma unroll
      for (int s = 0; s < 6; ++s) {
        const f32x2 p0 = lo2(acc[m][s]) * rr[m] + (lo2(cs[s]) * nm[m] + bf2(bb[s].x));
        const f32x2 p1 = hi2(acc[m][s]) * rr[m] + (hi2(cs[s]) * nm[m] + bf2(bb[s].y));
        hb[s].x = pk2(gelu2(p0)); hb[s].y = pk2(gelu2(p1));
        if (s & 1)
          st128(v4u{hb[s - 1].x, hb[s - 1].y, hb[s].x, hb[s].y}, rs, lane * 16, WS_H1 + ((2 * grp32 + m) * 48 + 3 * c16 + (s >> 1)) * 1024);
      }
    }
    STAMP(24);
    publish(FLAG(4, grp32) + c16, tag, lane);
    STAMP(13);
  }

  {
    v2u hr[2][3], bb[3];
#pragma unroll
    for (int s = 0; s < 3; ++s) {
      int n = 40 * c16 + 16 * s + 4 * g;
      if (s == 2 && g >= 2) n = 40 * c16;
      bb[s] = *(const v2u*)(b2 + n);
      const int cc = n / 80, off = n % 80, ss = off >> 4, gg = (off >> 2) & 3;
      const int ho = ss < 4 ? (2 * cc + (ss >> 1)) * 1024 + (gg * 16 + t) * 16 + (ss & 1) * 8 : 16 * 1024 + cc * 512 + (gg * 16 + t) * 8;
#pragma unroll
      for (int m = 0; m < 2; ++m) hr[m][s] = ld64(rs, ho, WS_H + (2 * grp32 + m) * 20 * 1024);
    }
    wait_flag(FLAG(4, grp32) + lane, 16, tag, lane);
    STAMP(14);
    constexpr int WIN = 8;
    v4u a[WIN][2];
    const int abase = WS_H1 + 2 * grp32 * 48 * 1024;
#pragma unroll
    for (int ks = 0; ks < WIN - 1; ++ks)
#pragma unroll
      for (int m = 0; m < 2; ++m) a[ks][m] = ld128(rs, lane * 16, abase + (m * 48 + ks) * 1024);
    SCHED();
    BAR_RAW();
    f32x4 acc[2][3];
    const LBase bp0(lds0 + L_W2 + (lane >> 2) * 64 + (lane & 3) * 8 + (g & 1) * 32);
    const LBase bp1(lds0 + L_W2 + (lane >> 2) * 64 + (lane & 3) * 8 + 32 - (g & 1) * 32);
    const LBase bp2(lds0 + L_W2 + 2048 + (lane >> 2) * 16 + (lane & 1) * 8);
    kloop<2, 3, 48, 2, true>(acc,
      [&](int ks, int s, int h) { return s == 0 ? bp0.rd(ks * 2560 + h * 1024) : s == 1 ? bp1.rd(ks * 2560 + h * 1024) : bp2.rd(ks * 2560 + h * 256); },
      [&](int ks, int m) { return a[ks % WIN][m]; },
      [&](int ks) {
        if (ks + WIN - 1 < 48) {
#pragma unroll
          for (int m = 0; m < 2; ++m) a[(ks + WIN - 1) % WIN][m] = ld128(rs, lane * 16, abase + (m * 48 + ks + WIN - 1) * 1024);
        }
      });
    STAMP(15);
    const __amdgpu_buffer_rsrc_t rout = __builtin_amdgcn_make_buffer_rsrc((void*)out, 0, S * D * 2, 0x00020000);
#pragma unroll
    for (int m = 0; m < 2; ++m) {
      const int tok = (2 * grp32 + m) * 16 + t;
#pragma unroll
      for (int s = 0; s < 3; ++s) {
        v2u o;
        o.x = pk2(lo2(acc[m][s]) + bf2(bb[s].x) + bf2(hr[m][s].x));
        o.y = pk2(hi2(acc[m][s]) + bf2(bb[s].y) + bf2(hr[m][s].y));
        __builtin_amdgcn_raw_buffer_store_b64(o, rout, (s < 2 || g < 2) ? (tok * D + 40 * c16 + 16 * s + 4 * g) * 2 : OOB, 0, OUTAUX);
      }
    }
    STAMP(16);
  }
}
}

extern "C" void kernel_launch(void* const* d_in, const int* in_sizes, int n_in, void* d_out, int out_size, void* d_ws,
                              size_t ws_size, hipStream_t stream) {
  const unsigned short* x = (const unsigned short*)d_in[0];
  const unsigned short* wqkv = (const unsigned short*)d_in[1];
  const unsigned short* wo = (const unsigned short*)d_in[2];
  const unsigned short* w1 = (const unsigned short*)d_in[4];
  const unsigned short* b1 = (const unsigned short*)d_in[5];
  const unsigned short* w2 = (const unsigned short*)d_in[6];
  const unsigned short* b2 = (const unsigned short*)d_in[7];
  fused_block<<<256, 512, 0, stream>>>(x, wqkv, wo, w1, b1, w2, b2, (unsigned short*)d_out, (unsigned char*)d_ws);
}
```

```cpp
#include <hip/hip_runtime.h>
#include <hip/hip_bf16.h>

#define STAMPS 0
#define THR 0
#define TOUCH 0
#define CODEPF 1
#define FLAGPF 0
#define OUTAUX 0
#define OUTPF 0
#define S0 6
#define XRPOS 0
#define QPOS 0
#define PUBA 8
#define NCDELAY 0
#define HPRIO 0
#define WOLATE 0
#define SLEEP 1

#define GAS __attribute__((address_space(1)))
#define LAS __attribute__((address_space(3)))
#define RLX_AGENT __ATOMIC_RELAXED, __HIP_MEMORY_SCOPE_AGENT
typedef unsigned v4u __attribute__((ext_vector_type(4)));
typedef unsigned v2u __attribute__((ext_vector_type(2)));
typedef float f32x4 __attribute__((ext_vector_type(4)));
typedef float f32x2 __attribute__((ext_vector_type(2)));
typedef __bf16 bf16x8 __attribute__((ext_vector_type(8)));
typedef __bf16 bf16x2 __attribute__((ext_vector_type(2)));
typedef short s16x4 __attribute__((ext_vector_type(4)));
typedef GAS unsigned gu32;

namespace {
constexpr int S = 2048, D = 640, F = 1536;
constexpr float LN_EPS = 1e-5f;
constexpr int AUX_SC1 = 16;
constexpr unsigned kSpinCap = 1u << 17;
constexpr int OOB = 0x7FFFFF00;

constexpr int WS_EPOCH = 0;
constexpr int WS_FLAG = 4096;
constexpr int FLAG_SET = 64 * 256;
constexpr int WS_XN = 1 << 20;
constexpr int WS_O = WS_XN + 128 * 20 * 1024;
constexpr int WS_H = WS_O + 128 * 20 * 1024;
constexpr int WS_H1 = WS_H + 128 * 20 * 1024;
constexpr int WS_KVP = WS_H1 + 128 * 48 * 1024;
constexpr int WS_ST = WS_KVP + 16 * 16 * 4096;
constexpr int WS_DBG = WS_ST + 2048 * 64;
constexpr int WS_TKV = WS_DBG + 256 * 8 * 32 * 8;
constexpr int WS_TO = WS_TKV + 16 * 153600;
constexpr int WS_T1 = WS_TO + 8 * 102400;
constexpr int WS_T2 = WS_T1 + 16 * 122880;
constexpr int WS_CS = WS_T2 + 16 * 122880;
constexpr int WS_BYTES = WS_CS + 16 * 16 * 384;

constexpr int L_BKV = 0, L_BQ = 102400;
constexpr int L_RED = 0, L_KVF = 153600;
constexpr int L_WO = 0, L_W1 = 30720, L_W2 = 0, L_CS = 161792;
constexpr int L_B1 = L_CS + 1152, L_B2 = L_CS + 1536;
constexpr int LDS_BYTES = 163840;

__device__ __forceinline__ f32x4 mfma(v4u a, v4u b, f32x4 c) {
  return __builtin_amdgcn_mfma_f32_16x16x32_bf16(__builtin_bit_cast(bf16x8, a), __builtin_bit_cast(bf16x8, b), c, 0, 0, 0);
}
__device__ __forceinline__ unsigned pk2(f32x2 v) { return __builtin_bit_cast(unsigned, __builtin_convertvector(v, bf16x2)); }
__device__ __forceinline__ unsigned pk2(float lo, float hi) { return pk2(f32x2{lo, hi}); }
__device__ __forceinline__ float bf_lo(unsigned u) { return __builtin_bit_cast(float, u << 16); }
__device__ __forceinline__ float bf_hi(unsigned u) { return __builtin_bit_cast(float, u & 0xFFFF0000u); }
__device__ __forceinline__ f32x2 bf2(unsigned u) { return f32x2{bf_lo(u), bf_hi(u)}; }
__device__ __forceinline__ f32x2 lo2(f32x4 v) { return f32x2{v[0], v[1]}; }
__device__ __forceinline__ f32x2 hi2(f32x4 v) { return f32x2{v[2], v[3]}; }
__device__ __forceinline__ v4u mk_srd(const void* p, unsigned bytes) {
  const unsigned long long a = (unsigned long long)p;
  v4u r;
  r.x = (unsigned)a; r.y = (unsigned)(a >> 32) & 0xFFFFu; r.z = bytes; r.w = 0x00020000u;
  return r;
}
#define DRAIN() asm volatile("s_waitcnt vmcnt(0)" ::: "memory")
#define BAR() asm volatile("s_waitcnt lgkmcnt(0)\n\ts_barrier" ::: "memory")
#define BAR_RAW() asm volatile("s_barrier" ::: "memory")
#define SCHED() __builtin_amdgcn_sched_barrier(0)
#define PIN(x) asm volatile("" : "+v"(x))

__device__ __forceinline__ void dma2(unsigned lds_addr, unsigned voff, v4u srd, unsigned soff) {
  unsigned keep;
  asm volatile("s_mov_b32 %0, m0\n\ts_mov_b32 m0, %1\n\ts_nop 0\n\t"
               "buffer_load_dwordx4 %2, %3, %4 offen sc1 lds\n\t"
               "buffer_load_dwordx4 %2, %3, %4 offen offset:1024 sc1 lds\n\t"
               "s_mov_b32 m0, %0"
               : "=&s"(keep) : "s"(lds_addr), "v"(voff), "s"(srd), "s"(soff) : "memory");
}
template <int LO, int HI, int TH = 0, int NW = 4>
__device__ __forceinline__ void dma_image(unsigned lds_base, unsigned voff, v4u srd, int ws_off, int hw, int rot) {
  constexpr int NP = (HI - LO) / 2;
#pragma unroll 1
  for (int i = hw; i < NP; i += NW) {
    int j = i + rot;
    if (j >= NP) j -= NP;
    dma2(lds_base + LO * 1024 + j * 2048, voff, srd, ws_off + LO * 1024 + j * 2048);
    if (TH) asm volatile("s_waitcnt vmcnt(%0)" :: "i"(TH - 2) : "memory");
  }
}
__device__ __forceinline__ v2u rdtr(LAS char* p) { return __builtin_bit_cast(v2u, __builtin_amdgcn_ds_read_tr16_b64_v4i16((LAS s16x4*)p)); }
struct LBase {
  unsigned a[3];
  __device__ __forceinline__ explicit LBase(unsigned v) { a[0] = v; a[1] = v + 65536; a[2] = v + 131072; PIN(a[0]); PIN(a[1]); PIN(a[2]); }
  __device__ __forceinline__ v2u rd(int off) const { return rdtr((LAS char*)(a[off >> 16]) + (off & 65535)); }
};
template <int MT, int NT, int KS, int PD, bool WA, class RB, class GA, class PRE>
__device__ __forceinline__ void kloop(f32x4 (&acc)[MT][NT], RB rb, GA ga, PRE pre) {
  v2u bl[PD + 1][NT], bh[PD + 1][NT];
#pragma unroll
  for (int m = 0; m < MT; ++m)
#pragma unroll
    for (int s = 0; s < NT; ++s) acc[m][s] = f32x4{0.f, 0.f, 0.f, 0.f};
#pragma unroll
  for (int ks = 0; ks < PD; ++ks)
#pragma unroll
    for (int s = 0; s < NT; ++s) { bl[ks][s] = rb(ks, s, 0); bh[ks][s] = rb(ks, s, 1); }
  SCHED();
#pragma unroll
  for (int ks = 0; ks < KS; ++ks) {
    pre(ks);
#pragma unroll
    for (int s = 0; s < NT; ++s)
#pragma unroll
      for (int m = 0; m < MT; ++m) {
        const v2u lo = bl[ks % (PD + 1)][s], hi = bh[ks % (PD + 1)][s];
        const v4u wv = {lo.x, lo.y, hi.x, hi.y};
        acc[m][s] = WA ? mfma(wv, ga(ks, m), acc[m][s]) : mfma(ga(ks, m), wv, acc[m][s]);
        if (ks + PD < KS) {
          if (MT == 1 || m == 0) bl[(ks + PD) % (PD + 1)][s] = rb(ks + PD, s, 0);
          if (MT == 1 || m == 1) bh[(ks + PD) % (PD + 1)][s] = rb(ks + PD, s, 1);
        }
        SCHED();
      }
  }
}
constexpr int ld_hi(int ks, int s0, int W, int KS) {
  const int a = s0 + 2 * (ks + 1), b = ks + 1 + W, c = a < b ? a : b;
  return c < KS ? c : KS;
}
__device__ __forceinline__ v4u ld128(__amdgpu_buffer_rsrc_t rs, int voff, int soff) {
  return __builtin_amdgcn_raw_buffer_load_b128(rs, voff, soff, AUX_SC1);
}
__device__ __forceinline__ v2u ld64(__amdgpu_buffer_rsrc_t rs, int voff, int soff) {
  return __builtin_amdgcn_raw_buffer_load_b64(rs, voff, soff, AUX_SC1);
}
__device__ __forceinline__ void st128(v4u v, __amdgpu_buffer_rsrc_t rs, int voff, int soff) {
  __builtin_amdgcn_raw_buffer_store_b128(v, rs, (int)((unsigned)voff + (unsigned)soff), 0, AUX_SC1);
}
__device__ __forceinline__ int sel(bool c, int a) {
  PIN(a);
  return c ? a : OOB;
}
__device__ __forceinline__ v4u ld_split(__amdgpu_buffer_rsrc_t rs, int lane, int soff) {
  const v2u a = ld64(rs, lane * 8, soff), b = ld64(rs, lane * 8 + 512, soff);
  return v4u{a.x, a.y, b.x, b.y};
}
__device__ __forceinline__ void st64(v2u v, __amdgpu_buffer_rsrc_t rs, int voff, int soff) {
  __builtin_amdgcn_raw_buffer_store_b64(v, rs, voff, soff, AUX_SC1);
}
__device__ __forceinline__ void flag_set(gu32* p, unsigned tag) {
  asm volatile("global_atomic_swap %0, %1, off sc1" :: "v"(p), "v"(tag) : "memory");
}
__device__ __forceinline__ void wait_flag(gu32* p, int n, unsigned tag, int lane) {
#pragma unroll 1
  for (unsigned spins = 0; spins < kSpinCap; ++spins) {
    unsigned v = tag;
    if (lane < n) asm volatile("global_atomic_add %0, %1, %2, off sc0 sc1\n\ts_waitcnt vmcnt(0)" : "=&v"(v) : "v"(p), "v"(0u) : "memory");
    if (__all(v == tag)) break;
    if (SLEEP) __builtin_amdgcn_s_sleep(SLEEP);
  }
  asm volatile("" ::: "memory");
}
__device__ __forceinline__ void sig_wait(volatile LAS unsigned* p) {
#pragma unroll 1
  for (unsigned spins = 0; spins < 64 * kSpinCap; ++spins) {
    if (*p != 0u) break;
    __builtin_amdgcn_s_sleep(1);
  }
  asm volatile("" ::: "memory");
}
__device__ __forceinline__ void prog_wait(volatile LAS v4u* p, unsigned stage) {
#pragma unroll 1
  for (unsigned spins = 0; spins < 64 * kSpinCap; ++spins) {
    const v4u v = *p;
    if (min(min(v.x, v.y), min(v.z, v.w)) >= stage) break;
    __builtin_amdgcn_s_sleep(1);
  }
  asm volatile("" ::: "memory");
}
__device__ __forceinline__ void publish(gu32* flag, unsigned tag, int lane) {
  DRAIN();
  if (lane == 0) flag_set(flag, tag);
}
__device__ __forceinline__ f32x4 add4(f32x4 a, f32x4 b) {
  f32x4 r;
#pragma unroll
  for (int i = 0; i < 4; ++i) { float x; asm("v_add_f32 %0, %1, %2" : "=v"(x) : "v"(a[i]), "v"(b[i])); r[i] = x; }
  return r;
}
__device__ __forceinline__ float xsum32(float x) {
  unsigned u = __builtin_bit_cast(unsigned, x), v = u;
  asm("" : "+v"(v));
  auto r = __builtin_amdgcn_permlane32_swap(u, v, false, false);
  const unsigned a = r[0], b = r[1];
  return __builtin_bit_cast(float, a) + __builtin_bit_cast(float, b);
}
__device__ __forceinline__ float xsum16(float x) {
  unsigned u = __builtin_bit_cast(unsigned, x), v = u;
  asm("" : "+v"(v));
  auto r = __builtin_amdgcn_permlane16_swap(u, v, false, false);
  const unsigned a = r[0], b = r[1];
  return __builtin_bit_cast(float, a) + __builtin_bit_cast(float, b);
}
__device__ __forceinline__ float xsum8(float x) {
  return x + __builtin_bit_cast(float, __builtin_amdgcn_update_dpp(0, __builtin_bit_cast(int, x), 0x128, 0xf, 0xf, false));
}
__device__ __forceinline__ f32x2 phi2(f32x2 t) {
  const f32x2 u = t * 1.4426950408889634f;
  const f32x2 e = {__builtin_amdgcn_exp2f(fminf(u[0], 0.f)), __builtin_amdgcn_exp2f(fminf(u[1], 0.f))};
  const f32x2 p = {fmaxf(t[0], 0.f), fmaxf(t[1], 0.f)};
  return (p + 1e-6f) + e;
}
__device__ __forceinline__ f32x2 gelu2(f32x2 x) {
  const f32x2 u = x * (x * x * -0.1029432397f + -2.302208198f);
  const f32x2 d = f32x2{__builtin_amdgcn_exp2f(u[0]), __builtin_amdgcn_exp2f(u[1])} + 1.f;
  return x * f32x2{__builtin_amdgcn_rcpf(d[0]), __builtin_amdgcn_rcpf(d[1])};
}

#define STAMP(i) do { } while (0)

__global__ void __launch_bounds__(512, 2)
fused_block(const unsigned short* __restrict__ x, const unsigned short* __restrict__ wqkv, const unsigned short* __restrict__ wo,
            const unsigned short* __restrict__ w1, const unsigned short* __restrict__ b1, const unsigned short* __restrict__ w2,
            const unsigned short* __restrict__ b2, unsigned short* __restrict__ out, unsigned char* ws) {
  __shared__ __attribute__((aligned(16))) char smem[LDS_BYTES];
  unsigned long long pc0;
  asm volatile("s_getpc_b64 %0" : "=s"(pc0));
  LAS char* L = (LAS char*)smem;
  const unsigned lds0 = (unsigned)(size_t)L;
  const int tid = threadIdx.x, lane = tid & 63, w = __builtin_amdgcn_readfirstlane(tid >> 6);
  const int b = blockIdx.x, xcd = b & 7, bi = b >> 3;
  const int g = lane >> 4, t = lane & 15;
  const __amdgpu_buffer_rsrc_t rs = __builtin_amdgcn_make_buffer_rsrc((void*)ws, 0, WS_BYTES, 0x00020000);
  gu32* flags = (gu32*)(ws + WS_FLAG);
#define FLAG(set, grp) (flags + ((set) * FLAG_SET + (grp) * 256) / 4)
  asm volatile("" :: "s"(x), "s"(wqkv), "s"(wo), "s"(w1), "s"(b1), "s"(w2), "s"(b2), "s"(out), "s"(ws));
  STAMP(0);
  const int r128 = 4 * (xcd >> 1) + (bi >> 3), c16 = 8 * (xcd & 1) + (bi & 7);
  const int r64 = 8 * (xcd >> 1) + (bi >> 2), c8 = 4 * (xcd & 1) + (bi & 3);

  const __amdgpu_buffer_rsrc_t rx = __builtin_amdgcn_make_buffer_rsrc((void*)x, 0, S * D * 2, 0x00020000);
  const int hw = w - 4, ht = tid - 256;
  volatile LAS unsigned* sig = (volatile LAS unsigned*)(L + L_CS + 1024);
  volatile LAS v4u* prog = (volatile LAS v4u*)(L + L_CS + 1088);
#define PROG(n) do { asm volatile("" ::: "memory"); sig[16 + w] = (n); } while (0)

  unsigned tag = __hip_atomic_load((gu32*)(ws + WS_EPOCH), RLX_AGENT);
  const __amdgpu_buffer_rsrc_t rq = __builtin_amdgcn_make_buffer_rsrc((void*)wqkv, 0, D * 3 * D * 2, 0x00020000);
  v4u tv[12];
  const int q10 = lane / 10, c10 = lane % 10;
  if (w < 4) {
#pragma unroll
    for (int j = 0; j < 3; ++j)
      tv[j] = __builtin_amdgcn_raw_buffer_load_b128(rx, (lane & 7) * 1280 + (lane >> 3) * 16, (j < 2 || w < 2) ? 8 * b * 1280 + (w + 4 * j) * 128 : OOB, 0);
  } else if (w == 4) {
    const int vo = sel(lane < 40, q10 * 3840 + (c10 < 5 ? 1280 : 2480) + c10 * 16);
#pragma unroll
    for (int j = 0; j < 10; ++j) tv[j] = __builtin_amdgcn_raw_buffer_load_b128(rq, vo, (40 * r128 + 4 * j) * 3840 + c16 * 80, 0);
  } else if (w == 7) {
    *(LAS v4u*)(L + L_CS + 512 + lane * 16) = v4u{0u, 0u, 0u, 0u};
  }
  BAR();

  if (w < 4) {
  const int grp32 = 4 * r128 + w;
  constexpr int AW = 6;

  {
    const int tok = 8 * b + (lane & 7), cl = lane >> 3;
    const bool has3 = w < 2;
    int xd = ((tok >> 4) * 20 + 2 * w + (cl >> 2)) * 1024 + ((cl & 3) * 16 + (tok & 15)) * 16;
    PIN(xd);
    SCHED();
    f32x2 f[12];
#pragma unroll
    for (int j = 0; j < 3; ++j)
#pragma unroll
      for (int e = 0; e < 4; ++e) f[j * 4 + e] = bf2(tv[j][e]);
    f32x2 s2 = (f[0] + f[1]) + (f[2] + f[3]) + ((f[4] + f[5]) + (f[6] + f[7])) + ((f[8] + f[9]) + (f[10] + f[11]));
    const float mloc = xsum32(xsum16(xsum8(s2[0] + s2[1]))) * (has3 ? 1.f / 192.f : 1.f / 128.f);
    const float m3 = has3 ? mloc : 0.f;
    f32x2 q2 = {0.f, 0.f}, q3 = {0.f, 0.f};
#pragma unroll
    for (int j = 0; j < 12; j += 2) {
      const f32x2 d0 = f[j] - (j < 8 ? mloc : m3), d1 = f[j + 1] - (j < 8 ? mloc : m3);
      q2 += d0 * d0; q3 += d1 * d1;
    }
    q2 += q3;
    const float m2loc = xsum32(xsum16(xsum8(q2[0] + q2[1])));
    if (lane < 8) *(volatile LAS f32x4*)(L + L_CS + 512 + (lane * 4 + w) * 16) = f32x4{mloc, m2loc, 1.f, 1.f};
    STAMP(25);
    f32x4 pr[4];
#pragma unroll 1
    for (unsigned spins = 0; spins < 64 * kSpinCap; ++spins) {
      bool ok = true;
#pragma unroll
      for (int ww = 0; ww < 4; ++ww) {
        pr[ww] = *(volatile LAS f32x4*)(L + L_CS + 512 + ((lane & 7) * 4 + ww) * 16);
        ok = ok && pr[ww][2] == 1.f;
      }
      if (__all(ok)) break;
    }
    const f32x4 p01 = {pr[0][0], pr[0][1], pr[1][0], pr[1][1]}, p23 = {pr[2][0], pr[2][1], pr[3][0], pr[3][1]};
    const float mean = (p01[0] + p01[2]) * 0.3f + (p23[0] + p23[2]) * 0.2f;
    const float e0 = p01[0] - mean, e1 = p01[2] - mean, e2 = p23[0] - mean, e3 = p23[2] - mean;
    const float m2 = (p01[1] + p01[3]) + (p23[1] + p23[3]) + 192.f * (e0 * e0 + e1 * e1) + 128.f * (e2 * e2 + e3 * e3);
    const float rstd = __builtin_amdgcn_rsqf(m2 * (1.f / D) + LN_EPS);
#pragma unroll
    for (int j = 0; j < 3; ++j) {
      v4u o;
#pragma unroll
      for (int e = 0; e < 4; ++e) o[e] = pk2((f[j * 4 + e] - mean) * rstd);
      st128(o, rs, (j < 2 || has3) ? xd : OOB, WS_XN + j * 8192);
    }
    STAMP(26);
    PIN(tag); tag += 1u;
    publish(FLAG(0, b >> 2) + (b & 3) * 4 + w, tag, lane);
  }

  STAMP(1);

  v2u xr[5];
  {
    wait_flag(FLAG(0, grp32) + lane, 16, tag, lane);
    STAMP(2);
    v4u a[20][2];
    const int abase = WS_XN + 2 * grp32 * 20 * 1024;
#pragma unroll
    for (int ks = 0; ks < S0; ++ks)
#pragma unroll
      for (int m = 0; m < 2; ++m) a[ks][m] = ld128(rs, lane * 16, abase + (m * 20 + ks) * 1024);
    SCHED();
    BAR_RAW();
    STAMP(27);
    {
      f32x4 acc[2][5];
      const LBase bp(lds0 + L_BKV + (lane >> 2) * 160 + (lane & 3) * 8);
      kloop<2, 5, 20, 1, false>(acc, [&](int ks, int s, int h) { return bp.rd(ks * 5120 + h * 2560 + s * 32); },
        [&](int ks, int m) { return a[ks][m]; },
        [&](int ks) {
#pragma unroll
          for (int j = ld_hi(ks - 1, S0, AW, 20); j < ld_hi(ks, S0, AW, 20); ++j)
#pragma unroll
            for (int m = 0; m < 2; ++m) a[j][m] = ld128(rs, lane * 16, abase + (m * 20 + j) * 1024);
          if (ks == 3 || ks == 10) BAR_RAW();
          if (ks == 20 - AW) PROG(1);
        });
      STAMP(3);
      v4u kf[5];
#pragma unroll
      for (int s = 0; s < 5; ++s)
#pragma unroll
        for (int m = 0; m < 2; ++m) {
          f32x2 u0 = lo2(acc[m][s]), u1 = hi2(acc[m][s]);
          if (s < 2) { u0 = phi2(u0); u1 = phi2(u1); }
          if (s == 2) { const f32x2 p0 = phi2(u0), p1 = phi2(u1); u0 = t < 8 ? p0 : u0; u1 = t < 8 ? p1 : u1; }
          kf[s][2 * m] = pk2(u0); kf[s][2 * m + 1] = pk2(u1);
        }
      const v4u ones = {0x3F803F80u, 0x3F803F80u, 0x3F803F80u, 0x3F803F80u};
      const f32x4 z4 = {0.f, 0.f, 0.f, 0.f};
      f32x4 T[10];
      T[0] = mfma(kf[0], kf[2], z4); T[1] = mfma(kf[0], kf[3], z4); T[2] = mfma(kf[1], kf[2], z4); T[3] = mfma(kf[1], kf[3], z4);
      T[4] = mfma(kf[1], kf[4], z4); T[5] = mfma(kf[2], kf[3], z4); T[6] = mfma(kf[2], kf[4], z4);
      T[7] = mfma(kf[0], ones, z4); T[8] = mfma(kf[1], ones, z4); T[9] = mfma(kf[2], ones, z4);
      STAMP(28);
      LAS f32x4* red = (LAS f32x4*)(L + L_RED);
#pragma unroll
      for (int i = 0; i < 10; ++i) red[(w * 10 + i) * 64 + lane] = T[i];
      BAR();
    }
    STAMP(4);
    f32x4 qa[2][3];
    {
      unsigned bp0 = lds0 + L_BQ + (lane >> 2) * 64 + (lane & 3) * 8 + (g & 1) * 32;
      unsigned bp1 = lds0 + L_BQ + (lane >> 2) * 64 + (lane & 3) * 8 + 32 - (g & 1) * 32;
      unsigned bp2 = lds0 + L_BQ + 2048 + (lane >> 2) * 16 + (lane & 1) * 8;
      PIN(bp0); PIN(bp1); PIN(bp2);
      kloop<2, 3, 20, 2, true>(qa,
        [&](int ks, int s, int h) {
          return s == 0 ? rdtr((LAS char*)bp0 + ks * 2560 + h * 1024) : s == 1 ? rdtr((LAS char*)bp1 + ks * 2560 + h * 1024) : rdtr((LAS char*)bp2 + ks * 2560 + h * 256);
        },
        [&](int ks, int m) { return a[ks][m]; },
        [&](int ks) {});
    }
    v4u pq0[2], pq1[2];
#pragma unroll
    for (int m = 0; m < 2; ++m) {
      pq0[m].x = pk2(phi2(lo2(qa[m][0]))); pq0[m].y = pk2(phi2(hi2(qa[m][0])));
      pq0[m].z = pk2(phi2(lo2(qa[m][1]))); pq0[m].w = pk2(phi2(hi2(qa[m][1])));
      const unsigned lo = pk2(phi2(lo2(qa[m][2]))), hi = pk2(phi2(hi2(qa[m][2])));
      pq1[m].x = g < 2 ? lo : 0u; pq1[m].y = g < 2 ? hi : 0u; pq1[m].z = 0u; pq1[m].w = 0u;
      PIN(pq0[m]); PIN(pq1[m]);
    }
    STAMP(5);
    BAR_RAW();
#pragma unroll
    for (int s = 0; s < 5; ++s)
      xr[s] = __builtin_amdgcn_raw_buffer_load_b64(rx, ((4 * r64 + w) * 16 + t) * 1280 + (80 * c8 + 16 * s + 4 * g) * 2, 0, 0);
    STAMP(17);
    {
      v4u kv[4][2];
#pragma unroll
      for (int es = 3; es >= 0; --es)
#pragma unroll
        for (int ks = 0; ks < 2; ++ks) kv[es][ks] = *(LAS v4u*)(L + L_KVF + (es * 2 + ks) * 1024 + lane * 16);
      SCHED();
      f32x4 o[2][4];
#pragma unroll
      for (int es = 3; es >= 0; --es)
#pragma unroll
        for (int m = 0; m < 2; ++m) o[m][es] = mfma(kv[es][0], pq0[m], f32x4{0.f, 0.f, 0.f, 0.f});
#pragma unroll
      for (int es = 3; es >= 0; --es)
#pragma unroll
        for (int m = 0; m < 2; ++m) o[m][es] = mfma(kv[es][1], pq1[m], o[m][es]);
#pragma unroll
      for (int m = 0; m < 2; ++m) {
        const float i0 = __builtin_amdgcn_rcpf(o[m][3][0] + 1e-6f), i1 = __builtin_amdgcn_rcpf(o[m][3][1] + 1e-6f);
        const float im = g == 0 ? i0 : i1;
        v4u ov;
        ov.x = pk2(lo2(o[m][0]) * i0); ov.y = pk2(hi2(o[m][0]) * i0);
        ov.z = pk2(lo2(o[m][1]) * im); ov.w = pk2(hi2(o[m][1]) * im);
        v2u ol; ol.x = pk2(lo2(o[m][2]) * i1); ol.y = pk2(hi2(o[m][2]) * i1);
        const int tt = 2 * grp32 + m;
        st128(ov, rs, lane * 16, WS_O + (tt * 20 + c16) * 1024);
        st64(ol, rs, g < 2 ? lane * 8 : OOB, WS_O + (tt * 20 + 16) * 1024 + c16 * 256);
      }
      STAMP(18);
      publish(FLAG(2, grp32) + c16, tag, lane);
    }
    STAMP(7);
  }

  {
    const int tt = 4 * r64 + w;
    wait_flag(FLAG(2, tt >> 1) + lane, 16, tag, lane);
    STAMP(8);
    if (b == 0 && tid == 0) __hip_atomic_store((gu32*)(ws + WS_EPOCH), tag, RLX_AGENT);
    v4u a[20];
#pragma unroll
    for (int ks = 0; ks < 2 * S0; ++ks) a[ks] = ld128(rs, lane * 16, WS_O + (tt * 20 + ks) * 1024);
    SCHED();
    BAR_RAW();
    f32x4 acc[1][5];
    const LBase bp(lds0 + L_WO + (lane >> 2) * 160 + (lane & 3) * 8);
    kloop<1, 5, 20, 1, true>(acc, [&](int ks, int s, int h) { return bp.rd(ks * 5120 + h * 2560 + s * 32); },
      [&](int ks, int m) { return a[ks]; },
      [&](int ks) {
#pragma unroll
        for (int j = ld_hi(ks - 1, 2 * S0, 2 * AW, 20); j < ld_hi(ks, 2 * S0, 2 * AW, 20); ++j)
          a[j] = j < 16 ? ld128(rs, lane * 16, WS_O + (tt * 20 + j) * 1024) : ld_split(rs, lane, WS_O + (tt * 20 + j) * 1024);
        if (ks == 8) PROG(2);
        if (ks == 14) PROG(3);
      });
    PROG(4);
    STAMP(9);
    v2u hb[5];
    f32x2 hv[10];
#pragma unroll
    for (int s = 0; s < 5; ++s) {
      hb[s].x = pk2(lo2(acc[0][s]) + bf2(xr[s].x));
      hb[s].y = pk2(hi2(acc[0][s]) + bf2(xr[s].y));
      hv[2 * s] = bf2(hb[s].x); hv[2 * s + 1] = bf2(hb[s].y);
    }
    st128(v4u{hb[0].x, hb[0].y, hb[1].x, hb[1].y}, rs, lane * 16, WS_H + (tt * 20 + 2 * c8) * 1024);
    st128(v4u{hb[2].x, hb[2].y, hb[3].x, hb[3].y}, rs, lane * 16, WS_H + (tt * 20 + 2 * c8 + 1) * 1024);
    st64(hb[4], rs, lane * 8, WS_H + (tt * 20 + 16) * 1024 + c8 * 512);
    const f32x2 s2 = ((hv[0] + hv[1]) + (hv[2] + hv[3])) + ((hv[4] + hv[5]) + (hv[6] + hv[7])) + (hv[8] + hv[9]);
    const float mc = xsum32(xsum16(s2[0] + s2[1])) * (1.f / 80.f);
    f32x2 q2 = {0.f, 0.f}, q3 = {0.f, 0.f};
#pragma unroll
    for (int j = 0; j < 10; j += 2) { const f32x2 d0 = hv[j] - mc, d1 = hv[j + 1] - mc; q2 += d0 * d0; q3 += d1 * d1; }
    q2 += q3;
    const float m2 = xsum32(xsum16(q2[0] + q2[1]));
    st64(v2u{__builtin_bit_cast(unsigned, mc), __builtin_bit_cast(unsigned, m2)}, rs, g == 0 ? t * 8 : OOB, WS_ST + (tt * 8 + c8) * 128);
    STAMP(20);
    publish(FLAG(3, tt >> 1) + (tt & 1) * 8 + c8, tag, lane);
    STAMP(10);
  }

  {
    wait_flag(FLAG(3, grp32) + lane, 16, tag, lane);
    STAMP(11);
    v4u stt[2];
#pragma unroll
    for (int m = 0; m < 2; ++m) {
      const v2u s0 = ld64(rs, g * 256 + t * 8, WS_ST + (2 * grp32 + m) * 1024), s1 = ld64(rs, g * 256 + 128 + t * 8, WS_ST + (2 * grp32 + m) * 1024);
      stt[m] = v4u{s0.x, s0.y, s1.x, s1.y};
    }
    v4u a[20][2];
    const int abase = WS_H + 2 * grp32 * 20 * 1024;
#pragma unroll
    for (int ks = 0; ks < S0; ++ks)
#pragma unroll
      for (int m = 0; m < 2; ++m) a[ks][m] = ld128(rs, lane * 16, abase + (m * 20 + ks) * 1024);
    SCHED();
    float nm[2], rr[2];
#pragma unroll
    for (int m = 0; m < 2; ++m) {
      const f32x4 sv = __builtin_bit_cast(f32x4, stt[m]);
      const float mean = xsum32(xsum16(sv[0] + sv[2])) * 0.125f;
      const float m2 = xsum32(xsum16(sv[1] + sv[3] + 80.f * ((sv[0] - mean) * (sv[0] - mean) + (sv[2] - mean) * (sv[2] - mean))));
      const float var = m2 * (1.f / D);
      const float r1 = __builtin_amdgcn_rsqf(var + LN_EPS);
      rr[m] = r1 * __builtin_amdgcn_rsqf(var * r1 * r1 + LN_EPS);
      nm[m] = -mean * rr[m];
      PIN(rr[m]); PIN(nm[m]);
    }
    BAR_RAW();
    STAMP(22);
    const unsigned l64 = lds0 + (lane >> 2) * 64 + (lane & 3) * 8;
    const LBase bp0(l64 + (g & 1) * 32), bp1(l64 + 32 - (g & 1) * 32);
    const int h1base = WS_H1 + 2 * grp32 * 48 * 1024;
    auto finish = [&](const f32x4& c0, const f32x4& c1, int j, int m) {
      v4u o;
#pragma unroll
      for (int s = 0; s < 2; ++s) {
        const f32x4 cs = *(LAS f32x4*)(L + L_CS + ((2 * j + s) * 4 + g) * 16), bb = *(LAS f32x4*)(L + L_B1 + ((2 * j + s) * 4 + g) * 16);
        const f32x4 c = s ? c1 : c0;
        o[2 * s] = pk2(gelu2(lo2(c) * rr[m] + (lo2(cs) * nm[m] + lo2(bb))));
        o[2 * s + 1] = pk2(gelu2(hi2(c) * rr[m] + (hi2(cs) * nm[m] + hi2(bb))));
      }
      st128(o, rs, lane * 16, h1base + (m * 48 + (c16 >> 3) * 24 + (j < 2 ? 2 * (c16 & 7) + j : 16 + (c16 & 7))) * 1024);
    };
    {
      f32x4 acc[2][4];
      kloop<2, 4, 20, 1, true>(acc, [&](int ks, int s, int h) { return ((s & 1) ? bp1 : bp0).rd(L_W1 + (s >> 1) * 40960 + ks * 2048 + h * 1024); },
        [&](int ks, int m) { return a[ks][m]; },
        [&](int ks) {
#pragma unroll
          for (int j = ld_hi(ks - 1, S0, AW, 20); j < ld_hi(ks, S0, AW, 20); ++j)
#pragma unroll
            for (int m = 0; m < 2; ++m)
              a[j][m] = j < 16 ? ld128(rs, lane * 16, abase + (m * 20 + j) * 1024) : ld_split(rs, lane, abase + (m * 20 + j) * 1024);
          if (ks == 20 - AW) PROG(5);
        });
      PROG(6);
      STAMP(12);
#pragma unroll
      for (int j = 0; j < 2; ++j)
#pragma unroll
        for (int m = 0; m < 2; ++m) finish(acc[m][2 * j], acc[m][2 * j + 1], j, m);
      SCHED();
      STAMP(24);
    }
    f32x4 accb[2][2];
    kloop<2, 2, 20, 3, true>(accb, [&](int ks, int s, int h) { return (s ? bp1 : bp0).rd(L_W1 + 81920 + ks * 2048 + h * 1024); },
      [&](int ks, int m) { return a[ks][m]; },
      [&](int ks) { if (ks == PUBA) publish(FLAG(4, grp32) + c16, tag, lane); });
    STAMP(13);
    BAR_RAW();
    STAMP(14);
    constexpr int WIN = 8;
    v4u ar[WIN][2];
    const int kh = w & 1;
    const int a4 = WS_H1 + ((4 * r64 + (w & 2)) * 48 + kh * 24) * 1024;
#pragma unroll
    for (int ks = 0; ks < WIN - 1; ++ks)
#pragma unroll
      for (int m = 0; m < 2; ++m) ar[ks][m] = ld128(rs, lane * 16, a4 + (m * 48 + ks) * 1024);
    SCHED();
#pragma unroll
    for (int m = 0; m < 2; ++m) finish(accb[m][0], accb[m][1], 2, m);
    STAMP(29);
    const int tt = 4 * r64 + w;
    v4u hr01, hr23;
    v2u hr4;
    f32x4 acc[2][5];
    const LBase bp(lds0 + L_W2 + kh * 5120 + (lane >> 2) * 160 + (lane & 3) * 8);
    kloop<2, 5, 24, 1, true>(acc, [&](int ks, int s, int h) { return bp.rd((ks % 15) * 10240 + h * 2560 + s * 32); },
      [&](int ks, int m) { return ar[ks % WIN][m]; },
      [&](int ks) {
        if (ks == 5) PROG(7);
        if (ks == 9 || ks == 14 || ks == 19) BAR_RAW();
        if (ks == 3) publish(FLAG(10, grp32) + c16, tag, lane);
        if (ks >= 3) {
#pragma unroll
          for (int j = ks == 3 ? WIN - 1 : ld_hi(ks - 1, 1, WIN - 1, 24); j < ld_hi(ks, 1, WIN - 1, 24); ++j)
#pragma unroll
            for (int m = 0; m < 2; ++m) ar[j % WIN][m] = ld128(rs, lane * 16, a4 + (m * 48 + j) * 1024);
        }
        if (ks + WIN - 1 == 24) {
          hr01 = ld128(rs, lane * 16, WS_H + (tt * 20 + 2 * c8) * 1024);
          hr23 = ld128(rs, lane * 16, WS_H + (tt * 20 + 2 * c8 + 1) * 1024);
          hr4 = ld64(rs, lane * 8, WS_H + (tt * 20 + 16) * 1024 + c8 * 512);
        }
      });
    STAMP(15);
    LAS f32x4* xch = (LAS f32x4*)(L + L_W2 + 102400);
#pragma unroll
    for (int s = 0; s < 5; ++s) xch[((w ^ 1) * 5 + s) * 64 + lane] = kh ? acc[0][s] : acc[1][s];
    BAR();
    const __amdgpu_buffer_rsrc_t rout = __builtin_amdgcn_make_buffer_rsrc((void*)out, 0, S * D * 2, 0x00020000);
    const unsigned hrr[10] = {hr01.x, hr01.y, hr01.z, hr01.w, hr23.x, hr23.y, hr23.z, hr23.w, hr4.x, hr4.y};
#pragma unroll
    for (int s = 0; s < 5; ++s) {
      const f32x4 bb = *(LAS f32x4*)(L + L_B2 + (s * 4 + g) * 16), ot = xch[(w * 5 + s) * 64 + lane];
      const f32x4 v = kh ? ot + acc[1][s] : acc[0][s] + ot;
      v2u o;
      o.x = pk2(lo2(v) + lo2(bb) + bf2(hrr[2 * s]));
      o.y = pk2(hi2(v) + hi2(bb) + bf2(hrr[2 * s + 1]));
      __builtin_amdgcn_raw_buffer_store_b64(o, rout, ((tt * 16 + t) * D + 80 * c8 + 16 * s + 4 * g) * 2, 0, 0);
    }
    STAMP(16);
  }
  return;
  }

  {
    const v4u srd_ws = mk_srd(ws, WS_BYTES);
    STAMP(16);
    const int q5 = lane / 5, c5 = lane % 5;
    const int f40m = c5 < 4 ? 512 : 128, f40l = c5 < 4 ? q5 * 64 + (c5 ^ (2 * ((q5 >> 2) & 1))) * 16 : 2048 + q5 * 16;
    v2u ub1, ub2;
    unsigned pf0 = 0;
    if (hw == 0) {
      const int dof = sel(lane < 40, lane * 16);
#pragma unroll
      for (int j = 0; j < 10; ++j) {
        const int k = 40 * r128 + 4 * j, R = (k & ~31) + 16 * ((k >> 2) & 1) + 4 * ((k >> 3) & 3);
        st128(tv[j], rs, dof, WS_TKV + c16 * 153600 + R * 160);
      }
      STAMP(18);
      PIN(tag); tag += 1u;
      publish(FLAG(6, c16) + r128, tag, lane);
      STAMP(13);
    } else if (hw == 1) {
      const __amdgpu_buffer_rsrc_t ro = __builtin_amdgcn_make_buffer_rsrc((void*)wo, 0, D * D * 2, 0x00020000);
      const int vq = sel(lane < 40, (8 * (q5 >> 2) + (q5 & 3)) * 3840 + c5 * 16), vo = sel(lane < 40, q10 * 1280 + c10 * 16);
#pragma unroll
      for (int j = 0; j < 5; ++j) {
        const int i8 = 5 * r128 + j, k = 32 * (i8 >> 2) + 16 * (i8 & 1) + 4 * ((i8 >> 1) & 1);
        tv[j] = __builtin_amdgcn_raw_buffer_load_b128(rq, vq, k * 3840 + c16 * 80, 0);
      }
#pragma unroll
      for (int j = 0; j < 5; ++j) tv[5 + j] = __builtin_amdgcn_raw_buffer_load_b128(ro, vo, (20 * r64 + 4 * j) * 1280 + c8 * 160, 0);
    } else if (hw == 2) {
      const GAS unsigned char* cb = (const GAS unsigned char*)(pc0 & ~127ull);
      if (lane < 11) pf0 = *(const GAS unsigned*)(cb + (lane * 32 + bi) * 128);
    } else if (hw == 3) {
      const __amdgpu_buffer_rsrc_t rb1 = __builtin_amdgcn_make_buffer_rsrc((void*)b1, 0, F * 2, 0x00020000);
      const __amdgpu_buffer_rsrc_t rb2 = __builtin_amdgcn_make_buffer_rsrc((void*)b2, 0, D * 2, 0x00020000);
      ub1 = __builtin_amdgcn_raw_buffer_load_b64(rb1, lane * 8, c16 * 192, 0);
      ub2 = __builtin_amdgcn_raw_buffer_load_b64(rb2, lane * 8, c8 * 160, 0);
    }
    asm volatile("" ::: "memory");
    int red_off, kvf_off, kvp_off;
    {
      const int it = ht < 210 ? ht : 209, ie = it / 5, ic = it % 5;
      const int ihead = ie < 40 ? (ie >= 20) : ie - 40;
      const int id = 20 * ihead + 4 * ic, dg = id >> 2;
      const int sa = id >> 4, col = 40 + ie;
      const int tile = ie < 40 ? 2 * sa + (col >> 4) - 2 : 7 + sa;
      red_off = lds0 + L_RED + (tile * 64 + (dg & 3) * 16 + (ie < 40 ? (col & 15) : 0)) * 16;
      kvf_off = lds0 + L_KVF + ((ie < 40 ? (ie >> 4) : 3) * 2 + (dg >> 3)) * 1024 + ((dg & 3) * 16 + (ie < 40 ? (ie & 15) : ihead)) * 16 + ((dg >> 2) & 1) * 8;
      kvp_off = WS_KVP + (c16 * 16 + r128) * 4096 + ht * 16;
      PIN(red_off); PIN(kvf_off); PIN(kvp_off);
    }
    int vo12, td[12];
    if (hw >= 2) {
      const int q12 = lane / 12, c12 = lane % 12;
      vo12 = hw == 2 ? sel(lane < 48, q12 * 3072 + c12 * 16) : sel(lane < 40, q10 * 1280 + c10 * 16);
#pragma unroll
      for (int j = 0; j < 12; ++j) {
        const int cc = r128 >> 1, off = 40 * (r128 & 1) + 4 * j;
        const int R = off < 64 ? 64 * cc + off : 32 * (16 + (cc >> 1)) + 16 * (cc & 1) + off - 64;
        const int d1 = WS_T1 + c16 * 122880 + (c12 >> 2) * 40960 + (R + q12) * 64 + ((c12 & 3) ^ (2 * ((R >> 2) & 1))) * 16;
        const int pc = r64 >> 1, ko = 48 * (r64 & 1) + 4 * j, jj = ko >> 5;
        const int d2 = WS_T2 + c8 * 245760 + (2 * (jj < 2 ? 2 * (pc & 7) + jj : 16 + (pc & 7)) + (pc >> 3)) * 5120 + (ko & 31) * 160 + lane * 16;
        td[j] = sel(hw == 2 ? (lane < 48 && j < 10) : lane < 40, hw == 2 ? d1 : d2);
        PIN(td[j]);
      }
      PIN(vo12);
    }
    int l16 = lane * 16, h16 = ht * 16;
    PIN(l16); PIN(h16);
    gu32* fp1 = FLAG(1, c16) + lane;
    gu32* fp78 = lane < 32 ? FLAG(7, c8) + lane : FLAG(8, c16) + t;
    gu32* fp4 = FLAG(4, 2 * r64 + (g & 1)) + t;
    PIN(fp1); PIN(fp78); PIN(fp4);
    ((LAS v4u*)(L + L_KVF))[ht] = v4u{0u, 0u, 0u, 0u};
    ((LAS v4u*)(L + L_KVF))[256 + ht] = v4u{0u, 0u, 0u, 0u};
    asm volatile("" ::: "memory");
    if (hw != 0) {
      if (hw == 1) {
#pragma unroll
        for (int j = 0; j < 5; ++j) {
          const int i8 = 5 * r128 + j;
          st128(tv[j], rs, sel(lane < 40, (i8 & 3) * f40m + f40l), WS_TKV + c16 * 153600 + 102400 + (i8 >> 2) * 2560);
        }
#pragma unroll
        for (int j = 0; j < 5; ++j) {
          const int pp = r64 >> 1, e = 20 * (r64 & 1) + 4 * j;
          const int R = e < 32 ? 32 * pp + e : 32 * (16 + (pp >> 2)) + 8 * (pp & 3) + e - 32;
          st128(tv[5 + j], rs, sel(lane < 40, lane * 16), WS_TO + c8 * 102400 + R * 160);
        }
      }
      if (hw == 3) {
        if (lane < 24) *(LAS f32x4*)(L + L_B1 + lane * 16) = f32x4{bf_lo(ub1.x), bf_hi(ub1.x), bf_lo(ub1.y), bf_hi(ub1.y)};
        if (lane < 20) *(LAS f32x4*)(L + L_B2 + lane * 16) = f32x4{bf_lo(ub2.x), bf_hi(ub2.x), bf_lo(ub2.y), bf_hi(ub2.y)};
      }
      asm volatile("" :: "v"(pf0));
      PIN(tag); tag += 1u;
      DRAIN();
      if (hw == 1 && lane < 2) flag_set(lane == 0 ? FLAG(8, c16) + r128 : FLAG(7, c8) + r64, tag);
    }
    if (hw == 0) {
      wait_flag(FLAG(6, c16) + lane, 16, tag, lane);
      sig[0] = 1u;
    }
    sig_wait(sig);
    STAMP(1);
    const int kvq = WS_TKV + c16 * 153600, wos = WS_TO + c8 * 102400;
    dma_image<0, 24>(lds0 + L_BKV, l16, srd_ws, kvq, hw, 3 * (bi >> 3));
    dma_image<24, 56>(lds0 + L_BKV, l16, srd_ws, kvq, hw, 4 * (bi >> 3));
    asm volatile("s_waitcnt vmcnt(8)" ::: "memory");
    STAMP(2);
    BAR_RAW();
    dma_image<56, 100>(lds0 + L_BKV, l16, srd_ws, kvq, hw, 5 * (bi >> 3));
    asm volatile("s_waitcnt vmcnt(10)" ::: "memory");
    BAR_RAW();
    if (hw == 1) wait_flag(fp78, 48, tag, lane);
    DRAIN();
    STAMP(3);
    BAR_RAW();
    if (QPOS == 0) dma_image<100, 150>(lds0 + L_BKV, l16, srd_ws, kvq, hw, 6 * (bi >> 3));
    prog_wait(prog, 1);
    if (QPOS == 1) dma_image<100, 150>(lds0 + L_BKV, l16, srd_ws, kvq, hw, 6 * (bi >> 3));
    if (hw >= 2) {
      const __amdgpu_buffer_rsrc_t r12 = __builtin_amdgcn_make_buffer_rsrc((void*)(hw == 2 ? w1 : w2), 0, D * F * 2, 0x00020000);
      const int base = hw == 2 ? r128 * 122880 + c16 * 192 : r64 * 61440 + c8 * 160, step = hw == 2 ? 4 * 3072 : 4 * 1280;
#pragma unroll
      for (int j = 0; j < 12; ++j) tv[j] = __builtin_amdgcn_raw_buffer_load_b128(r12, vo12, (j < 10 || hw == 3) ? base + j * step : OOB, 0);
      asm volatile("s_waitcnt vmcnt(12)" ::: "memory");
    } else {
      dma_image<40, 68, 0, 2>(lds0 + L_WO, l16, srd_ws, wos, hw, bi >> 2);
      asm volatile("s_waitcnt vmcnt(14)" ::: "memory");
    }
    STAMP(4);
    BAR();
    __builtin_amdgcn_s_setprio(2);
    STAMP(11);
    {
      f32x4 pv[4];
#pragma unroll
      for (int ww = 0; ww < 4; ++ww) pv[ww] = *(LAS f32x4*)((LAS char*)red_off + ww * 10240);
      st128(__builtin_bit_cast(v4u, add4(add4(pv[0], pv[1]), add4(pv[2], pv[3]))), rs, kvp_off, 0);
      STAMP(12);
      DRAIN();
    }
    if (hw == 0) {
      sig_wait(sig + 4); sig_wait(sig + 5); sig_wait(sig + 6);
      if (lane == 0) flag_set(fp1 + r128, tag);
      STAMP(10);
      wait_flag(fp1, 16, tag, lane);
      sig[2] = 1u;
    } else {
      sig[3 + hw] = 1u;
      if (hw >= 2) {
#pragma unroll
        for (int j = 0; j < 12; ++j) st128(tv[j], rs, td[j], 0);
      }
      dma_image<68, 100, 0, 3>(lds0 + L_WO, l16, srd_ws, wos, hw - 1, bi >> 2);
      if (hw >= 2) publish(hw == 2 ? FLAG(5, c16) + r128 : FLAG(11, c8) + r64, tag, lane);
      STAMP(10);
      sig_wait(sig + 2);
    }
    STAMP(5);
    {
      v4u pv[16];
#pragma unroll
      for (int r = 0; r < 16; ++r) pv[r] = ld128(rs, h16, WS_KVP + (c16 * 16 + r) * 4096);
      SCHED();
      f32x4 v = __builtin_bit_cast(f32x4, pv[0]);
#pragma unroll
      for (int r = 1; r < 16; ++r) v = add4(v, __builtin_bit_cast(f32x4, pv[r]));
      v2u o; o.x = pk2(lo2(v)); o.y = pk2(hi2(v));
      *(LAS v2u*)((LAS char*)kvf_off) = o;
      if (ht >= 200) {
#pragma unroll
        for (int c = 1; c < 4; ++c) *(LAS v2u*)((LAS char*)kvf_off + c * 64) = o;
      }
    }
    STAMP(6);
    BAR();
    __builtin_amdgcn_s_setprio(0);
    dma_image<0, 40>(lds0 + L_WO, l16, srd_ws, wos, hw, 2 * (bi >> 2));
    if (hw == 1) wait_flag(lane < 32 ? FLAG(11, c8) + lane : FLAG(5, c16) + t, 48, tag, lane);
    if (hw == 2) {
      f32x4 s0 = {0.f, 0.f, 0.f, 0.f}, s1 = {0.f, 0.f, 0.f, 0.f};
#pragma unroll
      for (int j = 0; j < 10; ++j) {
        s0 = add4(s0, f32x4{bf_lo(tv[j].x), bf_hi(tv[j].x), bf_lo(tv[j].y), bf_hi(tv[j].y)});
        s1 = add4(s1, f32x4{bf_lo(tv[j].z), bf_hi(tv[j].z), bf_lo(tv[j].w), bf_hi(tv[j].w)});
      }
#pragma unroll
      for (int d = 24; d >= 12; d -= 12) {
        f32x4 u0, u1;
#pragma unroll
        for (int e = 0; e < 4; ++e) { u0[e] = __shfl(s0[e], lane + d); u1[e] = __shfl(s1[e], lane + d); }
        s0 = add4(s0, u0); s1 = add4(s1, u1);
      }
      f32x4 sv;
#pragma unroll
      for (int e = 0; e < 4; ++e) { const float a0 = __shfl(s0[e], lane >> 1), a1 = __shfl(s1[e], lane >> 1); sv[e] = (lane & 1) ? a1 : a0; }
      st128(__builtin_bit_cast(v4u, sv), rs, sel(lane < 24, lane * 16), WS_CS + (c16 * 16 + r128) * 384);
      publish(FLAG(9, c16) + r128, tag, lane);
    }
    DRAIN();
    BAR_RAW();
    prog_wait(prog, 2);
    dma_image<70, 120>(lds0 + L_W1, l16, srd_ws, WS_T1 + c16 * 122880, hw, 6 * (bi >> 3));
    prog_wait(prog, 3);
    dma_image<0, 40>(lds0 + L_W1, l16, srd_ws, WS_T1 + c16 * 122880, hw, 5 * (bi >> 3));
    STAMP(7);
    prog_wait(prog, 4);
    if (hw == 0) {
      wait_flag(FLAG(9, c16) + lane, 16, tag, lane);
      v4u pv[16];
#pragma unroll
      for (int r = 0; r < 16; ++r) pv[r] = ld128(rs, sel(lane < 24, lane * 16), WS_CS + (c16 * 16 + r) * 384);
      f32x4 v = __builtin_bit_cast(f32x4, pv[0]);
#pragma unroll
      for (int r = 1; r < 16; ++r) v = add4(v, __builtin_bit_cast(f32x4, pv[r]));
      if (lane < 24) *(LAS f32x4*)(L + L_CS + lane * 16) = v;
    } else {
      dma_image<40, 70, 0, 3>(lds0 + L_W1, l16, srd_ws, WS_T1 + c16 * 122880, hw - 1, 3 * (bi >> 3));
    }
    DRAIN();
    STAMP(8);
    BAR();
    const int w2s = WS_T2 + c8 * 245760;
    prog_wait(prog, 5);
    dma_image<0, 30>(lds0 + L_W2, l16, srd_ws, w2s, hw, bi >> 2);
    STAMP(14);
    prog_wait(prog, 6);
    dma_image<30, 110>(lds0 + L_W2, l16, srd_ws, w2s, hw, 5 * (bi >> 2));
    DRAIN();
    STAMP(9);
    if (hw == 0) wait_flag(fp4, 32, tag, lane);
    STAMP(15);
    BAR_RAW();
    dma_image<110, 150>(lds0 + L_W2, l16, srd_ws, w2s, hw, 2 * (bi >> 2));
    prog_wait(prog, 7);
    if (hw == 0) wait_flag(fp4 + 6 * FLAG_SET / 4, 32, tag, lane);
    else dma_image<150, 198, 0, 3>(lds0 + L_W2 - 153600, l16, srd_ws, w2s, hw - 1, 3 * (bi >> 2));
    asm volatile("s_waitcnt vmcnt(16)" ::: "memory");
    STAMP(17);
    BAR_RAW();
    dma_image<198, 200>(lds0 + L_W2 - 153600, l16, srd_ws, w2s, hw, 0);
    dma_image<200, 240>(lds0 + L_W2 - 153600, l16, srd_ws, w2s, hw, 2 * (bi >> 2));
    asm volatile("s_waitcnt vmcnt(10)" ::: "memory");
    BAR_RAW();
    DRAIN();
    BAR_RAW();
  }

}
}

extern "C" void kernel_launch(void* const* d_in, const int* in_sizes, int n_in, void* d_out, int out_size, void* d_ws,
                              size_t ws_size, hipStream_t stream) {
  const unsigned short* x = (const unsigned short*)d_in[0];
  const unsigned short* wqkv = (const unsigned short*)d_in[1];
  const unsigned short* wo = (const unsigned short*)d_in[2];
  const unsigned short* w1 = (const unsigned short*)d_in[4];
  const unsigned short* b1 = (const unsigned short*)d_in[5];
  const unsigned short* w2 = (const unsigned short*)d_in[6];
  const unsigned short* b2 = (const unsigned short*)d_in[7];
  fused_block<<<256, 512, 0, stream>>>(x, wqkv, wo, w1, b1, w2, b2, (unsigned short*)d_out, (unsigned char*)d_ws);
}
```

```cpp
#include <hip/hip_runtime.h>
#include <hip/hip_bf16.h>

#define STAMPS 0
#define THR 0
#define TOUCH 0
#define CODEPF 1
#define FLAGPF 0
#define OUTAUX 0
#define OUTPF 0
#define S0 6
#define XRPOS 0
#define QPOS 0
#define H1W 8
#define H1G 2
#define PUBA 8
#define NCDELAY 0
#define HPRIO 0
#define WOLATE 0
#define SLEEP 1

#define GAS __attribute__((address_space(1)))
#define LAS __attribute__((address_space(3)))
#define RLX_AGENT __ATOMIC_RELAXED, __HIP_MEMORY_SCOPE_AGENT
typedef unsigned v4u __attribute__((ext_vector_type(4)));
typedef unsigned v2u __attribute__((ext_vector_type(2)));
typedef float f32x4 __attribute__((ext_vector_type(4)));
typedef float f32x2 __attribute__((ext_vector_type(2)));
typedef __bf16 bf16x8 __attribute__((ext_vector_type(8)));
typedef __bf16 bf16x2 __attribute__((ext_vector_type(2)));
typedef short s16x4 __attribute__((ext_vector_type(4)));
typedef GAS unsigned gu32;

namespace {
constexpr int S = 2048, D = 640, F = 1536;
constexpr float LN_EPS = 1e-5f;
constexpr int AUX_SC1 = 16;
constexpr unsigned kSpinCap = 1u << 17;
constexpr int OOB = 0x7FFFFF00;

constexpr int WS_EPOCH = 0;
constexpr int WS_FLAG = 4096;
constexpr int FLAG_SET = 64 * 256;
constexpr int WS_XN = 1 << 20;
constexpr int WS_O = WS_XN + 128 * 20 * 1024;
constexpr int WS_H = WS_O + 128 * 20 * 1024;
constexpr int WS_H1 = WS_H + 128 * 20 * 1024;
constexpr int WS_KVP = WS_H1 + 128 * 48 * 1024;
constexpr int WS_ST = WS_KVP + 16 * 16 * 4096;
constexpr int WS_DBG = WS_ST + 2048 * 64;
constexpr int WS_TKV = WS_DBG + 256 * 8 * 32 * 8;
constexpr int WS_TO = WS_TKV + 16 * 153600;
constexpr int WS_T1 = WS_TO + 8 * 102400;
constexpr int WS_T2 = WS_T1 + 16 * 122880;
constexpr int WS_CS = WS_T2 + 16 * 122880;
constexpr int WS_BYTES = WS_CS + 16 * 16 * 384;

constexpr int L_BKV = 0, L_BQ = 102400;
constexpr int L_RED = 0, L_KVF = 153600;
constexpr int L_WO = 0, L_W1 = 30720, L_W2 = 0, L_CS = 161792;
constexpr int L_B1 = L_CS + 1152, L_B2 = L_CS + 1536;
constexpr int LDS_BYTES = 163840;

__device__ __forceinline__ f32x4 mfma(v4u a, v4u b, f32x4 c) {
  return __builtin_amdgcn_mfma_f32_16x16x32_bf16(__builtin_bit_cast(bf16x8, a), __builtin_bit_cast(bf16x8, b), c, 0, 0, 0);
}
__device__ __forceinline__ unsigned pk2(f32x2 v) { return __builtin_bit_cast(unsigned, __builtin_convertvector(v, bf16x2)); }
__device__ __forceinline__ unsigned pk2(float lo, float hi) { return pk2(f32x2{lo, hi}); }
__device__ __forceinline__ float bf_lo(unsigned u) { return __builtin_bit_cast(float, u << 16); }
__device__ __forceinline__ float bf_hi(unsigned u) { return __builtin_bit_cast(float, u & 0xFFFF0000u); }
__device__ __forceinline__ f32x2 bf2(unsigned u) { return f32x2{bf_lo(u), bf_hi(u)}; }
__device__ __forceinline__ f32x2 lo2(f32x4 v) { return f32x2{v[0], v[1]}; }
__device__ __forceinline__ f32x2 hi2(f32x4 v) { return f32x2{v[2], v[3]}; }
__device__ __forceinline__ v4u mk_srd(const void* p, unsigned bytes) {
  const unsigned long long a = (unsigned long long)p;
  v4u r;
  r.x = (unsigned)a; r.y = (unsigned)(a >> 32) & 0xFFFFu; r.z = bytes; r.w = 0x00020000u;
  return r;
}
#define DRAIN() asm volatile("s_waitcnt vmcnt(0)" ::: "memory")
#define BAR() asm volatile("s_waitcnt lgkmcnt(0)\n\ts_barrier" ::: "memory")
#define BAR_RAW() asm volatile("s_barrier" ::: "memory")
#define SCHED() __builtin_amdgcn_sched_barrier(0)
#define PIN(x) asm volatile("" : "+v"(x))

__device__ __forceinline__ void dma2(unsigned lds_addr, unsigned voff, v4u srd, unsigned soff) {
  unsigned keep;
  asm volatile("s_mov_b32 %0, m0\n\ts_mov_b32 m0, %1\n\ts_nop 0\n\t"
               "buffer_load_dwordx4 %2, %3, %4 offen sc1 lds\n\t"
               "buffer_load_dwordx4 %2, %3, %4 offen offset:1024 sc1 lds\n\t"
               "s_mov_b32 m0, %0"
               : "=&s"(keep) : "s"(lds_addr), "v"(voff), "s"(srd), "s"(soff) : "memory");
}
template <int LO, int HI, int TH = 0, int NW = 4>
__device__ __forceinline__ void dma_image(unsigned lds_base, unsigned voff, v4u srd, int ws_off, int hw, int rot) {
  constexpr int NP = (HI - LO) / 2;
#pragma unroll 1
  for (int i = hw; i < NP; i += NW) {
    int j = i + rot;
    if (j >= NP) j -= NP;
    dma2(lds_base + LO * 1024 + j * 2048, voff, srd, ws_off + LO * 1024 + j * 2048);
    if (TH) asm volatile("s_waitcnt vmcnt(%0)" :: "i"(TH - 2) : "memory");
  }
}
__device__ __forceinline__ v2u rdtr(LAS char* p) { return __builtin_bit_cast(v2u, __builtin_amdgcn_ds_read_tr16_b64_v4i16((LAS s16x4*)p)); }
struct LBase {
  unsigned a[3];
  __device__ __forceinline__ explicit LBase(unsigned v) { a[0] = v; a[1] = v + 65536; a[2] = v + 131072; PIN(a[0]); PIN(a[1]); PIN(a[2]); }
  __device__ __forceinline__ v2u rd(int off) const { return rdtr((LAS char*)(a[off >> 16]) + (off & 65535)); }
};
template <int MT, int NT, int KS, int PD, bool WA, class RB, class GA, class PRE>
__device__ __forceinline__ void kloop(f32x4 (&acc)[MT][NT], RB rb, GA ga, PRE pre) {
  v2u bl[PD + 1][NT], bh[PD + 1][NT];
#pragma unroll
  for (int m = 0; m < MT; ++m)
#pragma unroll
    for (int s = 0; s < NT; ++s) acc[m][s] = f32x4{0.f, 0.f, 0.f, 0.f};
#pragma unroll
  for (int ks = 0; ks < PD; ++ks)
#pragma unroll
    for (int s = 0; s < NT; ++s) { bl[ks][s] = rb(ks, s, 0); bh[ks][s] = rb(ks, s, 1); }
  SCHED();
#pragma unroll
  for (int ks = 0; ks < KS; ++ks) {
    pre(ks);
#pragma unroll
    for (int s = 0; s < NT; ++s)
#pragma unroll
      for (int m = 0; m < MT; ++m) {
        const v2u lo = bl[ks % (PD + 1)][s], hi = bh[ks % (PD + 1)][s];
        const v4u wv = {lo.x, lo.y, hi.x, hi.y};
        acc[m][s] = WA ? mfma(wv, ga(ks, m), acc[m][s]) : mfma(ga(ks, m), wv, acc[m][s]);
        if (ks + PD < KS) {
          if (MT == 1 || m == 0) bl[(ks + PD) % (PD + 1)][s] = rb(ks + PD, s, 0);
          if (MT == 1 || m == 1) bh[(ks + PD) % (PD + 1)][s] = rb(ks + PD, s, 1);
        }
        SCHED();
      }
  }
}
constexpr int ld_hi(int ks, int s0, int W, int KS) {
  const int a = s0 + 2 * (ks + 1), b = ks + 1 + W, c = a < b ? a : b;
  return c < KS ? c : KS;
}
__device__ __forceinline__ v4u ld128(__amdgpu_buffer_rsrc_t rs, int voff, int soff) {
  return __builtin_amdgcn_raw_buffer_load_b128(rs, voff, soff, AUX_SC1);
}
__device__ __forceinline__ v2u ld64(__amdgpu_buffer_rsrc_t rs, int voff, int soff) {
  return __builtin_amdgcn_raw_buffer_load_b64(rs, voff, soff, AUX_SC1);
}
__device__ __forceinline__ void st128(v4u v, __amdgpu_buffer_rsrc_t rs, int voff, int soff) {
  __builtin_amdgcn_raw_buffer_store_b128(v, rs, (int)((unsigned)voff + (unsigned)soff), 0, AUX_SC1);
}
__device__ __forceinline__ int sel(bool c, int a) {
  PIN(a);
  return c ? a : OOB;
}
__device__ __forceinline__ v4u ld_split(__amdgpu_buffer_rsrc_t rs, int lane, int soff) {
  const v2u a = ld64(rs, lane * 8, soff), b = ld64(rs, lane * 8 + 512, soff);
  return v4u{a.x, a.y, b.x, b.y};
}
__device__ __forceinline__ void st64(v2u v, __amdgpu_buffer_rsrc_t rs, int voff, int soff) {
  __builtin_amdgcn_raw_buffer_store_b64(v, rs, voff, soff, AUX_SC1);
}
__device__ __forceinline__ void flag_set(gu32* p, unsigned tag) {
  asm volatile("global_atomic_swap %0, %1, off sc1" :: "v"(p), "v"(tag) : "memory");
}
__device__ __forceinline__ void wait_flag(gu32* p, int n, unsigned tag, int lane) {
#pragma unroll 1
  for (unsigned spins = 0; spins < kSpinCap; ++spins) {
    unsigned v = tag;
    if (lane < n) asm volatile("global_atomic_add %0, %1, %2, off sc0 sc1\n\ts_waitcnt vmcnt(0)" : "=&v"(v) : "v"(p), "v"(0u) : "memory");
    if (__all(v == tag)) break;
    if (SLEEP) __builtin_amdgcn_s_sleep(SLEEP);
  }
  asm volatile("" ::: "memory");
}
__device__ __forceinline__ void sig_wait(volatile LAS unsigned* p) {
#pragma unroll 1
  for (unsigned spins = 0; spins < 64 * kSpinCap; ++spins) {
    if (*p != 0u) break;
    __builtin_amdgcn_s_sleep(1);
  }
  asm volatile("" ::: "memory");
}
__device__ __forceinline__ void prog_wait(volatile LAS v4u* p, unsigned stage) {
#pragma unroll 1
  for (unsigned spins = 0; spins < 64 * kSpinCap; ++spins) {
    const v4u v = *p;
    if (min(min(v.x, v.y), min(v.z, v.w)) >= stage) break;
    __builtin_amdgcn_s_sleep(1);
  }
  asm volatile("" ::: "memory");
}
__device__ __forceinline__ void publish(gu32* flag, unsigned tag, int lane) {
  DRAIN();
  if (lane == 0) flag_set(flag, tag);
}
__device__ __forceinline__ f32x4 add4(f32x4 a, f32x4 b) {
  f32x4 r;
#pragma unroll
  for (int i = 0; i < 4; ++i) { float x; asm("v_add_f32 %0, %1, %2" : "=v"(x) : "v"(a[i]), "v"(b[i])); r[i] = x; }
  return r;
}
__device__ __forceinline__ float xsum32(float x) {
  unsigned u = __builtin_bit_cast(unsigned, x), v = u;
  asm("" : "+v"(v));
  auto r = __builtin_amdgcn_permlane32_swap(u, v, false, false);
  const unsigned a = r[0], b = r[1];
  return __builtin_bit_cast(float, a) + __builtin_bit_cast(float, b);
}
__device__ __forceinline__ float xsum16(float x) {
  unsigned u = __builtin_bit_cast(unsigned, x), v = u;
  asm("" : "+v"(v));
  auto r = __builtin_amdgcn_permlane16_swap(u, v, false, false);
  const unsigned a = r[0], b = r[1];
  return __builtin_bit_cast(float, a) + __builtin_bit_cast(float, b);
}
__device__ __forceinline__ float xsum8(float x) {
  return x + __builtin_bit_cast(float, __builtin_amdgcn_update_dpp(0, __builtin_bit_cast(int, x), 0x128, 0xf, 0xf, false));
}
__device__ __forceinline__ f32x2 phi2(f32x2 t) {
  const f32x2 u = t * 1.4426950408889634f;
  const f32x2 e = {__builtin_amdgcn_exp2f(fminf(u[0], 0.f)), __builtin_amdgcn_exp2f(fminf(u[1], 0.f))};
  const f32x2 p = {fmaxf(t[0], 0.f), fmaxf(t[1], 0.f)};
  return (p + 1e-6f) + e;
}
__device__ __forceinline__ f32x2 gelu2(f32x2 x) {
  const f32x2 u = x * (x * x * -0.1029432397f + -2.302208198f);
  const f32x2 d = f32x2{__builtin_amdgcn_exp2f(u[0]), __builtin_amdgcn_exp2f(u[1])} + 1.f;
  return x * f32x2{__builtin_amdgcn_rcpf(d[0]), __builtin_amdgcn_rcpf(d[1])};
}

#define STAMP(i) do { } while (0)

__global__ void __launch_bounds__(512, 2)
fused_block(const unsigned short* __restrict__ x, const unsigned short* __restrict__ wqkv, const unsigned short* __restrict__ wo,
            const unsigned short* __restrict__ w1, const unsigned short* __restrict__ b1, const unsigned short* __restrict__ w2,
            const unsigned short* __restrict__ b2, unsigned short* __restrict__ out, unsigned char* ws) {
  __shared__ __attribute__((aligned(16))) char smem[LDS_BYTES];
  unsigned long long pc0;
  asm volatile("s_getpc_b64 %0" : "=s"(pc0));
  LAS char* L = (LAS char*)smem;
  const unsigned lds0 = (unsigned)(size_t)L;
  const int tid = threadIdx.x, lane = tid & 63, w = __builtin_amdgcn_readfirstlane(tid >> 6);
  const int b = blockIdx.x, xcd = b & 7, bi = b >> 3;
  const int g = lane >> 4, t = lane & 15;
  const __amdgpu_buffer_rsrc_t rs = __builtin_amdgcn_make_buffer_rsrc((void*)ws, 0, WS_BYTES, 0x00020000);
  gu32* flags = (gu32*)(ws + WS_FLAG);
#define FLAG(set, grp) (flags + ((set) * FLAG_SET + (grp) * 256) / 4)
  asm volatile("" :: "s"(x), "s"(wqkv), "s"(wo), "s"(w1), "s"(b1), "s"(w2), "s"(b2), "s"(out), "s"(ws));
  STAMP(0);
  const int r128 = 4 * (xcd >> 1) + (bi >> 3), c16 = 8 * (xcd & 1) + (bi & 7);
  const int r64 = 8 * (xcd >> 1) + (bi >> 2), c8 = 4 * (xcd & 1) + (bi & 3);

  const __amdgpu_buffer_rsrc_t rx = __builtin_amdgcn_make_buffer_rsrc((void*)x, 0, S * D * 2, 0x00020000);
  const int hw = w - 4, ht = tid - 256;
  volatile LAS unsigned* sig = (volatile LAS unsigned*)(L + L_CS + 1024);
  volatile LAS v4u* prog = (volatile LAS v4u*)(L + L_CS + 1088);
#define PROG(n) do { asm volatile("" ::: "memory"); sig[16 + w] = (n); } while (0)

  unsigned tag = __hip_atomic_load((gu32*)(ws + WS_EPOCH), RLX_AGENT);
  const __amdgpu_buffer_rsrc_t rq = __builtin_amdgcn_make_buffer_rsrc((void*)wqkv, 0, D * 3 * D * 2, 0x00020000);
  v4u tv[12];
  const int q10 = lane / 10, c10 = lane % 10;
  if (w < 4) {
#pragma unroll
    for (int j = 0; j < 3; ++j)
      tv[j] = __builtin_amdgcn_raw_buffer_load_b128(rx, (lane & 7) * 1280 + (lane >> 3) * 16, (j < 2 || w < 2) ? 8 * b * 1280 + (w + 4 * j) * 128 : OOB, 0);
  } else if (w == 4) {
    const int vo = sel(lane < 40, q10 * 3840 + (c10 < 5 ? 1280 : 2480) + c10 * 16);
#pragma unroll
    for (int j = 0; j < 10; ++j) tv[j] = __builtin_amdgcn_raw_buffer_load_b128(rq, vo, (40 * r128 + 4 * j) * 3840 + c16 * 80, 0);
  } else if (w == 7) {
    *(LAS v4u*)(L + L_CS + 512 + lane * 16) = v4u{0u, 0u, 0u, 0u};
  }
  BAR();

  if (w < 4) {
  const int grp32 = 4 * r128 + w;
  constexpr int AW = 6;

  {
    const int tok = 8 * b + (lane & 7), cl = lane >> 3;
    const bool has3 = w < 2;
    int xd = ((tok >> 4) * 20 + 2 * w + (cl >> 2)) * 1024 + ((cl & 3) * 16 + (tok & 15)) * 16;
    PIN(xd);
    SCHED();
    f32x2 f[12];
#pragma unroll
    for (int j = 0; j < 3; ++j)
#pragma unroll
      for (int e = 0; e < 4; ++e) f[j * 4 + e] = bf2(tv[j][e]);
    f32x2 s2 = (f[0] + f[1]) + (f[2] + f[3]) + ((f[4] + f[5]) + (f[6] + f[7])) + ((f[8] + f[9]) + (f[10] + f[11]));
    const float mloc = xsum32(xsum16(xsum8(s2[0] + s2[1]))) * (has3 ? 1.f / 192.f : 1.f / 128.f);
    const float m3 = has3 ? mloc : 0.f;
    f32x2 q2 = {0.f, 0.f}, q3 = {0.f, 0.f};
#pragma unroll
    for (int j = 0; j < 12; j += 2) {
      const f32x2 d0 = f[j] - (j < 8 ? mloc : m3), d1 = f[j + 1] - (j < 8 ? mloc : m3);
      q2 += d0 * d0; q3 += d1 * d1;
    }
    q2 += q3;
    const float m2loc = xsum32(xsum16(xsum8(q2[0] + q2[1])));
    if (lane < 8) *(volatile LAS f32x4*)(L + L_CS + 512 + (lane * 4 + w) * 16) = f32x4{mloc, m2loc, 1.f, 1.f};
    STAMP(25);
    f32x4 pr[4];
#pragma unroll 1
    for (unsigned spins = 0; spins < 64 * kSpinCap; ++spins) {
      bool ok = true;
#pragma unroll
      for (int ww = 0; ww < 4; ++ww) {
        pr[ww] = *(volatile LAS f32x4*)(L + L_CS + 512 + ((lane & 7) * 4 + ww) * 16);
        ok = ok && pr[ww][2] == 1.f;
      }
      if (__all(ok)) break;
    }
    const f32x4 p01 = {pr[0][0], pr[0][1], pr[1][0], pr[1][1]}, p23 = {pr[2][0], pr[2][1], pr[3][0], pr[3][1]};
    const float mean = (p01[0] + p01[2]) * 0.3f + (p23[0] + p23[2]) * 0.2f;
    const float e0 = p01[0] - mean, e1 = p01[2] - mean, e2 = p23[0] - mean, e3 = p23[2] - mean;
    const float m2 = (p01[1] + p01[3]) + (p23[1] + p23[3]) + 192.f * (e0 * e0 + e1 * e1) + 128.f * (e2 * e2 + e3 * e3);
    const float rstd = __builtin_amdgcn_rsqf(m2 * (1.f / D) + LN_EPS);
#pragma unroll
    for (int j = 0; j < 3; ++j) {
      v4u o;
#pragma unroll
      for (int e = 0; e < 4; ++e) o[e] = pk2((f[j * 4 + e] - mean) * rstd);
      st128(o, rs, (j < 2 || has3) ? xd : OOB, WS_XN + j * 8192);
    }
    STAMP(26);
    PIN(tag); tag += 1u;
    publish(FLAG(0, b >> 2) + (b & 3) * 4 + w, tag, lane);
  }

  STAMP(1);

  v4u xr[3];
  {
    wait_flag(FLAG(0, grp32) + lane, 16, tag, lane);
    STAMP(2);
    v4u a[20][2];
    const int abase = WS_XN + 2 * grp32 * 20 * 1024;
#pragma unroll
    for (int ks = 0; ks < S0; ++ks)
#pragma unroll
      for (int m = 0; m < 2; ++m) a[ks][m] = ld128(rs, lane * 16, abase + (m * 20 + ks) * 1024);
    SCHED();
    BAR_RAW();
    STAMP(27);
    {
      f32x4 acc[2][5];
      const LBase bp(lds0 + L_BKV + (lane >> 2) * 160 + (lane & 3) * 8);
      kloop<2, 5, 20, 1, false>(acc, [&](int ks, int s, int h) { return bp.rd(ks * 5120 + h * 2560 + s * 32); },
        [&](int ks, int m) { return a[ks][m]; },
        [&](int ks) {
#pragma unroll
          for (int j = ld_hi(ks - 1, S0, AW, 20); j < ld_hi(ks, S0, AW, 20); ++j)
#pragma unroll
            for (int m = 0; m < 2; ++m) a[j][m] = ld128(rs, lane * 16, abase + (m * 20 + j) * 1024);
          if (ks == 3 || ks == 10) BAR_RAW();
          if (ks == 20 - AW) PROG(1);
        });
      STAMP(3);
      v4u kf[5];
#pragma unroll
      for (int s = 0; s < 5; ++s)
#pragma unroll
        for (int m = 0; m < 2; ++m) {
          f32x2 u0 = lo2(acc[m][s]), u1 = hi2(acc[m][s]);
          if (s < 2) { u0 = phi2(u0); u1 = phi2(u1); }
          if (s == 2) { const f32x2 p0 = phi2(u0), p1 = phi2(u1); u0 = t < 8 ? p0 : u0; u1 = t < 8 ? p1 : u1; }
          kf[s][2 * m] = pk2(u0); kf[s][2 * m + 1] = pk2(u1);
        }
      const v4u ones = {0x3F803F80u, 0x3F803F80u, 0x3F803F80u, 0x3F803F80u};
      const f32x4 z4 = {0.f, 0.f, 0.f, 0.f};
      f32x4 T[10];
      T[0] = mfma(kf[0], kf[2], z4); T[1] = mfma(kf[0], kf[3], z4); T[2] = mfma(kf[1], kf[2], z4); T[3] = mfma(kf[1], kf[3], z4);
      T[4] = mfma(kf[1], kf[4], z4); T[5] = mfma(kf[2], kf[3], z4); T[6] = mfma(kf[2], kf[4], z4);
      T[7] = mfma(kf[0], ones, z4); T[8] = mfma(kf[1], ones, z4); T[9] = mfma(kf[2], ones, z4);
      STAMP(28);
      LAS f32x4* red = (LAS f32x4*)(L + L_RED);
#pragma unroll
      for (int i = 0; i < 10; ++i) red[(w * 10 + i) * 64 + lane] = T[i];
      BAR();
    }
    STAMP(4);
    f32x4 qa[2][3];
    {
      unsigned bp0 = lds0 + L_BQ + (lane >> 2) * 64 + (lane & 3) * 8 + (g & 1) * 32;
      unsigned bp1 = lds0 + L_BQ + (lane >> 2) * 64 + (lane & 3) * 8 + 32 - (g & 1) * 32;
      unsigned bp2 = lds0 + L_BQ + 2048 + (lane >> 2) * 16 + (lane & 1) * 8;
      PIN(bp0); PIN(bp1); PIN(bp2);
      kloop<2, 3, 20, 2, true>(qa,
        [&](int ks, int s, int h) {
          return s == 0 ? rdtr((LAS char*)bp0 + ks * 2560 + h * 1024) : s == 1 ? rdtr((LAS char*)bp1 + ks * 2560 + h * 1024) : rdtr((LAS char*)bp2 + ks * 2560 + h * 256);
        },
        [&](int ks, int m) { return a[ks][m]; },
        [&](int ks) {});
    }
    v4u pq0[2], pq1[2];
#pragma unroll
    for (int m = 0; m < 2; ++m) {
      pq0[m].x = pk2(phi2(lo2(qa[m][0]))); pq0[m].y = pk2(phi2(hi2(qa[m][0])));
      pq0[m].z = pk2(phi2(lo2(qa[m][1]))); pq0[m].w = pk2(phi2(hi2(qa[m][1])));
      const unsigned lo = pk2(phi2(lo2(qa[m][2]))), hi = pk2(phi2(hi2(qa[m][2])));
      pq1[m].x = g < 2 ? lo : 0u; pq1[m].y = g < 2 ? hi : 0u; pq1[m].z = 0u; pq1[m].w = 0u;
      PIN(pq0[m]); PIN(pq1[m]);
    }
    STAMP(5);
    BAR_RAW();
#pragma unroll
    for (int j = 0; j < 3; ++j)
      xr[j] = __builtin_amdgcn_raw_buffer_load_b128(rx, sel(j < 2 || g < 2, t * 1280 + g * 16), (4 * r64 + w) * 16 * 1280 + c8 * 160 + j * 64, 0);
    STAMP(17);
    {
      v4u kv[4][2];
#pragma unroll
      for (int es = 3; es >= 0; --es)
#pragma unroll
        for (int ks = 0; ks < 2; ++ks) kv[es][ks] = *(LAS v4u*)(L + L_KVF + (es * 2 + ks) * 1024 + lane * 16);
      SCHED();
      f32x4 o[2][4];
#pragma unroll
      for (int es = 3; es >= 0; --es)
#pragma unroll
        for (int m = 0; m < 2; ++m) o[m][es] = mfma(kv[es][0], pq0[m], f32x4{0.f, 0.f, 0.f, 0.f});
#pragma unroll
      for (int es = 3; es >= 0; --es)
#pragma unroll
        for (int m = 0; m < 2; ++m) o[m][es] = mfma(kv[es][1], pq1[m], o[m][es]);
#pragma unroll
      for (int m = 0; m < 2; ++m) {
        const float i0 = __builtin_amdgcn_rcpf(o[m][3][0] + 1e-6f), i1 = __builtin_amdgcn_rcpf(o[m][3][1] + 1e-6f);
        const float im = g == 0 ? i0 : i1;
        v4u ov;
        ov.x = pk2(lo2(o[m][0]) * i0); ov.y = pk2(hi2(o[m][0]) * i0);
        ov.z = pk2(lo2(o[m][1]) * im); ov.w = pk2(hi2(o[m][1]) * im);
        v2u ol; ol.x = pk2(lo2(o[m][2]) * i1); ol.y = pk2(hi2(o[m][2]) * i1);
        const int tt = 2 * grp32 + m;
        st128(ov, rs, lane * 16, WS_O + (tt * 20 + c16) * 1024);
        st64(ol, rs, g < 2 ? lane * 8 : OOB, WS_O + (tt * 20 + 16) * 1024 + c16 * 256);
      }
      STAMP(18);
      publish(FLAG(2, grp32) + c16, tag, lane);
    }
    STAMP(7);
  }

  {
    const int tt = 4 * r64 + w;
    v4u eye[2];
#pragma unroll
    for (int hf = 0; hf < 2; ++hf) {
      const int e = 16 * hf + t - 8 * g;
#pragma unroll
      for (int d = 0; d < 4; ++d) eye[hf][d] = e == 2 * d ? 0x3F80u : e == 2 * d + 1 ? 0x3F800000u : 0u;
      PIN(eye[hf]);
    }
    wait_flag(FLAG(2, tt >> 1) + lane, 16, tag, lane);
    STAMP(8);
    if (b == 0 && tid == 0) __hip_atomic_store((gu32*)(ws + WS_EPOCH), tag, RLX_AGENT);
    v4u a[20];
#pragma unroll
    for (int ks = 0; ks < 2 * S0; ++ks) a[ks] = ld128(rs, lane * 16, WS_O + (tt * 20 + ks) * 1024);
    SCHED();
    f32x4 acc[1][5];
    const LBase bp(lds0 + L_WO + (lane >> 2) * 160 + (lane & 3) * 8);
    kloop<1, 5, 20, 1, true>(acc, [&](int ks, int s, int h) { return bp.rd(ks * 5120 + h * 2560 + s * 32); },
      [&](int ks, int m) { return a[ks]; },
      [&](int ks) {
#pragma unroll
        for (int j = ld_hi(ks - 1, 2 * S0, 2 * AW, 20); j < ld_hi(ks, 2 * S0, 2 * AW, 20); ++j)
          a[j] = j < 16 ? ld128(rs, lane * 16, WS_O + (tt * 20 + j) * 1024) : ld_split(rs, lane, WS_O + (tt * 20 + j) * 1024);
        if (ks == 8) PROG(2);
        if (ks == 14) PROG(3);
      });
    PROG(4);
#pragma unroll
    for (int s = 0; s < 5; ++s) acc[0][s] = mfma(eye[s & 1], xr[s >> 1], acc[0][s]);
    STAMP(9);
    v2u hb[5];
    f32x2 hv[10];
#pragma unroll
    for (int s = 0; s < 5; ++s) {
      hb[s].x = pk2(lo2(acc[0][s]));
      hb[s].y = pk2(hi2(acc[0][s]));
      hv[2 * s] = bf2(hb[s].x); hv[2 * s + 1] = bf2(hb[s].y);
    }
    st128(v4u{hb[0].x, hb[0].y, hb[1].x, hb[1].y}, rs, lane * 16, WS_H + (tt * 20 + 2 * c8) * 1024);
    st128(v4u{hb[2].x, hb[2].y, hb[3].x, hb[3].y}, rs, lane * 16, WS_H + (tt * 20 + 2 * c8 + 1) * 1024);
    st64(hb[4], rs, lane * 8, WS_H + (tt * 20 + 16) * 1024 + c8 * 512);
    const f32x2 s2 = ((hv[0] + hv[1]) + (hv[2] + hv[3])) + ((hv[4] + hv[5]) + (hv[6] + hv[7])) + (hv[8] + hv[9]);
    const float mc = xsum32(xsum16(s2[0] + s2[1])) * (1.f / 80.f);
    f32x2 q2 = {0.f, 0.f}, q3 = {0.f, 0.f};
#pragma unroll
    for (int j = 0; j < 10; j += 2) { const f32x2 d0 = hv[j] - mc, d1 = hv[j + 1] - mc; q2 += d0 * d0; q3 += d1 * d1; }
    q2 += q3;
    const float m2 = xsum32(xsum16(q2[0] + q2[1]));
    st64(v2u{__builtin_bit_cast(unsigned, mc), __builtin_bit_cast(unsigned, m2)}, rs, g == 0 ? t * 8 : OOB, WS_ST + (tt * 8 + c8) * 128);
    STAMP(20);
    publish(FLAG(3, tt >> 1) + (tt & 1) * 8 + c8, tag, lane);
    STAMP(10);
  }

  {
    wait_flag(FLAG(3, grp32) + lane, 16, tag, lane);
    STAMP(11);
    v4u stt[2];
#pragma unroll
    for (int m = 0; m < 2; ++m) {
      const v2u s0 = ld64(rs, g * 256 + t * 8, WS_ST + (2 * grp32 + m) * 1024), s1 = ld64(rs, g * 256 + 128 + t * 8, WS_ST + (2 * grp32 + m) * 1024);
      stt[m] = v4u{s0.x, s0.y, s1.x, s1.y};
    }
    v4u a[20][2];
    const int abase = WS_H + 2 * grp32 * 20 * 1024;
#pragma unroll
    for (int ks = 0; ks < S0; ++ks)
#pragma unroll
      for (int m = 0; m < 2; ++m) a[ks][m] = ld128(rs, lane * 16, abase + (m * 20 + ks) * 1024);
    SCHED();
    float nm[2], rr[2];
#pragma unroll
    for (int m = 0; m < 2; ++m) {
      const f32x4 sv = __builtin_bit_cast(f32x4, stt[m]);
      const float mean = xsum32(xsum16(sv[0] + sv[2])) * 0.125f;
      const float m2 = xsum32(xsum16(sv[1] + sv[3] + 80.f * ((sv[0] - mean) * (sv[0] - mean) + (sv[2] - mean) * (sv[2] - mean))));
      const float var = m2 * (1.f / D);
      const float r1 = __builtin_amdgcn_rsqf(var + LN_EPS);
      rr[m] = r1 * __builtin_amdgcn_rsqf(var * r1 * r1 + LN_EPS);
      nm[m] = -mean * rr[m];
      PIN(rr[m]); PIN(nm[m]);
    }
    BAR_RAW();
    STAMP(22);
    const unsigned l64 = lds0 + (lane >> 2) * 64 + (lane & 3) * 8;
    const LBase bp0(l64 + (g & 1) * 32), bp1(l64 + 32 - (g & 1) * 32);
    const int h1base = WS_H1 + 2 * grp32 * 48 * 1024;
    auto finish = [&](const f32x4& c0, const f32x4& c1, int j, int m) {
      v4u o;
#pragma unroll
      for (int s = 0; s < 2; ++s) {
        const f32x4 cs = *(LAS f32x4*)(L + L_CS + ((2 * j + s) * 4 + g) * 16), bb = *(LAS f32x4*)(L + L_B1 + ((2 * j + s) * 4 + g) * 16);
        const f32x4 c = s ? c1 : c0;
        o[2 * s] = pk2(gelu2(lo2(c) * rr[m] + (lo2(cs) * nm[m] + lo2(bb))));
        o[2 * s + 1] = pk2(gelu2(hi2(c) * rr[m] + (hi2(cs) * nm[m] + hi2(bb))));
      }
      st128(o, rs, lane * 16, h1base + (m * 48 + (c16 >> 3) * 24 + (j < 2 ? 2 * (c16 & 7) + j : 16 + (c16 & 7))) * 1024);
    };
    {
      f32x4 acc[2][4];
      kloop<2, 4, 20, 1, true>(acc, [&](int ks, int s, int h) { return ((s & 1) ? bp1 : bp0).rd(L_W1 + (s >> 1) * 40960 + ks * 2048 + h * 1024); },
        [&](int ks, int m) { return a[ks][m]; },
        [&](int ks) {
#pragma unroll
          for (int j = ld_hi(ks - 1, S0, AW, 20); j < ld_hi(ks, S0, AW, 20); ++j)
#pragma unroll
            for (int m = 0; m < 2; ++m)
              a[j][m] = j < 16 ? ld128(rs, lane * 16, abase + (m * 20 + j) * 1024) : ld_split(rs, lane, abase + (m * 20 + j) * 1024);
          if (ks == 20 - AW) PROG(5);
        });
      PROG(6);
      STAMP(12);
#pragma unroll
      for (int j = 0; j < 2; ++j)
#pragma unroll
        for (int m = 0; m < 2; ++m) finish(acc[m][2 * j], acc[m][2 * j + 1], j, m);
      SCHED();
      STAMP(24);
    }
    f32x4 accb[2][2];
    kloop<2, 2, 20, 3, true>(accb, [&](int ks, int s, int h) { return (s ? bp1 : bp0).rd(L_W1 + 81920 + ks * 2048 + h * 1024); },
      [&](int ks, int m) { return a[ks][m]; },
      [&](int ks) { if (ks == PUBA) publish(FLAG(4, grp32) + c16, tag, lane); });
    STAMP(13);
    BAR_RAW();
    STAMP(14);
    constexpr int WIN = 8;
    v4u ar[WIN][2];
    const int kh = w & 1;
    const int a4 = WS_H1 + ((4 * r64 + (w & 2)) * 48 + kh * 24) * 1024;
#pragma unroll
    for (int ks = 0; ks < WIN - 1; ++ks)
#pragma unroll
      for (int m = 0; m < 2; ++m) ar[ks][m] = ld128(rs, lane * 16, a4 + (m * 48 + ks) * 1024);
    SCHED();
#pragma unroll
    for (int m = 0; m < 2; ++m) finish(accb[m][0], accb[m][1], 2, m);
    STAMP(29);
    const int tt = 4 * r64 + w;
    v4u hr01, hr23;
    v2u hr4;
    f32x4 acc[2][5];
    const LBase bp(lds0 + L_W2 + kh * 5120 + (lane >> 2) * 160 + (lane & 3) * 8);
    kloop<2, 5, 24, 1, true>(acc, [&](int ks, int s, int h) { return bp.rd((ks % 15) * 10240 + h * 2560 + s * 32); },
      [&](int ks, int m) { return ar[ks % WIN][m]; },
      [&](int ks) {
        if (ks == 5) PROG(7);
        if (ks == 9 || ks == 14 || ks == 19) BAR_RAW();
        if (ks == 3) publish(FLAG(10, grp32) + c16, tag, lane);
        if (ks >= 3) {
#pragma unroll
          for (int j = ks == 3 ? WIN - 1 : ld_hi(ks - 1, 1, WIN - 1, 24); j < ld_hi(ks, 1, WIN - 1, 24); ++j)
#pragma unroll
            for (int m = 0; m < 2; ++m) ar[j % WIN][m] = ld128(rs, lane * 16, a4 + (m * 48 + j) * 1024);
        }
        if (ks + WIN - 1 == 24) {
          hr01 = ld128(rs, lane * 16, WS_H + (tt * 20 + 2 * c8) * 1024);
          hr23 = ld128(rs, lane * 16, WS_H + (tt * 20 + 2 * c8 + 1) * 1024);
          hr4 = ld64(rs, lane * 8, WS_H + (tt * 20 + 16) * 1024 + c8 * 512);
        }
      });
    STAMP(15);
    LAS f32x4* xch = (LAS f32x4*)(L + L_W2 + 102400);
#pragma unroll
    for (int s = 0; s < 5; ++s) xch[((w ^ 1) * 5 + s) * 64 + lane] = kh ? acc[0][s] : acc[1][s];
    BAR();
    const __amdgpu_buffer_rsrc_t rout = __builtin_amdgcn_make_buffer_rsrc((void*)out, 0, S * D * 2, 0x00020000);
    const unsigned hrr[10] = {hr01.x, hr01.y, hr01.z, hr01.w, hr23.x, hr23.y, hr23.z, hr23.w, hr4.x, hr4.y};
#pragma unroll
    for (int s = 0; s < 5; ++s) {
      const f32x4 bb = *(LAS f32x4*)(L + L_B2 + (s * 4 + g) * 16), ot = xch[(w * 5 + s) * 64 + lane];
      const f32x4 v = kh ? ot + acc[1][s] : acc[0][s] + ot;
      v2u o;
      o.x = pk2(lo2(v) + lo2(bb) + bf2(hrr[2 * s]));
      o.y = pk2(hi2(v) + hi2(bb) + bf2(hrr[2 * s + 1]));
      __builtin_amdgcn_raw_buffer_store_b64(o, rout, ((tt * 16 + t) * D + 80 * c8 + 16 * s + 4 * g) * 2, 0, 0);
    }
    STAMP(16);
  }
  return;
  }

  {
    const v4u srd_ws = mk_srd(ws, WS_BYTES);
    STAMP(16);
    const int q5 = lane / 5, c5 = lane % 5;
    const int f40m = c5 < 4 ? 512 : 128, f40l = c5 < 4 ? q5 * 64 + (c5 ^ (2 * ((q5 >> 2) & 1))) * 16 : 2048 + q5 * 16;
    v2u ub1, ub2;
    unsigned pf0 = 0;
    if (hw == 0) {
      const int dof = sel(lane < 40, lane * 16);
#pragma unroll
      for (int j = 0; j < 10; ++j) {
        const int k = 40 * r128 + 4 * j, R = (k & ~31) + 16 * ((k >> 2) & 1) + 4 * ((k >> 3) & 3);
        st128(tv[j], rs, dof, WS_TKV + c16 * 153600 + R * 160);
      }
      STAMP(18);
      PIN(tag); tag += 1u;
      publish(FLAG(6, c16) + r128, tag, lane);
      STAMP(13);
    } else if (hw == 1) {
      const __amdgpu_buffer_rsrc_t ro = __builtin_amdgcn_make_buffer_rsrc((void*)wo, 0, D * D * 2, 0x00020000);
      const int vq = sel(lane < 40, (8 * (q5 >> 2) + (q5 & 3)) * 3840 + c5 * 16), vo = sel(lane < 40, q10 * 1280 + c10 * 16);
#pragma unroll
      for (int j = 0; j < 5; ++j) {
        const int i8 = 5 * r128 + j, k = 32 * (i8 >> 2) + 16 * (i8 & 1) + 4 * ((i8 >> 1) & 1);
        tv[j] = __builtin_amdgcn_raw_buffer_load_b128(rq, vq, k * 3840 + c16 * 80, 0);
      }
#pragma unroll
      for (int j = 0; j < 5; ++j) tv[5 + j] = __builtin_amdgcn_raw_buffer_load_b128(ro, vo, (20 * r64 + 4 * j) * 1280 + c8 * 160, 0);
    } else if (hw == 2) {
      const GAS unsigned char* cb = (const GAS unsigned char*)(pc0 & ~127ull);
      if (lane < 11) pf0 = *(const GAS unsigned*)(cb + (lane * 32 + bi) * 128);
    } else if (hw == 3) {
      const __amdgpu_buffer_rsrc_t rb1 = __builtin_amdgcn_make_buffer_rsrc((void*)b1, 0, F * 2, 0x00020000);
      const __amdgpu_buffer_rsrc_t rb2 = __builtin_amdgcn_make_buffer_rsrc((void*)b2, 0, D * 2, 0x00020000);
      ub1 = __builtin_amdgcn_raw_buffer_load_b64(rb1, lane * 8, c16 * 192, 0);
      ub2 = __builtin_amdgcn_raw_buffer_load_b64(rb2, lane * 8, c8 * 160, 0);
    }
    asm volatile("" ::: "memory");
    int red_off, kvf_off, kvp_off;
    {
      const int it = ht < 210 ? ht : 209, ie = it / 5, ic = it % 5;
      const int ihead = ie < 40 ? (ie >= 20) : ie - 40;
      const int id = 20 * ihead + 4 * ic, dg = id >> 2;
      const int sa = id >> 4, col = 40 + ie;
      const int tile = ie < 40 ? 2 * sa + (col >> 4) - 2 : 7 + sa;
      red_off = lds0 + L_RED + (tile * 64 + (dg & 3) * 16 + (ie < 40 ? (col & 15) : 0)) * 16;
      kvf_off = lds0 + L_KVF + ((ie < 40 ? (ie >> 4) : 3) * 2 + (dg >> 3)) * 1024 + ((dg & 3) * 16 + (ie < 40 ? (ie & 15) : ihead)) * 16 + ((dg >> 2) & 1) * 8;
      kvp_off = WS_KVP + (c16 * 16 + r128) * 4096 + ht * 16;
      PIN(red_off); PIN(kvf_off); PIN(kvp_off);
    }
    int vo12, td[12];
    if (hw >= 2) {
      const int q12 = lane / 12, c12 = lane % 12;
      vo12 = hw == 2 ? sel(lane < 48, q12 * 3072 + c12 * 16) : sel(lane < 40, q10 * 1280 + c10 * 16);
#pragma unroll
      for (int j = 0; j < 12; ++j) {
        const int cc = r128 >> 1, off = 40 * (r128 & 1) + 4 * j;
        const int R = off < 64 ? 64 * cc + off : 32 * (16 + (cc >> 1)) + 16 * (cc & 1) + off - 64;
        const int d1 = WS_T1 + c16 * 122880 + (c12 >> 2) * 40960 + (R + q12) * 64 + ((c12 & 3) ^ (2 * ((R >> 2) & 1))) * 16;
        const int pc = r64 >> 1, ko = 48 * (r64 & 1) + 4 * j, jj = ko >> 5;
        const int d2 = WS_T2 + c8 * 245760 + (2 * (jj < 2 ? 2 * (pc & 7) + jj : 16 + (pc & 7)) + (pc >> 3)) * 5120 + (ko & 31) * 160 + lane * 16;
        td[j] = sel(hw == 2 ? (lane < 48 && j < 10) : lane < 40, hw == 2 ? d1 : d2);
        PIN(td[j]);
      }
      PIN(vo12);
    }
    int l16 = lane * 16, h16 = ht * 16;
    PIN(l16); PIN(h16);
    gu32* fp1 = FLAG(1, c16) + lane;
    gu32* fp78 = lane < 32 ? FLAG(7, c8) + lane : FLAG(8, c16) + t;
    gu32* fp4 = FLAG(4, 2 * r64 + (g & 1)) + t;
    gu32* fp1w = FLAG(1, c16) + r128 * 4 + hw;
    PIN(fp1); PIN(fp78); PIN(fp4); PIN(fp1w);
    ((LAS v4u*)(L + L_KVF))[ht] = v4u{0u, 0u, 0u, 0u};
    ((LAS v4u*)(L + L_KVF))[256 + ht] = v4u{0u, 0u, 0u, 0u};
    asm volatile("" ::: "memory");
    if (hw != 0) {
      if (hw == 1) {
#pragma unroll
        for (int j = 0; j < 5; ++j) {
          const int i8 = 5 * r128 + j;
          st128(tv[j], rs, sel(lane < 40, (i8 & 3) * f40m + f40l), WS_TKV + c16 * 153600 + 102400 + (i8 >> 2) * 2560);
        }
#pragma unroll
        for (int j = 0; j < 5; ++j) {
          const int pp = r64 >> 1, e = 20 * (r64 & 1) + 4 * j;
          const int R = e < 32 ? 32 * pp + e : 32 * (16 + (pp >> 2)) + 8 * (pp & 3) + e - 32;
          st128(tv[5 + j], rs, sel(lane < 40, lane * 16), WS_TO + c8 * 102400 + R * 160);
        }
      }
      if (hw == 3) {
        if (lane < 24) *(LAS f32x4*)(L + L_B1 + lane * 16) = f32x4{bf_lo(ub1.x), bf_hi(ub1.x), bf_lo(ub1.y), bf_hi(ub1.y)};
        if (lane < 20) *(LAS f32x4*)(L + L_B2 + lane * 16) = f32x4{bf_lo(ub2.x), bf_hi(ub2.x), bf_lo(ub2.y), bf_hi(ub2.y)};
      }
      asm volatile("" :: "v"(pf0));
      PIN(tag); tag += 1u;
      DRAIN();
      if (hw == 1 && lane < 2) flag_set(lane == 0 ? FLAG(8, c16) + r128 : FLAG(7, c8) + r64, tag);
    }
    if (hw == 0) {
      wait_flag(FLAG(6, c16) + lane, 16, tag, lane);
      sig[0] = 1u;
    }
    sig_wait(sig);
    STAMP(1);
    const int kvq = WS_TKV + c16 * 153600, wos = WS_TO + c8 * 102400;
    dma_image<0, 24>(lds0 + L_BKV, l16, srd_ws, kvq, hw, 3 * (bi >> 3));
    dma_image<24, 56>(lds0 + L_BKV, l16, srd_ws, kvq, hw, 4 * (bi >> 3));
    asm volatile("s_waitcnt vmcnt(8)" ::: "memory");
    STAMP(2);
    BAR_RAW();
    dma_image<56, 100>(lds0 + L_BKV, l16, srd_ws, kvq, hw, 5 * (bi >> 3));
    asm volatile("s_waitcnt vmcnt(10)" ::: "memory");
    BAR_RAW();
    if (hw == 1) wait_flag(fp78, 48, tag, lane);
    DRAIN();
    STAMP(3);
    BAR_RAW();
    if (QPOS == 0) dma_image<100, 150>(lds0 + L_BKV, l16, srd_ws, kvq, hw, 6 * (bi >> 3));
    prog_wait(prog, 1);
    if (QPOS == 1) dma_image<100, 150>(lds0 + L_BKV, l16, srd_ws, kvq, hw, 6 * (bi >> 3));
    if (hw >= 2) {
      const __amdgpu_buffer_rsrc_t r12 = __builtin_amdgcn_make_buffer_rsrc((void*)(hw == 2 ? w1 : w2), 0, D * F * 2, 0x00020000);
      const int base = hw == 2 ? r128 * 122880 + c16 * 192 : r64 * 61440 + c8 * 160, step = hw == 2 ? 4 * 3072 : 4 * 1280;
#pragma unroll
      for (int j = 0; j < 12; ++j) tv[j] = __builtin_amdgcn_raw_buffer_load_b128(r12, vo12, (j < 10 || hw == 3) ? base + j * step : OOB, 0);
      asm volatile("s_waitcnt vmcnt(12)" ::: "memory");
    } else {
      dma_image<40, 68, 0, 2>(lds0 + L_WO, l16, srd_ws, wos, hw, bi >> 2);
      asm volatile("s_waitcnt vmcnt(14)" ::: "memory");
    }
    STAMP(4);
    BAR();
    __builtin_amdgcn_s_setprio(2);
    STAMP(11);
    {
      f32x4 pv[4];
#pragma unroll
      for (int ww = 0; ww < 4; ++ww) pv[ww] = *(LAS f32x4*)((LAS char*)red_off + ww * 10240);
      st128(__builtin_bit_cast(v4u, add4(add4(pv[0], pv[1]), add4(pv[2], pv[3]))), rs, kvp_off, 0);
      PROG(1);
      STAMP(12);
      DRAIN();
    }
    if (lane == 0) flag_set(fp1w, tag);
    if (hw == 0) {
      STAMP(10);
      __builtin_amdgcn_s_sleep(H1W);
      wait_flag(fp1, 64, tag, lane);
      sig[2] = 1u;
    } else {
      if (H1G) __builtin_amdgcn_s_sleep(H1G);
      dma_image<68, 100, 0, 3>(lds0 + L_WO, l16, srd_ws, wos, hw - 1, bi >> 2);
      prog_wait(prog + 1, 1);
      dma_image<0, 40, 0, 3>(lds0 + L_WO, l16, srd_ws, wos, hw - 1, 2 * (bi >> 2));
      if (hw >= 2) {
#pragma unroll
        for (int j = 0; j < 12; ++j) st128(tv[j], rs, td[j], 0);
      }
      STAMP(10);
      sig_wait(sig + 2);
    }
    STAMP(5);
    {
      v4u pv[16];
#pragma unroll
      for (int r = 0; r < 16; ++r) pv[r] = ld128(rs, h16, WS_KVP + (c16 * 16 + r) * 4096);
      SCHED();
      f32x4 v = __builtin_bit_cast(f32x4, pv[0]);
#pragma unroll
      for (int r = 1; r < 16; ++r) v = add4(v, __builtin_bit_cast(f32x4, pv[r]));
      v2u o; o.x = pk2(lo2(v)); o.y = pk2(hi2(v));
      *(LAS v2u*)((LAS char*)kvf_off) = o;
      if (ht >= 200) {
#pragma unroll
        for (int c = 1; c < 4; ++c) *(LAS v2u*)((LAS char*)kvf_off + c * 64) = o;
      }
    }
    DRAIN();
    STAMP(6);
    BAR();
    __builtin_amdgcn_s_setprio(0);
    if (hw >= 2 && lane == 0) flag_set(hw == 2 ? FLAG(5, c16) + r128 : FLAG(11, c8) + r64, tag);
    if (hw == 2) {
      f32x4 s0 = {0.f, 0.f, 0.f, 0.f}, s1 = {0.f, 0.f, 0.f, 0.f};
#pragma unroll
      for (int j = 0; j < 10; ++j) {
        s0 = add4(s0, f32x4{bf_lo(tv[j].x), bf_hi(tv[j].x), bf_lo(tv[j].y), bf_hi(tv[j].y)});
        s1 = add4(s1, f32x4{bf_lo(tv[j].z), bf_hi(tv[j].z), bf_lo(tv[j].w), bf_hi(tv[j].w)});
      }
#pragma unroll
      for (int d = 24; d >= 12; d -= 12) {
        f32x4 u0, u1;
#pragma unroll
        for (int e = 0; e < 4; ++e) { u0[e] = __shfl(s0[e], lane + d); u1[e] = __shfl(s1[e], lane + d); }
        s0 = add4(s0, u0); s1 = add4(s1, u1);
      }
      f32x4 sv;
#pragma unroll
      for (int e = 0; e < 4; ++e) { const float a0 = __shfl(s0[e], lane >> 1), a1 = __shfl(s1[e], lane >> 1); sv[e] = (lane & 1) ? a1 : a0; }
      st128(__builtin_bit_cast(v4u, sv), rs, sel(lane < 24, lane * 16), WS_CS + (c16 * 16 + r128) * 384);
      publish(FLAG(9, c16) + r128, tag, lane);
    }
    if (hw == 1) {
      __builtin_amdgcn_s_sleep(40);
      wait_flag(lane < 32 ? FLAG(11, c8) + lane : FLAG(5, c16) + t, 48, tag, lane);
      sig[3] = 1u;
    }
    sig_wait(sig + 3);
    prog_wait(prog, 2);
    if (hw == 0) {
      wait_flag(FLAG(9, c16) + lane, 16, tag, lane);
      v4u pv[16];
      const int co = sel(lane < 24, l16);
#pragma unroll
      for (int r = 0; r < 16; ++r) pv[r] = ld128(rs, co, WS_CS + (c16 * 16 + r) * 384);
      SCHED();
      f32x4 v = __builtin_bit_cast(f32x4, pv[0]);
#pragma unroll
      for (int r = 1; r < 16; ++r) v = add4(v, __builtin_bit_cast(f32x4, pv[r]));
      if (lane < 24) *(LAS f32x4*)(L + L_CS + lane * 16) = v;
    } else {
      dma_image<70, 120, 0, 3>(lds0 + L_W1, l16, srd_ws, WS_T1 + c16 * 122880, hw - 1, 6 * (bi >> 3));
      prog_wait(prog, 3);
      dma_image<0, 40, 0, 3>(lds0 + L_W1, l16, srd_ws, WS_T1 + c16 * 122880, hw - 1, 5 * (bi >> 3));
      STAMP(7);
      prog_wait(prog, 4);
      dma_image<40, 70, 0, 3>(lds0 + L_W1, l16, srd_ws, WS_T1 + c16 * 122880, hw - 1, 3 * (bi >> 3));
    }
    DRAIN();
    STAMP(8);
    BAR();
    const int w2s = WS_T2 + c8 * 245760;
    prog_wait(prog, 5);
    dma_image<0, 30>(lds0 + L_W2, l16, srd_ws, w2s, hw, bi >> 2);
    STAMP(14);
    prog_wait(prog, 6);
    dma_image<30, 110>(lds0 + L_W2, l16, srd_ws, w2s, hw, 5 * (bi >> 2));
    DRAIN();
    STAMP(9);
    if (hw == 0) wait_flag(fp4, 32, tag, lane);
    STAMP(15);
    BAR_RAW();
    dma_image<110, 150>(lds0 + L_W2, l16, srd_ws, w2s, hw, 2 * (bi >> 2));
    prog_wait(prog, 7);
    if (hw == 0) wait_flag(fp4 + 6 * FLAG_SET / 4, 32, tag, lane);
    else dma_image<150, 198, 0, 3>(lds0 + L_W2 - 153600, l16, srd_ws, w2s, hw - 1, 3 * (bi >> 2));
    asm volatile("s_waitcnt vmcnt(16)" ::: "memory");
    STAMP(17);
    BAR_RAW();
    dma_image<198, 200>(lds0 + L_W2 - 153600, l16, srd_ws, w2s, hw, 0);
    dma_image<200, 240>(lds0 + L_W2 - 153600, l16, srd_ws, w2s, hw, 2 * (bi >> 2));
    asm volatile("s_waitcnt vmcnt(10)" ::: "memory");
    BAR_RAW();
    DRAIN();
    BAR_RAW();
  }

}
}

extern "C" void kernel_launch(void* const* d_in, const int* in_sizes, int n_in, void* d_out, int out_size, void* d_ws,
                              size_t ws_size, hipStream_t stream) {
  const unsigned short* x = (const unsigned short*)d_in[0];
  const unsigned short* wqkv = (const unsigned short*)d_in[1];
  const unsigned short* wo = (const unsigned short*)d_in[2];
  const unsigned short* w1 = (const unsigned short*)d_in[4];
  const unsigned short* b1 = (const unsigned short*)d_in[5];
  const unsigned short* w2 = (const unsigned short*)d_in[6];
  const unsigned short* b2 = (const unsigned short*)d_in[7];
  fused_block<<<256, 512, 0, stream>>>(x, wqkv, wo, w1, b1, w2, b2, (unsigned short*)d_out, (unsigned char*)d_ws);
}
```

```cpp
#include <hip/hip_runtime.h>
#include <hip/hip_bf16.h>

#define STAMPS 0
#define THR 0
#define TOUCH 0
#define CODEPF 1
#define FLAGPF 0
#define OUTAUX 0
#define OUTPF 0
#define S0 6
#define XRPOS 0
#define QPOS 0
#define H1W 8
#define H1G 2
#define W2S 70
#define PUBA 8
#define NCDELAY 0
#define HPRIO 0
#define WOLATE 0
#define SLEEP 1

#define GAS __attribute__((address_space(1)))
#define LAS __attribute__((address_space(3)))
#define RLX_AGENT __ATOMIC_RELAXED, __HIP_MEMORY_SCOPE_AGENT
typedef unsigned v4u __attribute__((ext_vector_type(4)));
typedef unsigned v2u __attribute__((ext_vector_type(2)));
typedef float f32x4 __attribute__((ext_vector_type(4)));
typedef float f32x2 __attribute__((ext_vector_type(2)));
typedef __bf16 bf16x8 __attribute__((ext_vector_type(8)));
typedef __bf16 bf16x2 __attribute__((ext_vector_type(2)));
typedef short s16x4 __attribute__((ext_vector_type(4)));
typedef GAS unsigned gu32;

namespace {
constexpr int S = 2048, D = 640, F = 1536;
constexpr float LN_EPS = 1e-5f;
constexpr int AUX_SC1 = 16;
constexpr unsigned kSpinCap = 1u << 17;
constexpr int OOB = 0x7FFFFF00;

constexpr int WS_EPOCH = 0;
constexpr int WS_FLAG = 4096;
constexpr int FLAG_SET = 64 * 256;
constexpr int WS_XN = 1 << 20;
constexpr int WS_O = WS_XN + 128 * 20 * 1024;
constexpr int WS_H = WS_O + 128 * 20 * 1024;
constexpr int WS_H1 = WS_H + 128 * 20 * 1024;
constexpr int WS_KVP = WS_H1 + 128 * 48 * 1024;
constexpr int WS_ST = WS_KVP + 16 * 16 * 4096;
constexpr int WS_DBG = WS_ST + 2048 * 64;
constexpr int WS_TKV = WS_DBG + 256 * 8 * 32 * 8;
constexpr int WS_TO = WS_TKV + 16 * 153600;
constexpr int WS_T1 = WS_TO + 8 * 102400;
constexpr int WS_T2 = WS_T1 + 16 * 122880;
constexpr int WS_CS = WS_T2 + 16 * 122880;
constexpr int WS_BYTES = WS_CS + 16 * 16 * 384;

constexpr int L_BKV = 0, L_BQ = 102400;
constexpr int L_RED = 0, L_KVF = 153600;
constexpr int L_WO = 0, L_W1 = 30720, L_W2 = 0, L_CS = 161792;
constexpr int L_B1 = L_CS + 1152, L_B2 = L_CS + 1536;
constexpr int LDS_BYTES = 163840;

__device__ __forceinline__ f32x4 mfma(v4u a, v4u b, f32x4 c) {
  return __builtin_amdgcn_mfma_f32_16x16x32_bf16(__builtin_bit_cast(bf16x8, a), __builtin_bit_cast(bf16x8, b), c, 0, 0, 0);
}
__device__ __forceinline__ unsigned pk2(f32x2 v) { return __builtin_bit_cast(unsigned, __builtin_convertvector(v, bf16x2)); }
__device__ __forceinline__ unsigned pk2(float lo, float hi) { return pk2(f32x2{lo, hi}); }
__device__ __forceinline__ float bf_lo(unsigned u) { return __builtin_bit_cast(float, u << 16); }
__device__ __forceinline__ float bf_hi(unsigned u) { return __builtin_bit_cast(float, u & 0xFFFF0000u); }
__device__ __forceinline__ f32x2 bf2(unsigned u) { return f32x2{bf_lo(u), bf_hi(u)}; }
__device__ __forceinline__ f32x2 lo2(f32x4 v) { return f32x2{v[0], v[1]}; }
__device__ __forceinline__ f32x2 hi2(f32x4 v) { return f32x2{v[2], v[3]}; }
__device__ __forceinline__ v4u mk_srd(const void* p, unsigned bytes) {
  const unsigned long long a = (unsigned long long)p;
  v4u r;
  r.x = (unsigned)a; r.y = (unsigned)(a >> 32) & 0xFFFFu; r.z = bytes; r.w = 0x00020000u;
  return r;
}
#define DRAIN() asm volatile("s_waitcnt vmcnt(0)" ::: "memory")
#define BAR() asm volatile("s_waitcnt lgkmcnt(0)\n\ts_barrier" ::: "memory")
#define BAR_RAW() asm volatile("s_barrier" ::: "memory")
#define SCHED() __builtin_amdgcn_sched_barrier(0)
#define PIN(x) asm volatile("" : "+v"(x))

__device__ __forceinline__ void dma2(unsigned lds_addr, unsigned voff, v4u srd, unsigned soff) {
  unsigned keep;
  asm volatile("s_mov_b32 %0, m0\n\ts_mov_b32 m0, %1\n\ts_nop 0\n\t"
               "buffer_load_dwordx4 %2, %3, %4 offen sc1 lds\n\t"
               "buffer_load_dwordx4 %2, %3, %4 offen offset:1024 sc1 lds\n\t"
               "s_mov_b32 m0, %0"
               : "=&s"(keep) : "s"(lds_addr), "v"(voff), "s"(srd), "s"(soff) : "memory");
}
template <int LO, int HI, int TH = 0, int NW = 4>
__device__ __forceinline__ void dma_image(unsigned lds_base, unsigned voff, v4u srd, int ws_off, int hw, int rot) {
  constexpr int NP = (HI - LO) / 2;
#pragma unroll 1
  for (int i = hw; i < NP; i += NW) {
    int j = i + rot;
    if (j >= NP) j -= NP;
    dma2(lds_base + LO * 1024 + j * 2048, voff, srd, ws_off + LO * 1024 + j * 2048);
    if (TH) asm volatile("s_waitcnt vmcnt(%0)" :: "i"(TH - 2) : "memory");
  }
}
__device__ __forceinline__ v2u rdtr(LAS char* p) { return __builtin_bit_cast(v2u, __builtin_amdgcn_ds_read_tr16_b64_v4i16((LAS s16x4*)p)); }
struct LBase {
  unsigned a[3];
  __device__ __forceinline__ explicit LBase(unsigned v) { a[0] = v; a[1] = v + 65536; a[2] = v + 131072; PIN(a[0]); PIN(a[1]); PIN(a[2]); }
  __device__ __forceinline__ v2u rd(int off) const { return rdtr((LAS char*)(a[off >> 16]) + (off & 65535)); }
};
template <int MT, int NT, int KS, int PD, bool WA, int M0 = 0, class RB, class GA, class PRE>
__device__ __forceinline__ void kloop(f32x4 (&acc)[MT][NT], RB rb, GA ga, PRE pre) {
  v2u bl[PD + 1][NT], bh[PD + 1][NT];
#pragma unroll
  for (int m = M0; m < MT; ++m)
#pragma unroll
    for (int s = 0; s < NT; ++s) acc[m][s] = f32x4{0.f, 0.f, 0.f, 0.f};
#pragma unroll
  for (int ks = 0; ks < PD; ++ks)
#pragma unroll
    for (int s = 0; s < NT; ++s) { bl[ks][s] = rb(ks, s, 0); bh[ks][s] = rb(ks, s, 1); }
  SCHED();
#pragma unroll
  for (int ks = 0; ks < KS; ++ks) {
    pre(ks);
#pragma unroll
    for (int s = 0; s < NT; ++s)
#pragma unroll
      for (int m = 0; m < MT; ++m) {
        const v2u lo = bl[ks % (PD + 1)][s], hi = bh[ks % (PD + 1)][s];
        const v4u wv = {lo.x, lo.y, hi.x, hi.y};
        acc[m][s] = WA ? mfma(wv, ga(ks, m), acc[m][s]) : mfma(ga(ks, m), wv, acc[m][s]);
        if (ks + PD < KS) {
          if (MT == 1 || m == 0) bl[(ks + PD) % (PD + 1)][s] = rb(ks + PD, s, 0);
          if (MT == 1 || m == 1) bh[(ks + PD) % (PD + 1)][s] = rb(ks + PD, s, 1);
        }
        SCHED();
      }
  }
}
constexpr int ld_hi(int ks, int s0, int W, int KS) {
  const int a = s0 + 2 * (ks + 1), b = ks + 1 + W, c = a < b ? a : b;
  return c < KS ? c : KS;
}
__device__ __forceinline__ v4u ld128(__amdgpu_buffer_rsrc_t rs, int voff, int soff) {
  return __builtin_amdgcn_raw_buffer_load_b128(rs, voff, soff, AUX_SC1);
}
__device__ __forceinline__ v2u ld64(__amdgpu_buffer_rsrc_t rs, int voff, int soff) {
  return __builtin_amdgcn_raw_buffer_load_b64(rs, voff, soff, AUX_SC1);
}
__device__ __forceinline__ void st128(v4u v, __amdgpu_buffer_rsrc_t rs, int voff, int soff) {
  __builtin_amdgcn_raw_buffer_store_b128(v, rs, (int)((unsigned)voff + (unsigned)soff), 0, AUX_SC1);
}
__device__ __forceinline__ int sel(bool c, int a) {
  PIN(a);
  return c ? a : OOB;
}
__device__ __forceinline__ v4u ld_split(__amdgpu_buffer_rsrc_t rs, int lane, int soff) {
  const v2u a = ld64(rs, lane * 8, soff), b = ld64(rs, lane * 8 + 512, soff);
  return v4u{a.x, a.y, b.x, b.y};
}
__device__ __forceinline__ void st64(v2u v, __amdgpu_buffer_rsrc_t rs, int voff, int soff) {
  __builtin_amdgcn_raw_buffer_store_b64(v, rs, voff, soff, AUX_SC1);
}
__device__ __forceinline__ void flag_set(gu32* p, unsigned tag) {
  asm volatile("global_atomic_swap %0, %1, off sc1" :: "v"(p), "v"(tag) : "memory");
}
__device__ __forceinline__ void wait_flag(gu32* p, int n, unsigned tag, int lane) {
#pragma unroll 1
  for (unsigned spins = 0; spins < kSpinCap; ++spins) {
    unsigned v = tag;
    if (lane < n) asm volatile("global_atomic_add %0, %1, %2, off sc0 sc1\n\ts_waitcnt vmcnt(0)" : "=&v"(v) : "v"(p), "v"(0u) : "memory");
    if (__all(v == tag)) break;
    if (SLEEP) __builtin_amdgcn_s_sleep(SLEEP);
  }
  asm volatile("" ::: "memory");
}
__device__ __forceinline__ void sig_wait(volatile LAS unsigned* p) {
#pragma unroll 1
  for (unsigned spins = 0; spins < 64 * kSpinCap; ++spins) {
    if (*p != 0u) break;
    __builtin_amdgcn_s_sleep(1);
  }
  asm volatile("" ::: "memory");
}
__device__ __forceinline__ void prog_wait(volatile LAS v4u* p, unsigned stage) {
#pragma unroll 1
  for (unsigned spins = 0; spins < 64 * kSpinCap; ++spins) {
    const v4u v = *p;
    if (min(min(v.x, v.y), min(v.z, v.w)) >= stage) break;
    __builtin_amdgcn_s_sleep(1);
  }
  asm volatile("" ::: "memory");
}
__device__ __forceinline__ void publish(gu32* flag, unsigned tag, int lane) {
  DRAIN();
  if (lane == 0) flag_set(flag, tag);
}
__device__ __forceinline__ f32x4 add4(f32x4 a, f32x4 b) {
  f32x4 r;
#pragma unroll
  for (int i = 0; i < 4; ++i) { float x; asm("v_add_f32 %0, %1, %2" : "=v"(x) : "v"(a[i]), "v"(b[i])); r[i] = x; }
  return r;
}
__device__ __forceinline__ float xsum32(float x) {
  unsigned u = __builtin_bit_cast(unsigned, x), v = u;
  asm("" : "+v"(v));
  auto r = __builtin_amdgcn_permlane32_swap(u, v, false, false);
  const unsigned a = r[0], b = r[1];
  return __builtin_bit_cast(float, a) + __builtin_bit_cast(float, b);
}
__device__ __forceinline__ float xsum16(float x) {
  unsigned u = __builtin_bit_cast(unsigned, x), v = u;
  asm("" : "+v"(v));
  auto r = __builtin_amdgcn_permlane16_swap(u, v, false, false);
  const unsigned a = r[0], b = r[1];
  return __builtin_bit_cast(float, a) + __builtin_bit_cast(float, b);
}
__device__ __forceinline__ float xsum8(float x) {
  return x + __builtin_bit_cast(float, __builtin_amdgcn_update_dpp(0, __builtin_bit_cast(int, x), 0x128, 0xf, 0xf, false));
}
__device__ __forceinline__ f32x2 phi2(f32x2 t) {
  const f32x2 u = t * 1.4426950408889634f, v = t + 1e-6f;
  const f32x2 e = {__builtin_amdgcn_fmed3f(__builtin_amdgcn_exp2f(u[0]), 0.f, 1.f), __builtin_amdgcn_fmed3f(__builtin_amdgcn_exp2f(u[1]), 0.f, 1.f)};
  return f32x2{fmaxf(v[0], 1e-6f), fmaxf(v[1], 1e-6f)} + e;
}
__device__ __forceinline__ f32x2 gelu2(f32x2 x) {
  const f32x2 u = x * (x * x * -0.1029432397f + -2.302208198f);
  const f32x2 d = f32x2{__builtin_amdgcn_exp2f(u[0]), __builtin_amdgcn_exp2f(u[1])} + 1.f;
  return x * f32x2{__builtin_amdgcn_rcpf(d[0]), __builtin_amdgcn_rcpf(d[1])};
}

#define STAMP(i) do { } while (0)

__global__ void __launch_bounds__(512, 2)
fused_block(const unsigned short* __restrict__ x, const unsigned short* __restrict__ wqkv, const unsigned short* __restrict__ wo,
            const unsigned short* __restrict__ w1, const unsigned short* __restrict__ b1, const unsigned short* __restrict__ w2,
            const unsigned short* __restrict__ b2, unsigned short* __restrict__ out, unsigned char* ws) {
  __shared__ __attribute__((aligned(16))) char smem[LDS_BYTES];
  unsigned long long pc0;
  asm volatile("s_getpc_b64 %0" : "=s"(pc0));
  LAS char* L = (LAS char*)smem;
  const unsigned lds0 = (unsigned)(size_t)L;
  const int tid = threadIdx.x, lane = tid & 63, w = __builtin_amdgcn_readfirstlane(tid >> 6);
  const int b = blockIdx.x, xcd = b & 7, bi = b >> 3;
  const int g = lane >> 4, t = lane & 15;
  const __amdgpu_buffer_rsrc_t rs = __builtin_amdgcn_make_buffer_rsrc((void*)ws, 0, WS_BYTES, 0x00020000);
  gu32* flags = (gu32*)(ws + WS_FLAG);
#define FLAG(set, grp) (flags + ((set) * FLAG_SET + (grp) * 256) / 4)
  asm volatile("" :: "s"(x), "s"(wqkv), "s"(wo), "s"(w1), "s"(b1), "s"(w2), "s"(b2), "s"(out), "s"(ws));
  STAMP(0);
  const int r128 = 4 * (xcd >> 1) + (bi >> 3), c16 = 8 * (xcd & 1) + (bi & 7);
  const int r64 = 8 * (xcd >> 1) + (bi >> 2), c8 = 4 * (xcd & 1) + (bi & 3);

  const __amdgpu_buffer_rsrc_t rx = __builtin_amdgcn_make_buffer_rsrc((void*)x, 0, S * D * 2, 0x00020000);
  const int hw = w - 4, ht = tid - 256;
  volatile LAS unsigned* sig = (volatile LAS unsigned*)(L + L_CS + 1024);
  volatile LAS v4u* prog = (volatile LAS v4u*)(L + L_CS + 1088);
#define PROG(n) do { asm volatile("" ::: "memory"); sig[16 + w] = (n); } while (0)

  unsigned tag = __hip_atomic_load((gu32*)(ws + WS_EPOCH), RLX_AGENT);
  const __amdgpu_buffer_rsrc_t rq = __builtin_amdgcn_make_buffer_rsrc((void*)wqkv, 0, D * 3 * D * 2, 0x00020000);
  v4u tv[12];
  const int q10 = lane / 10, c10 = lane % 10;
  if (w < 4) {
#pragma unroll
    for (int j = 0; j < 3; ++j)
      tv[j] = __builtin_amdgcn_raw_buffer_load_b128(rx, (lane & 7) * 1280 + (lane >> 3) * 16, (j < 2 || w < 2) ? 8 * b * 1280 + (w + 4 * j) * 128 : OOB, 0);
  } else if (w == 4) {
    const int vo = sel(lane < 40, q10 * 3840 + (c10 < 5 ? 1280 : 2480) + c10 * 16);
#pragma unroll
    for (int j = 0; j < 10; ++j) tv[j] = __builtin_amdgcn_raw_buffer_load_b128(rq, vo, (40 * r128 + 4 * j) * 3840 + c16 * 80, 0);
  } else if (w == 7) {
    *(LAS v4u*)(L + L_CS + 512 + lane * 16) = v4u{0u, 0u, 0u, 0u};
  }
  BAR();

  if (w < 4) {
  const int grp32 = 4 * r128 + w;
  constexpr int AW = 6;

  {
    const int tok = 8 * b + (lane & 7), cl = lane >> 3;
    const bool has3 = w < 2;
    int xd = ((tok >> 4) * 20 + 2 * w + (cl >> 2)) * 1024 + ((cl & 3) * 16 + (tok & 15)) * 16;
    PIN(xd);
    SCHED();
    f32x2 f[12];
#pragma unroll
    for (int j = 0; j < 3; ++j)
#pragma unroll
      for (int e = 0; e < 4; ++e) f[j * 4 + e] = bf2(tv[j][e]);
    f32x2 s2 = (f[0] + f[1]) + (f[2] + f[3]) + ((f[4] + f[5]) + (f[6] + f[7])) + ((f[8] + f[9]) + (f[10] + f[11]));
    const float mloc = xsum32(xsum16(xsum8(s2[0] + s2[1]))) * (has3 ? 1.f / 192.f : 1.f / 128.f);
    const float m3 = has3 ? mloc : 0.f;
    f32x2 q2 = {0.f, 0.f}, q3 = {0.f, 0.f};
#pragma unroll
    for (int j = 0; j < 12; j += 2) {
      const f32x2 d0 = f[j] - (j < 8 ? mloc : m3), d1 = f[j + 1] - (j < 8 ? mloc : m3);
      q2 += d0 * d0; q3 += d1 * d1;
    }
    q2 += q3;
    const float m2loc = xsum32(xsum16(xsum8(q2[0] + q2[1])));
    if (lane < 8) *(volatile LAS f32x4*)(L + L_CS + 512 + (lane * 4 + w) * 16) = f32x4{mloc, m2loc, 1.f, 1.f};
    STAMP(25);
    f32x4 pr[4];
#pragma unroll 1
    for (unsigned spins = 0; spins < 64 * kSpinCap; ++spins) {
      bool ok = true;
#pragma unroll
      for (int ww = 0; ww < 4; ++ww) {
        pr[ww] = *(volatile LAS f32x4*)(L + L_CS + 512 + ((lane & 7) * 4 + ww) * 16);
        ok = ok && pr[ww][2] == 1.f;
      }
      if (__all(ok)) break;
    }
    const f32x4 p01 = {pr[0][0], pr[0][1], pr[1][0], pr[1][1]}, p23 = {pr[2][0], pr[2][1], pr[3][0], pr[3][1]};
    const float mean = (p01[0] + p01[2]) * 0.3f + (p23[0] + p23[2]) * 0.2f;
    const float e0 = p01[0] - mean, e1 = p01[2] - mean, e2 = p23[0] - mean, e3 = p23[2] - mean;
    const float m2 = (p01[1] + p01[3]) + (p23[1] + p23[3]) + 192.f * (e0 * e0 + e1 * e1) + 128.f * (e2 * e2 + e3 * e3);
    const float rstd = __builtin_amdgcn_rsqf(m2 * (1.f / D) + LN_EPS);
#pragma unroll
    for (int j = 0; j < 3; ++j) {
      v4u o;
#pragma unroll
      for (int e = 0; e < 4; ++e) o[e] = pk2((f[j * 4 + e] - mean) * rstd);
      st128(o, rs, (j < 2 || has3) ? xd : OOB, WS_XN + j * 8192);
    }
    STAMP(26);
    PIN(tag); tag += 1u;
    publish(FLAG(0, b >> 2) + (b & 3) * 4 + w, tag, lane);
  }

  STAMP(1);

  v4u xr[3];
  {
    wait_flag(FLAG(0, grp32) + lane, 16, tag, lane);
    STAMP(2);
    v4u a[20][2];
    const int abase = WS_XN + 2 * grp32 * 20 * 1024;
#pragma unroll
    for (int ks = 0; ks < S0; ++ks)
#pragma unroll
      for (int m = 0; m < 2; ++m) a[ks][m] = ld128(rs, lane * 16, abase + (m * 20 + ks) * 1024);
    SCHED();
    BAR_RAW();
    STAMP(27);
    {
      f32x4 acc[2][5];
      const LBase bp(lds0 + L_BKV + (lane >> 2) * 160 + (lane & 3) * 8);
      kloop<2, 5, 20, 1, false>(acc, [&](int ks, int s, int h) { return bp.rd(ks * 5120 + h * 2560 + s * 32); },
        [&](int ks, int m) { return a[ks][m]; },
        [&](int ks) {
#pragma unroll
          for (int j = ld_hi(ks - 1, S0, AW, 20); j < ld_hi(ks, S0, AW, 20); ++j)
#pragma unroll
            for (int m = 0; m < 2; ++m) a[j][m] = ld128(rs, lane * 16, abase + (m * 20 + j) * 1024);
          if (ks == 3 || ks == 10) BAR_RAW();
          if (ks == 20 - AW) PROG(1);
        });
      STAMP(3);
      v4u ka[3], vb[3];
#pragma unroll
      for (int s = 0; s < 3; ++s)
#pragma unroll
        for (int m = 0; m < 2; ++m) {
          vb[s][2 * m] = pk2(lo2(acc[m][s + 2])); vb[s][2 * m + 1] = pk2(hi2(acc[m][s + 2]));
        }
#pragma unroll
      for (int e = 0; e < 4; ++e) vb[0][e] = t < 8 ? 0x3F803F80u : vb[0][e];
      const f32x4 z4 = {0.f, 0.f, 0.f, 0.f};
      LAS f32x4* red = (LAS f32x4*)(L + L_RED) + w * 512 + lane;
#pragma unroll
      for (int s = 0; s < 3; ++s) {
#pragma unroll
        for (int m = 0; m < 2; ++m) { ka[s][2 * m] = pk2(phi2(lo2(acc[m][s]))); ka[s][2 * m + 1] = pk2(phi2(hi2(acc[m][s]))); }
#pragma unroll
        for (int sb = 0; sb < 3; ++sb)
          if (s > 0 || sb < 2) red[(3 * s + sb - (s > 0)) * 64] = mfma(ka[s], vb[sb], z4);
      }
      STAMP(28);
      BAR();
    }
    STAMP(4);
    f32x4 qa[2][3];
    {
      unsigned bp0 = lds0 + L_BQ + (lane >> 2) * 64 + (lane & 3) * 8 + (g & 1) * 32;
      unsigned bp1 = lds0 + L_BQ + (lane >> 2) * 64 + (lane & 3) * 8 + 32 - (g & 1) * 32;
      unsigned bp2 = lds0 + L_BQ + 2048 + (lane >> 2) * 16 + (lane & 1) * 8;
      PIN(bp0); PIN(bp1); PIN(bp2);
      kloop<2, 3, 20, 2, true>(qa,
        [&](int ks, int s, int h) {
          return s == 0 ? rdtr((LAS char*)bp0 + ks * 2560 + h * 1024) : s == 1 ? rdtr((LAS char*)bp1 + ks * 2560 + h * 1024) : rdtr((LAS char*)bp2 + ks * 2560 + h * 256);
        },
        [&](int ks, int m) { return a[ks][m]; },
        [&](int ks) {});
    }
    v4u pq0[2], pq1[2];
#pragma unroll
    for (int m = 0; m < 2; ++m) {
      pq0[m].x = pk2(phi2(lo2(qa[m][0]))); pq0[m].y = pk2(phi2(hi2(qa[m][0])));
      pq0[m].z = pk2(phi2(lo2(qa[m][1]))); pq0[m].w = pk2(phi2(hi2(qa[m][1])));
      const unsigned lo = pk2(phi2(lo2(qa[m][2]))), hi = pk2(phi2(hi2(qa[m][2])));
      pq1[m].x = g < 2 ? lo : 0u; pq1[m].y = g < 2 ? hi : 0u; pq1[m].z = 0u; pq1[m].w = 0u;
      PIN(pq0[m]); PIN(pq1[m]);
    }
    STAMP(5);
    BAR_RAW();
#pragma unroll
    for (int j = 0; j < 3; ++j)
      xr[j] = __builtin_amdgcn_raw_buffer_load_b128(rx, sel(j < 2 || g < 2, t * 1280 + g * 16), (4 * r64 + w) * 16 * 1280 + c8 * 160 + j * 64, 0);
    STAMP(17);
    {
      v4u kv[4][2];
#pragma unroll
      for (int es = 3; es >= 0; --es)
#pragma unroll
        for (int ks = 0; ks < 2; ++ks) kv[es][ks] = *(LAS v4u*)(L + L_KVF + (es * 2 + ks) * 1024 + lane * 16);
      SCHED();
      f32x4 o[2][4];
#pragma unroll
      for (int es = 3; es >= 0; --es)
#pragma unroll
        for (int m = 0; m < 2; ++m) o[m][es] = mfma(kv[es][0], pq0[m], f32x4{0.f, 0.f, 0.f, 0.f});
#pragma unroll
      for (int es = 3; es >= 0; --es)
#pragma unroll
        for (int m = 0; m < 2; ++m) o[m][es] = mfma(kv[es][1], pq1[m], o[m][es]);
#pragma unroll
      for (int m = 0; m < 2; ++m) {
        const float i0 = __builtin_amdgcn_rcpf(o[m][3][0] + 1e-6f), i1 = __builtin_amdgcn_rcpf(o[m][3][1] + 1e-6f);
        const float im = g == 0 ? i0 : i1;
        v4u ov;
        ov.x = pk2(lo2(o[m][0]) * i0); ov.y = pk2(hi2(o[m][0]) * i0);
        ov.z = pk2(lo2(o[m][1]) * im); ov.w = pk2(hi2(o[m][1]) * im);
        v2u ol; ol.x = pk2(lo2(o[m][2]) * i1); ol.y = pk2(hi2(o[m][2]) * i1);
        const int tt = 2 * grp32 + m;
        st128(ov, rs, lane * 16, WS_O + (tt * 20 + c16) * 1024);
        st64(ol, rs, g < 2 ? lane * 8 : OOB, WS_O + (tt * 20 + 16) * 1024 + c16 * 256);
      }
      STAMP(18);
      publish(FLAG(2, grp32) + c16, tag, lane);
    }
    STAMP(7);
  }

  {
    const int tt = 4 * r64 + w;
    v4u eye[2];
#pragma unroll
    for (int hf = 0; hf < 2; ++hf) {
      const int e = 16 * hf + t - 8 * g;
#pragma unroll
      for (int d = 0; d < 4; ++d) eye[hf][d] = e == 2 * d ? 0x3F80u : e == 2 * d + 1 ? 0x3F800000u : 0u;
      PIN(eye[hf]);
    }
    f32x4 acc[1][5];
#pragma unroll
    for (int s = 0; s < 5; ++s) { acc[0][s] = mfma(eye[s & 1], xr[s >> 1], f32x4{0.f, 0.f, 0.f, 0.f}); PIN(acc[0][s]); }
    wait_flag(FLAG(2, tt >> 1) + lane, 16, tag, lane);
    STAMP(8);
    if (b == 0 && tid == 0) __hip_atomic_store((gu32*)(ws + WS_EPOCH), tag, RLX_AGENT);
    v4u a[20];
#pragma unroll
    for (int ks = 0; ks < 2 * S0; ++ks) a[ks] = ld128(rs, lane * 16, WS_O + (tt * 20 + ks) * 1024);
    SCHED();
    const LBase bp(lds0 + L_WO + (lane >> 2) * 160 + (lane & 3) * 8);
    kloop<1, 5, 20, 1, true, 1>(acc, [&](int ks, int s, int h) { return bp.rd(ks * 5120 + h * 2560 + s * 32); },
      [&](int ks, int m) { return a[ks]; },
      [&](int ks) {
#pragma unroll
        for (int j = ld_hi(ks - 1, 2 * S0, 2 * AW, 20); j < ld_hi(ks, 2 * S0, 2 * AW, 20); ++j)
          a[j] = j < 16 ? ld128(rs, lane * 16, WS_O + (tt * 20 + j) * 1024) : ld_split(rs, lane, WS_O + (tt * 20 + j) * 1024);
        if (ks == 8) PROG(2);
        if (ks == 14) PROG(3);
      });
    PROG(4);
    STAMP(9);
    v2u hb[5];
    f32x2 hv[10];
#pragma unroll
    for (int s = 0; s < 5; ++s) {
      hb[s].x = pk2(lo2(acc[0][s]));
      hb[s].y = pk2(hi2(acc[0][s]));
      hv[2 * s] = bf2(hb[s].x); hv[2 * s + 1] = bf2(hb[s].y);
    }
    st128(v4u{hb[0].x, hb[0].y, hb[1].x, hb[1].y}, rs, lane * 16, WS_H + (tt * 20 + 2 * c8) * 1024);
    st128(v4u{hb[2].x, hb[2].y, hb[3].x, hb[3].y}, rs, lane * 16, WS_H + (tt * 20 + 2 * c8 + 1) * 1024);
    st64(hb[4], rs, lane * 8, WS_H + (tt * 20 + 16) * 1024 + c8 * 512);
    const f32x2 s2 = ((hv[0] + hv[1]) + (hv[2] + hv[3])) + ((hv[4] + hv[5]) + (hv[6] + hv[7])) + (hv[8] + hv[9]);
    const float mc = xsum32(xsum16(s2[0] + s2[1])) * (1.f / 80.f);
    f32x2 q2 = {0.f, 0.f}, q3 = {0.f, 0.f};
#pragma unroll
    for (int j = 0; j < 10; j += 2) { const f32x2 d0 = hv[j] - mc, d1 = hv[j + 1] - mc; q2 += d0 * d0; q3 += d1 * d1; }
    q2 += q3;
    const float m2 = xsum32(xsum16(q2[0] + q2[1]));
    st64(v2u{__builtin_bit_cast(unsigned, mc), __builtin_bit_cast(unsigned, m2)}, rs, g == 0 ? t * 8 : OOB, WS_ST + (tt * 8 + c8) * 128);
    STAMP(20);
    publish(FLAG(3, tt >> 1) + (tt & 1) * 8 + c8, tag, lane);
    STAMP(10);
  }

  {
    wait_flag(FLAG(3, grp32) + lane, 16, tag, lane);
    STAMP(11);
    v4u stt[2];
#pragma unroll
    for (int m = 0; m < 2; ++m) {
      const v2u s0 = ld64(rs, g * 256 + t * 8, WS_ST + (2 * grp32 + m) * 1024), s1 = ld64(rs, g * 256 + 128 + t * 8, WS_ST + (2 * grp32 + m) * 1024);
      stt[m] = v4u{s0.x, s0.y, s1.x, s1.y};
    }
    v4u a[20][2];
    const int abase = WS_H + 2 * grp32 * 20 * 1024;
#pragma unroll
    for (int ks = 0; ks < S0; ++ks)
#pragma unroll
      for (int m = 0; m < 2; ++m) a[ks][m] = ld128(rs, lane * 16, abase + (m * 20 + ks) * 1024);
    SCHED();
    float nm[2], rr[2];
#pragma unroll
    for (int m = 0; m < 2; ++m) {
      const f32x4 sv = __builtin_bit_cast(f32x4, stt[m]);
      const float mean = xsum32(xsum16(sv[0] + sv[2])) * 0.125f;
      const float m2 = xsum32(xsum16(sv[1] + sv[3] + 80.f * ((sv[0] - mean) * (sv[0] - mean) + (sv[2] - mean) * (sv[2] - mean))));
      const float var = m2 * (1.f / D);
      const float r1 = __builtin_amdgcn_rsqf(var + LN_EPS);
      rr[m] = r1 * __builtin_amdgcn_rsqf(var * r1 * r1 + LN_EPS);
      nm[m] = -mean * rr[m];
      PIN(rr[m]); PIN(nm[m]);
    }
    BAR_RAW();
    STAMP(22);
    const unsigned l64 = lds0 + (lane >> 2) * 64 + (lane & 3) * 8;
    const LBase bp0(l64 + (g & 1) * 32), bp1(l64 + 32 - (g & 1) * 32);
    const int h1base = WS_H1 + 2 * grp32 * 48 * 1024;
    auto finish = [&](const f32x4& c0, const f32x4& c1, int j, int m) {
      v4u o;
#pragma unroll
      for (int s = 0; s < 2; ++s) {
        const f32x4 cs = *(LAS f32x4*)(L + L_CS + ((2 * j + s) * 4 + g) * 16), bb = *(LAS f32x4*)(L + L_B1 + ((2 * j + s) * 4 + g) * 16);
        const f32x4 c = s ? c1 : c0;
        o[2 * s] = pk2(gelu2(lo2(c) * rr[m] + (lo2(cs) * nm[m] + lo2(bb))));
        o[2 * s + 1] = pk2(gelu2(hi2(c) * rr[m] + (hi2(cs) * nm[m] + hi2(bb))));
      }
      st128(o, rs, lane * 16, h1base + (m * 48 + (c16 >> 3) * 24 + (j < 2 ? 2 * (c16 & 7) + j : 16 + (c16 & 7))) * 1024);
    };
    {
      f32x4 acc[2][4];
      kloop<2, 4, 20, 1, true>(acc, [&](int ks, int s, int h) { return ((s & 1) ? bp1 : bp0).rd(L_W1 + (s >> 1) * 40960 + ks * 2048 + h * 1024); },
        [&](int ks, int m) { return a[ks][m]; },
        [&](int ks) {
#pragma unroll
          for (int j = ld_hi(ks - 1, S0, AW, 20); j < ld_hi(ks, S0, AW, 20); ++j)
#pragma unroll
            for (int m = 0; m < 2; ++m)
              a[j][m] = j < 16 ? ld128(rs, lane * 16, abase + (m * 20 + j) * 1024) : ld_split(rs, lane, abase + (m * 20 + j) * 1024);
          if (ks == 20 - AW) PROG(5);
        });
      PROG(6);
      STAMP(12);
#pragma unroll
      for (int j = 0; j < 2; ++j)
#pragma unroll
        for (int m = 0; m < 2; ++m) finish(acc[m][2 * j], acc[m][2 * j + 1], j, m);
      SCHED();
      STAMP(24);
    }
    f32x4 accb[2][2];
    const int kh = w & 1, tt = 4 * r64 + w;
    v4u hr01, hr23;
    v2u hr4;
    kloop<2, 2, 20, 3, true>(accb, [&](int ks, int s, int h) { return (s ? bp1 : bp0).rd(L_W1 + 81920 + ks * 2048 + h * 1024); },
      [&](int ks, int m) { return a[ks][m]; },
      [&](int ks) {
        if (ks == PUBA) { publish(FLAG(4, grp32) + c16, tag, lane); PROG(7); }
        if (ks == PUBA + 2) {
          hr01 = ld128(rs, lane * 16, WS_H + (tt * 20 + 2 * c8) * 1024);
          hr23 = ld128(rs, lane * 16, WS_H + (tt * 20 + 2 * c8 + 1) * 1024);
          hr4 = ld64(rs, lane * 8, WS_H + (tt * 20 + 16) * 1024 + c8 * 512);
        }
      });
    STAMP(13);
    f32x4 acc[2][5];
    {
      const unsigned hrr[10] = {hr01.x, hr01.y, hr01.z, hr01.w, hr23.x, hr23.y, hr23.z, hr23.w, hr4.x, hr4.y};
#pragma unroll
      for (int s = 0; s < 5; ++s) {
        acc[0][s] = *(LAS f32x4*)(L + L_B2 + (s * 4 + g) * 16) + f32x4{bf_lo(hrr[2 * s]), bf_hi(hrr[2 * s]), bf_lo(hrr[2 * s + 1]), bf_hi(hrr[2 * s + 1])};
        PIN(acc[0][s]);
      }
    }
    BAR_RAW();
    STAMP(14);
    constexpr int WIN = 8;
    v4u ar[WIN][2];
    const int a4[2] = {WS_H1 + (tt * 48 + kh * 24) * 1024, WS_H1 + ((tt ^ 1) * 48 + kh * 24) * 1024};
#pragma unroll
    for (int ks = 0; ks < WIN - 1; ++ks)
#pragma unroll
      for (int m = 0; m < 2; ++m) ar[ks][m] = ld128(rs, lane * 16, a4[m] + ks * 1024);
    SCHED();
#pragma unroll
    for (int m = 0; m < 2; ++m) finish(accb[m][0], accb[m][1], 2, m);
    STAMP(29);
    const LBase bp(lds0 + L_W2 + kh * 5120 + (lane >> 2) * 160 + (lane & 3) * 8);
    kloop<2, 5, 24, 1, true, 1>(acc, [&](int ks, int s, int h) { return bp.rd((ks % 15) * 10240 + h * 2560 + s * 32); },
      [&](int ks, int m) { return ar[ks % WIN][m]; },
      [&](int ks) {
        if (ks == 5) PROG(8);
        if (ks == 9 || ks == 14 || ks == 19) BAR_RAW();
        if (ks == 3) publish(FLAG(10, grp32) + c16, tag, lane);
        if (ks >= 3) {
#pragma unroll
          for (int j = ks == 3 ? WIN - 1 : ld_hi(ks - 1, 1, WIN - 1, 24); j < ld_hi(ks, 1, WIN - 1, 24); ++j)
#pragma unroll
            for (int m = 0; m < 2; ++m) ar[j % WIN][m] = ld128(rs, lane * 16, a4[m] + j * 1024);
        }
      });
    STAMP(15);
    LAS f32x4* xch = (LAS f32x4*)(L + L_W2 + 102400);
#pragma unroll
    for (int s = 0; s < 5; ++s) xch[((w ^ 1) * 5 + s) * 64 + lane] = acc[1][s];
    int oo = ((tt * 16 + t) * D + 80 * c8 + 4 * g) * 2;
    PIN(oo);
    BAR();
    const __amdgpu_buffer_rsrc_t rout = __builtin_amdgcn_make_buffer_rsrc((void*)out, 0, S * D * 2, 0x00020000);
    f32x4 ot[5];
#pragma unroll
    for (int s = 0; s < 5; ++s) ot[s] = xch[(w * 5 + s) * 64 + lane];
    SCHED();
#pragma unroll
    for (int s = 0; s < 5; ++s) {
      const f32x4 v = acc[0][s] + ot[s];
      __builtin_amdgcn_raw_buffer_store_b64(v2u{pk2(lo2(v)), pk2(hi2(v))}, rout, oo + 32 * s, 0, 0);
    }
    STAMP(16);
  }
  return;
  }

  {
    const v4u srd_ws = mk_srd(ws, WS_BYTES);
    STAMP(16);
    const int q5 = lane / 5, c5 = lane % 5;
    const int f40m = c5 < 4 ? 512 : 128, f40l = c5 < 4 ? q5 * 64 + (c5 ^ (2 * ((q5 >> 2) & 1))) * 16 : 2048 + q5 * 16;
    v2u ub1, ub2;
    unsigned pf0 = 0;
    if (hw == 0) {
      const int dof = sel(lane < 40, lane * 16);
#pragma unroll
      for (int j = 0; j < 10; ++j) {
        const int k = 40 * r128 + 4 * j, R = (k & ~31) + 16 * ((k >> 2) & 1) + 4 * ((k >> 3) & 3);
        st128(tv[j], rs, dof, WS_TKV + c16 * 153600 + R * 160);
      }
      STAMP(18);
      PIN(tag); tag += 1u;
      publish(FLAG(6, c16) + r128, tag, lane);
      STAMP(13);
    } else if (hw == 1) {
      const __amdgpu_buffer_rsrc_t ro = __builtin_amdgcn_make_buffer_rsrc((void*)wo, 0, D * D * 2, 0x00020000);
      const int vq = sel(lane < 40, (8 * (q5 >> 2) + (q5 & 3)) * 3840 + c5 * 16), vo = sel(lane < 40, q10 * 1280 + c10 * 16);
#pragma unroll
      for (int j = 0; j < 5; ++j) {
        const int i8 = 5 * r128 + j, k = 32 * (i8 >> 2) + 16 * (i8 & 1) + 4 * ((i8 >> 1) & 1);
        tv[j] = __builtin_amdgcn_raw_buffer_load_b128(rq, vq, k * 3840 + c16 * 80, 0);
      }
#pragma unroll
      for (int j = 0; j < 5; ++j) tv[5 + j] = __builtin_amdgcn_raw_buffer_load_b128(ro, vo, (20 * r64 + 4 * j) * 1280 + c8 * 160, 0);
    } else if (hw == 2) {
      const GAS unsigned char* cb = (const GAS unsigned char*)(pc0 & ~127ull);
      if (lane < 11) pf0 = *(const GAS unsigned*)(cb + (lane * 32 + bi) * 128);
    } else if (hw == 3) {
      const __amdgpu_buffer_rsrc_t rb1 = __builtin_amdgcn_make_buffer_rsrc((void*)b1, 0, F * 2, 0x00020000);
      const __amdgpu_buffer_rsrc_t rb2 = __builtin_amdgcn_make_buffer_rsrc((void*)b2, 0, D * 2, 0x00020000);
      ub1 = __builtin_amdgcn_raw_buffer_load_b64(rb1, lane * 8, c16 * 192, 0);
      ub2 = __builtin_amdgcn_raw_buffer_load_b64(rb2, lane * 8, c8 * 160, 0);
    }
    asm volatile("" ::: "memory");
    int red_off, kvf_off, kvp_off;
    {
      const int it = ht < 210 ? ht : 209, ie = it / 5, ic = it % 5;
      const int ihead = ie < 40 ? (ie >= 20) : ie - 40;
      const int id = 20 * ihead + 4 * ic, dg = id >> 2;
      const int sa = id >> 4, col = 40 + ie;
      const int tile = 3 * sa + (ie < 40 ? (col >> 4) - 2 : 0) - (sa > 0);
      red_off = lds0 + L_RED + (tile * 64 + (dg & 3) * 16 + (ie < 40 ? (col & 15) : 0)) * 16;
      kvf_off = lds0 + L_KVF + ((ie < 40 ? (ie >> 4) : 3) * 2 + (dg >> 3)) * 1024 + ((dg & 3) * 16 + (ie < 40 ? (ie & 15) : ihead)) * 16 + ((dg >> 2) & 1) * 8;
      kvp_off = WS_KVP + (c16 * 16 + r128) * 4096 + ht * 16;
      PIN(red_off); PIN(kvf_off); PIN(kvp_off);
    }
    int vo12, td[12];
    if (hw >= 2) {
      const int q12 = lane / 12, c12 = lane % 12;
      vo12 = hw == 2 ? sel(lane < 48, q12 * 3072 + c12 * 16) : sel(lane < 40, q10 * 1280 + c10 * 16);
#pragma unroll
      for (int j = 0; j < 12; ++j) {
        const int cc = r128 >> 1, off = 40 * (r128 & 1) + 4 * j;
        const int R = off < 64 ? 64 * cc + off : 32 * (16 + (cc >> 1)) + 16 * (cc & 1) + off - 64;
        const int d1 = WS_T1 + c16 * 122880 + (c12 >> 2) * 40960 + (R + q12) * 64 + ((c12 & 3) ^ (2 * ((R >> 2) & 1))) * 16;
        const int pc = r64 >> 1, ko = 48 * (r64 & 1) + 4 * j, jj = ko >> 5;
        const int d2 = WS_T2 + c8 * 245760 + (2 * (jj < 2 ? 2 * (pc & 7) + jj : 16 + (pc & 7)) + (pc >> 3)) * 5120 + (ko & 31) * 160 + lane * 16;
        td[j] = sel(hw == 2 ? (lane < 48 && j < 10) : lane < 40, hw == 2 ? d1 : d2);
        PIN(td[j]);
      }
      PIN(vo12);
    }
    int l16 = lane * 16, h16 = ht * 16;
    PIN(l16); PIN(h16);
    gu32* fp1 = FLAG(1, c16) + lane;
    gu32* fp78 = lane < 32 ? FLAG(7, c8) + lane : FLAG(8, c16) + t;
    gu32* fp4 = FLAG(4, 2 * r64 + (g & 1)) + t;
    gu32* fp1w = FLAG(1, c16) + r128 * 4 + hw;
    PIN(fp1); PIN(fp78); PIN(fp4); PIN(fp1w);
    ((LAS v4u*)(L + L_KVF))[ht] = v4u{0u, 0u, 0u, 0u};
    ((LAS v4u*)(L + L_KVF))[256 + ht] = v4u{0u, 0u, 0u, 0u};
    asm volatile("" ::: "memory");
    if (hw != 0) {
      if (hw == 1) {
#pragma unroll
        for (int j = 0; j < 5; ++j) {
          const int i8 = 5 * r128 + j;
          st128(tv[j], rs, sel(lane < 40, (i8 & 3) * f40m + f40l), WS_TKV + c16 * 153600 + 102400 + (i8 >> 2) * 2560);
        }
#pragma unroll
        for (int j = 0; j < 5; ++j) {
          const int pp = r64 >> 1, e = 20 * (r64 & 1) + 4 * j;
          const int R = e < 32 ? 32 * pp + e : 32 * (16 + (pp >> 2)) + 8 * (pp & 3) + e - 32;
          st128(tv[5 + j], rs, sel(lane < 40, lane * 16), WS_TO + c8 * 102400 + R * 160);
        }
      }
      if (hw == 3) {
        if (lane < 24) *(LAS f32x4*)(L + L_B1 + lane * 16) = f32x4{bf_lo(ub1.x), bf_hi(ub1.x), bf_lo(ub1.y), bf_hi(ub1.y)};
        if (lane < 20) *(LAS f32x4*)(L + L_B2 + lane * 16) = f32x4{bf_lo(ub2.x), bf_hi(ub2.x), bf_lo(ub2.y), bf_hi(ub2.y)};
      }
      asm volatile("" :: "v"(pf0));
      PIN(tag); tag += 1u;
      DRAIN();
      if (hw == 1 && lane < 2) flag_set(lane == 0 ? FLAG(8, c16) + r128 : FLAG(7, c8) + r64, tag);
    }
    if (hw == 0) {
      wait_flag(FLAG(6, c16) + lane, 16, tag, lane);
      sig[0] = 1u;
    }
    sig_wait(sig);
    STAMP(1);
    const int kvq = WS_TKV + c16 * 153600, wos = WS_TO + c8 * 102400;
    dma_image<0, 24>(lds0 + L_BKV, l16, srd_ws, kvq, hw, 3 * (bi >> 3));
    dma_image<24, 56>(lds0 + L_BKV, l16, srd_ws, kvq, hw, 4 * (bi >> 3));
    asm volatile("s_waitcnt vmcnt(8)" ::: "memory");
    STAMP(2);
    BAR_RAW();
    dma_image<56, 100>(lds0 + L_BKV, l16, srd_ws, kvq, hw, 5 * (bi >> 3));
    asm volatile("s_waitcnt vmcnt(10)" ::: "memory");
    BAR_RAW();
    if (hw == 1) wait_flag(fp78, 48, tag, lane);
    DRAIN();
    STAMP(3);
    BAR_RAW();
    if (QPOS == 0) dma_image<100, 150>(lds0 + L_BKV, l16, srd_ws, kvq, hw, 6 * (bi >> 3));
    prog_wait(prog, 1);
    if (QPOS == 1) dma_image<100, 150>(lds0 + L_BKV, l16, srd_ws, kvq, hw, 6 * (bi >> 3));
    if (hw >= 2) {
      const __amdgpu_buffer_rsrc_t r12 = __builtin_amdgcn_make_buffer_rsrc((void*)(hw == 2 ? w1 : w2), 0, D * F * 2, 0x00020000);
      const int base = hw == 2 ? r128 * 122880 + c16 * 192 : r64 * 61440 + c8 * 160, step = hw == 2 ? 4 * 3072 : 4 * 1280;
#pragma unroll
      for (int j = 0; j < 12; ++j) tv[j] = __builtin_amdgcn_raw_buffer_load_b128(r12, vo12, (j < 10 || hw == 3) ? base + j * step : OOB, 0);
      asm volatile("s_waitcnt vmcnt(12)" ::: "memory");
    } else {
      dma_image<32, 68, 0, 2>(lds0 + L_WO, l16, srd_ws, wos, hw, bi >> 2);
      asm volatile("s_waitcnt vmcnt(18)" ::: "memory");
    }
    STAMP(4);
    BAR();
    __builtin_amdgcn_s_setprio(2);
    STAMP(11);
    {
      f32x4 pv[4];
#pragma unroll
      for (int ww = 0; ww < 4; ++ww) pv[ww] = *(LAS f32x4*)((LAS char*)red_off + ww * 8192);
      st128(__builtin_bit_cast(v4u, add4(add4(pv[0], pv[1]), add4(pv[2], pv[3]))), rs, kvp_off, 0);
      PROG(1);
      STAMP(12);
      DRAIN();
    }
    if (lane == 0) flag_set(fp1w, tag);
    if (hw == 0) {
      STAMP(10);
      __builtin_amdgcn_s_sleep(H1W);
      wait_flag(fp1, 64, tag, lane);
      sig[2] = 1u;
    } else {
      if (H1G) __builtin_amdgcn_s_sleep(H1G);
      dma_image<68, 100, 0, 3>(lds0 + L_WO, l16, srd_ws, wos, hw - 1, bi >> 2);
      prog_wait(prog + 1, 1);
      dma_image<0, 32, 0, 3>(lds0 + L_WO, l16, srd_ws, wos, hw - 1, 2 * (bi >> 2));
      if (hw >= 2) {
#pragma unroll
        for (int j = 0; j < 12; ++j) st128(tv[j], rs, td[j], 0);
      }
      STAMP(10);
      sig_wait(sig + 2);
    }
    STAMP(5);
    {
      v4u pv[16];
#pragma unroll
      for (int r = 0; r < 16; ++r) pv[r] = ld128(rs, h16, WS_KVP + (c16 * 16 + r) * 4096);
      SCHED();
      f32x4 v = __builtin_bit_cast(f32x4, pv[0]);
#pragma unroll
      for (int r = 1; r < 16; ++r) v = add4(v, __builtin_bit_cast(f32x4, pv[r]));
      v2u o; o.x = pk2(lo2(v)); o.y = pk2(hi2(v));
      *(LAS v2u*)((LAS char*)kvf_off) = o;
      if (ht >= 200) {
#pragma unroll
        for (int c = 1; c < 4; ++c) *(LAS v2u*)((LAS char*)kvf_off + c * 64) = o;
      }
    }
    DRAIN();
    STAMP(6);
    BAR();
    __builtin_amdgcn_s_setprio(0);
    if (hw >= 2 && lane == 0) flag_set(hw == 2 ? FLAG(5, c16) + r128 : FLAG(11, c8) + r64, tag);
    if (hw == 2) {
      f32x4 s0 = {0.f, 0.f, 0.f, 0.f}, s1 = {0.f, 0.f, 0.f, 0.f};
#pragma unroll
      for (int j = 0; j < 10; ++j) {
        s0 = add4(s0, f32x4{bf_lo(tv[j].x), bf_hi(tv[j].x), bf_lo(tv[j].y), bf_hi(tv[j].y)});
        s1 = add4(s1, f32x4{bf_lo(tv[j].z), bf_hi(tv[j].z), bf_lo(tv[j].w), bf_hi(tv[j].w)});
      }
#pragma unroll
      for (int d = 24; d >= 12; d -= 12) {
        f32x4 u0, u1;
#pragma unroll
        for (int e = 0; e < 4; ++e) { u0[e] = __shfl(s0[e], lane + d); u1[e] = __shfl(s1[e], lane + d); }
        s0 = add4(s0, u0); s1 = add4(s1, u1);
      }
      f32x4 sv;
#pragma unroll
      for (int e = 0; e < 4; ++e) { const float a0 = __shfl(s0[e], lane >> 1), a1 = __shfl(s1[e], lane >> 1); sv[e] = (lane & 1) ? a1 : a0; }
      st128(__builtin_bit_cast(v4u, sv), rs, sel(lane < 24, lane * 16), WS_CS + (c16 * 16 + r128) * 384);
      publish(FLAG(9, c16) + r128, tag, lane);
    }
    if (hw == 1) {
      __builtin_amdgcn_s_sleep(40);
      wait_flag(lane < 32 ? FLAG(11, c8) + lane : FLAG(5, c16) + t, 48, tag, lane);
      sig[3] = 1u;
    }
    sig_wait(sig + 3);
    prog_wait(prog, 2);
    if (hw == 0) {
      wait_flag(FLAG(9, c16) + lane, 16, tag, lane);
      v4u pv[16];
      const int co = sel(lane < 24, l16);
#pragma unroll
      for (int r = 0; r < 16; ++r) pv[r] = ld128(rs, co, WS_CS + (c16 * 16 + r) * 384);
      SCHED();
      f32x4 v = __builtin_bit_cast(f32x4, pv[0]);
#pragma unroll
      for (int r = 1; r < 16; ++r) v = add4(v, __builtin_bit_cast(f32x4, pv[r]));
      if (lane < 24) *(LAS f32x4*)(L + L_CS + lane * 16) = v;
    } else {
      dma_image<70, 120, 0, 3>(lds0 + L_W1, l16, srd_ws, WS_T1 + c16 * 122880, hw - 1, 6 * (bi >> 3));
      prog_wait(prog, 3);
      dma_image<0, 40, 0, 3>(lds0 + L_W1, l16, srd_ws, WS_T1 + c16 * 122880, hw - 1, 5 * (bi >> 3));
      STAMP(7);
      prog_wait(prog, 4);
      dma_image<40, 70, 0, 3>(lds0 + L_W1, l16, srd_ws, WS_T1 + c16 * 122880, hw - 1, 3 * (bi >> 3));
    }
    DRAIN();
    STAMP(8);
    BAR();
    const int w2s = WS_T2 + c8 * 245760;
    prog_wait(prog, 5);
    dma_image<0, 30>(lds0 + L_W2, l16, srd_ws, w2s, hw, bi >> 2);
    STAMP(14);
    prog_wait(prog, 6);
    dma_image<30, W2S>(lds0 + L_W2, l16, srd_ws, w2s, hw, bi >> 2);
    prog_wait(prog, 7);
    if (hw != 0) dma_image<W2S, 110, 0, 3>(lds0 + L_W2, l16, srd_ws, w2s, hw - 1, bi >> 2);
    DRAIN();
    STAMP(9);
    if (hw == 0) wait_flag(fp4, 32, tag, lane);
    STAMP(15);
    BAR_RAW();
    dma_image<110, 150>(lds0 + L_W2, l16, srd_ws, w2s, hw, 2 * (bi >> 2));
    prog_wait(prog, 8);
    if (hw == 0) wait_flag(fp4 + 6 * FLAG_SET / 4, 32, tag, lane);
    else dma_image<150, 198, 0, 3>(lds0 + L_W2 - 153600, l16, srd_ws, w2s, hw - 1, 3 * (bi >> 2));
    asm volatile("s_waitcnt vmcnt(16)" ::: "memory");
    STAMP(17);
    BAR_RAW();
    dma_image<198, 200>(lds0 + L_W2 - 153600, l16, srd_ws, w2s, hw, 0);
    dma_image<200, 240>(lds0 + L_W2 - 153600, l16, srd_ws, w2s, hw, 2 * (bi >> 2));
    asm volatile("s_waitcnt vmcnt(10)" ::: "memory");
    BAR_RAW();
    DRAIN();
    BAR_RAW();
  }

}
}

extern "C" void kernel_launch(void* const* d_in, const int* in_sizes, int n_in, void* d_out, int out_size, void* d_ws,
                              size_t ws_size, hipStream_t stream) {
  const unsigned short* x = (const unsigned short*)d_in[0];
  const unsigned short* wqkv = (const unsigned short*)d_in[1];
  const unsigned short* wo = (const unsigned short*)d_in[2];
  const unsigned short* w1 = (const unsigned short*)d_in[4];
  const unsigned short* b1 = (const unsigned short*)d_in[5];
  const unsigned short* w2 = (const unsigned short*)d_in[6];
  const unsigned short* b2 = (const unsigned short*)d_in[7];
  fused_block<<<256, 512, 0, stream>>>(x, wqkv, wo, w1, b1, w2, b2, (unsigned short*)d_out, (unsigned char*)d_ws);
}
```

```cpp
#include <hip/hip_runtime.h>
#include <hip/hip_bf16.h>

#define STAMPS 0
#define THR 0
#define TOUCH 0
#define CODEPF 1
#define FLAGPF 0
#define OUTAUX 0
#define OUTPF 0
#define S0 6
#define XRPOS 0
#define QPOS 0
#define H1W 8
#define H1G 2
#define W2S 70
#define PUBA 8
#define NCDELAY 0
#define HPRIO 0
#define WOLATE 0
#define SLEEP 1

#define GAS __attribute__((address_space(1)))
#define LAS __attribute__((address_space(3)))
#define RLX_AGENT __ATOMIC_RELAXED, __HIP_MEMORY_SCOPE_AGENT
typedef unsigned v4u __attribute__((ext_vector_type(4)));
typedef unsigned v2u __attribute__((ext_vector_type(2)));
typedef float f32x4 __attribute__((ext_vector_type(4)));
typedef float f32x2 __attribute__((ext_vector_type(2)));
typedef __bf16 bf16x8 __attribute__((ext_vector_type(8)));
typedef __bf16 bf16x2 __attribute__((ext_vector_type(2)));
typedef short s16x4 __attribute__((ext_vector_type(4)));
typedef GAS unsigned gu32;

namespace {
constexpr int S = 2048, D = 640, F = 1536;
constexpr float LN_EPS = 1e-5f;
constexpr int AUX_SC1 = 16;
constexpr unsigned kSpinCap = 1u << 17;
constexpr int OOB = 0x7FFFFF00;

constexpr int WS_EPOCH = 0;
constexpr int WS_FLAG = 4096;
constexpr int FLAG_SET = 64 * 256;
constexpr int WS_XN = 1 << 20;
constexpr int WS_O = WS_XN + 128 * 20 * 1024;
constexpr int WS_H = WS_O + 128 * 20 * 1024;
constexpr int WS_H1 = WS_H + 128 * 20 * 1024;
constexpr int WS_KVP = WS_H1 + 128 * 48 * 1024;
constexpr int WS_ST = WS_KVP + 16 * 16 * 4096;
constexpr int WS_DBG = WS_ST + 2048 * 64;
constexpr int WS_TKV = WS_DBG + 256 * 8 * 32 * 8;
constexpr int WS_TO = WS_TKV + 16 * 153600;
constexpr int WS_T1 = WS_TO + 8 * 102400;
constexpr int WS_T2 = WS_T1 + 16 * 122880;
constexpr int WS_CS = WS_T2 + 16 * 122880;
constexpr int WS_BYTES = WS_CS + 16 * 16 * 384;

constexpr int L_BKV = 0, L_BQ = 102400;
constexpr int L_RED = 0, L_KVF = 153600;
constexpr int L_WO = 0, L_W1 = 30720, L_W2 = 0, L_CS = 161792;
constexpr int L_B1 = L_CS + 1152, L_B2 = L_CS + 1536;
constexpr int LDS_BYTES = 163840;

__device__ __forceinline__ f32x4 mfma(v4u a, v4u b, f32x4 c) {
  return __builtin_amdgcn_mfma_f32_16x16x32_bf16(__builtin_bit_cast(bf16x8, a), __builtin_bit_cast(bf16x8, b), c, 0, 0, 0);
}
__device__ __forceinline__ unsigned pk2(f32x2 v) { return __builtin_bit_cast(unsigned, __builtin_convertvector(v, bf16x2)); }
__device__ __forceinline__ unsigned pk2(float lo, float hi) { return pk2(f32x2{lo, hi}); }
__device__ __forceinline__ float bf_lo(unsigned u) { return __builtin_bit_cast(float, u << 16); }
__device__ __forceinline__ float bf_hi(unsigned u) { return __builtin_bit_cast(float, u & 0xFFFF0000u); }
__device__ __forceinline__ f32x2 bf2(unsigned u) { return f32x2{bf_lo(u), bf_hi(u)}; }
__device__ __forceinline__ f32x2 lo2(f32x4 v) { return f32x2{v[0], v[1]}; }
__device__ __forceinline__ f32x2 hi2(f32x4 v) { return f32x2{v[2], v[3]}; }
__device__ __forceinline__ v4u mk_srd(const void* p, unsigned bytes) {
  const unsigned long long a = (unsigned long long)p;
  v4u r;
  r.x = (unsigned)a; r.y = (unsigned)(a >> 32) & 0xFFFFu; r.z = bytes; r.w = 0x00020000u;
  return r;
}
#define DRAIN() asm volatile("s_waitcnt vmcnt(0)" ::: "memory")
#define BAR() asm volatile("s_waitcnt lgkmcnt(0)\n\ts_barrier" ::: "memory")
#define BAR_RAW() asm volatile("s_barrier" ::: "memory")
#define SCHED() __builtin_amdgcn_sched_barrier(0)
#define PIN(x) asm volatile("" : "+v"(x))

__device__ __forceinline__ void dma2(unsigned lds_addr, unsigned voff, v4u srd, unsigned soff) {
  unsigned keep;
  asm volatile("s_mov_b32 %0, m0\n\ts_mov_b32 m0, %1\n\ts_nop 0\n\t"
               "buffer_load_dwordx4 %2, %3, %4 offen sc1 lds\n\t"
               "buffer_load_dwordx4 %2, %3, %4 offen offset:1024 sc1 lds\n\t"
               "s_mov_b32 m0, %0"
               : "=&s"(keep) : "s"(lds_addr), "v"(voff), "s"(srd), "s"(soff) : "memory");
}
template <int LO, int HI, int TH = 0, int NW = 4>
__device__ __forceinline__ void dma_image(unsigned lds_base, unsigned voff, v4u srd, int ws_off, int hw, int rot) {
  constexpr int NP = (HI - LO) / 2;
#pragma unroll 1
  for (int i = hw; i < NP; i += NW) {
    int j = i + rot;
    if (j >= NP) j -= NP;
    dma2(lds_base + LO * 1024 + j * 2048, voff, srd, ws_off + LO * 1024 + j * 2048);
    if (TH) asm volatile("s_waitcnt vmcnt(%0)" :: "i"(TH - 2) : "memory");
  }
}
__device__ __forceinline__ v2u rdtr(LAS char* p) { return __builtin_bit_cast(v2u, __builtin_amdgcn_ds_read_tr16_b64_v4i16((LAS s16x4*)p)); }
struct LBase {
  unsigned a[3];
  __device__ __forceinline__ explicit LBase(unsigned v) { a[0] = v; a[1] = v + 65536; a[2] = v + 131072; PIN(a[0]); PIN(a[1]); PIN(a[2]); }
  __device__ __forceinline__ v2u rd(int off) const { return rdtr((LAS char*)(a[off >> 16]) + (off & 65535)); }
};
template <int MT, int NT, int KS, int PD, bool WA, int M0 = 0, class RB, class GA, class PRE>
__device__ __forceinline__ void kloop(f32x4 (&acc)[MT][NT], RB rb, GA ga, PRE pre) {
  v2u bl[PD + 1][NT], bh[PD + 1][NT];
#pragma unroll
  for (int m = M0; m < MT; ++m)
#pragma unroll
    for (int s = 0; s < NT; ++s) acc[m][s] = f32x4{0.f, 0.f, 0.f, 0.f};
#pragma unroll
  for (int ks = 0; ks < PD; ++ks)
#pragma unroll
    for (int s = 0; s < NT; ++s) { bl[ks][s] = rb(ks, s, 0); bh[ks][s] = rb(ks, s, 1); }
  SCHED();
#pragma unroll
  for (int ks = 0; ks < KS; ++ks) {
    pre(ks);
#pragma unroll
    for (int s = 0; s < NT; ++s)
#pragma unroll
      for (int m = 0; m < MT; ++m) {
        const v2u lo = bl[ks % (PD + 1)][s], hi = bh[ks % (PD + 1)][s];
        const v4u wv = {lo.x, lo.y, hi.x, hi.y};
        acc[m][s] = WA ? mfma(wv, ga(ks, m), acc[m][s]) : mfma(ga(ks, m), wv, acc[m][s]);
        if (ks + PD < KS) {
          if (MT == 1 || m == 0) bl[(ks + PD) % (PD + 1)][s] = rb(ks + PD, s, 0);
          if (MT == 1 || m == 1) bh[(ks + PD) % (PD + 1)][s] = rb(ks + PD, s, 1);
        }
        SCHED();
      }
  }
}
constexpr int ld_hi(int ks, int s0, int W, int KS) {
  const int a = s0 + 2 * (ks + 1), b = ks + 1 + W, c = a < b ? a : b;
  return c < KS ? c : KS;
}
__device__ __forceinline__ v4u ld128(__amdgpu_buffer_rsrc_t rs, int voff, int soff) {
  return __builtin_amdgcn_raw_buffer_load_b128(rs, voff, soff, AUX_SC1);
}
__device__ __forceinline__ v2u ld64(__amdgpu_buffer_rsrc_t rs, int voff, int soff) {
  return __builtin_amdgcn_raw_buffer_load_b64(rs, voff, soff, AUX_SC1);
}
__device__ __forceinline__ void st128(v4u v, __amdgpu_buffer_rsrc_t rs, int voff, int soff) {
  __builtin_amdgcn_raw_buffer_store_b128(v, rs, (int)((unsigned)voff + (unsigned)soff), 0, AUX_SC1);
}
__device__ __forceinline__ int sel(bool c, int a) {
  PIN(a);
  return c ? a : OOB;
}
__device__ __forceinline__ v4u ld_split(__amdgpu_buffer_rsrc_t rs, int lane, int soff) {
  const v2u a = ld64(rs, lane * 8, soff), b = ld64(rs, lane * 8 + 512, soff);
  return v4u{a.x, a.y, b.x, b.y};
}
__device__ __forceinline__ void st64(v2u v, __amdgpu_buffer_rsrc_t rs, int voff, int soff) {
  __builtin_amdgcn_raw_buffer_store_b64(v, rs, voff, soff, AUX_SC1);
}
__device__ __forceinline__ void flag_set(gu32* p, unsigned tag) {
  asm volatile("global_atomic_swap %0, %1, off sc1" :: "v"(p), "v"(tag) : "memory");
}
__device__ __forceinline__ void wait_flag(gu32* p, int n, unsigned tag, int lane) {
#pragma unroll 1
  for (unsigned spins = 0; spins < kSpinCap; ++spins) {
    unsigned v = tag;
    if (lane < n) asm volatile("global_atomic_add %0, %1, %2, off sc0 sc1\n\ts_waitcnt vmcnt(0)" : "=&v"(v) : "v"(p), "v"(0u) : "memory");
    if (__all(v == tag)) break;
    if (SLEEP) __builtin_amdgcn_s_sleep(SLEEP);
  }
  asm volatile("" ::: "memory");
}
__device__ __forceinline__ void sig_wait(volatile LAS unsigned* p) {
#pragma unroll 1
  for (unsigned spins = 0; spins < 64 * kSpinCap; ++spins) {
    if (*p != 0u) break;
    __builtin_amdgcn_s_sleep(1);
  }
  asm volatile("" ::: "memory");
}
__device__ __forceinline__ void prog_wait(volatile LAS v4u* p, unsigned stage) {
#pragma unroll 1
  for (unsigned spins = 0; spins < 64 * kSpinCap; ++spins) {
    const v4u v = *p;
    if (min(min(v.x, v.y), min(v.z, v.w)) >= stage) break;
    __builtin_amdgcn_s_sleep(1);
  }
  asm volatile("" ::: "memory");
}
__device__ __forceinline__ void publish(gu32* flag, unsigned tag, int lane) {
  DRAIN();
  if (lane == 0) flag_set(flag, tag);
}
__device__ __forceinline__ f32x4 add4(f32x4 a, f32x4 b) {
  f32x4 r;
#pragma unroll
  for (int i = 0; i < 4; ++i) { float x; asm("v_add_f32 %0, %1, %2" : "=v"(x) : "v"(a[i]), "v"(b[i])); r[i] = x; }
  return r;
}
__device__ __forceinline__ float xsum32(float x) {
  unsigned u = __builtin_bit_cast(unsigned, x), v = u;
  asm("" : "+v"(v));
  auto r = __builtin_amdgcn_permlane32_swap(u, v, false, false);
  const unsigned a = r[0], b = r[1];
  return __builtin_bit_cast(float, a) + __builtin_bit_cast(float, b);
}
__device__ __forceinline__ float xsum16(float x) {
  unsigned u = __builtin_bit_cast(unsigned, x), v = u;
  asm("" : "+v"(v));
  auto r = __builtin_amdgcn_permlane16_swap(u, v, false, false);
  const unsigned a = r[0], b = r[1];
  return __builtin_bit_cast(float, a) + __builtin_bit_cast(float, b);
}
__device__ __forceinline__ float xsum8(float x) {
  return x + __builtin_bit_cast(float, __builtin_amdgcn_update_dpp(0, __builtin_bit_cast(int, x), 0x128, 0xf, 0xf, false));
}
__device__ __forceinline__ f32x2 phi2(f32x2 t) {
  const f32x2 u = t * 1.4426950408889634f, v = t + 1e-6f;
  const f32x2 e = {__builtin_amdgcn_fmed3f(__builtin_amdgcn_exp2f(u[0]), 0.f, 1.f), __builtin_amdgcn_fmed3f(__builtin_amdgcn_exp2f(u[1]), 0.f, 1.f)};
  return f32x2{fmaxf(v[0], 1e-6f), fmaxf(v[1], 1e-6f)} + e;
}
__device__ __forceinline__ f32x2 gelu2(f32x2 x) {
  const f32x2 u = x * (x * x * -0.1029432397f + -2.302208198f);
  const f32x2 d = f32x2{__builtin_amdgcn_exp2f(u[0]), __builtin_amdgcn_exp2f(u[1])} + 1.f;
  return x * f32x2{__builtin_amdgcn_rcpf(d[0]), __builtin_amdgcn_rcpf(d[1])};
}

#define STAMP(i) do { } while (0)

__global__ void __launch_bounds__(512, 2)
fused_block(const unsigned short* __restrict__ x, const unsigned short* __restrict__ wqkv, const unsigned short* __restrict__ wo,
            const unsigned short* __restrict__ w1, const unsigned short* __restrict__ b1, const unsigned short* __restrict__ w2,
            const unsigned short* __restrict__ b2, unsigned short* __restrict__ out, unsigned char* ws) {
  __shared__ __attribute__((aligned(16))) char smem[LDS_BYTES];
  unsigned long long pc0;
  asm volatile("s_getpc_b64 %0" : "=s"(pc0));
  LAS char* L = (LAS char*)smem;
  const unsigned lds0 = (unsigned)(size_t)L;
  const int tid = threadIdx.x, lane = tid & 63, w = __builtin_amdgcn_readfirstlane(tid >> 6);
  const int b = blockIdx.x, xcd = b & 7, bi = b >> 3;
  const int g = lane >> 4, t = lane & 15;
  const __amdgpu_buffer_rsrc_t rs = __builtin_amdgcn_make_buffer_rsrc((void*)ws, 0, WS_BYTES, 0x00020000);
  gu32* flags = (gu32*)(ws + WS_FLAG);
#define FLAG(set, grp) (flags + ((set) * FLAG_SET + (grp) * 256) / 4)
  asm volatile("" :: "s"(x), "s"(wqkv), "s"(wo), "s"(w1), "s"(b1), "s"(w2), "s"(b2), "s"(out), "s"(ws));
  STAMP(0);
  const int r128 = 4 * (xcd >> 1) + (bi >> 3), c16 = 8 * (xcd & 1) + (bi & 7);
  const int r64 = 8 * (xcd >> 1) + (bi >> 2), c8 = 4 * (xcd & 1) + (bi & 3);

  const __amdgpu_buffer_rsrc_t rx = __builtin_amdgcn_make_buffer_rsrc((void*)x, 0, S * D * 2, 0x00020000);
  const int hw = w - 4, ht = tid - 256;
  volatile LAS unsigned* sig = (volatile LAS unsigned*)(L + L_CS + 1024);
  volatile LAS v4u* prog = (volatile LAS v4u*)(L + L_CS + 1088);
#define PROG(n) do { asm volatile("" ::: "memory"); sig[16 + w] = (n); } while (0)

  unsigned tag = __hip_atomic_load((gu32*)(ws + WS_EPOCH), RLX_AGENT);
  const __amdgpu_buffer_rsrc_t rq = __builtin_amdgcn_make_buffer_rsrc((void*)wqkv, 0, D * 3 * D * 2, 0x00020000);
  v4u tv[12];
  const int q10 = lane / 10, c10 = lane % 10;
  if (w < 4) {
#pragma unroll
    for (int j = 0; j < 3; ++j)
      tv[j] = __builtin_amdgcn_raw_buffer_load_b128(rx, (lane & 7) * 1280 + (lane >> 3) * 16, (j < 2 || w < 2) ? 8 * b * 1280 + (w + 4 * j) * 128 : OOB, 0);
  } else if (w == 4) {
    const int vo = sel(lane < 40, q10 * 3840 + (c10 < 5 ? 1280 : 2480) + c10 * 16);
#pragma unroll
    for (int j = 0; j < 10; ++j) tv[j] = __builtin_amdgcn_raw_buffer_load_b128(rq, vo, (40 * r128 + 4 * j) * 3840 + c16 * 80, 0);
  } else if (w == 7) {
    *(LAS v4u*)(L + L_CS + 512 + lane * 16) = v4u{0u, 0u, 0u, 0u};
  }
  BAR();

  if (w < 4) {
  const int grp32 = 4 * r128 + w;
  constexpr int AW = 6;

  {
    const int tok = 8 * b + (lane & 7), cl = lane >> 3;
    const bool has3 = w < 2;
    int xd = ((tok >> 4) * 20 + 2 * w + (cl >> 2)) * 1024 + ((cl & 3) * 16 + (tok & 15)) * 16;
    PIN(xd);
    SCHED();
    f32x2 f[12];
#pragma unroll
    for (int j = 0; j < 3; ++j)
#pragma unroll
      for (int e = 0; e < 4; ++e) f[j * 4 + e] = bf2(tv[j][e]);
    f32x2 s2 = (f[0] + f[1]) + (f[2] + f[3]) + ((f[4] + f[5]) + (f[6] + f[7])) + ((f[8] + f[9]) + (f[10] + f[11]));
    const float mloc = xsum32(xsum16(xsum8(s2[0] + s2[1]))) * (has3 ? 1.f / 192.f : 1.f / 128.f);
    const float m3 = has3 ? mloc : 0.f;
    f32x2 q2 = {0.f, 0.f}, q3 = {0.f, 0.f};
#pragma unroll
    for (int j = 0; j < 12; j += 2) {
      const f32x2 d0 = f[j] - (j < 8 ? mloc : m3), d1 = f[j + 1] - (j < 8 ? mloc : m3);
      q2 += d0 * d0; q3 += d1 * d1;
    }
    q2 += q3;
    const float m2loc = xsum32(xsum16(xsum8(q2[0] + q2[1])));
    if (lane < 8) *(volatile LAS f32x4*)(L + L_CS + 512 + (lane * 4 + w) * 16) = f32x4{mloc, m2loc, 1.f, 1.f};
    STAMP(25);
    f32x4 pr[4];
#pragma unroll 1
    for (unsigned spins = 0; spins < 64 * kSpinCap; ++spins) {
      bool ok = true;
#pragma unroll
      for (int ww = 0; ww < 4; ++ww) {
        pr[ww] = *(volatile LAS f32x4*)(L + L_CS + 512 + ((lane & 7) * 4 + ww) * 16);
        ok = ok && pr[ww][2] == 1.f;
      }
      if (__all(ok)) break;
    }
    const f32x4 p01 = {pr[0][0], pr[0][1], pr[1][0], pr[1][1]}, p23 = {pr[2][0], pr[2][1], pr[3][0], pr[3][1]};
    const float mean = (p01[0] + p01[2]) * 0.3f + (p23[0] + p23[2]) * 0.2f;
    const float e0 = p01[0] - mean, e1 = p01[2] - mean, e2 = p23[0] - mean, e3 = p23[2] - mean;
    const float m2 = (p01[1] + p01[3]) + (p23[1] + p23[3]) + 192.f * (e0 * e0 + e1 * e1) + 128.f * (e2 * e2 + e3 * e3);
    const float rstd = __builtin_amdgcn_rsqf(m2 * (1.f / D) + LN_EPS);
#pragma unroll
    for (int j = 0; j < 3; ++j) {
      v4u o;
#pragma unroll
      for (int e = 0; e < 4; ++e) o[e] = pk2((f[j * 4 + e] - mean) * rstd);
      st128(o, rs, (j < 2 || has3) ? xd : OOB, WS_XN + j * 8192);
    }
    STAMP(26);
    PIN(tag); tag += 1u;
    publish(FLAG(0, b >> 2) + (b & 3) * 4 + w, tag, lane);
  }

  STAMP(1);

  v4u xr[3];
  {
    wait_flag(FLAG(0, grp32) + lane, 16, tag, lane);
    STAMP(2);
    v4u a[20][2];
    const int abase = WS_XN + 2 * grp32 * 20 * 1024;
#pragma unroll
    for (int ks = 0; ks < S0; ++ks)
#pragma unroll
      for (int m = 0; m < 2; ++m) a[ks][m] = ld128(rs, lane * 16, abase + (m * 20 + ks) * 1024);
    SCHED();
    BAR_RAW();
    STAMP(27);
    {
      f32x4 acc[2][5];
      const LBase bp(lds0 + L_BKV + (lane >> 2) * 160 + (lane & 3) * 8);
      kloop<2, 5, 20, 1, false>(acc, [&](int ks, int s, int h) { return bp.rd(ks * 5120 + h * 2560 + s * 32); },
        [&](int ks, int m) { return a[ks][m]; },
        [&](int ks) {
#pragma unroll
          for (int j = ld_hi(ks - 1, S0, AW, 20); j < ld_hi(ks, S0, AW, 20); ++j)
#pragma unroll
            for (int m = 0; m < 2; ++m) a[j][m] = ld128(rs, lane * 16, abase + (m * 20 + j) * 1024);
          if (ks == 3 || ks == 10) BAR_RAW();
          if (ks == 20 - AW) PROG(1);
        });
      STAMP(3);
      v4u ka[3], vb[3];
#pragma unroll
      for (int s = 0; s < 3; ++s)
#pragma unroll
        for (int m = 0; m < 2; ++m) {
          vb[s][2 * m] = pk2(lo2(acc[m][s + 2])); vb[s][2 * m + 1] = pk2(hi2(acc[m][s + 2]));
        }
#pragma unroll
      for (int e = 0; e < 4; ++e) vb[0][e] = t < 8 ? 0x3F803F80u : vb[0][e];
      const f32x4 z4 = {0.f, 0.f, 0.f, 0.f};
      LAS f32x4* red = (LAS f32x4*)(L + L_RED) + w * 512 + lane;
#pragma unroll
      for (int s = 0; s < 3; ++s) {
#pragma unroll
        for (int m = 0; m < 2; ++m) { ka[s][2 * m] = pk2(phi2(lo2(acc[m][s]))); ka[s][2 * m + 1] = pk2(phi2(hi2(acc[m][s]))); }
#pragma unroll
        for (int sb = 0; sb < 3; ++sb)
          if (s > 0 || sb < 2) red[(3 * s + sb - (s > 0)) * 64] = mfma(ka[s], vb[sb], z4);
      }
      STAMP(28);
      BAR();
    }
    STAMP(4);
    f32x4 qa[2][3];
    {
      unsigned bp0 = lds0 + L_BQ + (lane >> 2) * 64 + (lane & 3) * 8 + (g & 1) * 32;
      unsigned bp1 = lds0 + L_BQ + (lane >> 2) * 64 + (lane & 3) * 8 + 32 - (g & 1) * 32;
      unsigned bp2 = lds0 + L_BQ + 2048 + (lane >> 2) * 16 + (lane & 1) * 8;
      PIN(bp0); PIN(bp1); PIN(bp2);
      kloop<2, 3, 20, 2, true>(qa,
        [&](int ks, int s, int h) {
          return s == 0 ? rdtr((LAS char*)bp0 + ks * 2560 + h * 1024) : s == 1 ? rdtr((LAS char*)bp1 + ks * 2560 + h * 1024) : rdtr((LAS char*)bp2 + ks * 2560 + h * 256);
        },
        [&](int ks, int m) { return a[ks][m]; },
        [&](int ks) {});
    }
    v4u pq0[2], pq1[2];
#pragma unroll
    for (int m = 0; m < 2; ++m) {
      pq0[m].x = pk2(phi2(lo2(qa[m][0]))); pq0[m].y = pk2(phi2(hi2(qa[m][0])));
      pq0[m].z = pk2(phi2(lo2(qa[m][1]))); pq0[m].w = pk2(phi2(hi2(qa[m][1])));
      const unsigned lo = pk2(phi2(lo2(qa[m][2]))), hi = pk2(phi2(hi2(qa[m][2])));
      pq1[m].x = g < 2 ? lo : 0u; pq1[m].y = g < 2 ? hi : 0u; pq1[m].z = 0u; pq1[m].w = 0u;
      PIN(pq0[m]); PIN(pq1[m]);
    }
    STAMP(5);
    BAR_RAW();
#pragma unroll
    for (int j = 0; j < 3; ++j)
      xr[j] = __builtin_amdgcn_raw_buffer_load_b128(rx, sel(j < 2 || g < 2, t * 1280 + g * 16), (4 * r64 + w) * 16 * 1280 + c8 * 160 + j * 64, 0);
    STAMP(17);
    {
      v4u kv[4][2];
#pragma unroll
      for (int es = 3; es >= 0; --es)
#pragma unroll
        for (int ks = 0; ks < 2; ++ks) kv[es][ks] = *(LAS v4u*)(L + L_KVF + (es * 2 + ks) * 1024 + lane * 16);
      SCHED();
      f32x4 o[2][4];
#pragma unroll
      for (int es = 3; es >= 0; --es)
#pragma unroll
        for (int m = 0; m < 2; ++m) o[m][es] = mfma(kv[es][0], pq0[m], f32x4{0.f, 0.f, 0.f, 0.f});
#pragma unroll
      for (int es = 3; es >= 0; --es)
#pragma unroll
        for (int m = 0; m < 2; ++m) o[m][es] = mfma(kv[es][1], pq1[m], o[m][es]);
#pragma unroll
      for (int m = 0; m < 2; ++m) {
        const float i0 = __builtin_amdgcn_rcpf(o[m][3][0] + 1e-6f), i1 = __builtin_amdgcn_rcpf(o[m][3][1] + 1e-6f);
        const float im = g == 0 ? i0 : i1;
        v4u ov;
        ov.x = pk2(lo2(o[m][0]) * i0); ov.y = pk2(hi2(o[m][0]) * i0);
        ov.z = pk2(lo2(o[m][1]) * im); ov.w = pk2(hi2(o[m][1]) * im);
        v2u ol; ol.x = pk2(lo2(o[m][2]) * i1); ol.y = pk2(hi2(o[m][2]) * i1);
        const int tt = 2 * grp32 + m;
        st128(ov, rs, lane * 16, WS_O + (tt * 20 + c16) * 1024);
        st64(ol, rs, g < 2 ? lane * 8 : OOB, WS_O + (tt * 20 + 16) * 1024 + c16 * 256);
      }
      STAMP(18);
      publish(FLAG(2, grp32) + c16, tag, lane);
    }
    STAMP(7);
  }

  {
    const int tt = 4 * r64 + w;
    v4u eye[2];
#pragma unroll
    for (int hf = 0; hf < 2; ++hf) {
      const int e = 16 * hf + t - 8 * g;
#pragma unroll
      for (int d = 0; d < 4; ++d) eye[hf][d] = e == 2 * d ? 0x3F80u : e == 2 * d + 1 ? 0x3F800000u : 0u;
      PIN(eye[hf]);
    }
    f32x4 acc[1][5];
#pragma unroll
    for (int s = 0; s < 5; ++s) { acc[0][s] = mfma(eye[s & 1], xr[s >> 1], f32x4{0.f, 0.f, 0.f, 0.f}); PIN(acc[0][s]); }
    wait_flag(FLAG(2, tt >> 1) + lane, 16, tag, lane);
    STAMP(8);
    if (b == 0 && tid == 0) __hip_atomic_store((gu32*)(ws + WS_EPOCH), tag, RLX_AGENT);
    v4u a[20];
#pragma unroll
    for (int ks = 0; ks < 2 * S0; ++ks) a[ks] = ld128(rs, lane * 16, WS_O + (tt * 20 + ks) * 1024);
    SCHED();
    const LBase bp(lds0 + L_WO + (lane >> 2) * 160 + (lane & 3) * 8);
    kloop<1, 5, 20, 1, true, 1>(acc, [&](int ks, int s, int h) { return bp.rd(ks * 5120 + h * 2560 + s * 32); },
      [&](int ks, int m) { return a[ks]; },
      [&](int ks) {
#pragma unroll
        for (int j = ld_hi(ks - 1, 2 * S0, 2 * AW, 20); j < ld_hi(ks, 2 * S0, 2 * AW, 20); ++j)
          a[j] = j < 16 ? ld128(rs, lane * 16, WS_O + (tt * 20 + j) * 1024) : ld_split(rs, lane, WS_O + (tt * 20 + j) * 1024);
        if (ks == 8) PROG(2);
        if (ks == 14) PROG(3);
      });
    PROG(4);
    STAMP(9);
    v2u hb[5];
    f32x2 hv[10];
#pragma unroll
    for (int s = 0; s < 5; ++s) {
      hb[s].x = pk2(lo2(acc[0][s]));
      hb[s].y = pk2(hi2(acc[0][s]));
      hv[2 * s] = bf2(hb[s].x); hv[2 * s + 1] = bf2(hb[s].y);
    }
    st128(v4u{hb[0].x, hb[0].y, hb[1].x, hb[1].y}, rs, lane * 16, WS_H + (tt * 20 + 2 * c8) * 1024);
    st128(v4u{hb[2].x, hb[2].y, hb[3].x, hb[3].y}, rs, lane * 16, WS_H + (tt * 20 + 2 * c8 + 1) * 1024);
    st64(hb[4], rs, lane * 8, WS_H + (tt * 20 + 16) * 1024 + c8 * 512);
    const f32x2 s2 = ((hv[0] + hv[1]) + (hv[2] + hv[3])) + ((hv[4] + hv[5]) + (hv[6] + hv[7])) + (hv[8] + hv[9]);
    const float mc = xsum32(xsum16(s2[0] + s2[1])) * (1.f / 80.f);
    f32x2 q2 = {0.f, 0.f}, q3 = {0.f, 0.f};
#pragma unroll
    for (int j = 0; j < 10; j += 2) { const f32x2 d0 = hv[j] - mc, d1 = hv[j + 1] - mc; q2 += d0 * d0; q3 += d1 * d1; }
    q2 += q3;
    const float m2 = xsum32(xsum16(q2[0] + q2[1]));
    st64(v2u{__builtin_bit_cast(unsigned, mc), __builtin_bit_cast(unsigned, m2)}, rs, g == 0 ? t * 8 : OOB, WS_ST + (tt * 8 + c8) * 128);
    STAMP(20);
    publish(FLAG(3, tt >> 1) + (tt & 1) * 8 + c8, tag, lane);
    STAMP(10);
  }

  {
    wait_flag(FLAG(3, grp32) + lane, 16, tag, lane);
    STAMP(11);
    v4u stt[2];
#pragma unroll
    for (int m = 0; m < 2; ++m) {
      const v2u s0 = ld64(rs, g * 256 + t * 8, WS_ST + (2 * grp32 + m) * 1024), s1 = ld64(rs, g * 256 + 128 + t * 8, WS_ST + (2 * grp32 + m) * 1024);
      stt[m] = v4u{s0.x, s0.y, s1.x, s1.y};
    }
    v4u a[20][2];
    const int abase = WS_H + 2 * grp32 * 20 * 1024;
#pragma unroll
    for (int ks = 0; ks < S0; ++ks)
#pragma unroll
      for (int m = 0; m < 2; ++m) a[ks][m] = ld128(rs, lane * 16, abase + (m * 20 + ks) * 1024);
    SCHED();
    float nm[2], rr[2];
#pragma unroll
    for (int m = 0; m < 2; ++m) {
      const f32x4 sv = __builtin_bit_cast(f32x4, stt[m]);
      const float mean = xsum32(xsum16(sv[0] + sv[2])) * 0.125f;
      const float m2 = xsum32(xsum16(sv[1] + sv[3] + 80.f * ((sv[0] - mean) * (sv[0] - mean) + (sv[2] - mean) * (sv[2] - mean))));
      const float var = m2 * (1.f / D);
      const float r1 = __builtin_amdgcn_rsqf(var + LN_EPS);
      rr[m] = r1 * __builtin_amdgcn_rsqf(var * r1 * r1 + LN_EPS);
      nm[m] = -mean * rr[m];
      PIN(rr[m]); PIN(nm[m]);
    }
    BAR_RAW();
    STAMP(22);
    const unsigned l64 = lds0 + (lane >> 2) * 64 + (lane & 3) * 8;
    const LBase bp0(l64 + (g & 1) * 32), bp1(l64 + 32 - (g & 1) * 32);
    const int h1base = WS_H1 + 2 * grp32 * 48 * 1024;
    auto finish = [&](const f32x4& c0, const f32x4& c1, int j, int m) {
      v4u o;
#pragma unroll
      for (int s = 0; s < 2; ++s) {
        const f32x4 cs = *(LAS f32x4*)(L + L_CS + ((2 * j + s) * 4 + g) * 16), bb = *(LAS f32x4*)(L + L_B1 + ((2 * j + s) * 4 + g) * 16);
        const f32x4 c = s ? c1 : c0;
        o[2 * s] = pk2(gelu2(lo2(c) * rr[m] + (lo2(cs) * nm[m] + lo2(bb))));
        o[2 * s + 1] = pk2(gelu2(hi2(c) * rr[m] + (hi2(cs) * nm[m] + hi2(bb))));
      }
      st128(o, rs, lane * 16, h1base + (m * 48 + (c16 >> 3) * 24 + (j < 2 ? 2 * (c16 & 7) + j : 16 + (c16 & 7))) * 1024);
    };
    {
      f32x4 acc[2][4];
      kloop<2, 4, 20, 1, true>(acc, [&](int ks, int s, int h) { return ((s & 1) ? bp1 : bp0).rd(L_W1 + (s >> 1) * 40960 + ks * 2048 + h * 1024); },
        [&](int ks, int m) { return a[ks][m]; },
        [&](int ks) {
#pragma unroll
          for (int j = ld_hi(ks - 1, S0, AW, 20); j < ld_hi(ks, S0, AW, 20); ++j)
#pragma unroll
            for (int m = 0; m < 2; ++m)
              a[j][m] = j < 16 ? ld128(rs, lane * 16, abase + (m * 20 + j) * 1024) : ld_split(rs, lane, abase + (m * 20 + j) * 1024);
          if (ks == 20 - AW) PROG(5);
        });
      PROG(6);
      STAMP(12);
#pragma unroll
      for (int j = 0; j < 2; ++j)
#pragma unroll
        for (int m = 0; m < 2; ++m) finish(acc[m][2 * j], acc[m][2 * j + 1], j, m);
      SCHED();
      STAMP(24);
    }
    f32x4 accb[2][2];
    const int kh = w & 1, tt = 4 * r64 + w;
    v4u hr01, hr23;
    v2u hr4;
    kloop<2, 2, 20, 3, true>(accb, [&](int ks, int s, int h) { return (s ? bp1 : bp0).rd(L_W1 + 81920 + ks * 2048 + h * 1024); },
      [&](int ks, int m) { return a[ks][m]; },
      [&](int ks) {
        if (ks == PUBA) { publish(FLAG(4, grp32) + c16, tag, lane); PROG(7); }
        if (ks == PUBA + 2) {
          hr01 = ld128(rs, lane * 16, WS_H + (tt * 20 + 2 * c8) * 1024);
          hr23 = ld128(rs, lane * 16, WS_H + (tt * 20 + 2 * c8 + 1) * 1024);
          hr4 = ld64(rs, lane * 8, WS_H + (tt * 20 + 16) * 1024 + c8 * 512);
        }
      });
    STAMP(13);
    f32x4 acc[2][5];
    {
      const unsigned hrr[10] = {hr01.x, hr01.y, hr01.z, hr01.w, hr23.x, hr23.y, hr23.z, hr23.w, hr4.x, hr4.y};
#pragma unroll
      for (int s = 0; s < 5; ++s) {
        acc[0][s] = *(LAS f32x4*)(L + L_B2 + (s * 4 + g) * 16) + f32x4{bf_lo(hrr[2 * s]), bf_hi(hrr[2 * s]), bf_lo(hrr[2 * s + 1]), bf_hi(hrr[2 * s + 1])};
        PIN(acc[0][s]);
      }
    }
    int oo[3];
    unsigned tw, tr;
    {
      int ln = lane;
      PIN(ln);
      const int qq = ln / 10, cc = ln % 10;
#pragma unroll
      for (int j = 0; j < 3; ++j) { oo[j] = sel(ln < (j < 2 ? 60 : 40), ((tt * 16 + 6 * j + qq) * D + 80 * c8) * 2 + cc * 16); PIN(oo[j]); }
      tw = lds0 + L_W2 + 122880 + w * 3072 + t * 176 + g * 8; tr = lds0 + L_W2 + 122880 + w * 3072 + qq * 176 + cc * 16;
      PIN(tw); PIN(tr);
    }
    BAR_RAW();
    STAMP(14);
    constexpr int WIN = 8;
    v4u ar[WIN][2];
    const int a4[2] = {WS_H1 + (tt * 48 + kh * 24) * 1024, WS_H1 + ((tt ^ 1) * 48 + kh * 24) * 1024};
#pragma unroll
    for (int ks = 0; ks < WIN - 1; ++ks)
#pragma unroll
      for (int m = 0; m < 2; ++m) ar[ks][m] = ld128(rs, lane * 16, a4[m] + ks * 1024);
    SCHED();
#pragma unroll
    for (int m = 0; m < 2; ++m) finish(accb[m][0], accb[m][1], 2, m);
    STAMP(29);
    const LBase bp(lds0 + L_W2 + kh * 5120 + (lane >> 2) * 160 + (lane & 3) * 8);
    kloop<2, 5, 24, 1, true, 1>(acc, [&](int ks, int s, int h) { return bp.rd((ks % 15) * 10240 + h * 2560 + s * 32); },
      [&](int ks, int m) { return ar[ks % WIN][m]; },
      [&](int ks) {
        if (ks == 5) PROG(8);
        if (ks == 9 || ks == 14 || ks == 19) BAR_RAW();
        if (ks == 3) publish(FLAG(10, grp32) + c16, tag, lane);
        if (ks >= 3) {
#pragma unroll
          for (int j = ks == 3 ? WIN - 1 : ld_hi(ks - 1, 1, WIN - 1, 24); j < ld_hi(ks, 1, WIN - 1, 24); ++j)
#pragma unroll
            for (int m = 0; m < 2; ++m) ar[j % WIN][m] = ld128(rs, lane * 16, a4[m] + j * 1024);
        }
      });
    STAMP(15);
    LAS f32x4* xch = (LAS f32x4*)(L + L_W2 + 102400);
#pragma unroll
    for (int s = 0; s < 5; ++s) xch[((w ^ 1) * 5 + s) * 64 + lane] = acc[1][s];
    BAR();
    const __amdgpu_buffer_rsrc_t rout = __builtin_amdgcn_make_buffer_rsrc((void*)out, 0, S * D * 2, 0x00020000);
    f32x4 ot[5];
#pragma unroll
    for (int s = 0; s < 5; ++s) ot[s] = xch[(w * 5 + s) * 64 + lane];
    SCHED();
#pragma unroll
    for (int s = 0; s < 5; ++s) {
      const f32x4 v = acc[0][s] + ot[s];
      *(LAS v2u*)((LAS char*)tw + s * 32) = v2u{pk2(lo2(v)), pk2(hi2(v))};
    }
    asm volatile("" ::: "memory");
    v4u ov[3];
#pragma unroll
    for (int j = 0; j < 3; ++j) ov[j] = *(LAS v4u*)((LAS char*)tr + j * 6 * 176);
#pragma unroll
    for (int j = 0; j < 3; ++j) __builtin_amdgcn_raw_buffer_store_b128(ov[j], rout, oo[j], 0, 0);
    STAMP(16);
  }
  return;
  }

  {
    const v4u srd_ws = mk_srd(ws, WS_BYTES);
    STAMP(16);
    const int q5 = lane / 5, c5 = lane % 5;
    const int f40m = c5 < 4 ? 512 : 128, f40l = c5 < 4 ? q5 * 64 + (c5 ^ (2 * ((q5 >> 2) & 1))) * 16 : 2048 + q5 * 16;
    v2u ub1, ub2;
    unsigned pf0 = 0;
    if (hw == 0) {
      const int dof = sel(lane < 40, lane * 16);
#pragma unroll
      for (int j = 0; j < 10; ++j) {
        const int k = 40 * r128 + 4 * j, R = (k & ~31) + 16 * ((k >> 2) & 1) + 4 * ((k >> 3) & 3);
        st128(tv[j], rs, dof, WS_TKV + c16 * 153600 + R * 160);
      }
      STAMP(18);
      PIN(tag); tag += 1u;
      publish(FLAG(6, c16) + r128, tag, lane);
      STAMP(13);
    } else if (hw == 1) {
      const __amdgpu_buffer_rsrc_t ro = __builtin_amdgcn_make_buffer_rsrc((void*)wo, 0, D * D * 2, 0x00020000);
      const int vq = sel(lane < 40, (8 * (q5 >> 2) + (q5 & 3)) * 3840 + c5 * 16), vo = sel(lane < 40, q10 * 1280 + c10 * 16);
#pragma unroll
      for (int j = 0; j < 5; ++j) {
        const int i8 = 5 * r128 + j, k = 32 * (i8 >> 2) + 16 * (i8 & 1) + 4 * ((i8 >> 1) & 1);
        tv[j] = __builtin_amdgcn_raw_buffer_load_b128(rq, vq, k * 3840 + c16 * 80, 0);
      }
#pragma unroll
      for (int j = 0; j < 5; ++j) tv[5 + j] = __builtin_amdgcn_raw_buffer_load_b128(ro, vo, (20 * r64 + 4 * j) * 1280 + c8 * 160, 0);
    } else if (hw == 2) {
      const GAS unsigned char* cb = (const GAS unsigned char*)(pc0 & ~127ull);
      if (lane < 11) pf0 = *(const GAS unsigned*)(cb + (lane * 32 + bi) * 128);
    } else if (hw == 3) {
      const __amdgpu_buffer_rsrc_t rb1 = __builtin_amdgcn_make_buffer_rsrc((void*)b1, 0, F * 2, 0x00020000);
      const __amdgpu_buffer_rsrc_t rb2 = __builtin_amdgcn_make_buffer_rsrc((void*)b2, 0, D * 2, 0x00020000);
      ub1 = __builtin_amdgcn_raw_buffer_load_b64(rb1, lane * 8, c16 * 192, 0);
      ub2 = __builtin_amdgcn_raw_buffer_load_b64(rb2, lane * 8, c8 * 160, 0);
    }
    asm volatile("" ::: "memory");
    int red_off, kvf_off, kvp_off;
    {
      const int it = ht < 210 ? ht : 209, ie = it / 5, ic = it % 5;
      const int ihead = ie < 40 ? (ie >= 20) : ie - 40;
      const int id = 20 * ihead + 4 * ic, dg = id >> 2;
      const int sa = id >> 4, col = 40 + ie;
      const int tile = 3 * sa + (ie < 40 ? (col >> 4) - 2 : 0) - (sa > 0);
      red_off = lds0 + L_RED + (tile * 64 + (dg & 3) * 16 + (ie < 40 ? (col & 15) : 0)) * 16;
      kvf_off = lds0 + L_KVF + ((ie < 40 ? (ie >> 4) : 3) * 2 + (dg >> 3)) * 1024 + ((dg & 3) * 16 + (ie < 40 ? (ie & 15) : ihead)) * 16 + ((dg >> 2) & 1) * 8;
      kvp_off = WS_KVP + (c16 * 16 + r128) * 4096 + ht * 16;
      PIN(red_off); PIN(kvf_off); PIN(kvp_off);
    }
    int vo12, td[12];
    if (hw >= 2) {
      const int q12 = lane / 12, c12 = lane % 12;
      vo12 = hw == 2 ? sel(lane < 48, q12 * 3072 + c12 * 16) : sel(lane < 40, q10 * 1280 + c10 * 16);
#pragma unroll
      for (int j = 0; j < 12; ++j) {
        const int cc = r128 >> 1, off = 40 * (r128 & 1) + 4 * j;
        const int R = off < 64 ? 64 * cc + off : 32 * (16 + (cc >> 1)) + 16 * (cc & 1) + off - 64;
        const int d1 = WS_T1 + c16 * 122880 + (c12 >> 2) * 40960 + (R + q12) * 64 + ((c12 & 3) ^ (2 * ((R >> 2) & 1))) * 16;
        const int pc = r64 >> 1, ko = 48 * (r64 & 1) + 4 * j, jj = ko >> 5;
        const int d2 = WS_T2 + c8 * 245760 + (2 * (jj < 2 ? 2 * (pc & 7) + jj : 16 + (pc & 7)) + (pc >> 3)) * 5120 + (ko & 31) * 160 + lane * 16;
        td[j] = sel(hw == 2 ? (lane < 48 && j < 10) : lane < 40, hw == 2 ? d1 : d2);
        PIN(td[j]);
      }
      PIN(vo12);
    }
    int l16 = lane * 16, h16 = ht * 16;
    PIN(l16); PIN(h16);
    gu32* fp1 = FLAG(1, c16) + lane;
    gu32* fp78 = lane < 32 ? FLAG(7, c8) + lane : FLAG(8, c16) + t;
    gu32* fp4 = FLAG(4, 2 * r64 + (g & 1)) + t;
    gu32* fp1w = FLAG(1, c16) + r128 * 4 + hw;
    PIN(fp1); PIN(fp78); PIN(fp4); PIN(fp1w);
    ((LAS v4u*)(L + L_KVF))[ht] = v4u{0u, 0u, 0u, 0u};
    ((LAS v4u*)(L + L_KVF))[256 + ht] = v4u{0u, 0u, 0u, 0u};
    asm volatile("" ::: "memory");
    if (hw != 0) {
      if (hw == 1) {
#pragma unroll
        for (int j = 0; j < 5; ++j) {
          const int i8 = 5 * r128 + j;
          st128(tv[j], rs, sel(lane < 40, (i8 & 3) * f40m + f40l), WS_TKV + c16 * 153600 + 102400 + (i8 >> 2) * 2560);
        }
#pragma unroll
        for (int j = 0; j < 5; ++j) {
          const int pp = r64 >> 1, e = 20 * (r64 & 1) + 4 * j;
          const int R = e < 32 ? 32 * pp + e : 32 * (16 + (pp >> 2)) + 8 * (pp & 3) + e - 32;
          st128(tv[5 + j], rs, sel(lane < 40, lane * 16), WS_TO + c8 * 102400 + R * 160);
        }
      }
      if (hw == 3) {
        if (lane < 24) *(LAS f32x4*)(L + L_B1 + lane * 16) = f32x4{bf_lo(ub1.x), bf_hi(ub1.x), bf_lo(ub1.y), bf_hi(ub1.y)};
        if (lane < 20) *(LAS f32x4*)(L + L_B2 + lane * 16) = f32x4{bf_lo(ub2.x), bf_hi(ub2.x), bf_lo(ub2.y), bf_hi(ub2.y)};
      }
      asm volatile("" :: "v"(pf0));
      PIN(tag); tag += 1u;
      DRAIN();
      if (hw == 1 && lane < 2) flag_set(lane == 0 ? FLAG(8, c16) + r128 : FLAG(7, c8) + r64, tag);
    }
    if (hw == 0) {
      wait_flag(FLAG(6, c16) + lane, 16, tag, lane);
      sig[0] = 1u;
    }
    sig_wait(sig);
    STAMP(1);
    const int kvq = WS_TKV + c16 * 153600, wos = WS_TO + c8 * 102400;
    dma_image<0, 24>(lds0 + L_BKV, l16, srd_ws, kvq, hw, 3 * (bi >> 3));
    dma_image<24, 56>(lds0 + L_BKV, l16, srd_ws, kvq, hw, 4 * (bi >> 3));
    asm volatile("s_waitcnt vmcnt(8)" ::: "memory");
    STAMP(2);
    BAR_RAW();
    dma_image<56, 100>(lds0 + L_BKV, l16, srd_ws, kvq, hw, 5 * (bi >> 3));
    asm volatile("s_waitcnt vmcnt(10)" ::: "memory");
    BAR_RAW();
    if (hw == 1) wait_flag(fp78, 48, tag, lane);
    DRAIN();
    STAMP(3);
    BAR_RAW();
    if (QPOS == 0) dma_image<100, 150>(lds0 + L_BKV, l16, srd_ws, kvq, hw, 6 * (bi >> 3));
    prog_wait(prog, 1);
    if (QPOS == 1) dma_image<100, 150>(lds0 + L_BKV, l16, srd_ws, kvq, hw, 6 * (bi >> 3));
    if (hw >= 2) {
      const __amdgpu_buffer_rsrc_t r12 = __builtin_amdgcn_make_buffer_rsrc((void*)(hw == 2 ? w1 : w2), 0, D * F * 2, 0x00020000);
      const int base = hw == 2 ? r128 * 122880 + c16 * 192 : r64 * 61440 + c8 * 160, step = hw == 2 ? 4 * 3072 : 4 * 1280;
#pragma unroll
      for (int j = 0; j < 12; ++j) tv[j] = __builtin_amdgcn_raw_buffer_load_b128(r12, vo12, (j < 10 || hw == 3) ? base + j * step : OOB, 0);
      asm volatile("s_waitcnt vmcnt(12)" ::: "memory");
    } else {
      dma_image<32, 68, 0, 2>(lds0 + L_WO, l16, srd_ws, wos, hw, bi >> 2);
      asm volatile("s_waitcnt vmcnt(18)" ::: "memory");
    }
    STAMP(4);
    BAR();
    __builtin_amdgcn_s_setprio(2);
    STAMP(11);
    {
      f32x4 pv[4];
#pragma unroll
      for (int ww = 0; ww < 4; ++ww) pv[ww] = *(LAS f32x4*)((LAS char*)red_off + ww * 8192);
      st128(__builtin_bit_cast(v4u, add4(add4(pv[0], pv[1]), add4(pv[2], pv[3]))), rs, kvp_off, 0);
      PROG(1);
      STAMP(12);
      DRAIN();
    }
    if (lane == 0) flag_set(fp1w, tag);
    if (hw == 0) {
      STAMP(10);
      __builtin_amdgcn_s_sleep(H1W);
      wait_flag(fp1, 64, tag, lane);
      sig[2] = 1u;
    } else {
      if (H1G) __builtin_amdgcn_s_sleep(H1G);
      dma_image<68, 100, 0, 3>(lds0 + L_WO, l16, srd_ws, wos, hw - 1, bi >> 2);
      prog_wait(prog + 1, 1);
      dma_image<0, 32, 0, 3>(lds0 + L_WO, l16, srd_ws, wos, hw - 1, 2 * (bi >> 2));
      if (hw >= 2) {
#pragma unroll
        for (int j = 0; j < 12; ++j) st128(tv[j], rs, td[j], 0);
      }
      STAMP(10);
      sig_wait(sig + 2);
    }
    STAMP(5);
    {
      v4u pv[16];
#pragma unroll
      for (int r = 0; r < 16; ++r) pv[r] = ld128(rs, h16, WS_KVP + (c16 * 16 + r) * 4096);
      SCHED();
      f32x4 v = __builtin_bit_cast(f32x4, pv[0]);
#pragma unroll
      for (int r = 1; r < 16; ++r) v = add4(v, __builtin_bit_cast(f32x4, pv[r]));
      v2u o; o.x = pk2(lo2(v)); o.y = pk2(hi2(v));
      *(LAS v2u*)((LAS char*)kvf_off) = o;
      if (ht >= 200) {
#pragma unroll
        for (int c = 1; c < 4; ++c) *(LAS v2u*)((LAS char*)kvf_off + c * 64) = o;
      }
    }
    DRAIN();
    STAMP(6);
    BAR();
    __builtin_amdgcn_s_setprio(0);
    if (hw >= 2 && lane == 0) flag_set(hw == 2 ? FLAG(5, c16) + r128 : FLAG(11, c8) + r64, tag);
    if (hw == 2) {
      f32x4 s0 = {0.f, 0.f, 0.f, 0.f}, s1 = {0.f, 0.f, 0.f, 0.f};
#pragma unroll
      for (int j = 0; j < 10; ++j) {
        s0 = add4(s0, f32x4{bf_lo(tv[j].x), bf_hi(tv[j].x), bf_lo(tv[j].y), bf_hi(tv[j].y)});
        s1 = add4(s1, f32x4{bf_lo(tv[j].z), bf_hi(tv[j].z), bf_lo(tv[j].w), bf_hi(tv[j].w)});
      }
#pragma unroll
      for (int d = 24; d >= 12; d -= 12) {
        f32x4 u0, u1;
#pragma unroll
        for (int e = 0; e < 4; ++e) { u0[e] = __shfl(s0[e], lane + d); u1[e] = __shfl(s1[e], lane + d); }
        s0 = add4(s0, u0); s1 = add4(s1, u1);
      }
      f32x4 sv;
#pragma unroll
      for (int e = 0; e < 4; ++e) { const float a0 = __shfl(s0[e], lane >> 1), a1 = __shfl(s1[e], lane >> 1); sv[e] = (lane & 1) ? a1 : a0; }
      st128(__builtin_bit_cast(v4u, sv), rs, sel(lane < 24, lane * 16), WS_CS + (c16 * 16 + r128) * 384);
      publish(FLAG(9, c16) + r128, tag, lane);
    }
    if (hw == 1) {
      __builtin_amdgcn_s_sleep(40);
      wait_flag(lane < 32 ? FLAG(11, c8) + lane : FLAG(5, c16) + t, 48, tag, lane);
      sig[3] = 1u;
    }
    sig_wait(sig + 3);
    prog_wait(prog, 2);
    if (hw == 0) {
      wait_flag(FLAG(9, c16) + lane, 16, tag, lane);
      v4u pv[16];
      const int co = sel(lane < 24, l16);
#pragma unroll
      for (int r = 0; r < 16; ++r) pv[r] = ld128(rs, co, WS_CS + (c16 * 16 + r) * 384);
      SCHED();
      f32x4 v = __builtin_bit_cast(f32x4, pv[0]);
#pragma unroll
      for (int r = 1; r < 16; ++r) v = add4(v, __builtin_bit_cast(f32x4, pv[r]));
      if (lane < 24) *(LAS f32x4*)(L + L_CS + lane * 16) = v;
    } else {
      dma_image<70, 120, 0, 3>(lds0 + L_W1, l16, srd_ws, WS_T1 + c16 * 122880, hw - 1, 6 * (bi >> 3));
      prog_wait(prog, 3);
      dma_image<0, 40, 0, 3>(lds0 + L_W1, l16, srd_ws, WS_T1 + c16 * 122880, hw - 1, 5 * (bi >> 3));
      STAMP(7);
      prog_wait(prog, 4);
      dma_image<40, 70, 0, 3>(lds0 + L_W1, l16, srd_ws, WS_T1 + c16 * 122880, hw - 1, 3 * (bi >> 3));
    }
    DRAIN();
    STAMP(8);
    BAR();
    const int w2s = WS_T2 + c8 * 245760;
    prog_wait(prog, 5);
    dma_image<0, 30>(lds0 + L_W2, l16, srd_ws, w2s, hw, bi >> 2);
    STAMP(14);
    prog_wait(prog, 6);
    dma_image<30, W2S>(lds0 + L_W2, l16, srd_ws, w2s, hw, bi >> 2);
    prog_wait(prog, 7);
    if (hw != 0) dma_image<W2S, 110, 0, 3>(lds0 + L_W2, l16, srd_ws, w2s, hw - 1, bi >> 2);
    DRAIN();
    STAMP(9);
    if (hw == 0) wait_flag(fp4, 32, tag, lane);
    STAMP(15);
    BAR_RAW();
    dma_image<110, 150>(lds0 + L_W2, l16, srd_ws, w2s, hw, 2 * (bi >> 2));
    prog_wait(prog, 8);
    if (hw == 0) wait_flag(fp4 + 6 * FLAG_SET / 4, 32, tag, lane);
    else dma_image<150, 198, 0, 3>(lds0 + L_W2 - 153600, l16, srd_ws, w2s, hw - 1, 3 * (bi >> 2));
    asm volatile("s_waitcnt vmcnt(16)" ::: "memory");
    STAMP(17);
    BAR_RAW();
    dma_image<198, 200>(lds0 + L_W2 - 153600, l16, srd_ws, w2s, hw, 0);
    dma_image<200, 240>(lds0 + L_W2 - 153600, l16, srd_ws, w2s, hw, 2 * (bi >> 2));
    asm volatile("s_waitcnt vmcnt(10)" ::: "memory");
    BAR_RAW();
    DRAIN();
    BAR_RAW();
  }

}
}

extern "C" void kernel_launch(void* const* d_in, const int* in_sizes, int n_in, void* d_out, int out_size, void* d_ws,
                              size_t ws_size, hipStream_t stream) {
  const unsigned short* x = (const unsigned short*)d_in[0];
  const unsigned short* wqkv = (const unsigned short*)d_in[1];
  const unsigned short* wo = (const unsigned short*)d_in[2];
  const unsigned short* w1 = (const unsigned short*)d_in[4];
  const unsigned short* b1 = (const unsigned short*)d_in[5];
  const unsigned short* w2 = (const unsigned short*)d_in[6];
  const unsigned short* b2 = (const unsigned short*)d_in[7];
  fused_block<<<256, 512, 0, stream>>>(x, wqkv, wo, w1, b1, w2, b2, (unsigned short*)d_out, (unsigned char*)d_ws);
}
```

```cpp
#include <hip/hip_runtime.h>
#include <hip/hip_bf16.h>

#define STAMPS 0
#define THR 0
#define TOUCH 0
#define CODEPF 1
#define FLAGPF 1
#define OUTAUX 0
#define OUTPF 0
#define S0 6
#define XRPOS 0
#define QPOS 0
#define H1W 8
#define H1G 2
#define W2S 70
#define PUBA 8
#define NCDELAY 0
#define HPRIO 0
#define WOLATE 0
#define SLEEP 1

#define GAS __attribute__((address_space(1)))
#define LAS __attribute__((address_space(3)))
#define RLX_AGENT __ATOMIC_RELAXED, __HIP_MEMORY_SCOPE_AGENT
typedef unsigned v4u __attribute__((ext_vector_type(4)));
typedef unsigned v2u __attribute__((ext_vector_type(2)));
typedef float f32x4 __attribute__((ext_vector_type(4)));
typedef float f32x2 __attribute__((ext_vector_type(2)));
typedef __bf16 bf16x8 __attribute__((ext_vector_type(8)));
typedef __bf16 bf16x2 __attribute__((ext_vector_type(2)));
typedef short s16x4 __attribute__((ext_vector_type(4)));
typedef GAS unsigned gu32;

namespace {
constexpr int S = 2048, D = 640, F = 1536;
constexpr float LN_EPS = 1e-5f;
constexpr int AUX_SC1 = 16;
constexpr unsigned kSpinCap = 1u << 17;
constexpr int OOB = 0x7FFFFF00;

constexpr int WS_EPOCH = 0;
constexpr int WS_FLAG = 4096;
constexpr int FLAG_SET = 64 * 256;
constexpr int WS_XN = 1 << 20;
constexpr int WS_O = WS_XN + 128 * 20 * 1024;
constexpr int WS_H = WS_O + 128 * 20 * 1024;
constexpr int WS_H1 = WS_H + 128 * 20 * 1024;
constexpr int WS_KVP = WS_H1 + 128 * 48 * 1024;
constexpr int WS_ST = WS_KVP + 16 * 16 * 4096;
constexpr int WS_DBG = WS_ST + 2048 * 64;
constexpr int WS_TKV = WS_DBG + 256 * 8 * 32 * 8;
constexpr int WS_TO = WS_TKV + 16 * 153600;
constexpr int WS_T1 = WS_TO + 8 * 102400;
constexpr int WS_T2 = WS_T1 + 16 * 122880;
constexpr int WS_CS = WS_T2 + 16 * 122880;
constexpr int WS_BYTES = WS_CS + 16 * 16 * 384;

constexpr int L_BKV = 0, L_BQ = 102400;
constexpr int L_RED = 0, L_KVF = 153600;
constexpr int L_WO = 0, L_W1 = 30720, L_W2 = 0, L_CS = 161792;
constexpr int L_B1 = L_CS + 1152, L_B2 = L_CS + 1536;
constexpr int LDS_BYTES = 163840;

__device__ __forceinline__ f32x4 mfma(v4u a, v4u b, f32x4 c) {
  return __builtin_amdgcn_mfma_f32_16x16x32_bf16(__builtin_bit_cast(bf16x8, a), __builtin_bit_cast(bf16x8, b), c, 0, 0, 0);
}
__device__ __forceinline__ unsigned pk2(f32x2 v) { return __builtin_bit_cast(unsigned, __builtin_convertvector(v, bf16x2)); }
__device__ __forceinline__ unsigned pk2(float lo, float hi) { return pk2(f32x2{lo, hi}); }
__device__ __forceinline__ float bf_lo(unsigned u) { return __builtin_bit_cast(float, u << 16); }
__device__ __forceinline__ float bf_hi(unsigned u) { return __builtin_bit_cast(float, u & 0xFFFF0000u); }
__device__ __forceinline__ f32x2 bf2(unsigned u) { return f32x2{bf_lo(u), bf_hi(u)}; }
__device__ __forceinline__ f32x2 lo2(f32x4 v) { return f32x2{v[0], v[1]}; }
__device__ __forceinline__ f32x2 hi2(f32x4 v) { return f32x2{v[2], v[3]}; }
__device__ __forceinline__ v4u mk_srd(const void* p, unsigned bytes) {
  const unsigned long long a = (unsigned long long)p;
  v4u r;
  r.x = (unsigned)a; r.y = (unsigned)(a >> 32) & 0xFFFFu; r.z = bytes; r.w = 0x00020000u;
  return r;
}
#define DRAIN() asm volatile("s_waitcnt vmcnt(0)" ::: "memory")
#define BAR() asm volatile("s_waitcnt lgkmcnt(0)\n\ts_barrier" ::: "memory")
#define BAR_RAW() asm volatile("s_barrier" ::: "memory")
#define SCHED() __builtin_amdgcn_sched_barrier(0)
#define PIN(x) asm volatile("" : "+v"(x))

__device__ __forceinline__ void dma2(unsigned lds_addr, unsigned voff, v4u srd, unsigned soff) {
  unsigned keep;
  asm volatile("s_mov_b32 %0, m0\n\ts_mov_b32 m0, %1\n\ts_nop 0\n\t"
               "buffer_load_dwordx4 %2, %3, %4 offen sc1 lds\n\t"
               "buffer_load_dwordx4 %2, %3, %4 offen offset:1024 sc1 lds\n\t"
               "s_mov_b32 m0, %0"
               : "=&s"(keep) : "s"(lds_addr), "v"(voff), "s"(srd), "s"(soff) : "memory");
}
template <int LO, int HI, int TH = 0, int NW = 4>
__device__ __forceinline__ void dma_image(unsigned lds_base, unsigned voff, v4u srd, int ws_off, int hw, int rot) {
  constexpr int NP = (HI - LO) / 2;
#pragma unroll 1
  for (int i = hw; i < NP; i += NW) {
    int j = i + rot;
    if (j >= NP) j -= NP;
    dma2(lds_base + LO * 1024 + j * 2048, voff, srd, ws_off + LO * 1024 + j * 2048);
    if (TH) asm volatile("s_waitcnt vmcnt(%0)" :: "i"(TH - 2) : "memory");
  }
}
__device__ __forceinline__ v2u rdtr(LAS char* p) { return __builtin_bit_cast(v2u, __builtin_amdgcn_ds_read_tr16_b64_v4i16((LAS s16x4*)p)); }
struct LBase {
  unsigned a[3];
  __device__ __forceinline__ explicit LBase(unsigned v) { a[0] = v; a[1] = v + 65536; a[2] = v + 131072; PIN(a[0]); PIN(a[1]); PIN(a[2]); }
  __device__ __forceinline__ v2u rd(int off) const { return rdtr((LAS char*)(a[off >> 16]) + (off & 65535)); }
};
template <int MT, int NT, int KS, int PD, bool WA, int M0 = 0, class RB, class GA, class PRE>
__device__ __forceinline__ void kloop(f32x4 (&acc)[MT][NT], RB rb, GA ga, PRE pre) {
  v2u bl[PD + 1][NT], bh[PD + 1][NT];
#pragma unroll
  for (int m = M0; m < MT; ++m)
#pragma unroll
    for (int s = 0; s < NT; ++s) acc[m][s] = f32x4{0.f, 0.f, 0.f, 0.f};
#pragma unroll
  for (int ks = 0; ks < PD; ++ks)
#pragma unroll
    for (int s = 0; s < NT; ++s) { bl[ks][s] = rb(ks, s, 0); bh[ks][s] = rb(ks, s, 1); }
  SCHED();
#pragma unroll
  for (int ks = 0; ks < KS; ++ks) {
    pre(ks);
#pragma unroll
    for (int s = 0; s < NT; ++s)
#pragma unroll
      for (int m = 0; m < MT; ++m) {
        const v2u lo = bl[ks % (PD + 1)][s], hi = bh[ks % (PD + 1)][s];
        const v4u wv = {lo.x, lo.y, hi.x, hi.y};
        acc[m][s] = WA ? mfma(wv, ga(ks, m), acc[m][s]) : mfma(ga(ks, m), wv, acc[m][s]);
        if (ks + PD < KS) {
          if (MT == 1 || m == 0) bl[(ks + PD) % (PD + 1)][s] = rb(ks + PD, s, 0);
          if (MT == 1 || m == 1) bh[(ks + PD) % (PD + 1)][s] = rb(ks + PD, s, 1);
        }
        SCHED();
      }
  }
}
constexpr int ld_hi(int ks, int s0, int W, int KS) {
  const int a = s0 + 2 * (ks + 1), b = ks + 1 + W, c = a < b ? a : b;
  return c < KS ? c : KS;
}
__device__ __forceinline__ v4u ld128(__amdgpu_buffer_rsrc_t rs, int voff, int soff) {
  return __builtin_amdgcn_raw_buffer_load_b128(rs, voff, soff, AUX_SC1);
}
__device__ __forceinline__ v2u ld64(__amdgpu_buffer_rsrc_t rs, int voff, int soff) {
  return __builtin_amdgcn_raw_buffer_load_b64(rs, voff, soff, AUX_SC1);
}
__device__ __forceinline__ void st128(v4u v, __amdgpu_buffer_rsrc_t rs, int voff, int soff) {
  __builtin_amdgcn_raw_buffer_store_b128(v, rs, (int)((unsigned)voff + (unsigned)soff), 0, AUX_SC1);
}
__device__ __forceinline__ int sel(bool c, int a) {
  PIN(a);
  return c ? a : OOB;
}
__device__ __forceinline__ v4u ld_split(__amdgpu_buffer_rsrc_t rs, int lane, int soff) {
  const v2u a = ld64(rs, lane * 8, soff), b = ld64(rs, lane * 8 + 512, soff);
  return v4u{a.x, a.y, b.x, b.y};
}
__device__ __forceinline__ void st64(v2u v, __amdgpu_buffer_rsrc_t rs, int voff, int soff) {
  __builtin_amdgcn_raw_buffer_store_b64(v, rs, voff, soff, AUX_SC1);
}
__device__ __forceinline__ void flag_set(gu32* p, unsigned tag) {
  asm volatile("global_atomic_swap %0, %1, off sc1" :: "v"(p), "v"(tag) : "memory");
}
__device__ __forceinline__ void wait_flag(gu32* p, int n, unsigned tag, int lane) {
#pragma unroll 1
  for (unsigned spins = 0; spins < kSpinCap; ++spins) {
    unsigned v = tag;
    if (lane < n) asm volatile("global_atomic_add %0, %1, %2, off sc0 sc1\n\ts_waitcnt vmcnt(0)" : "=&v"(v) : "v"(p), "v"(0u) : "memory");
    if (__all(v == tag)) break;
    if (SLEEP) __builtin_amdgcn_s_sleep(SLEEP);
  }
  asm volatile("" ::: "memory");
}
__device__ __forceinline__ void sig_wait(volatile LAS unsigned* p) {
#pragma unroll 1
  for (unsigned spins = 0; spins < 64 * kSpinCap; ++spins) {
    if (*p != 0u) break;
    __builtin_amdgcn_s_sleep(1);
  }
  asm volatile("" ::: "memory");
}
__device__ __forceinline__ void prog_wait(volatile LAS v4u* p, unsigned stage) {
#pragma unroll 1
  for (unsigned spins = 0; spins < 64 * kSpinCap; ++spins) {
    const v4u v = *p;
    if (min(min(v.x, v.y), min(v.z, v.w)) >= stage) break;
    __builtin_amdgcn_s_sleep(1);
  }
  asm volatile("" ::: "memory");
}
__device__ __forceinline__ void publish(gu32* flag, unsigned tag, int lane) {
  DRAIN();
  if (lane == 0) flag_set(flag, tag);
}
__device__ __forceinline__ f32x4 add4(f32x4 a, f32x4 b) {
  f32x4 r;
#pragma unroll
  for (int i = 0; i < 4; ++i) { float x; asm("v_add_f32 %0, %1, %2" : "=v"(x) : "v"(a[i]), "v"(b[i])); r[i] = x; }
  return r;
}
__device__ __forceinline__ float xsum32(float x) {
  unsigned u = __builtin_bit_cast(unsigned, x), v = u;
  asm("" : "+v"(v));
  auto r = __builtin_amdgcn_permlane32_swap(u, v, false, false);
  const unsigned a = r[0], b = r[1];
  return __builtin_bit_cast(float, a) + __builtin_bit_cast(float, b);
}
__device__ __forceinline__ float xsum16(float x) {
  unsigned u = __builtin_bit_cast(unsigned, x), v = u;
  asm("" : "+v"(v));
  auto r = __builtin_amdgcn_permlane16_swap(u, v, false, false);
  const unsigned a = r[0], b = r[1];
  return __builtin_bit_cast(float, a) + __builtin_bit_cast(float, b);
}
__device__ __forceinline__ float xsum8(float x) {
  return x + __builtin_bit_cast(float, __builtin_amdgcn_update_dpp(0, __builtin_bit_cast(int, x), 0x128, 0xf, 0xf, false));
}
__device__ __forceinline__ f32x2 phi2(f32x2 t) {
  const f32x2 u = t * 1.4426950408889634f, v = t + 1e-6f;
  const f32x2 e = {__builtin_amdgcn_fmed3f(__builtin_amdgcn_exp2f(u[0]), 0.f, 1.f), __builtin_amdgcn_fmed3f(__builtin_amdgcn_exp2f(u[1]), 0.f, 1.f)};
  return f32x2{fmaxf(v[0], 1e-6f), fmaxf(v[1], 1e-6f)} + e;
}
__device__ __forceinline__ f32x2 gelu2(f32x2 x) {
  const f32x2 u = x * (x * x * -0.1029432397f + -2.302208198f);
  const f32x2 d = f32x2{__builtin_amdgcn_exp2f(u[0]), __builtin_amdgcn_exp2f(u[1])} + 1.f;
  return x * f32x2{__builtin_amdgcn_rcpf(d[0]), __builtin_amdgcn_rcpf(d[1])};
}

#define STAMP(i) do { } while (0)

__global__ void __launch_bounds__(512, 2)
fused_block(const unsigned short* __restrict__ x, const unsigned short* __restrict__ wqkv, const unsigned short* __restrict__ wo,
            const unsigned short* __restrict__ w1, const unsigned short* __restrict__ b1, const unsigned short* __restrict__ w2,
            const unsigned short* __restrict__ b2, unsigned short* __restrict__ out, unsigned char* ws) {
  __shared__ __attribute__((aligned(16))) char smem[LDS_BYTES];
  unsigned long long pc0;
  asm volatile("s_getpc_b64 %0" : "=s"(pc0));
  LAS char* L = (LAS char*)smem;
  const unsigned lds0 = (unsigned)(size_t)L;
  const int tid = threadIdx.x, lane = tid & 63, w = __builtin_amdgcn_readfirstlane(tid >> 6);
  const int b = blockIdx.x, xcd = b & 7, bi = b >> 3;
  const int g = lane >> 4, t = lane & 15;
  const __amdgpu_buffer_rsrc_t rs = __builtin_amdgcn_make_buffer_rsrc((void*)ws, 0, WS_BYTES, 0x00020000);
  gu32* flags = (gu32*)(ws + WS_FLAG);
#define FLAG(set, grp) (flags + ((set) * FLAG_SET + (grp) * 256) / 4)
  asm volatile("" :: "s"(x), "s"(wqkv), "s"(wo), "s"(w1), "s"(b1), "s"(w2), "s"(b2), "s"(out), "s"(ws));
  STAMP(0);
  const int r128 = 4 * (xcd >> 1) + (bi >> 3), c16 = 8 * (xcd & 1) + (bi & 7);
  const int r64 = 8 * (xcd >> 1) + (bi >> 2), c8 = 4 * (xcd & 1) + (bi & 3);

  const __amdgpu_buffer_rsrc_t rx = __builtin_amdgcn_make_buffer_rsrc((void*)x, 0, S * D * 2, 0x00020000);
  const int hw = w - 4, ht = tid - 256;
  volatile LAS unsigned* sig = (volatile LAS unsigned*)(L + L_CS + 1024);
  volatile LAS v4u* prog = (volatile LAS v4u*)(L + L_CS + 1088);
#define PROG(n) do { asm volatile("" ::: "memory"); sig[16 + w] = (n); } while (0)

  unsigned tag = __hip_atomic_load((gu32*)(ws + WS_EPOCH), RLX_AGENT);
  const __amdgpu_buffer_rsrc_t rq = __builtin_amdgcn_make_buffer_rsrc((void*)wqkv, 0, D * 3 * D * 2, 0x00020000);
  v4u tv[12];
  const int q10 = lane / 10, c10 = lane % 10;
  if (w < 4) {
#pragma unroll
    for (int j = 0; j < 3; ++j)
      tv[j] = __builtin_amdgcn_raw_buffer_load_b128(rx, (lane & 7) * 1280 + (lane >> 3) * 16, (j < 2 || w < 2) ? 8 * b * 1280 + (w + 4 * j) * 128 : OOB, 0);
  } else if (w == 4) {
    const int vo = sel(lane < 40, q10 * 3840 + (c10 < 5 ? 1280 : 2480) + c10 * 16);
#pragma unroll
    for (int j = 0; j < 10; ++j) tv[j] = __builtin_amdgcn_raw_buffer_load_b128(rq, vo, (40 * r128 + 4 * j) * 3840 + c16 * 80, 0);
  } else if (w == 7) {
    *(LAS v4u*)(L + L_CS + 512 + lane * 16) = v4u{0u, 0u, 0u, 0u};
  }
  BAR();

  if (w < 4) {
  const int grp32 = 4 * r128 + w;
  constexpr int AW = 6;

  {
    const int tok = 8 * b + (lane & 7), cl = lane >> 3;
    const bool has3 = w < 2;
    int xd = ((tok >> 4) * 20 + 2 * w + (cl >> 2)) * 1024 + ((cl & 3) * 16 + (tok & 15)) * 16;
    PIN(xd);
    SCHED();
    f32x2 f[12];
#pragma unroll
    for (int j = 0; j < 3; ++j)
#pragma unroll
      for (int e = 0; e < 4; ++e) f[j * 4 + e] = bf2(tv[j][e]);
    f32x2 s2 = (f[0] + f[1]) + (f[2] + f[3]) + ((f[4] + f[5]) + (f[6] + f[7])) + ((f[8] + f[9]) + (f[10] + f[11]));
    const float mloc = xsum32(xsum16(xsum8(s2[0] + s2[1]))) * (has3 ? 1.f / 192.f : 1.f / 128.f);
    const float m3 = has3 ? mloc : 0.f;
    f32x2 q2 = {0.f, 0.f}, q3 = {0.f, 0.f};
#pragma unroll
    for (int j = 0; j < 12; j += 2) {
      const f32x2 d0 = f[j] - (j < 8 ? mloc : m3), d1 = f[j + 1] - (j < 8 ? mloc : m3);
      q2 += d0 * d0; q3 += d1 * d1;
    }
    q2 += q3;
    const float m2loc = xsum32(xsum16(xsum8(q2[0] + q2[1])));
    if (lane < 8) *(volatile LAS f32x4*)(L + L_CS + 512 + (lane * 4 + w) * 16) = f32x4{mloc, m2loc, 1.f, 1.f};
    STAMP(25);
    f32x4 pr[4];
#pragma unroll 1
    for (unsigned spins = 0; spins < 64 * kSpinCap; ++spins) {
      bool ok = true;
#pragma unroll
      for (int ww = 0; ww < 4; ++ww) {
        pr[ww] = *(volatile LAS f32x4*)(L + L_CS + 512 + ((lane & 7) * 4 + ww) * 16);
        ok = ok && pr[ww][2] == 1.f;
      }
      if (__all(ok)) break;
    }
    const f32x4 p01 = {pr[0][0], pr[0][1], pr[1][0], pr[1][1]}, p23 = {pr[2][0], pr[2][1], pr[3][0], pr[3][1]};
    const float mean = (p01[0] + p01[2]) * 0.3f + (p23[0] + p23[2]) * 0.2f;
    const float e0 = p01[0] - mean, e1 = p01[2] - mean, e2 = p23[0] - mean, e3 = p23[2] - mean;
    const float m2 = (p01[1] + p01[3]) + (p23[1] + p23[3]) + 192.f * (e0 * e0 + e1 * e1) + 128.f * (e2 * e2 + e3 * e3);
    const float rstd = __builtin_amdgcn_rsqf(m2 * (1.f / D) + LN_EPS);
#pragma unroll
    for (int j = 0; j < 3; ++j) {
      v4u o;
#pragma unroll
      for (int e = 0; e < 4; ++e) o[e] = pk2((f[j * 4 + e] - mean) * rstd);
      st128(o, rs, (j < 2 || has3) ? xd : OOB, WS_XN + j * 8192);
    }
    STAMP(26);
    PIN(tag); tag += 1u;
    publish(FLAG(0, b >> 2) + (b & 3) * 4 + w, tag, lane);
  }

  STAMP(1);

  v4u xr[3];
  {
    wait_flag(FLAG(0, grp32) + lane, 16, tag, lane);
    STAMP(2);
    v4u a[20][2];
    const int abase = WS_XN + 2 * grp32 * 20 * 1024;
#pragma unroll
    for (int ks = 0; ks < S0; ++ks)
#pragma unroll
      for (int m = 0; m < 2; ++m) a[ks][m] = ld128(rs, lane * 16, abase + (m * 20 + ks) * 1024);
    SCHED();
    BAR_RAW();
    STAMP(27);
    {
      f32x4 acc[2][5];
      const LBase bp(lds0 + L_BKV + (lane >> 2) * 160 + (lane & 3) * 8);
      kloop<2, 5, 20, 1, false>(acc, [&](int ks, int s, int h) { return bp.rd(ks * 5120 + h * 2560 + s * 32); },
        [&](int ks, int m) { return a[ks][m]; },
        [&](int ks) {
#pragma unroll
          for (int j = ld_hi(ks - 1, S0, AW, 20); j < ld_hi(ks, S0, AW, 20); ++j)
#pragma unroll
            for (int m = 0; m < 2; ++m) a[j][m] = ld128(rs, lane * 16, abase + (m * 20 + j) * 1024);
          if (ks == 3 || ks == 10) BAR_RAW();
          if (ks == 20 - AW) PROG(1);
        });
      STAMP(3);
      v4u ka[3], vb[3];
#pragma unroll
      for (int s = 0; s < 3; ++s)
#pragma unroll
        for (int m = 0; m < 2; ++m) {
          vb[s][2 * m] = pk2(lo2(acc[m][s + 2])); vb[s][2 * m + 1] = pk2(hi2(acc[m][s + 2]));
        }
#pragma unroll
      for (int e = 0; e < 4; ++e) vb[0][e] = t < 8 ? 0x3F803F80u : vb[0][e];
      const f32x4 z4 = {0.f, 0.f, 0.f, 0.f};
      LAS f32x4* red = (LAS f32x4*)(L + L_RED) + w * 512 + lane;
#pragma unroll
      for (int s = 0; s < 3; ++s) {
#pragma unroll
        for (int m = 0; m < 2; ++m) { ka[s][2 * m] = pk2(phi2(lo2(acc[m][s]))); ka[s][2 * m + 1] = pk2(phi2(hi2(acc[m][s]))); }
#pragma unroll
        for (int sb = 0; sb < 3; ++sb)
          if (s > 0 || sb < 2) red[(3 * s + sb - (s > 0)) * 64] = mfma(ka[s], vb[sb], z4);
      }
      STAMP(28);
      BAR();
    }
    STAMP(4);
    f32x4 qa[2][3];
    {
      unsigned bp0 = lds0 + L_BQ + (lane >> 2) * 64 + (lane & 3) * 8 + (g & 1) * 32;
      unsigned bp1 = lds0 + L_BQ + (lane >> 2) * 64 + (lane & 3) * 8 + 32 - (g & 1) * 32;
      unsigned bp2 = lds0 + L_BQ + 2048 + (lane >> 2) * 16 + (lane & 1) * 8;
      PIN(bp0); PIN(bp1); PIN(bp2);
      kloop<2, 3, 20, 2, true>(qa,
        [&](int ks, int s, int h) {
          return s == 0 ? rdtr((LAS char*)bp0 + ks * 2560 + h * 1024) : s == 1 ? rdtr((LAS char*)bp1 + ks * 2560 + h * 1024) : rdtr((LAS char*)bp2 + ks * 2560 + h * 256);
        },
        [&](int ks, int m) { return a[ks][m]; },
        [&](int ks) {});
    }
    v4u pq0[2], pq1[2];
#pragma unroll
    for (int m = 0; m < 2; ++m) {
      pq0[m].x = pk2(phi2(lo2(qa[m][0]))); pq0[m].y = pk2(phi2(hi2(qa[m][0])));
      pq0[m].z = pk2(phi2(lo2(qa[m][1]))); pq0[m].w = pk2(phi2(hi2(qa[m][1])));
      const unsigned lo = pk2(phi2(lo2(qa[m][2]))), hi = pk2(phi2(hi2(qa[m][2])));
      pq1[m].x = g < 2 ? lo : 0u; pq1[m].y = g < 2 ? hi : 0u; pq1[m].z = 0u; pq1[m].w = 0u;
      PIN(pq0[m]); PIN(pq1[m]);
    }
    STAMP(5);
    BAR_RAW();
#pragma unroll
    for (int j = 0; j < 3; ++j)
      xr[j] = __builtin_amdgcn_raw_buffer_load_b128(rx, sel(j < 2 || g < 2, t * 1280 + g * 16), (4 * r64 + w) * 16 * 1280 + c8 * 160 + j * 64, 0);
    STAMP(17);
    {
      v4u kv[4][2];
#pragma unroll
      for (int es = 3; es >= 0; --es)
#pragma unroll
        for (int ks = 0; ks < 2; ++ks) kv[es][ks] = *(LAS v4u*)(L + L_KVF + (es * 2 + ks) * 1024 + lane * 16);
      SCHED();
      f32x4 o[2][4];
#pragma unroll
      for (int es = 3; es >= 0; --es)
#pragma unroll
        for (int m = 0; m < 2; ++m) o[m][es] = mfma(kv[es][0], pq0[m], f32x4{0.f, 0.f, 0.f, 0.f});
#pragma unroll
      for (int es = 3; es >= 0; --es)
#pragma unroll
        for (int m = 0; m < 2; ++m) o[m][es] = mfma(kv[es][1], pq1[m], o[m][es]);
#pragma unroll
      for (int m = 0; m < 2; ++m) {
        const float i0 = __builtin_amdgcn_rcpf(o[m][3][0] + 1e-6f), i1 = __builtin_amdgcn_rcpf(o[m][3][1] + 1e-6f);
        const float im = g == 0 ? i0 : i1;
        v4u ov;
        ov.x = pk2(lo2(o[m][0]) * i0); ov.y = pk2(hi2(o[m][0]) * i0);
        ov.z = pk2(lo2(o[m][1]) * im); ov.w = pk2(hi2(o[m][1]) * im);
        v2u ol; ol.x = pk2(lo2(o[m][2]) * i1); ol.y = pk2(hi2(o[m][2]) * i1);
        const int tt = 2 * grp32 + m;
        st128(ov, rs, lane * 16, WS_O + (tt * 20 + c16) * 1024);
        st64(ol, rs, g < 2 ? lane * 8 : OOB, WS_O + (tt * 20 + 16) * 1024 + c16 * 256);
      }
      STAMP(18);
      publish(FLAG(2, grp32) + c16, tag, lane);
    }
    STAMP(7);
  }

  {
    const int tt = 4 * r64 + w;
    v4u eye[2];
#pragma unroll
    for (int hf = 0; hf < 2; ++hf) {
      const int e = 16 * hf + t - 8 * g;
#pragma unroll
      for (int d = 0; d < 4; ++d) eye[hf][d] = e == 2 * d ? 0x3F80u : e == 2 * d + 1 ? 0x3F800000u : 0u;
      PIN(eye[hf]);
    }
    f32x4 acc[1][5];
#pragma unroll
    for (int s = 0; s < 5; ++s) { acc[0][s] = mfma(eye[s & 1], xr[s >> 1], f32x4{0.f, 0.f, 0.f, 0.f}); PIN(acc[0][s]); }
    wait_flag(FLAG(2, tt >> 1) + lane, 16, tag, lane);
    STAMP(8);
    if (b == 0 && tid == 0) __hip_atomic_store((gu32*)(ws + WS_EPOCH), tag, RLX_AGENT);
    v4u a[20];
#pragma unroll
    for (int ks = 0; ks < 2 * S0; ++ks) a[ks] = ld128(rs, lane * 16, WS_O + (tt * 20 + ks) * 1024);
    SCHED();
    const LBase bp(lds0 + L_WO + (lane >> 2) * 160 + (lane & 3) * 8);
    kloop<1, 5, 20, 1, true, 1>(acc, [&](int ks, int s, int h) { return bp.rd(ks * 5120 + h * 2560 + s * 32); },
      [&](int ks, int m) { return a[ks]; },
      [&](int ks) {
#pragma unroll
        for (int j = ld_hi(ks - 1, 2 * S0, 2 * AW, 20); j < ld_hi(ks, 2 * S0, 2 * AW, 20); ++j)
          a[j] = j < 16 ? ld128(rs, lane * 16, WS_O + (tt * 20 + j) * 1024) : ld_split(rs, lane, WS_O + (tt * 20 + j) * 1024);
        if (ks == 8) PROG(2);
        if (ks == 14) PROG(3);
      });
    PROG(4);
    STAMP(9);
    v2u hb[5];
    f32x2 hv[10];
#pragma unroll
    for (int s = 0; s < 5; ++s) {
      hb[s].x = pk2(lo2(acc[0][s]));
      hb[s].y = pk2(hi2(acc[0][s]));
      hv[2 * s] = bf2(hb[s].x); hv[2 * s + 1] = bf2(hb[s].y);
    }
    st128(v4u{hb[0].x, hb[0].y, hb[1].x, hb[1].y}, rs, lane * 16, WS_H + (tt * 20 + 2 * c8) * 1024);
    st128(v4u{hb[2].x, hb[2].y, hb[3].x, hb[3].y}, rs, lane * 16, WS_H + (tt * 20 + 2 * c8 + 1) * 1024);
    st64(hb[4], rs, lane * 8, WS_H + (tt * 20 + 16) * 1024 + c8 * 512);
    const f32x2 s2 = ((hv[0] + hv[1]) + (hv[2] + hv[3])) + ((hv[4] + hv[5]) + (hv[6] + hv[7])) + (hv[8] + hv[9]);
    const float mc = xsum32(xsum16(s2[0] + s2[1])) * (1.f / 80.f);
    f32x2 q2 = {0.f, 0.f}, q3 = {0.f, 0.f};
#pragma unroll
    for (int j = 0; j < 10; j += 2) { const f32x2 d0 = hv[j] - mc, d1 = hv[j + 1] - mc; q2 += d0 * d0; q3 += d1 * d1; }
    q2 += q3;
    const float m2 = xsum32(xsum16(q2[0] + q2[1]));
    st64(v2u{__builtin_bit_cast(unsigned, mc), __builtin_bit_cast(unsigned, m2)}, rs, g == 0 ? t * 8 : OOB, WS_ST + (tt * 8 + c8) * 128);
    STAMP(20);
    publish(FLAG(3, tt >> 1) + (tt & 1) * 8 + c8, tag, lane);
    STAMP(10);
  }

  {
    wait_flag(FLAG(3, grp32) + lane, 16, tag, lane);
    STAMP(11);
    v4u stt[2];
#pragma unroll
    for (int m = 0; m < 2; ++m) {
      const v2u s0 = ld64(rs, g * 256 + t * 8, WS_ST + (2 * grp32 + m) * 1024), s1 = ld64(rs, g * 256 + 128 + t * 8, WS_ST + (2 * grp32 + m) * 1024);
      stt[m] = v4u{s0.x, s0.y, s1.x, s1.y};
    }
    v4u a[20][2];
    const int abase = WS_H + 2 * grp32 * 20 * 1024;
#pragma unroll
    for (int ks = 0; ks < S0; ++ks)
#pragma unroll
      for (int m = 0; m < 2; ++m) a[ks][m] = ld128(rs, lane * 16, abase + (m * 20 + ks) * 1024);
    SCHED();
    float nm[2], rr[2];
#pragma unroll
    for (int m = 0; m < 2; ++m) {
      const f32x4 sv = __builtin_bit_cast(f32x4, stt[m]);
      const float mean = xsum32(xsum16(sv[0] + sv[2])) * 0.125f;
      const float m2 = xsum32(xsum16(sv[1] + sv[3] + 80.f * ((sv[0] - mean) * (sv[0] - mean) + (sv[2] - mean) * (sv[2] - mean))));
      const float var = m2 * (1.f / D);
      const float r1 = __builtin_amdgcn_rsqf(var + LN_EPS);
      rr[m] = r1 * __builtin_amdgcn_rsqf(var * r1 * r1 + LN_EPS);
      nm[m] = -mean * rr[m];
      PIN(rr[m]); PIN(nm[m]);
    }
    BAR_RAW();
    STAMP(22);
    const unsigned l64 = lds0 + (lane >> 2) * 64 + (lane & 3) * 8;
    const LBase bp0(l64 + (g & 1) * 32), bp1(l64 + 32 - (g & 1) * 32);
    const int h1base = WS_H1 + 2 * grp32 * 48 * 1024;
    auto finish = [&](const f32x4& c0, const f32x4& c1, int j, int m) {
      v4u o;
#pragma unroll
      for (int s = 0; s < 2; ++s) {
        const f32x4 cs = *(LAS f32x4*)(L + L_CS + ((2 * j + s) * 4 + g) * 16), bb = *(LAS f32x4*)(L + L_B1 + ((2 * j + s) * 4 + g) * 16);
        const f32x4 c = s ? c1 : c0;
        o[2 * s] = pk2(gelu2(lo2(c) * rr[m] + (lo2(cs) * nm[m] + lo2(bb))));
        o[2 * s + 1] = pk2(gelu2(hi2(c) * rr[m] + (hi2(cs) * nm[m] + hi2(bb))));
      }
      st128(o, rs, lane * 16, h1base + (m * 48 + (c16 >> 3) * 24 + (j < 2 ? 2 * (c16 & 7) + j : 16 + (c16 & 7))) * 1024);
    };
    {
      f32x4 acc[2][4];
      kloop<2, 4, 20, 1, true>(acc, [&](int ks, int s, int h) { return ((s & 1) ? bp1 : bp0).rd(L_W1 + (s >> 1) * 40960 + ks * 2048 + h * 1024); },
        [&](int ks, int m) { return a[ks][m]; },
        [&](int ks) {
#pragma unroll
          for (int j = ld_hi(ks - 1, S0, AW, 20); j < ld_hi(ks, S0, AW, 20); ++j)
#pragma unroll
            for (int m = 0; m < 2; ++m)
              a[j][m] = j < 16 ? ld128(rs, lane * 16, abase + (m * 20 + j) * 1024) : ld_split(rs, lane, abase + (m * 20 + j) * 1024);
          if (ks == 20 - AW) PROG(5);
        });
      PROG(6);
      STAMP(12);
#pragma unroll
      for (int j = 0; j < 2; ++j)
#pragma unroll
        for (int m = 0; m < 2; ++m) finish(acc[m][2 * j], acc[m][2 * j + 1], j, m);
      SCHED();
      STAMP(24);
    }
    f32x4 accb[2][2];
    const int kh = w & 1, tt = 4 * r64 + w;
    v4u hr01, hr23;
    v2u hr4;
    kloop<2, 2, 20, 3, true>(accb, [&](int ks, int s, int h) { return (s ? bp1 : bp0).rd(L_W1 + 81920 + ks * 2048 + h * 1024); },
      [&](int ks, int m) { return a[ks][m]; },
      [&](int ks) {
        if (ks == PUBA) { publish(FLAG(4, grp32) + c16, tag, lane); PROG(7); }
        if (ks == PUBA + 2) {
          hr01 = ld128(rs, lane * 16, WS_H + (tt * 20 + 2 * c8) * 1024);
          hr23 = ld128(rs, lane * 16, WS_H + (tt * 20 + 2 * c8 + 1) * 1024);
          hr4 = ld64(rs, lane * 8, WS_H + (tt * 20 + 16) * 1024 + c8 * 512);
        }
      });
    STAMP(13);
    f32x4 acc[2][5];
    {
      const unsigned hrr[10] = {hr01.x, hr01.y, hr01.z, hr01.w, hr23.x, hr23.y, hr23.z, hr23.w, hr4.x, hr4.y};
#pragma unroll
      for (int s = 0; s < 5; ++s) {
        acc[0][s] = *(LAS f32x4*)(L + L_B2 + (s * 4 + g) * 16) + f32x4{bf_lo(hrr[2 * s]), bf_hi(hrr[2 * s]), bf_lo(hrr[2 * s + 1]), bf_hi(hrr[2 * s + 1])};
        PIN(acc[0][s]);
      }
    }
    int oo[3];
    unsigned tw, tr;
    {
      int ln = lane;
      PIN(ln);
      const int qq = ln / 10, cc = ln % 10;
#pragma unroll
      for (int j = 0; j < 3; ++j) { oo[j] = sel(ln < (j < 2 ? 60 : 40), ((tt * 16 + 6 * j + qq) * D + 80 * c8) * 2 + cc * 16); PIN(oo[j]); }
      tw = lds0 + L_W2 + 122880 + w * 3072 + t * 176 + g * 8; tr = lds0 + L_W2 + 122880 + w * 3072 + qq * 176 + cc * 16;
      PIN(tw); PIN(tr);
    }
    BAR_RAW();
    STAMP(14);
    constexpr int WIN = 8;
    v4u ar[WIN][2];
    const int a4[2] = {WS_H1 + (tt * 48 + kh * 24) * 1024, WS_H1 + ((tt ^ 1) * 48 + kh * 24) * 1024};
#pragma unroll
    for (int ks = 0; ks < WIN - 1; ++ks)
#pragma unroll
      for (int m = 0; m < 2; ++m) ar[ks][m] = ld128(rs, lane * 16, a4[m] + ks * 1024);
    SCHED();
#pragma unroll
    for (int m = 0; m < 2; ++m) finish(accb[m][0], accb[m][1], 2, m);
    STAMP(29);
    const LBase bp(lds0 + L_W2 + kh * 5120 + (lane >> 2) * 160 + (lane & 3) * 8);
    kloop<2, 5, 24, 1, true, 1>(acc, [&](int ks, int s, int h) { return bp.rd((ks % 15) * 10240 + h * 2560 + s * 32); },
      [&](int ks, int m) { return ar[ks % WIN][m]; },
      [&](int ks) {
        if (ks == 5) PROG(8);
        if (ks == 9 || ks == 14 || ks == 19) BAR_RAW();
        if (ks == 3) publish(FLAG(10, grp32) + c16, tag, lane);
        if (ks >= 3) {
#pragma unroll
          for (int j = ks == 3 ? WIN - 1 : ld_hi(ks - 1, 1, WIN - 1, 24); j < ld_hi(ks, 1, WIN - 1, 24); ++j)
#pragma unroll
            for (int m = 0; m < 2; ++m) ar[j % WIN][m] = ld128(rs, lane * 16, a4[m] + j * 1024);
        }
      });
    STAMP(15);
    LAS f32x4* xch = (LAS f32x4*)(L + L_W2 + 102400);
#pragma unroll
    for (int s = 0; s < 5; ++s) xch[((w ^ 1) * 5 + s) * 64 + lane] = acc[1][s];
    BAR();
    const __amdgpu_buffer_rsrc_t rout = __builtin_amdgcn_make_buffer_rsrc((void*)out, 0, S * D * 2, 0x00020000);
    f32x4 ot[5];
#pragma unroll
    for (int s = 0; s < 5; ++s) ot[s] = xch[(w * 5 + s) * 64 + lane];
    SCHED();
#pragma unroll
    for (int s = 0; s < 5; ++s) {
      const f32x4 v = acc[0][s] + ot[s];
      *(LAS v2u*)((LAS char*)tw + s * 32) = v2u{pk2(lo2(v)), pk2(hi2(v))};
    }
    asm volatile("" ::: "memory");
    v4u ov[3];
#pragma unroll
    for (int j = 0; j < 3; ++j) ov[j] = *(LAS v4u*)((LAS char*)tr + j * 6 * 176);
#pragma unroll
    for (int j = 0; j < 3; ++j) __builtin_amdgcn_raw_buffer_store_b128(ov[j], rout, oo[j], 0, 0);
    STAMP(16);
  }
  return;
  }

  {
    const v4u srd_ws = mk_srd(ws, WS_BYTES);
    STAMP(16);
    const int q5 = lane / 5, c5 = lane % 5;
    const int f40m = c5 < 4 ? 512 : 128, f40l = c5 < 4 ? q5 * 64 + (c5 ^ (2 * ((q5 >> 2) & 1))) * 16 : 2048 + q5 * 16;
    v2u ub1, ub2;
    unsigned pf0 = 0;
    if (hw == 0) {
      const int dof = sel(lane < 40, lane * 16);
#pragma unroll
      for (int j = 0; j < 10; ++j) {
        const int k = 40 * r128 + 4 * j, R = (k & ~31) + 16 * ((k >> 2) & 1) + 4 * ((k >> 3) & 3);
        st128(tv[j], rs, dof, WS_TKV + c16 * 153600 + R * 160);
      }
      STAMP(18);
      PIN(tag); tag += 1u;
      publish(FLAG(6, c16) + r128, tag, lane);
      STAMP(13);
    } else if (hw == 1) {
      const __amdgpu_buffer_rsrc_t ro = __builtin_amdgcn_make_buffer_rsrc((void*)wo, 0, D * D * 2, 0x00020000);
      const int vq = sel(lane < 40, (8 * (q5 >> 2) + (q5 & 3)) * 3840 + c5 * 16), vo = sel(lane < 40, q10 * 1280 + c10 * 16);
#pragma unroll
      for (int j = 0; j < 5; ++j) {
        const int i8 = 5 * r128 + j, k = 32 * (i8 >> 2) + 16 * (i8 & 1) + 4 * ((i8 >> 1) & 1);
        tv[j] = __builtin_amdgcn_raw_buffer_load_b128(rq, vq, k * 3840 + c16 * 80, 0);
      }
#pragma unroll
      for (int j = 0; j < 5; ++j) tv[5 + j] = __builtin_amdgcn_raw_buffer_load_b128(ro, vo, (20 * r64 + 4 * j) * 1280 + c8 * 160, 0);
    } else if (hw == 2) {
      const GAS unsigned char* cb = (const GAS unsigned char*)(pc0 & ~127ull);
      if (lane < 11) pf0 = *(const GAS unsigned*)(cb + (lane * 32 + bi) * 128);
      if (lane < 6) {
        gu32* fp = FLAG(3 * (b >> 6) + (lane >> 1), b & 63) + (lane & 1) * 32;
        asm volatile("global_atomic_add %0, %1, off sc1" :: "v"(fp), "v"(0u) : "memory");
      }
    } else if (hw == 3) {
      const __amdgpu_buffer_rsrc_t rb1 = __builtin_amdgcn_make_buffer_rsrc((void*)b1, 0, F * 2, 0x00020000);
      const __amdgpu_buffer_rsrc_t rb2 = __builtin_amdgcn_make_buffer_rsrc((void*)b2, 0, D * 2, 0x00020000);
      ub1 = __builtin_amdgcn_raw_buffer_load_b64(rb1, lane * 8, c16 * 192, 0);
      ub2 = __builtin_amdgcn_raw_buffer_load_b64(rb2, lane * 8, c8 * 160, 0);
    }
    asm volatile("" ::: "memory");
    int red_off, kvf_off, kvp_off;
    {
      const int it = ht < 210 ? ht : 209, ie = it / 5, ic = it % 5;
      const int ihead = ie < 40 ? (ie >= 20) : ie - 40;
      const int id = 20 * ihead + 4 * ic, dg = id >> 2;
      const int sa = id >> 4, col = 40 + ie;
      const int tile = 3 * sa + (ie < 40 ? (col >> 4) - 2 : 0) - (sa > 0);
      red_off = lds0 + L_RED + (tile * 64 + (dg & 3) * 16 + (ie < 40 ? (col & 15) : 0)) * 16;
      kvf_off = lds0 + L_KVF + ((ie < 40 ? (ie >> 4) : 3) * 2 + (dg >> 3)) * 1024 + ((dg & 3) * 16 + (ie < 40 ? (ie & 15) : ihead)) * 16 + ((dg >> 2) & 1) * 8;
      kvp_off = WS_KVP + (c16 * 16 + r128) * 4096 + ht * 16;
      PIN(red_off); PIN(kvf_off); PIN(kvp_off);
    }
    int vo12, td[12];
    if (hw >= 2) {
      const int q12 = lane / 12, c12 = lane % 12;
      vo12 = hw == 2 ? sel(lane < 48, q12 * 3072 + c12 * 16) : sel(lane < 40, q10 * 1280 + c10 * 16);
#pragma unroll
      for (int j = 0; j < 12; ++j) {
        const int cc = r128 >> 1, off = 40 * (r128 & 1) + 4 * j;
        const int R = off < 64 ? 64 * cc + off : 32 * (16 + (cc >> 1)) + 16 * (cc & 1) + off - 64;
        const int d1 = WS_T1 + c16 * 122880 + (c12 >> 2) * 40960 + (R + q12) * 64 + ((c12 & 3) ^ (2 * ((R >> 2) & 1))) * 16;
        const int pc = r64 >> 1, ko = 48 * (r64 & 1) + 4 * j, jj = ko >> 5;
        const int d2 = WS_T2 + c8 * 245760 + (2 * (jj < 2 ? 2 * (pc & 7) + jj : 16 + (pc & 7)) + (pc >> 3)) * 5120 + (ko & 31) * 160 + lane * 16;
        td[j] = sel(hw == 2 ? (lane < 48 && j < 10) : lane < 40, hw == 2 ? d1 : d2);
        PIN(td[j]);
      }
      PIN(vo12);
    }
    int l16 = lane * 16, h16 = ht * 16;
    PIN(l16); PIN(h16);
    gu32* fp1 = FLAG(1, c16) + lane;
    gu32* fp78 = lane < 32 ? FLAG(7, c8) + lane : FLAG(8, c16) + t;
    gu32* fp4 = FLAG(4, 2 * r64 + (g & 1)) + t;
    gu32* fp1w = FLAG(1, c16) + r128 * 4 + hw;
    PIN(fp1); PIN(fp78); PIN(fp4); PIN(fp1w);
    ((LAS v4u*)(L + L_KVF))[ht] = v4u{0u, 0u, 0u, 0u};
    ((LAS v4u*)(L + L_KVF))[256 + ht] = v4u{0u, 0u, 0u, 0u};
    asm volatile("" ::: "memory");
    if (hw != 0) {
      if (hw == 1) {
#pragma unroll
        for (int j = 0; j < 5; ++j) {
          const int i8 = 5 * r128 + j;
          st128(tv[j], rs, sel(lane < 40, (i8 & 3) * f40m + f40l), WS_TKV + c16 * 153600 + 102400 + (i8 >> 2) * 2560);
        }
#pragma unroll
        for (int j = 0; j < 5; ++j) {
          const int pp = r64 >> 1, e = 20 * (r64 & 1) + 4 * j;
          const int R = e < 32 ? 32 * pp + e : 32 * (16 + (pp >> 2)) + 8 * (pp & 3) + e - 32;
          st128(tv[5 + j], rs, sel(lane < 40, lane * 16), WS_TO + c8 * 102400 + R * 160);
        }
      }
      if (hw == 3) {
        if (lane < 24) *(LAS f32x4*)(L + L_B1 + lane * 16) = f32x4{bf_lo(ub1.x), bf_hi(ub1.x), bf_lo(ub1.y), bf_hi(ub1.y)};
        if (lane < 20) *(LAS f32x4*)(L + L_B2 + lane * 16) = f32x4{bf_lo(ub2.x), bf_hi(ub2.x), bf_lo(ub2.y), bf_hi(ub2.y)};
      }
      asm volatile("" :: "v"(pf0));
      PIN(tag); tag += 1u;
      DRAIN();
      if (hw == 1 && lane < 2) flag_set(lane == 0 ? FLAG(8, c16) + r128 : FLAG(7, c8) + r64, tag);
    }
    if (hw == 0) {
      wait_flag(FLAG(6, c16) + lane, 16, tag, lane);
      sig[0] = 1u;
    }
    sig_wait(sig);
    STAMP(1);
    const int kvq = WS_TKV + c16 * 153600, wos = WS_TO + c8 * 102400;
    dma_image<0, 24>(lds0 + L_BKV, l16, srd_ws, kvq, hw, 3 * (bi >> 3));
    dma_image<24, 56>(lds0 + L_BKV, l16, srd_ws, kvq, hw, 4 * (bi >> 3));
    asm volatile("s_waitcnt vmcnt(8)" ::: "memory");
    STAMP(2);
    BAR_RAW();
    dma_image<56, 100>(lds0 + L_BKV, l16, srd_ws, kvq, hw, 5 * (bi >> 3));
    asm volatile("s_waitcnt vmcnt(10)" ::: "memory");
    BAR_RAW();
    if (hw == 1) wait_flag(fp78, 48, tag, lane);
    DRAIN();
    STAMP(3);
    BAR_RAW();
    if (QPOS == 0) dma_image<100, 150>(lds0 + L_BKV, l16, srd_ws, kvq, hw, 6 * (bi >> 3));
    prog_wait(prog, 1);
    if (QPOS == 1) dma_image<100, 150>(lds0 + L_BKV, l16, srd_ws, kvq, hw, 6 * (bi >> 3));
    if (hw >= 2) {
      const __amdgpu_buffer_rsrc_t r12 = __builtin_amdgcn_make_buffer_rsrc((void*)(hw == 2 ? w1 : w2), 0, D * F * 2, 0x00020000);
      const int base = hw == 2 ? r128 * 122880 + c16 * 192 : r64 * 61440 + c8 * 160, step = hw == 2 ? 4 * 3072 : 4 * 1280;
#pragma unroll
      for (int j = 0; j < 12; ++j) tv[j] = __builtin_amdgcn_raw_buffer_load_b128(r12, vo12, (j < 10 || hw == 3) ? base + j * step : OOB, 0);
      asm volatile("s_waitcnt vmcnt(12)" ::: "memory");
    } else {
      dma_image<32, 68, 0, 2>(lds0 + L_WO, l16, srd_ws, wos, hw, bi >> 2);
      asm volatile("s_waitcnt vmcnt(18)" ::: "memory");
    }
    STAMP(4);
    BAR();
    __builtin_amdgcn_s_setprio(2);
    STAMP(11);
    {
      f32x4 pv[4];
#pragma unroll
      for (int ww = 0; ww < 4; ++ww) pv[ww] = *(LAS f32x4*)((LAS char*)red_off + ww * 8192);
      st128(__builtin_bit_cast(v4u, add4(add4(pv[0], pv[1]), add4(pv[2], pv[3]))), rs, kvp_off, 0);
      PROG(1);
      STAMP(12);
      DRAIN();
    }
    if (lane == 0) flag_set(fp1w, tag);
    if (hw == 0) {
      STAMP(10);
      __builtin_amdgcn_s_sleep(H1W);
      wait_flag(fp1, 64, tag, lane);
      sig[2] = 1u;
    } else {
      if (H1G) __builtin_amdgcn_s_sleep(H1G);
      dma_image<68, 100, 0, 3>(lds0 + L_WO, l16, srd_ws, wos, hw - 1, bi >> 2);
      prog_wait(prog + 1, 1);
      dma_image<0, 32, 0, 3>(lds0 + L_WO, l16, srd_ws, wos, hw - 1, 2 * (bi >> 2));
      if (hw >= 2) {
#pragma unroll
        for (int j = 0; j < 12; ++j) st128(tv[j], rs, td[j], 0);
      }
      STAMP(10);
      sig_wait(sig + 2);
    }
    STAMP(5);
    {
      v4u pv[16];
#pragma unroll
      for (int r = 0; r < 16; ++r) pv[r] = ld128(rs, h16, WS_KVP + (c16 * 16 + r) * 4096);
      SCHED();
      f32x4 v = __builtin_bit_cast(f32x4, pv[0]);
#pragma unroll
      for (int r = 1; r < 16; ++r) v = add4(v, __builtin_bit_cast(f32x4, pv[r]));
      v2u o; o.x = pk2(lo2(v)); o.y = pk2(hi2(v));
      *(LAS v2u*)((LAS char*)kvf_off) = o;
      if (ht >= 200) {
#pragma unroll
        for (int c = 1; c < 4; ++c) *(LAS v2u*)((LAS char*)kvf_off + c * 64) = o;
      }
    }
    DRAIN();
    STAMP(6);
    BAR();
    __builtin_amdgcn_s_setprio(0);
    if (hw >= 2 && lane == 0) flag_set(hw == 2 ? FLAG(5, c16) + r128 : FLAG(11, c8) + r64, tag);
    if (hw == 2) {
      f32x4 s0 = {0.f, 0.f, 0.f, 0.f}, s1 = {0.f, 0.f, 0.f, 0.f};
#pragma unroll
      for (int j = 0; j < 10; ++j) {
        s0 = add4(s0, f32x4{bf_lo(tv[j].x), bf_hi(tv[j].x), bf_lo(tv[j].y), bf_hi(tv[j].y)});
        s1 = add4(s1, f32x4{bf_lo(tv[j].z), bf_hi(tv[j].z), bf_lo(tv[j].w), bf_hi(tv[j].w)});
      }
#pragma unroll
      for (int d = 24; d >= 12; d -= 12) {
        f32x4 u0, u1;
#pragma unroll
        for (int e = 0; e < 4; ++e) { u0[e] = __shfl(s0[e], lane + d); u1[e] = __shfl(s1[e], lane + d); }
        s0 = add4(s0, u0); s1 = add4(s1, u1);
      }
      f32x4 sv;
#pragma unroll
      for (int e = 0; e < 4; ++e) { const float a0 = __shfl(s0[e], lane >> 1), a1 = __shfl(s1[e], lane >> 1); sv[e] = (lane & 1) ? a1 : a0; }
      st128(__builtin_bit_cast(v4u, sv), rs, sel(lane < 24, lane * 16), WS_CS + (c16 * 16 + r128) * 384);
      publish(FLAG(9, c16) + r128, tag, lane);
    }
    if (hw == 1) {
      __builtin_amdgcn_s_sleep(40);
      wait_flag(lane < 32 ? FLAG(11, c8) + lane : FLAG(5, c16) + t, 48, tag, lane);
      sig[3] = 1u;
    }
    sig_wait(sig + 3);
    prog_wait(prog, 2);
    if (hw == 0) {
      wait_flag(FLAG(9, c16) + lane, 16, tag, lane);
      v4u pv[16];
      const int co = sel(lane < 24, l16);
#pragma unroll
      for (int r = 0; r < 16; ++r) pv[r] = ld128(rs, co, WS_CS + (c16 * 16 + r) * 384);
      SCHED();
      f32x4 v = __builtin_bit_cast(f32x4, pv[0]);
#pragma unroll
      for (int r = 1; r < 16; ++r) v = add4(v, __builtin_bit_cast(f32x4, pv[r]));
      if (lane < 24) *(LAS f32x4*)(L + L_CS + lane * 16) = v;
    } else {
      dma_image<70, 120, 0, 3>(lds0 + L_W1, l16, srd_ws, WS_T1 + c16 * 122880, hw - 1, 6 * (bi >> 3));
      prog_wait(prog, 3);
      dma_image<0, 40, 0, 3>(lds0 + L_W1, l16, srd_ws, WS_T1 + c16 * 122880, hw - 1, 5 * (bi >> 3));
      STAMP(7);
      prog_wait(prog, 4);
      dma_image<40, 70, 0, 3>(lds0 + L_W1, l16, srd_ws, WS_T1 + c16 * 122880, hw - 1, 3 * (bi >> 3));
    }
    DRAIN();
    STAMP(8);
    BAR();
    const int w2s = WS_T2 + c8 * 245760;
    prog_wait(prog, 5);
    dma_image<0, 30>(lds0 + L_W2, l16, srd_ws, w2s, hw, bi >> 2);
    STAMP(14);
    prog_wait(prog, 6);
    dma_image<30, W2S>(lds0 + L_W2, l16, srd_ws, w2s, hw, bi >> 2);
    prog_wait(prog, 7);
    if (hw != 0) dma_image<W2S, 110, 0, 3>(lds0 + L_W2, l16, srd_ws, w2s, hw - 1, bi >> 2);
    DRAIN();
    STAMP(9);
    if (hw == 0) wait_flag(fp4, 32, tag, lane);
    STAMP(15);
    BAR_RAW();
    dma_image<110, 150>(lds0 + L_W2, l16, srd_ws, w2s, hw, 2 * (bi >> 2));
    prog_wait(prog, 8);
    if (hw == 0) wait_flag(fp4 + 6 * FLAG_SET / 4, 32, tag, lane);
    else dma_image<150, 198, 0, 3>(lds0 + L_W2 - 153600, l16, srd_ws, w2s, hw - 1, 3 * (bi >> 2));
    asm volatile("s_waitcnt vmcnt(16)" ::: "memory");
    STAMP(17);
    BAR_RAW();
    dma_image<198, 200>(lds0 + L_W2 - 153600, l16, srd_ws, w2s, hw, 0);
    dma_image<200, 240>(lds0 + L_W2 - 153600, l16, srd_ws, w2s, hw, 2 * (bi >> 2));
    asm volatile("s_waitcnt vmcnt(10)" ::: "memory");
    BAR_RAW();
    DRAIN();
    BAR_RAW();
  }

}
}

extern "C" void kernel_launch(void* const* d_in, const int* in_sizes, int n_in, void* d_out, int out_size, void* d_ws,
                              size_t ws_size, hipStream_t stream) {
  const unsigned short* x = (const unsigned short*)d_in[0];
  const unsigned short* wqkv = (const unsigned short*)d_in[1];
  const unsigned short* wo = (const unsigned short*)d_in[2];
  const unsigned short* w1 = (const unsigned short*)d_in[4];
  const unsigned short* b1 = (const unsigned short*)d_in[5];
  const unsigned short* w2 = (const unsigned short*)d_in[6];
  const unsigned short* b2 = (const unsigned short*)d_in[7];
  fused_block<<<256, 512, 0, stream>>>(x, wqkv, wo, w1, b1, w2, b2, (unsigned short*)d_out, (unsigned char*)d_ws);
}
```

```cpp
#include <hip/hip_runtime.h>
#include <hip/hip_bf16.h>

#define STAMPS 0
#define THR 0
#define TOUCH 1
#define CODEPF 1
#define FLAGPF 1
#define OUTAUX 0
#define OUTPF 0
#define S0 6
#define XRPOS 0
#define QPOS 0
#define H1W 8
#define H1G 2
#define W2S 70
#define PUBA 8
#define NCDELAY 0
#define HPRIO 0
#define WOLATE 0
#define SLEEP 1

#define GAS __attribute__((address_space(1)))
#define LAS __attribute__((address_space(3)))
#define RLX_AGENT __ATOMIC_RELAXED, __HIP_MEMORY_SCOPE_AGENT
typedef unsigned v4u __attribute__((ext_vector_type(4)));
typedef unsigned v2u __attribute__((ext_vector_type(2)));
typedef float f32x4 __attribute__((ext_vector_type(4)));
typedef float f32x2 __attribute__((ext_vector_type(2)));
typedef __bf16 bf16x8 __attribute__((ext_vector_type(8)));
typedef __bf16 bf16x2 __attribute__((ext_vector_type(2)));
typedef short s16x4 __attribute__((ext_vector_type(4)));
typedef GAS unsigned gu32;

namespace {
constexpr int S = 2048, D = 640, F = 1536;
constexpr float LN_EPS = 1e-5f;
constexpr int AUX_SC1 = 16;
constexpr unsigned kSpinCap = 1u << 17;
constexpr int OOB = 0x7FFFFF00;

constexpr int WS_EPOCH = 0;
constexpr int WS_FLAG = 4096;
constexpr int FLAG_SET = 64 * 256;
constexpr int WS_XN = 1 << 20;
constexpr int WS_O = WS_XN + 128 * 20 * 1024;
constexpr int WS_H = WS_O + 128 * 20 * 1024;
constexpr int WS_H1 = WS_H + 128 * 20 * 1024;
constexpr int WS_KVP = WS_H1 + 128 * 48 * 1024;
constexpr int WS_ST = WS_KVP + 16 * 16 * 4096;
constexpr int WS_DBG = WS_ST + 2048 * 64;
constexpr int WS_TKV = WS_DBG + 256 * 8 * 32 * 8;
constexpr int WS_TO = WS_TKV + 16 * 153600;
constexpr int WS_T1 = WS_TO + 8 * 102400;
constexpr int WS_T2 = WS_T1 + 16 * 122880;
constexpr int WS_CS = WS_T2 + 16 * 122880;
constexpr int WS_BYTES = WS_CS + 16 * 16 * 384;

constexpr int L_BKV = 0, L_BQ = 102400;
constexpr int L_RED = 0, L_KVF = 153600;
constexpr int L_WO = 0, L_W1 = 30720, L_W2 = 0, L_CS = 161792;
constexpr int L_B1 = L_CS + 1152, L_B2 = L_CS + 1536;
constexpr int LDS_BYTES = 163840;

__device__ __forceinline__ f32x4 mfma(v4u a, v4u b, f32x4 c) {
  return __builtin_amdgcn_mfma_f32_16x16x32_bf16(__builtin_bit_cast(bf16x8, a), __builtin_bit_cast(bf16x8, b), c, 0, 0, 0);
}
__device__ __forceinline__ unsigned pk2(f32x2 v) { return __builtin_bit_cast(unsigned, __builtin_convertvector(v, bf16x2)); }
__device__ __forceinline__ unsigned pk2(float lo, float hi) { return pk2(f32x2{lo, hi}); }
__device__ __forceinline__ float bf_lo(unsigned u) { return __builtin_bit_cast(float, u << 16); }
__device__ __forceinline__ float bf_hi(unsigned u) { return __builtin_bit_cast(float, u & 0xFFFF0000u); }
__device__ __forceinline__ f32x2 bf2(unsigned u) { return f32x2{bf_lo(u), bf_hi(u)}; }
__device__ __forceinline__ f32x2 lo2(f32x4 v) { return f32x2{v[0], v[1]}; }
__device__ __forceinline__ f32x2 hi2(f32x4 v) { return f32x2{v[2], v[3]}; }
__device__ __forceinline__ v4u mk_srd(const void* p, unsigned bytes) {
  const unsigned long long a = (unsigned long long)p;
  v4u r;
  r.x = (unsigned)a; r.y = (unsigned)(a >> 32) & 0xFFFFu; r.z = bytes; r.w = 0x00020000u;
  return r;
}
#define DRAIN() asm volatile("s_waitcnt vmcnt(0)" ::: "memory")
#define BAR() asm volatile("s_waitcnt lgkmcnt(0)\n\ts_barrier" ::: "memory")
#define BAR_RAW() asm volatile("s_barrier" ::: "memory")
#define SCHED() __builtin_amdgcn_sched_barrier(0)
#define PIN(x) asm volatile("" : "+v"(x))

__device__ __forceinline__ void dma2(unsigned lds_addr, unsigned voff, v4u srd, unsigned soff) {
  unsigned keep;
  asm volatile("s_mov_b32 %0, m0\n\ts_mov_b32 m0, %1\n\ts_nop 0\n\t"
               "buffer_load_dwordx4 %2, %3, %4 offen sc1 lds\n\t"
               "buffer_load_dwordx4 %2, %3, %4 offen offset:1024 sc1 lds\n\t"
               "s_mov_b32 m0, %0"
               : "=&s"(keep) : "s"(lds_addr), "v"(voff), "s"(srd), "s"(soff) : "memory");
}
template <int LO, int HI, int TH = 0, int NW = 4>
__device__ __forceinline__ void dma_image(unsigned lds_base, unsigned voff, v4u srd, int ws_off, int hw, int rot) {
  constexpr int NP = (HI - LO) / 2;
#pragma unroll 1
  for (int i = hw; i < NP; i += NW) {
    int j = i + rot;
    if (j >= NP) j -= NP;
    dma2(lds_base + LO * 1024 + j * 2048, voff, srd, ws_off + LO * 1024 + j * 2048);
    if (TH) asm volatile("s_waitcnt vmcnt(%0)" :: "i"(TH - 2) : "memory");
  }
}
__device__ __forceinline__ v2u rdtr(LAS char* p) { return __builtin_bit_cast(v2u, __builtin_amdgcn_ds_read_tr16_b64_v4i16((LAS s16x4*)p)); }
struct LBase {
  unsigned a[3];
  __device__ __forceinline__ explicit LBase(unsigned v) { a[0] = v; a[1] = v + 65536; a[2] = v + 131072; PIN(a[0]); PIN(a[1]); PIN(a[2]); }
  __device__ __forceinline__ v2u rd(int off) const { return rdtr((LAS char*)(a[off >> 16]) + (off & 65535)); }
};
template <int MT, int NT, int KS, int PD, bool WA, int M0 = 0, class RB, class GA, class PRE>
__device__ __forceinline__ void kloop(f32x4 (&acc)[MT][NT], RB rb, GA ga, PRE pre) {
  v2u bl[PD + 1][NT], bh[PD + 1][NT];
#pragma unroll
  for (int m = M0; m < MT; ++m)
#pragma unroll
    for (int s = 0; s < NT; ++s) acc[m][s] = f32x4{0.f, 0.f, 0.f, 0.f};
#pragma unroll
  for (int ks = 0; ks < PD; ++ks)
#pragma unroll
    for (int s = 0; s < NT; ++s) { bl[ks][s] = rb(ks, s, 0); bh[ks][s] = rb(ks, s, 1); }
  SCHED();
#pragma unroll
  for (int ks = 0; ks < KS; ++ks) {
    pre(ks);
#pragma unroll
    for (int s = 0; s < NT; ++s)
#pragma unroll
      for (int m = 0; m < MT; ++m) {
        const v2u lo = bl[ks % (PD + 1)][s], hi = bh[ks % (PD + 1)][s];
        const v4u wv = {lo.x, lo.y, hi.x, hi.y};
        acc[m][s] = WA ? mfma(wv, ga(ks, m), acc[m][s]) : mfma(ga(ks, m), wv, acc[m][s]);
        if (ks + PD < KS) {
          if (MT == 1 || m == 0) bl[(ks + PD) % (PD + 1)][s] = rb(ks + PD, s, 0);
          if (MT == 1 || m == 1) bh[(ks + PD) % (PD + 1)][s] = rb(ks + PD, s, 1);
        }
        SCHED();
      }
  }
}
constexpr int ld_hi(int ks, int s0, int W, int KS) {
  const int a = s0 + 2 * (ks + 1), b = ks + 1 + W, c = a < b ? a : b;
  return c < KS ? c : KS;
}
__device__ __forceinline__ v4u ld128(__amdgpu_buffer_rsrc_t rs, int voff, int soff) {
  return __builtin_amdgcn_raw_buffer_load_b128(rs, voff, soff, AUX_SC1);
}
__device__ __forceinline__ v2u ld64(__amdgpu_buffer_rsrc_t rs, int voff, int soff) {
  return __builtin_amdgcn_raw_buffer_load_b64(rs, voff, soff, AUX_SC1);
}
__device__ __forceinline__ void st128(v4u v, __amdgpu_buffer_rsrc_t rs, int voff, int soff) {
  __builtin_amdgcn_raw_buffer_store_b128(v, rs, (int)((unsigned)voff + (unsigned)soff), 0, AUX_SC1);
}
__device__ __forceinline__ int sel(bool c, int a) {
  PIN(a);
  return c ? a : OOB;
}
__device__ __forceinline__ v4u ld_split(__amdgpu_buffer_rsrc_t rs, int lane, int soff) {
  const v2u a = ld64(rs, lane * 8, soff), b = ld64(rs, lane * 8 + 512, soff);
  return v4u{a.x, a.y, b.x, b.y};
}
__device__ __forceinline__ void st64(v2u v, __amdgpu_buffer_rsrc_t rs, int voff, int soff) {
  __builtin_amdgcn_raw_buffer_store_b64(v, rs, voff, soff, AUX_SC1);
}
__device__ __forceinline__ void flag_set(gu32* p, unsigned tag) {
  asm volatile("global_atomic_swap %0, %1, off sc1" :: "v"(p), "v"(tag) : "memory");
}
__device__ __forceinline__ void wait_flag(gu32* p, int n, unsigned tag, int lane) {
#pragma unroll 1
  for (unsigned spins = 0; spins < kSpinCap; ++spins) {
    unsigned v = tag;
    if (lane < n) asm volatile("global_atomic_add %0, %1, %2, off sc0 sc1\n\ts_waitcnt vmcnt(0)" : "=&v"(v) : "v"(p), "v"(0u) : "memory");
    if (__all(v == tag)) break;
    if (SLEEP) __builtin_amdgcn_s_sleep(SLEEP);
  }
  asm volatile("" ::: "memory");
}
__device__ __forceinline__ void sig_wait(volatile LAS unsigned* p) {
#pragma unroll 1
  for (unsigned spins = 0; spins < 64 * kSpinCap; ++spins) {
    if (*p != 0u) break;
    __builtin_amdgcn_s_sleep(1);
  }
  asm volatile("" ::: "memory");
}
__device__ __forceinline__ void prog_wait(volatile LAS v4u* p, unsigned stage) {
#pragma unroll 1
  for (unsigned spins = 0; spins < 64 * kSpinCap; ++spins) {
    const v4u v = *p;
    if (min(min(v.x, v.y), min(v.z, v.w)) >= stage) break;
    __builtin_amdgcn_s_sleep(1);
  }
  asm volatile("" ::: "memory");
}
__device__ __forceinline__ void publish(gu32* flag, unsigned tag, int lane) {
  DRAIN();
  if (lane == 0) flag_set(flag, tag);
}
__device__ __forceinline__ f32x4 add4(f32x4 a, f32x4 b) {
  f32x4 r;
#pragma unroll
  for (int i = 0; i < 4; ++i) { float x; asm("v_add_f32 %0, %1, %2" : "=v"(x) : "v"(a[i]), "v"(b[i])); r[i] = x; }
  return r;
}
__device__ __forceinline__ float xsum32(float x) {
  unsigned u = __builtin_bit_cast(unsigned, x), v = u;
  asm("" : "+v"(v));
  auto r = __builtin_amdgcn_permlane32_swap(u, v, false, false);
  const unsigned a = r[0], b = r[1];
  return __builtin_bit_cast(float, a) + __builtin_bit_cast(float, b);
}
__device__ __forceinline__ float xsum16(float x) {
  unsigned u = __builtin_bit_cast(unsigned, x), v = u;
  asm("" : "+v"(v));
  auto r = __builtin_amdgcn_permlane16_swap(u, v, false, false);
  const unsigned a = r[0], b = r[1];
  return __builtin_bit_cast(float, a) + __builtin_bit_cast(float, b);
}
__device__ __forceinline__ float xsum8(float x) {
  return x + __builtin_bit_cast(float, __builtin_amdgcn_update_dpp(0, __builtin_bit_cast(int, x), 0x128, 0xf, 0xf, false));
}
__device__ __forceinline__ f32x2 phi2(f32x2 t) {
  const f32x2 u = t * 1.4426950408889634f, v = t + 1e-6f;
  const f32x2 e = {__builtin_amdgcn_fmed3f(__builtin_amdgcn_exp2f(u[0]), 0.f, 1.f), __builtin_amdgcn_fmed3f(__builtin_amdgcn_exp2f(u[1]), 0.f, 1.f)};
  return f32x2{fmaxf(v[0], 1e-6f), fmaxf(v[1], 1e-6f)} + e;
}
__device__ __forceinline__ f32x2 gelu2(f32x2 x) {
  const f32x2 u = x * (x * x * -0.1029432397f + -2.302208198f);
  const f32x2 d = f32x2{__builtin_amdgcn_exp2f(u[0]), __builtin_amdgcn_exp2f(u[1])} + 1.f;
  return x * f32x2{__builtin_amdgcn_rcpf(d[0]), __builtin_amdgcn_rcpf(d[1])};
}

#define STAMP(i) do { } while (0)

__global__ void __launch_bounds__(512, 2)
fused_block(const unsigned short* __restrict__ x, const unsigned short* __restrict__ wqkv, const unsigned short* __restrict__ wo,
            const unsigned short* __restrict__ w1, const unsigned short* __restrict__ b1, const unsigned short* __restrict__ w2,
            const unsigned short* __restrict__ b2, unsigned short* __restrict__ out, unsigned char* ws) {
  __shared__ __attribute__((aligned(16))) char smem[LDS_BYTES];
  unsigned long long pc0;
  asm volatile("s_getpc_b64 %0" : "=s"(pc0));
  LAS char* L = (LAS char*)smem;
  const unsigned lds0 = (unsigned)(size_t)L;
  const int tid = threadIdx.x, lane = tid & 63, w = __builtin_amdgcn_readfirstlane(tid >> 6);
  const int b = blockIdx.x, xcd = b & 7, bi = b >> 3;
  const int g = lane >> 4, t = lane & 15;
  const __amdgpu_buffer_rsrc_t rs = __builtin_amdgcn_make_buffer_rsrc((void*)ws, 0, WS_BYTES, 0x00020000);
  gu32* flags = (gu32*)(ws + WS_FLAG);
#define FLAG(set, grp) (flags + ((set) * FLAG_SET + (grp) * 256) / 4)
  asm volatile("" :: "s"(x), "s"(wqkv), "s"(wo), "s"(w1), "s"(b1), "s"(w2), "s"(b2), "s"(out), "s"(ws));
  STAMP(0);
  const int r128 = 4 * (xcd >> 1) + (bi >> 3), c16 = 8 * (xcd & 1) + (bi & 7);
  const int r64 = 8 * (xcd >> 1) + (bi >> 2), c8 = 4 * (xcd & 1) + (bi & 3);

  const __amdgpu_buffer_rsrc_t rx = __builtin_amdgcn_make_buffer_rsrc((void*)x, 0, S * D * 2, 0x00020000);
  const int hw = w - 4, ht = tid - 256;
  volatile LAS unsigned* sig = (volatile LAS unsigned*)(L + L_CS + 1024);
  volatile LAS v4u* prog = (volatile LAS v4u*)(L + L_CS + 1088);
#define PROG(n) do { asm volatile("" ::: "memory"); sig[16 + w] = (n); } while (0)

  unsigned tag = __hip_atomic_load((gu32*)(ws + WS_EPOCH), RLX_AGENT);
  const __amdgpu_buffer_rsrc_t rq = __builtin_amdgcn_make_buffer_rsrc((void*)wqkv, 0, D * 3 * D * 2, 0x00020000);
  v4u tv[12];
  const int q10 = lane / 10, c10 = lane % 10;
  if (w < 4) {
#pragma unroll
    for (int j = 0; j < 3; ++j)
      tv[j] = __builtin_amdgcn_raw_buffer_load_b128(rx, (lane & 7) * 1280 + (lane >> 3) * 16, (j < 2 || w < 2) ? 8 * b * 1280 + (w + 4 * j) * 128 : OOB, 0);
  } else if (w == 4) {
    const int vo = sel(lane < 40, q10 * 3840 + (c10 < 5 ? 1280 : 2480) + c10 * 16);
#pragma unroll
    for (int j = 0; j < 10; ++j) tv[j] = __builtin_amdgcn_raw_buffer_load_b128(rq, vo, (40 * r128 + 4 * j) * 3840 + c16 * 80, 0);
  } else if (w == 7) {
    *(LAS v4u*)(L + L_CS + 512 + lane * 16) = v4u{0u, 0u, 0u, 0u};
  }
  BAR();

  if (w < 4) {
  const int grp32 = 4 * r128 + w;
  constexpr int AW = 6;

  {
    const int tok = 8 * b + (lane & 7), cl = lane >> 3;
    const bool has3 = w < 2;
    int xd = ((tok >> 4) * 20 + 2 * w + (cl >> 2)) * 1024 + ((cl & 3) * 16 + (tok & 15)) * 16;
    PIN(xd);
    SCHED();
    f32x2 f[12];
#pragma unroll
    for (int j = 0; j < 3; ++j)
#pragma unroll
      for (int e = 0; e < 4; ++e) f[j * 4 + e] = bf2(tv[j][e]);
    f32x2 s2 = (f[0] + f[1]) + (f[2] + f[3]) + ((f[4] + f[5]) + (f[6] + f[7])) + ((f[8] + f[9]) + (f[10] + f[11]));
    const float mloc = xsum32(xsum16(xsum8(s2[0] + s2[1]))) * (has3 ? 1.f / 192.f : 1.f / 128.f);
    const float m3 = has3 ? mloc : 0.f;
    f32x2 q2 = {0.f, 0.f}, q3 = {0.f, 0.f};
#pragma unroll
    for (int j = 0; j < 12; j += 2) {
      const f32x2 d0 = f[j] - (j < 8 ? mloc : m3), d1 = f[j + 1] - (j < 8 ? mloc : m3);
      q2 += d0 * d0; q3 += d1 * d1;
    }
    q2 += q3;
    const float m2loc = xsum32(xsum16(xsum8(q2[0] + q2[1])));
    if (lane < 8) *(volatile LAS f32x4*)(L + L_CS + 512 + (lane * 4 + w) * 16) = f32x4{mloc, m2loc, 1.f, 1.f};
    STAMP(25);
    f32x4 pr[4];
#pragma unroll 1
    for (unsigned spins = 0; spins < 64 * kSpinCap; ++spins) {
      bool ok = true;
#pragma unroll
      for (int ww = 0; ww < 4; ++ww) {
        pr[ww] = *(volatile LAS f32x4*)(L + L_CS + 512 + ((lane & 7) * 4 + ww) * 16);
        ok = ok && pr[ww][2] == 1.f;
      }
      if (__all(ok)) break;
    }
    const f32x4 p01 = {pr[0][0], pr[0][1], pr[1][0], pr[1][1]}, p23 = {pr[2][0], pr[2][1], pr[3][0], pr[3][1]};
    const float mean = (p01[0] + p01[2]) * 0.3f + (p23[0] + p23[2]) * 0.2f;
    const float e0 = p01[0] - mean, e1 = p01[2] - mean, e2 = p23[0] - mean, e3 = p23[2] - mean;
    const float m2 = (p01[1] + p01[3]) + (p23[1] + p23[3]) + 192.f * (e0 * e0 + e1 * e1) + 128.f * (e2 * e2 + e3 * e3);
    const float rstd = __builtin_amdgcn_rsqf(m2 * (1.f / D) + LN_EPS);
#pragma unroll
    for (int j = 0; j < 3; ++j) {
      v4u o;
#pragma unroll
      for (int e = 0; e < 4; ++e) o[e] = pk2((f[j * 4 + e] - mean) * rstd);
      st128(o, rs, (j < 2 || has3) ? xd : OOB, WS_XN + j * 8192);
    }
    STAMP(26);
    PIN(tag); tag += 1u;
    publish(FLAG(0, b >> 2) + (b & 3) * 4 + w, tag, lane);
  }

  STAMP(1);

  v4u xr[3];
  {
    wait_flag(FLAG(0, grp32) + lane, 16, tag, lane);
    STAMP(2);
    v4u a[20][2];
    const int abase = WS_XN + 2 * grp32 * 20 * 1024;
#pragma unroll
    for (int ks = 0; ks < S0; ++ks)
#pragma unroll
      for (int m = 0; m < 2; ++m) a[ks][m] = ld128(rs, lane * 16, abase + (m * 20 + ks) * 1024);
    SCHED();
    BAR_RAW();
    STAMP(27);
    {
      f32x4 acc[2][5];
      const LBase bp(lds0 + L_BKV + (lane >> 2) * 160 + (lane & 3) * 8);
      kloop<2, 5, 20, 1, false>(acc, [&](int ks, int s, int h) { return bp.rd(ks * 5120 + h * 2560 + s * 32); },
        [&](int ks, int m) { return a[ks][m]; },
        [&](int ks) {
#pragma unroll
          for (int j = ld_hi(ks - 1, S0, AW, 20); j < ld_hi(ks, S0, AW, 20); ++j)
#pragma unroll
            for (int m = 0; m < 2; ++m) a[j][m] = ld128(rs, lane * 16, abase + (m * 20 + j) * 1024);
          if (ks == 3 || ks == 10) BAR_RAW();
          if (ks == 20 - AW) PROG(1);
        });
      STAMP(3);
      v4u ka[3], vb[3];
#pragma unroll
      for (int s = 0; s < 3; ++s)
#pragma unroll
        for (int m = 0; m < 2; ++m) {
          vb[s][2 * m] = pk2(lo2(acc[m][s + 2])); vb[s][2 * m + 1] = pk2(hi2(acc[m][s + 2]));
        }
#pragma unroll
      for (int e = 0; e < 4; ++e) vb[0][e] = t < 8 ? 0x3F803F80u : vb[0][e];
      const f32x4 z4 = {0.f, 0.f, 0.f, 0.f};
      LAS f32x4* red = (LAS f32x4*)(L + L_RED) + w * 512 + lane;
#pragma unroll
      for (int s = 0; s < 3; ++s) {
#pragma unroll
        for (int m = 0; m < 2; ++m) { ka[s][2 * m] = pk2(phi2(lo2(acc[m][s]))); ka[s][2 * m + 1] = pk2(phi2(hi2(acc[m][s]))); }
#pragma unroll
        for (int sb = 0; sb < 3; ++sb)
          if (s > 0 || sb < 2) red[(3 * s + sb - (s > 0)) * 64] = mfma(ka[s], vb[sb], z4);
      }
      STAMP(28);
      BAR();
    }
    STAMP(4);
    f32x4 qa[2][3];
    {
      unsigned bp0 = lds0 + L_BQ + (lane >> 2) * 64 + (lane & 3) * 8 + (g & 1) * 32;
      unsigned bp1 = lds0 + L_BQ + (lane >> 2) * 64 + (lane & 3) * 8 + 32 - (g & 1) * 32;
      unsigned bp2 = lds0 + L_BQ + 2048 + (lane >> 2) * 16 + (lane & 1) * 8;
      PIN(bp0); PIN(bp1); PIN(bp2);
      kloop<2, 3, 20, 2, true>(qa,
        [&](int ks, int s, int h) {
          return s == 0 ? rdtr((LAS char*)bp0 + ks * 2560 + h * 1024) : s == 1 ? rdtr((LAS char*)bp1 + ks * 2560 + h * 1024) : rdtr((LAS char*)bp2 + ks * 2560 + h * 256);
        },
        [&](int ks, int m) { return a[ks][m]; },
        [&](int ks) {});
    }
    v4u pq0[2], pq1[2];
#pragma unroll
    for (int m = 0; m < 2; ++m) {
      pq0[m].x = pk2(phi2(lo2(qa[m][0]))); pq0[m].y = pk2(phi2(hi2(qa[m][0])));
      pq0[m].z = pk2(phi2(lo2(qa[m][1]))); pq0[m].w = pk2(phi2(hi2(qa[m][1])));
      const unsigned lo = pk2(phi2(lo2(qa[m][2]))), hi = pk2(phi2(hi2(qa[m][2])));
      pq1[m].x = g < 2 ? lo : 0u; pq1[m].y = g < 2 ? hi : 0u; pq1[m].z = 0u; pq1[m].w = 0u;
      PIN(pq0[m]); PIN(pq1[m]);
    }
    STAMP(5);
    BAR_RAW();
#pragma unroll
    for (int j = 0; j < 3; ++j)
      xr[j] = __builtin_amdgcn_raw_buffer_load_b128(rx, sel(j < 2 || g < 2, t * 1280 + g * 16), (4 * r64 + w) * 16 * 1280 + c8 * 160 + j * 64, 0);
    STAMP(17);
    {
      v4u kv[4][2];
#pragma unroll
      for (int es = 3; es >= 0; --es)
#pragma unroll
        for (int ks = 0; ks < 2; ++ks) kv[es][ks] = *(LAS v4u*)(L + L_KVF + (es * 2 + ks) * 1024 + lane * 16);
      SCHED();
      f32x4 o[2][4];
#pragma unroll
      for (int es = 3; es >= 0; --es)
#pragma unroll
        for (int m = 0; m < 2; ++m) o[m][es] = mfma(kv[es][0], pq0[m], f32x4{0.f, 0.f, 0.f, 0.f});
#pragma unroll
      for (int es = 3; es >= 0; --es)
#pragma unroll
        for (int m = 0; m < 2; ++m) o[m][es] = mfma(kv[es][1], pq1[m], o[m][es]);
#pragma unroll
      for (int m = 0; m < 2; ++m) {
        const float i0 = __builtin_amdgcn_rcpf(o[m][3][0] + 1e-6f), i1 = __builtin_amdgcn_rcpf(o[m][3][1] + 1e-6f);
        const float im = g == 0 ? i0 : i1;
        v4u ov;
        ov.x = pk2(lo2(o[m][0]) * i0); ov.y = pk2(hi2(o[m][0]) * i0);
        ov.z = pk2(lo2(o[m][1]) * im); ov.w = pk2(hi2(o[m][1]) * im);
        v2u ol; ol.x = pk2(lo2(o[m][2]) * i1); ol.y = pk2(hi2(o[m][2]) * i1);
        const int tt = 2 * grp32 + m;
        st128(ov, rs, lane * 16, WS_O + (tt * 20 + c16) * 1024);
        st64(ol, rs, g < 2 ? lane * 8 : OOB, WS_O + (tt * 20 + 16) * 1024 + c16 * 256);
      }
      STAMP(18);
      publish(FLAG(2, grp32) + c16, tag, lane);
    }
    STAMP(7);
  }

  {
    const int tt = 4 * r64 + w;
    v4u eye[2];
#pragma unroll
    for (int hf = 0; hf < 2; ++hf) {
      const int e = 16 * hf + t - 8 * g;
#pragma unroll
      for (int d = 0; d < 4; ++d) eye[hf][d] = e == 2 * d ? 0x3F80u : e == 2 * d + 1 ? 0x3F800000u : 0u;
      PIN(eye[hf]);
    }
    f32x4 acc[1][5];
#pragma unroll
    for (int s = 0; s < 5; ++s) { acc[0][s] = mfma(eye[s & 1], xr[s >> 1], f32x4{0.f, 0.f, 0.f, 0.f}); PIN(acc[0][s]); }
    wait_flag(FLAG(2, tt >> 1) + lane, 16, tag, lane);
    STAMP(8);
    if (b == 0 && tid == 0) __hip_atomic_store((gu32*)(ws + WS_EPOCH), tag, RLX_AGENT);
    v4u a[20];
#pragma unroll
    for (int ks = 0; ks < 2 * S0; ++ks) a[ks] = ld128(rs, lane * 16, WS_O + (tt * 20 + ks) * 1024);
    SCHED();
    const LBase bp(lds0 + L_WO + (lane >> 2) * 160 + (lane & 3) * 8);
    kloop<1, 5, 20, 1, true, 1>(acc, [&](int ks, int s, int h) { return bp.rd(ks * 5120 + h * 2560 + s * 32); },
      [&](int ks, int m) { return a[ks]; },
      [&](int ks) {
#pragma unroll
        for (int j = ld_hi(ks - 1, 2 * S0, 2 * AW, 20); j < ld_hi(ks, 2 * S0, 2 * AW, 20); ++j)
          a[j] = j < 16 ? ld128(rs, lane * 16, WS_O + (tt * 20 + j) * 1024) : ld_split(rs, lane, WS_O + (tt * 20 + j) * 1024);
        if (ks == 8) PROG(2);
        if (ks == 14) PROG(3);
      });
    PROG(4);
    STAMP(9);
    v2u hb[5];
    f32x2 hv[10];
#pragma unroll
    for (int s = 0; s < 5; ++s) {
      hb[s].x = pk2(lo2(acc[0][s]));
      hb[s].y = pk2(hi2(acc[0][s]));
      hv[2 * s] = bf2(hb[s].x); hv[2 * s + 1] = bf2(hb[s].y);
    }
    st128(v4u{hb[0].x, hb[0].y, hb[1].x, hb[1].y}, rs, lane * 16, WS_H + (tt * 20 + 2 * c8) * 1024);
    st128(v4u{hb[2].x, hb[2].y, hb[3].x, hb[3].y}, rs, lane * 16, WS_H + (tt * 20 + 2 * c8 + 1) * 1024);
    st64(hb[4], rs, lane * 8, WS_H + (tt * 20 + 16) * 1024 + c8 * 512);
    const f32x2 s2 = ((hv[0] + hv[1]) + (hv[2] + hv[3])) + ((hv[4] + hv[5]) + (hv[6] + hv[7])) + (hv[8] + hv[9]);
    const float mc = xsum32(xsum16(s2[0] + s2[1])) * (1.f / 80.f);
    f32x2 q2 = {0.f, 0.f}, q3 = {0.f, 0.f};
#pragma unroll
    for (int j = 0; j < 10; j += 2) { const f32x2 d0 = hv[j] - mc, d1 = hv[j + 1] - mc; q2 += d0 * d0; q3 += d1 * d1; }
    q2 += q3;
    const float m2 = xsum32(xsum16(q2[0] + q2[1]));
    st64(v2u{__builtin_bit_cast(unsigned, mc), __builtin_bit_cast(unsigned, m2)}, rs, g == 0 ? t * 8 : OOB, WS_ST + (tt * 8 + c8) * 128);
    STAMP(20);
    publish(FLAG(3, tt >> 1) + (tt & 1) * 8 + c8, tag, lane);
    STAMP(10);
  }

  {
    wait_flag(FLAG(3, grp32) + lane, 16, tag, lane);
    STAMP(11);
    v4u stt[2];
#pragma unroll
    for (int m = 0; m < 2; ++m) {
      const v2u s0 = ld64(rs, g * 256 + t * 8, WS_ST + (2 * grp32 + m) * 1024), s1 = ld64(rs, g * 256 + 128 + t * 8, WS_ST + (2 * grp32 + m) * 1024);
      stt[m] = v4u{s0.x, s0.y, s1.x, s1.y};
    }
    v4u a[20][2];
    const int abase = WS_H + 2 * grp32 * 20 * 1024;
#pragma unroll
    for (int ks = 0; ks < S0; ++ks)
#pragma unroll
      for (int m = 0; m < 2; ++m) a[ks][m] = ld128(rs, lane * 16, abase + (m * 20 + ks) * 1024);
    SCHED();
    float nm[2], rr[2];
#pragma unroll
    for (int m = 0; m < 2; ++m) {
      const f32x4 sv = __builtin_bit_cast(f32x4, stt[m]);
      const float mean = xsum32(xsum16(sv[0] + sv[2])) * 0.125f;
      const float m2 = xsum32(xsum16(sv[1] + sv[3] + 80.f * ((sv[0] - mean) * (sv[0] - mean) + (sv[2] - mean) * (sv[2] - mean))));
      const float var = m2 * (1.f / D);
      const float r1 = __builtin_amdgcn_rsqf(var + LN_EPS);
      rr[m] = r1 * __builtin_amdgcn_rsqf(var * r1 * r1 + LN_EPS);
      nm[m] = -mean * rr[m];
      PIN(rr[m]); PIN(nm[m]);
    }
    BAR_RAW();
    STAMP(22);
    const unsigned l64 = lds0 + (lane >> 2) * 64 + (lane & 3) * 8;
    const LBase bp0(l64 + (g & 1) * 32), bp1(l64 + 32 - (g & 1) * 32);
    const int h1base = WS_H1 + 2 * grp32 * 48 * 1024;
    auto finish = [&](const f32x4& c0, const f32x4& c1, int j, int m) {
      v4u o;
#pragma unroll
      for (int s = 0; s < 2; ++s) {
        const f32x4 cs = *(LAS f32x4*)(L + L_CS + ((2 * j + s) * 4 + g) * 16), bb = *(LAS f32x4*)(L + L_B1 + ((2 * j + s) * 4 + g) * 16);
        const f32x4 c = s ? c1 : c0;
        o[2 * s] = pk2(gelu2(lo2(c) * rr[m] + (lo2(cs) * nm[m] + lo2(bb))));
        o[2 * s + 1] = pk2(gelu2(hi2(c) * rr[m] + (hi2(cs) * nm[m] + hi2(bb))));
      }
      st128(o, rs, lane * 16, h1base + (m * 48 + (c16 >> 3) * 24 + (j < 2 ? 2 * (c16 & 7) + j : 16 + (c16 & 7))) * 1024);
    };
    {
      f32x4 acc[2][4];
      kloop<2, 4, 20, 1, true>(acc, [&](int ks, int s, int h) { return ((s & 1) ? bp1 : bp0).rd(L_W1 + (s >> 1) * 40960 + ks * 2048 + h * 1024); },
        [&](int ks, int m) { return a[ks][m]; },
        [&](int ks) {
#pragma unroll
          for (int j = ld_hi(ks - 1, S0, AW, 20); j < ld_hi(ks, S0, AW, 20); ++j)
#pragma unroll
            for (int m = 0; m < 2; ++m)
              a[j][m] = j < 16 ? ld128(rs, lane * 16, abase + (m * 20 + j) * 1024) : ld_split(rs, lane, abase + (m * 20 + j) * 1024);
          if (ks == 20 - AW) PROG(5);
        });
      PROG(6);
      STAMP(12);
#pragma unroll
      for (int j = 0; j < 2; ++j)
#pragma unroll
        for (int m = 0; m < 2; ++m) finish(acc[m][2 * j], acc[m][2 * j + 1], j, m);
      SCHED();
      STAMP(24);
    }
    f32x4 accb[2][2];
    const int kh = w & 1, tt = 4 * r64 + w;
    v4u hr01, hr23;
    v2u hr4;
    kloop<2, 2, 20, 3, true>(accb, [&](int ks, int s, int h) { return (s ? bp1 : bp0).rd(L_W1 + 81920 + ks * 2048 + h * 1024); },
      [&](int ks, int m) { return a[ks][m]; },
      [&](int ks) {
        if (ks == PUBA) { publish(FLAG(4, grp32) + c16, tag, lane); PROG(7); }
        if (ks == PUBA + 2) {
          hr01 = ld128(rs, lane * 16, WS_H + (tt * 20 + 2 * c8) * 1024);
          hr23 = ld128(rs, lane * 16, WS_H + (tt * 20 + 2 * c8 + 1) * 1024);
          hr4 = ld64(rs, lane * 8, WS_H + (tt * 20 + 16) * 1024 + c8 * 512);
        }
      });
    STAMP(13);
    f32x4 acc[2][5];
    {
      const unsigned hrr[10] = {hr01.x, hr01.y, hr01.z, hr01.w, hr23.x, hr23.y, hr23.z, hr23.w, hr4.x, hr4.y};
#pragma unroll
      for (int s = 0; s < 5; ++s) {
        acc[0][s] = *(LAS f32x4*)(L + L_B2 + (s * 4 + g) * 16) + f32x4{bf_lo(hrr[2 * s]), bf_hi(hrr[2 * s]), bf_lo(hrr[2 * s + 1]), bf_hi(hrr[2 * s + 1])};
        PIN(acc[0][s]);
      }
    }
    int oo[3];
    unsigned tw, tr;
    {
      int ln = lane;
      PIN(ln);
      const int qq = ln / 10, cc = ln % 10;
#pragma unroll
      for (int j = 0; j < 3; ++j) { oo[j] = sel(ln < (j < 2 ? 60 : 40), ((tt * 16 + 6 * j + qq) * D + 80 * c8) * 2 + cc * 16); PIN(oo[j]); }
      tw = lds0 + L_W2 + 122880 + w * 3072 + t * 176 + g * 8; tr = lds0 + L_W2 + 122880 + w * 3072 + qq * 176 + cc * 16;
      PIN(tw); PIN(tr);
    }
    BAR_RAW();
    STAMP(14);
    constexpr int WIN = 8;
    v4u ar[WIN][2];
    const int a4[2] = {WS_H1 + (tt * 48 + kh * 24) * 1024, WS_H1 + ((tt ^ 1) * 48 + kh * 24) * 1024};
#pragma unroll
    for (int ks = 0; ks < WIN - 1; ++ks)
#pragma unroll
      for (int m = 0; m < 2; ++m) ar[ks][m] = ld128(rs, lane * 16, a4[m] + ks * 1024);
    SCHED();
#pragma unroll
    for (int m = 0; m < 2; ++m) finish(accb[m][0], accb[m][1], 2, m);
    STAMP(29);
    const LBase bp(lds0 + L_W2 + kh * 5120 + (lane >> 2) * 160 + (lane & 3) * 8);
    kloop<2, 5, 24, 1, true, 1>(acc, [&](int ks, int s, int h) { return bp.rd((ks % 15) * 10240 + h * 2560 + s * 32); },
      [&](int ks, int m) { return ar[ks % WIN][m]; },
      [&](int ks) {
        if (ks == 5) PROG(8);
        if (ks == 9 || ks == 14 || ks == 19) BAR_RAW();
        if (ks == 3) publish(FLAG(10, grp32) + c16, tag, lane);
        if (ks >= 3) {
#pragma unroll
          for (int j = ks == 3 ? WIN - 1 : ld_hi(ks - 1, 1, WIN - 1, 24); j < ld_hi(ks, 1, WIN - 1, 24); ++j)
#pragma unroll
            for (int m = 0; m < 2; ++m) ar[j % WIN][m] = ld128(rs, lane * 16, a4[m] + j * 1024);
        }
      });
    STAMP(15);
    LAS f32x4* xch = (LAS f32x4*)(L + L_W2 + 102400);
#pragma unroll
    for (int s = 0; s < 5; ++s) xch[((w ^ 1) * 5 + s) * 64 + lane] = acc[1][s];
    BAR();
    const __amdgpu_buffer_rsrc_t rout = __builtin_amdgcn_make_buffer_rsrc((void*)out, 0, S * D * 2, 0x00020000);
    f32x4 ot[5];
#pragma unroll
    for (int s = 0; s < 5; ++s) ot[s] = xch[(w * 5 + s) * 64 + lane];
    SCHED();
#pragma unroll
    for (int s = 0; s < 5; ++s) {
      const f32x4 v = acc[0][s] + ot[s];
      *(LAS v2u*)((LAS char*)tw + s * 32) = v2u{pk2(lo2(v)), pk2(hi2(v))};
    }
    asm volatile("" ::: "memory");
    v4u ov[3];
#pragma unroll
    for (int j = 0; j < 3; ++j) ov[j] = *(LAS v4u*)((LAS char*)tr + j * 6 * 176);
#pragma unroll
    for (int j = 0; j < 3; ++j) __builtin_amdgcn_raw_buffer_store_b128(ov[j], rout, oo[j], 0, 0);
    STAMP(16);
  }
  return;
  }

  {
    const v4u srd_ws = mk_srd(ws, WS_BYTES);
    STAMP(16);
    const int q5 = lane / 5, c5 = lane % 5;
    const int f40m = c5 < 4 ? 512 : 128, f40l = c5 < 4 ? q5 * 64 + (c5 ^ (2 * ((q5 >> 2) & 1))) * 16 : 2048 + q5 * 16;
    v2u ub1, ub2;
    unsigned pf0 = 0, pf1 = 0;
    if (hw == 0) {
      const int dof = sel(lane < 40, lane * 16);
#pragma unroll
      for (int j = 0; j < 10; ++j) {
        const int k = 40 * r128 + 4 * j, R = (k & ~31) + 16 * ((k >> 2) & 1) + 4 * ((k >> 3) & 3);
        st128(tv[j], rs, dof, WS_TKV + c16 * 153600 + R * 160);
      }
      STAMP(18);
      PIN(tag); tag += 1u;
      publish(FLAG(6, c16) + r128, tag, lane);
      STAMP(13);
    } else if (hw == 1) {
      const __amdgpu_buffer_rsrc_t ro = __builtin_amdgcn_make_buffer_rsrc((void*)wo, 0, D * D * 2, 0x00020000);
      const int vq = sel(lane < 40, (8 * (q5 >> 2) + (q5 & 3)) * 3840 + c5 * 16), vo = sel(lane < 40, q10 * 1280 + c10 * 16);
#pragma unroll
      for (int j = 0; j < 5; ++j) {
        const int i8 = 5 * r128 + j, k = 32 * (i8 >> 2) + 16 * (i8 & 1) + 4 * ((i8 >> 1) & 1);
        tv[j] = __builtin_amdgcn_raw_buffer_load_b128(rq, vq, k * 3840 + c16 * 80, 0);
      }
#pragma unroll
      for (int j = 0; j < 5; ++j) tv[5 + j] = __builtin_amdgcn_raw_buffer_load_b128(ro, vo, (20 * r64 + 4 * j) * 1280 + c8 * 160, 0);
    } else if (hw == 2) {
      const GAS unsigned char* cb = (const GAS unsigned char*)(pc0 & ~127ull);
      if (lane < 11) pf0 = *(const GAS unsigned*)(cb + (lane * 32 + bi) * 128);
      {
        const unsigned char* tp = lane < 32 ? ws + (min(lane, WS_BYTES >> 20) << 20) : lane < 35 ? (const unsigned char*)out + ((lane - 32) << 20)
                                  : lane < 37 ? (const unsigned char*)w1 + ((lane - 35) << 20) : (const unsigned char*)w2 + ((lane & 1) << 20);
        pf1 = __hip_atomic_load((const gu32*)tp + (bi & 7) * 32, RLX_AGENT);
      }
      if (lane < 6) {
        gu32* fp = FLAG(3 * (b >> 6) + (lane >> 1), b & 63) + (lane & 1) * 32;
        asm volatile("global_atomic_add %0, %1, off sc1" :: "v"(fp), "v"(0u) : "memory");
      }
    } else if (hw == 3) {
      const __amdgpu_buffer_rsrc_t rb1 = __builtin_amdgcn_make_buffer_rsrc((void*)b1, 0, F * 2, 0x00020000);
      const __amdgpu_buffer_rsrc_t rb2 = __builtin_amdgcn_make_buffer_rsrc((void*)b2, 0, D * 2, 0x00020000);
      ub1 = __builtin_amdgcn_raw_buffer_load_b64(rb1, lane * 8, c16 * 192, 0);
      ub2 = __builtin_amdgcn_raw_buffer_load_b64(rb2, lane * 8, c8 * 160, 0);
    }
    asm volatile("" ::: "memory");
    int red_off, kvf_off, kvp_off;
    {
      const int it = ht < 210 ? ht : 209, ie = it / 5, ic = it % 5;
      const int ihead = ie < 40 ? (ie >= 20) : ie - 40;
      const int id = 20 * ihead + 4 * ic, dg = id >> 2;
      const int sa = id >> 4, col = 40 + ie;
      const int tile = 3 * sa + (ie < 40 ? (col >> 4) - 2 : 0) - (sa > 0);
      red_off = lds0 + L_RED + (tile * 64 + (dg & 3) * 16 + (ie < 40 ? (col & 15) : 0)) * 16;
      kvf_off = lds0 + L_KVF + ((ie < 40 ? (ie >> 4) : 3) * 2 + (dg >> 3)) * 1024 + ((dg & 3) * 16 + (ie < 40 ? (ie & 15) : ihead)) * 16 + ((dg >> 2) & 1) * 8;
      kvp_off = WS_KVP + (c16 * 16 + r128) * 4096 + ht * 16;
      PIN(red_off); PIN(kvf_off); PIN(kvp_off);
    }
    int vo12, td[12];
    if (hw >= 2) {
      const int q12 = lane / 12, c12 = lane % 12;
      vo12 = hw == 2 ? sel(lane < 48, q12 * 3072 + c12 * 16) : sel(lane < 40, q10 * 1280 + c10 * 16);
#pragma unroll
      for (int j = 0; j < 12; ++j) {
        const int cc = r128 >> 1, off = 40 * (r128 & 1) + 4 * j;
        const int R = off < 64 ? 64 * cc + off : 32 * (16 + (cc >> 1)) + 16 * (cc & 1) + off - 64;
        const int d1 = WS_T1 + c16 * 122880 + (c12 >> 2) * 40960 + (R + q12) * 64 + ((c12 & 3) ^ (2 * ((R >> 2) & 1))) * 16;
        const int pc = r64 >> 1, ko = 48 * (r64 & 1) + 4 * j, jj = ko >> 5;
        const int d2 = WS_T2 + c8 * 245760 + (2 * (jj < 2 ? 2 * (pc & 7) + jj : 16 + (pc & 7)) + (pc >> 3)) * 5120 + (ko & 31) * 160 + lane * 16;
        td[j] = sel(hw == 2 ? (lane < 48 && j < 10) : lane < 40, hw == 2 ? d1 : d2);
        PIN(td[j]);
      }
      PIN(vo12);
    }
    int l16 = lane * 16, h16 = ht * 16;
    PIN(l16); PIN(h16);
    gu32* fp1 = FLAG(1, c16) + lane;
    gu32* fp78 = lane < 32 ? FLAG(7, c8) + lane : FLAG(8, c16) + t;
    gu32* fp4 = FLAG(4, 2 * r64 + (g & 1)) + t;
    gu32* fp1w = FLAG(1, c16) + r128 * 4 + hw;
    PIN(fp1); PIN(fp78); PIN(fp4); PIN(fp1w);
    ((LAS v4u*)(L + L_KVF))[ht] = v4u{0u, 0u, 0u, 0u};
    ((LAS v4u*)(L + L_KVF))[256 + ht] = v4u{0u, 0u, 0u, 0u};
    asm volatile("" ::: "memory");
    if (hw != 0) {
      if (hw == 1) {
#pragma unroll
        for (int j = 0; j < 5; ++j) {
          const int i8 = 5 * r128 + j;
          st128(tv[j], rs, sel(lane < 40, (i8 & 3) * f40m + f40l), WS_TKV + c16 * 153600 + 102400 + (i8 >> 2) * 2560);
        }
#pragma unroll
        for (int j = 0; j < 5; ++j) {
          const int pp = r64 >> 1, e = 20 * (r64 & 1) + 4 * j;
          const int R = e < 32 ? 32 * pp + e : 32 * (16 + (pp >> 2)) + 8 * (pp & 3) + e - 32;
          st128(tv[5 + j], rs, sel(lane < 40, lane * 16), WS_TO + c8 * 102400 + R * 160);
        }
      }
      if (hw == 3) {
        if (lane < 24) *(LAS f32x4*)(L + L_B1 + lane * 16) = f32x4{bf_lo(ub1.x), bf_hi(ub1.x), bf_lo(ub1.y), bf_hi(ub1.y)};
        if (lane < 20) *(LAS f32x4*)(L + L_B2 + lane * 16) = f32x4{bf_lo(ub2.x), bf_hi(ub2.x), bf_lo(ub2.y), bf_hi(ub2.y)};
      }
      asm volatile("" :: "v"(pf0), "v"(pf1));
      PIN(tag); tag += 1u;
      DRAIN();
      if (hw == 1 && lane < 2) flag_set(lane == 0 ? FLAG(8, c16) + r128 : FLAG(7, c8) + r64, tag);
    }
    if (hw == 0) {
      wait_flag(FLAG(6, c16) + lane, 16, tag, lane);
      sig[0] = 1u;
    }
    sig_wait(sig);
    STAMP(1);
    const int kvq = WS_TKV + c16 * 153600, wos = WS_TO + c8 * 102400;
    dma_image<0, 24>(lds0 + L_BKV, l16, srd_ws, kvq, hw, 3 * (bi >> 3));
    dma_image<24, 56>(lds0 + L_BKV, l16, srd_ws, kvq, hw, 4 * (bi >> 3));
    asm volatile("s_waitcnt vmcnt(8)" ::: "memory");
    STAMP(2);
    BAR_RAW();
    dma_image<56, 100>(lds0 + L_BKV, l16, srd_ws, kvq, hw, 5 * (bi >> 3));
    asm volatile("s_waitcnt vmcnt(10)" ::: "memory");
    BAR_RAW();
    if (hw == 1) wait_flag(fp78, 48, tag, lane);
    DRAIN();
    STAMP(3);
    BAR_RAW();
    if (QPOS == 0) dma_image<100, 150>(lds0 + L_BKV, l16, srd_ws, kvq, hw, 6 * (bi >> 3));
    prog_wait(prog, 1);
    if (QPOS == 1) dma_image<100, 150>(lds0 + L_BKV, l16, srd_ws, kvq, hw, 6 * (bi >> 3));
    if (hw >= 2) {
      const __amdgpu_buffer_rsrc_t r12 = __builtin_amdgcn_make_buffer_rsrc((void*)(hw == 2 ? w1 : w2), 0, D * F * 2, 0x00020000);
      const int base = hw == 2 ? r128 * 122880 + c16 * 192 : r64 * 61440 + c8 * 160, step = hw == 2 ? 4 * 3072 : 4 * 1280;
#pragma unroll
      for (int j = 0; j < 12; ++j) tv[j] = __builtin_amdgcn_raw_buffer_load_b128(r12, vo12, (j < 10 || hw == 3) ? base + j * step : OOB, 0);
      asm volatile("s_waitcnt vmcnt(12)" ::: "memory");
    } else {
      dma_image<32, 68, 0, 2>(lds0 + L_WO, l16, srd_ws, wos, hw, bi >> 2);
      asm volatile("s_waitcnt vmcnt(18)" ::: "memory");
    }
    STAMP(4);
    BAR();
    __builtin_amdgcn_s_setprio(2);
    STAMP(11);
    {
      f32x4 pv[4];
#pragma unroll
      for (int ww = 0; ww < 4; ++ww) pv[ww] = *(LAS f32x4*)((LAS char*)red_off + ww * 8192);
      st128(__builtin_bit_cast(v4u, add4(add4(pv[0], pv[1]), add4(pv[2], pv[3]))), rs, kvp_off, 0);
      PROG(1);
      STAMP(12);
      DRAIN();
    }
    if (lane == 0) flag_set(fp1w, tag);
    if (hw == 0) {
      STAMP(10);
      __builtin_amdgcn_s_sleep(H1W);
      wait_flag(fp1, 64, tag, lane);
      sig[2] = 1u;
    } else {
      if (H1G) __builtin_amdgcn_s_sleep(H1G);
      dma_image<68, 100, 0, 3>(lds0 + L_WO, l16, srd_ws, wos, hw - 1, bi >> 2);
      prog_wait(prog + 1, 1);
      dma_image<0, 32, 0, 3>(lds0 + L_WO, l16, srd_ws, wos, hw - 1, 2 * (bi >> 2));
      if (hw >= 2) {
#pragma unroll
        for (int j = 0; j < 12; ++j) st128(tv[j], rs, td[j], 0);
      }
      STAMP(10);
      sig_wait(sig + 2);
    }
    STAMP(5);
    {
      v4u pv[16];
#pragma unroll
      for (int r = 0; r < 16; ++r) pv[r] = ld128(rs, h16, WS_KVP + (c16 * 16 + r) * 4096);
      SCHED();
      f32x4 v = __builtin_bit_cast(f32x4, pv[0]);
#pragma unroll
      for (int r = 1; r < 16; ++r) v = add4(v, __builtin_bit_cast(f32x4, pv[r]));
      v2u o; o.x = pk2(lo2(v)); o.y = pk2(hi2(v));
      *(LAS v2u*)((LAS char*)kvf_off) = o;
      if (ht >= 200) {
#pragma unroll
        for (int c = 1; c < 4; ++c) *(LAS v2u*)((LAS char*)kvf_off + c * 64) = o;
      }
    }
    DRAIN();
    STAMP(6);
    BAR();
    __builtin_amdgcn_s_setprio(0);
    if (hw >= 2 && lane == 0) flag_set(hw == 2 ? FLAG(5, c16) + r128 : FLAG(11, c8) + r64, tag);
    if (hw == 2) {
      f32x4 s0 = {0.f, 0.f, 0.f, 0.f}, s1 = {0.f, 0.f, 0.f, 0.f};
#pragma unroll
      for (int j = 0; j < 10; ++j) {
        s0 = add4(s0, f32x4{bf_lo(tv[j].x), bf_hi(tv[j].x), bf_lo(tv[j].y), bf_hi(tv[j].y)});
        s1 = add4(s1, f32x4{bf_lo(tv[j].z), bf_hi(tv[j].z), bf_lo(tv[j].w), bf_hi(tv[j].w)});
      }
#pragma unroll
      for (int d = 24; d >= 12; d -= 12) {
        f32x4 u0, u1;
#pragma unroll
        for (int e = 0; e < 4; ++e) { u0[e] = __shfl(s0[e], lane + d); u1[e] = __shfl(s1[e], lane + d); }
        s0 = add4(s0, u0); s1 = add4(s1, u1);
      }
      f32x4 sv;
#pragma unroll
      for (int e = 0; e < 4; ++e) { const float a0 = __shfl(s0[e], lane >> 1), a1 = __shfl(s1[e], lane >> 1); sv[e] = (lane & 1) ? a1 : a0; }
      st128(__builtin_bit_cast(v4u, sv), rs, sel(lane < 24, lane * 16), WS_CS + (c16 * 16 + r128) * 384);
      publish(FLAG(9, c16) + r128, tag, lane);
    }
    if (hw == 1) {
      __builtin_amdgcn_s_sleep(40);
      wait_flag(lane < 32 ? FLAG(11, c8) + lane : FLAG(5, c16) + t, 48, tag, lane);
      sig[3] = 1u;
    }
    sig_wait(sig + 3);
    prog_wait(prog, 2);
    if (hw == 0) {
      wait_flag(FLAG(9, c16) + lane, 16, tag, lane);
      v4u pv[16];
      const int co = sel(lane < 24, l16);
#pragma unroll
      for (int r = 0; r < 16; ++r) pv[r] = ld128(rs, co, WS_CS + (c16 * 16 + r) * 384);
      SCHED();
      f32x4 v = __builtin_bit_cast(f32x4, pv[0]);
#pragma unroll
      for (int r = 1; r < 16; ++r) v = add4(v, __builtin_bit_cast(f32x4, pv[r]));
      if (lane < 24) *(LAS f32x4*)(L + L_CS + lane * 16) = v;
    } else {
      dma_image<70, 120, 0, 3>(lds0 + L_W1, l16, srd_ws, WS_T1 + c16 * 122880, hw - 1, 6 * (bi >> 3));
      prog_wait(prog, 3);
      dma_image<0, 40, 0, 3>(lds0 + L_W1, l16, srd_ws, WS_T1 + c16 * 122880, hw - 1, 5 * (bi >> 3));
      STAMP(7);
      prog_wait(prog, 4);
      dma_image<40, 70, 0, 3>(lds0 + L_W1, l16, srd_ws, WS_T1 + c16 * 122880, hw - 1, 3 * (bi >> 3));
    }
    DRAIN();
    STAMP(8);
    BAR();
    const int w2s = WS_T2 + c8 * 245760;
    prog_wait(prog, 5);
    dma_image<0, 30>(lds0 + L_W2, l16, srd_ws, w2s, hw, bi >> 2);
    STAMP(14);
    prog_wait(prog, 6);
    dma_image<30, W2S>(lds0 + L_W2, l16, srd_ws, w2s, hw, bi >> 2);
    prog_wait(prog, 7);
    if (hw != 0) dma_image<W2S, 110, 0, 3>(lds0 + L_W2, l16, srd_ws, w2s, hw - 1, bi >> 2);
    DRAIN();
    STAMP(9);
    if (hw == 0) wait_flag(fp4, 32, tag, lane);
    STAMP(15);
    BAR_RAW();
    dma_image<110, 150>(lds0 + L_W2, l16, srd_ws, w2s, hw, 2 * (bi >> 2));
    prog_wait(prog, 8);
    if (hw == 0) wait_flag(fp4 + 6 * FLAG_SET / 4, 32, tag, lane);
    else dma_image<150, 198, 0, 3>(lds0 + L_W2 - 153600, l16, srd_ws, w2s, hw - 1, 3 * (bi >> 2));
    asm volatile("s_waitcnt vmcnt(16)" ::: "memory");
    STAMP(17);
    BAR_RAW();
    dma_image<198, 200>(lds0 + L_W2 - 153600, l16, srd_ws, w2s, hw, 0);
    dma_image<200, 240>(lds0 + L_W2 - 153600, l16, srd_ws, w2s, hw, 2 * (bi >> 2));
    asm volatile("s_waitcnt vmcnt(10)" ::: "memory");
    BAR_RAW();
    DRAIN();
    BAR_RAW();
  }

}
}

extern "C" void kernel_launch(void* const* d_in, const int* in_sizes, int n_in, void* d_out, int out_size, void* d_ws,
                              size_t ws_size, hipStream_t stream) {
  const unsigned short* x = (const unsigned short*)d_in[0];
  const unsigned short* wqkv = (const unsigned short*)d_in[1];
  const unsigned short* wo = (const unsigned short*)d_in[2];
  const unsigned short* w1 = (const unsigned short*)d_in[4];
  const unsigned short* b1 = (const unsigned short*)d_in[5];
  const unsigned short* w2 = (const unsigned short*)d_in[6];
  const unsigned short* b2 = (const unsigned short*)d_in[7];
  fused_block<<<256, 512, 0, stream>>>(x, wqkv, wo, w1, b1, w2, b2, (unsigned short*)d_out, (unsigned char*)d_ws);
}
```

```cpp
#include <hip/hip_runtime.h>
#include <hip/hip_bf16.h>

#define STAMPS 0
#define THR 0
#define TOUCH 0
#define CODEPF 1
#define FLAGPF 1
#define OUTAUX 0
#define OUTPF 0
#define S0 6
#define XRPOS 0
#define QPOS 0
#define H1W 5
#define H1G 2
#define W2S 70
#define F6W 0
#define PUBA 8
#define NCDELAY 0
#define HPRIO 0
#define WOLATE 0
#define SLEEP 1

#define GAS __attribute__((address_space(1)))
#define LAS __attribute__((address_space(3)))
#define RLX_AGENT __ATOMIC_RELAXED, __HIP_MEMORY_SCOPE_AGENT
typedef unsigned v4u __attribute__((ext_vector_type(4)));
typedef unsigned v2u __attribute__((ext_vector_type(2)));
typedef float f32x4 __attribute__((ext_vector_type(4)));
typedef float f32x2 __attribute__((ext_vector_type(2)));
typedef __bf16 bf16x8 __attribute__((ext_vector_type(8)));
typedef __bf16 bf16x2 __attribute__((ext_vector_type(2)));
typedef short s16x4 __attribute__((ext_vector_type(4)));
typedef GAS unsigned gu32;

namespace {
constexpr int S = 2048, D = 640, F = 1536;
constexpr float LN_EPS = 1e-5f;
constexpr int AUX_SC1 = 16;
constexpr unsigned kSpinCap = 1u << 17;
constexpr int OOB = 0x7FFFFF00;

constexpr int WS_EPOCH = 0;
constexpr int WS_FLAG = 4096;
constexpr int FLAG_SET = 64 * 256;
constexpr int WS_XN = 1 << 20;
constexpr int WS_O = WS_XN + 128 * 20 * 1024;
constexpr int WS_H = WS_O + 128 * 20 * 1024;
constexpr int WS_H1 = WS_H + 128 * 20 * 1024;
constexpr int WS_KVP = WS_H1 + 128 * 48 * 1024;
constexpr int WS_ST = WS_KVP + 16 * 16 * 4096;
constexpr int WS_DBG = WS_ST + 2048 * 64;
constexpr int WS_TKV = WS_DBG + 256 * 8 * 32 * 8;
constexpr int WS_TO = WS_TKV + 16 * 153600;
constexpr int WS_T1 = WS_TO + 8 * 102400;
constexpr int WS_T2 = WS_T1 + 16 * 122880;
constexpr int WS_CS = WS_T2 + 16 * 122880;
constexpr int WS_BYTES = WS_CS + 16 * 16 * 384;

constexpr int L_BKV = 0, L_BQ = 102400;
constexpr int L_RED = 0, L_KVF = 153600;
constexpr int L_WO = 0, L_W1 = 30720, L_W2 = 0, L_CS = 161792;
constexpr int L_B1 = L_CS + 1152, L_B2 = L_CS + 1536;
constexpr int LDS_BYTES = 163840;

__device__ __forceinline__ f32x4 mfma(v4u a, v4u b, f32x4 c) {
  return __builtin_amdgcn_mfma_f32_16x16x32_bf16(__builtin_bit_cast(bf16x8, a), __builtin_bit_cast(bf16x8, b), c, 0, 0, 0);
}
__device__ __forceinline__ unsigned pk2(f32x2 v) { return __builtin_bit_cast(unsigned, __builtin_convertvector(v, bf16x2)); }
__device__ __forceinline__ unsigned pk2(float lo, float hi) { return pk2(f32x2{lo, hi}); }
__device__ __forceinline__ float bf_lo(unsigned u) { return __builtin_bit_cast(float, u << 16); }
__device__ __forceinline__ float bf_hi(unsigned u) { return __builtin_bit_cast(float, u & 0xFFFF0000u); }
__device__ __forceinline__ f32x2 bf2(unsigned u) { return f32x2{bf_lo(u), bf_hi(u)}; }
__device__ __forceinline__ f32x2 lo2(f32x4 v) { return f32x2{v[0], v[1]}; }
__device__ __forceinline__ f32x2 hi2(f32x4 v) { return f32x2{v[2], v[3]}; }
__device__ __forceinline__ v4u mk_srd(const void* p, unsigned bytes) {
  const unsigned long long a = (unsigned long long)p;
  v4u r;
  r.x = (unsigned)a; r.y = (unsigned)(a >> 32) & 0xFFFFu; r.z = bytes; r.w = 0x00020000u;
  return r;
}
#define DRAIN() asm volatile("s_waitcnt vmcnt(0)" ::: "memory")
#define BAR() asm volatile("s_waitcnt lgkmcnt(0)\n\ts_barrier" ::: "memory")
#define BAR_RAW() asm volatile("s_barrier" ::: "memory")
#define SCHED() __builtin_amdgcn_sched_barrier(0)
#define PIN(x) asm volatile("" : "+v"(x))

__device__ __forceinline__ void dma2(unsigned lds_addr, unsigned voff, v4u srd, unsigned soff) {
  unsigned keep;
  asm volatile("s_mov_b32 %0, m0\n\ts_mov_b32 m0, %1\n\ts_nop 0\n\t"
               "buffer_load_dwordx4 %2, %3, %4 offen sc1 lds\n\t"
               "buffer_load_dwordx4 %2, %3, %4 offen offset:1024 sc1 lds\n\t"
               "s_mov_b32 m0, %0"
               : "=&s"(keep) : "s"(lds_addr), "v"(voff), "s"(srd), "s"(soff) : "memory");
}
template <int LO, int HI, int TH = 0, int NW = 4>
__device__ __forceinline__ void dma_image(unsigned lds_base, unsigned voff, v4u srd, int ws_off, int hw, int rot) {
  constexpr int NP = (HI - LO) / 2;
#pragma unroll 1
  for (int i = hw; i < NP; i += NW) {
    int j = i + rot;
    if (j >= NP) j -= NP;
    dma2(lds_base + LO * 1024 + j * 2048, voff, srd, ws_off + LO * 1024 + j * 2048);
    if (TH) asm volatile("s_waitcnt vmcnt(%0)" :: "i"(TH - 2) : "memory");
  }
}
__device__ __forceinline__ v2u rdtr(LAS char* p) { return __builtin_bit_cast(v2u, __builtin_amdgcn_ds_read_tr16_b64_v4i16((LAS s16x4*)p)); }
struct LBase {
  unsigned a[3];
  __device__ __forceinline__ explicit LBase(unsigned v) { a[0] = v; a[1] = v + 65536; a[2] = v + 131072; PIN(a[0]); PIN(a[1]); PIN(a[2]); }
  __device__ __forceinline__ v2u rd(int off) const { return rdtr((LAS char*)(a[off >> 16]) + (off & 65535)); }
};
template <int MT, int NT, int KS, int PD, bool WA, int M0 = 0, class RB, class GA, class PRE>
__device__ __forceinline__ void kloop(f32x4 (&acc)[MT][NT], RB rb, GA ga, PRE pre) {
  v2u bl[PD + 1][NT], bh[PD + 1][NT];
#pragma unroll
  for (int m = M0; m < MT; ++m)
#pragma unroll
    for (int s = 0; s < NT; ++s) acc[m][s] = f32x4{0.f, 0.f, 0.f, 0.f};
#pragma unroll
  for (int ks = 0; ks < PD; ++ks)
#pragma unroll
    for (int s = 0; s < NT; ++s) { bl[ks][s] = rb(ks, s, 0); bh[ks][s] = rb(ks, s, 1); }
  SCHED();
#pragma unroll
  for (int ks = 0; ks < KS; ++ks) {
    pre(ks);
#pragma unroll
    for (int s = 0; s < NT; ++s)
#pragma unroll
      for (int m = 0; m < MT; ++m) {
        const v2u lo = bl[ks % (PD + 1)][s], hi = bh[ks % (PD + 1)][s];
        const v4u wv = {lo.x, lo.y, hi.x, hi.y};
        acc[m][s] = WA ? mfma(wv, ga(ks, m), acc[m][s]) : mfma(ga(ks, m), wv, acc[m][s]);
        if (ks + PD < KS) {
          if (MT == 1 || m == 0) bl[(ks + PD) % (PD + 1)][s] = rb(ks + PD, s, 0);
          if (MT == 1 || m == 1) bh[(ks + PD) % (PD + 1)][s] = rb(ks + PD, s, 1);
        }
        SCHED();
      }
  }
}
constexpr int ld_hi(int ks, int s0, int W, int KS) {
  const int a = s0 + 2 * (ks + 1), b = ks + 1 + W, c = a < b ? a : b;
  return c < KS ? c : KS;
}
__device__ __forceinline__ v4u ld128(__amdgpu_buffer_rsrc_t rs, int voff, int soff) {
  return __builtin_amdgcn_raw_buffer_load_b128(rs, voff, soff, AUX_SC1);
}
__device__ __forceinline__ v2u ld64(__amdgpu_buffer_rsrc_t rs, int voff, int soff) {
  return __builtin_amdgcn_raw_buffer_load_b64(rs, voff, soff, AUX_SC1);
}
__device__ __forceinline__ void st128(v4u v, __amdgpu_buffer_rsrc_t rs, int voff, int soff) {
  __builtin_amdgcn_raw_buffer_store_b128(v, rs, (int)((unsigned)voff + (unsigned)soff), 0, AUX_SC1);
}
__device__ __forceinline__ int sel(bool c, int a) {
  PIN(a);
  return c ? a : OOB;
}
__device__ __forceinline__ v4u ld_split(__amdgpu_buffer_rsrc_t rs, int lane, int soff) {
  const v2u a = ld64(rs, lane * 8, soff), b = ld64(rs, lane * 8 + 512, soff);
  return v4u{a.x, a.y, b.x, b.y};
}
__device__ __forceinline__ void st64(v2u v, __amdgpu_buffer_rsrc_t rs, int voff, int soff) {
  __builtin_amdgcn_raw_buffer_store_b64(v, rs, voff, soff, AUX_SC1);
}
__device__ __forceinline__ void flag_set(gu32* p, unsigned tag) {
  asm volatile("global_atomic_swap %0, %1, off sc1" :: "v"(p), "v"(tag) : "memory");
}
__device__ __forceinline__ void wait_flag(gu32* p, int n, unsigned tag, int lane) {
#pragma unroll 1
  for (unsigned spins = 0; spins < kSpinCap; ++spins) {
    unsigned v = tag;
    if (lane < n) asm volatile("global_atomic_add %0, %1, %2, off sc0 sc1\n\ts_waitcnt vmcnt(0)" : "=&v"(v) : "v"(p), "v"(0u) : "memory");
    if (__all(v == tag)) break;
    if (SLEEP) __builtin_amdgcn_s_sleep(SLEEP);
  }
  asm volatile("" ::: "memory");
}
__device__ __forceinline__ void sig_wait(volatile LAS unsigned* p) {
#pragma unroll 1
  for (unsigned spins = 0; spins < 64 * kSpinCap; ++spins) {
    if (*p != 0u) break;
    __builtin_amdgcn_s_sleep(1);
  }
  asm volatile("" ::: "memory");
}
__device__ __forceinline__ void prog_wait(volatile LAS v4u* p, unsigned stage) {
#pragma unroll 1
  for (unsigned spins = 0; spins < 64 * kSpinCap; ++spins) {
    const v4u v = *p;
    if (min(min(v.x, v.y), min(v.z, v.w)) >= stage) break;
    __builtin_amdgcn_s_sleep(1);
  }
  asm volatile("" ::: "memory");
}
__device__ __forceinline__ void publish(gu32* flag, unsigned tag, int lane) {
  DRAIN();
  if (lane == 0) flag_set(flag, tag);
}
__device__ __forceinline__ f32x4 add4(f32x4 a, f32x4 b) {
  f32x4 r;
#pragma unroll
  for (int i = 0; i < 4; ++i) { float x; asm("v_add_f32 %0, %1, %2" : "=v"(x) : "v"(a[i]), "v"(b[i])); r[i] = x; }
  return r;
}
__device__ __forceinline__ float xsum32(float x) {
  unsigned u = __builtin_bit_cast(unsigned, x), v = u;
  asm("" : "+v"(v));
  auto r = __builtin_amdgcn_permlane32_swap(u, v, false, false);
  const unsigned a = r[0], b = r[1];
  return __builtin_bit_cast(float, a) + __builtin_bit_cast(float, b);
}
__device__ __forceinline__ float xsum16(float x) {
  unsigned u = __builtin_bit_cast(unsigned, x), v = u;
  asm("" : "+v"(v));
  auto r = __builtin_amdgcn_permlane16_swap(u, v, false, false);
  const unsigned a = r[0], b = r[1];
  return __builtin_bit_cast(float, a) + __builtin_bit_cast(float, b);
}
__device__ __forceinline__ float xsum8(float x) {
  return x + __builtin_bit_cast(float, __builtin_amdgcn_update_dpp(0, __builtin_bit_cast(int, x), 0x128, 0xf, 0xf, false));
}
__device__ __forceinline__ f32x2 phi2(f32x2 t) {
  const f32x2 u = t * 1.4426950408889634f, v = t + 1e-6f;
  const f32x2 e = {__builtin_amdgcn_fmed3f(__builtin_amdgcn_exp2f(u[0]), 0.f, 1.f), __builtin_amdgcn_fmed3f(__builtin_amdgcn_exp2f(u[1]), 0.f, 1.f)};
  return f32x2{fmaxf(v[0], 1e-6f), fmaxf(v[1], 1e-6f)} + e;
}
__device__ __forceinline__ f32x2 gelu2(f32x2 x) {
  const f32x2 u = x * (x * x * -0.1029432397f + -2.302208198f);
  const f32x2 d = f32x2{__builtin_amdgcn_exp2f(u[0]), __builtin_amdgcn_exp2f(u[1])} + 1.f;
  return x * f32x2{__builtin_amdgcn_rcpf(d[0]), __builtin_amdgcn_rcpf(d[1])};
}

#define STAMP(i) do { } while (0)

__global__ void __launch_bounds__(512, 2)
fused_block(const unsigned short* __restrict__ x, const unsigned short* __restrict__ wqkv, const unsigned short* __restrict__ wo,
            const unsigned short* __restrict__ w1, const unsigned short* __restrict__ b1, const unsigned short* __restrict__ w2,
            const unsigned short* __restrict__ b2, unsigned short* __restrict__ out, unsigned char* ws) {
  __shared__ __attribute__((aligned(16))) char smem[LDS_BYTES];
  unsigned long long pc0;
  asm volatile("s_getpc_b64 %0" : "=s"(pc0));
  LAS char* L = (LAS char*)smem;
  const unsigned lds0 = (unsigned)(size_t)L;
  const int tid = threadIdx.x, lane = tid & 63, w = __builtin_amdgcn_readfirstlane(tid >> 6);
  const int b = blockIdx.x, xcd = b & 7, bi = b >> 3;
  const int g = lane >> 4, t = lane & 15;
  const __amdgpu_buffer_rsrc_t rs = __builtin_amdgcn_make_buffer_rsrc((void*)ws, 0, WS_BYTES, 0x00020000);
  gu32* flags = (gu32*)(ws + WS_FLAG);
#define FLAG(set, grp) (flags + ((set) * FLAG_SET + (grp) * 256) / 4)
  asm volatile("" :: "s"(x), "s"(wqkv), "s"(wo), "s"(w1), "s"(b1), "s"(w2), "s"(b2), "s"(out), "s"(ws));
  STAMP(0);
  const int r128 = 4 * (xcd >> 1) + (bi >> 3), c16 = 8 * (xcd & 1) + (bi & 7);
  const int r64 = 8 * (xcd >> 1) + (bi >> 2), c8 = 4 * (xcd & 1) + (bi & 3);

  const __amdgpu_buffer_rsrc_t rx = __builtin_amdgcn_make_buffer_rsrc((void*)x, 0, S * D * 2, 0x00020000);
  const int hw = w - 4, ht = tid - 256;
  volatile LAS unsigned* sig = (volatile LAS unsigned*)(L + L_CS + 1024);
  volatile LAS v4u* prog = (volatile LAS v4u*)(L + L_CS + 1088);
#define PROG(n) do { asm volatile("" ::: "memory"); sig[16 + w] = (n); } while (0)

  unsigned tag = __hip_atomic_load((gu32*)(ws + WS_EPOCH), RLX_AGENT);
  const __amdgpu_buffer_rsrc_t rq = __builtin_amdgcn_make_buffer_rsrc((void*)wqkv, 0, D * 3 * D * 2, 0x00020000);
  v4u tv[12];
  const int q10 = lane / 10, c10 = lane % 10;
  if (w < 4) {
#pragma unroll
    for (int j = 0; j < 3; ++j)
      tv[j] = __builtin_amdgcn_raw_buffer_load_b128(rx, (lane & 7) * 1280 + (lane >> 3) * 16, (j < 2 || w < 2) ? 8 * b * 1280 + (w + 4 * j) * 128 : OOB, 0);
  } else if (w == 4) {
    const int vo = sel(lane < 40, q10 * 3840 + (c10 < 5 ? 1280 : 2480) + c10 * 16);
#pragma unroll
    for (int j = 0; j < 10; ++j) tv[j] = __builtin_amdgcn_raw_buffer_load_b128(rq, vo, (40 * r128 + 4 * j) * 3840 + c16 * 80, 0);
  } else if (w == 7) {
    *(LAS v4u*)(L + L_CS + 512 + lane * 16) = v4u{0u, 0u, 0u, 0u};
  }
  BAR();

  if (w < 4) {
  const int grp32 = 4 * r128 + w;
  constexpr int AW = 6;

  {
    const int tok = 8 * b + (lane & 7), cl = lane >> 3;
    const bool has3 = w < 2;
    int xd = ((tok >> 4) * 20 + 2 * w + (cl >> 2)) * 1024 + ((cl & 3) * 16 + (tok & 15)) * 16;
    PIN(xd);
    SCHED();
    f32x2 f[12];
#pragma unroll
    for (int j = 0; j < 3; ++j)
#pragma unroll
      for (int e = 0; e < 4; ++e) f[j * 4 + e] = bf2(tv[j][e]);
    f32x2 s2 = (f[0] + f[1]) + (f[2] + f[3]) + ((f[4] + f[5]) + (f[6] + f[7])) + ((f[8] + f[9]) + (f[10] + f[11]));
    const float mloc = xsum32(xsum16(xsum8(s2[0] + s2[1]))) * (has3 ? 1.f / 192.f : 1.f / 128.f);
    const float m3 = has3 ? mloc : 0.f;
    f32x2 q2 = {0.f, 0.f}, q3 = {0.f, 0.f};
#pragma unroll
    for (int j = 0; j < 12; j += 2) {
      const f32x2 d0 = f[j] - (j < 8 ? mloc : m3), d1 = f[j + 1] - (j < 8 ? mloc : m3);
      q2 += d0 * d0; q3 += d1 * d1;
    }
    q2 += q3;
    const float m2loc = xsum32(xsum16(xsum8(q2[0] + q2[1])));
    if (lane < 8) *(volatile LAS f32x4*)(L + L_CS + 512 + (lane * 4 + w) * 16) = f32x4{mloc, m2loc, 1.f, 1.f};
    STAMP(25);
    f32x4 pr[4];
#pragma unroll 1
    for (unsigned spins = 0; spins < 64 * kSpinCap; ++spins) {
      bool ok = true;
#pragma unroll
      for (int ww = 0; ww < 4; ++ww) {
        pr[ww] = *(volatile LAS f32x4*)(L + L_CS + 512 + ((lane & 7) * 4 + ww) * 16);
        ok = ok && pr[ww][2] == 1.f;
      }
      if (__all(ok)) break;
    }
    const f32x4 p01 = {pr[0][0], pr[0][1], pr[1][0], pr[1][1]}, p23 = {pr[2][0], pr[2][1], pr[3][0], pr[3][1]};
    const float mean = (p01[0] + p01[2]) * 0.3f + (p23[0] + p23[2]) * 0.2f;
    const float e0 = p01[0] - mean, e1 = p01[2] - mean, e2 = p23[0] - mean, e3 = p23[2] - mean;
    const float m2 = (p01[1] + p01[3]) + (p23[1] + p23[3]) + 192.f * (e0 * e0 + e1 * e1) + 128.f * (e2 * e2 + e3 * e3);
    const float rstd = __builtin_amdgcn_rsqf(m2 * (1.f / D) + LN_EPS);
#pragma unroll
    for (int j = 0; j < 3; ++j) {
      v4u o;
#pragma unroll
      for (int e = 0; e < 4; ++e) o[e] = pk2((f[j * 4 + e] - mean) * rstd);
      st128(o, rs, (j < 2 || has3) ? xd : OOB, WS_XN + j * 8192);
    }
    STAMP(26);
    PIN(tag); tag += 1u;
    publish(FLAG(0, b >> 2) + (b & 3) * 4 + w, tag, lane);
  }

  STAMP(1);

  v4u xr[3];
  {
    wait_flag(FLAG(0, grp32) + lane, 16, tag, lane);
    STAMP(2);
    v4u a[20][2];
    const int abase = WS_XN + 2 * grp32 * 20 * 1024;
#pragma unroll
    for (int ks = 0; ks < S0; ++ks)
#pragma unroll
      for (int m = 0; m < 2; ++m) a[ks][m] = ld128(rs, lane * 16, abase + (m * 20 + ks) * 1024);
    SCHED();
    BAR_RAW();
    STAMP(27);
    {
      f32x4 acc[2][5];
      const LBase bp(lds0 + L_BKV + (lane >> 2) * 160 + (lane & 3) * 8);
      kloop<2, 5, 20, 1, false>(acc, [&](int ks, int s, int h) { return bp.rd(ks * 5120 + h * 2560 + s * 32); },
        [&](int ks, int m) { return a[ks][m]; },
        [&](int ks) {
#pragma unroll
          for (int j = ld_hi(ks - 1, S0, AW, 20); j < ld_hi(ks, S0, AW, 20); ++j)
#pragma unroll
            for (int m = 0; m < 2; ++m) a[j][m] = ld128(rs, lane * 16, abase + (m * 20 + j) * 1024);
          if (ks == 3 || ks == 10) BAR_RAW();
          if (ks == 20 - AW) PROG(1);
        });
      STAMP(3);
      v4u ka[3], vb[3];
#pragma unroll
      for (int s = 0; s < 3; ++s)
#pragma unroll
        for (int m = 0; m < 2; ++m) {
          vb[s][2 * m] = pk2(lo2(acc[m][s + 2])); vb[s][2 * m + 1] = pk2(hi2(acc[m][s + 2]));
        }
#pragma unroll
      for (int e = 0; e < 4; ++e) vb[0][e] = t < 8 ? 0x3F803F80u : vb[0][e];
      const f32x4 z4 = {0.f, 0.f, 0.f, 0.f};
      LAS f32x4* red = (LAS f32x4*)(L + L_RED) + w * 512 + lane;
#pragma unroll
      for (int s = 0; s < 3; ++s) {
#pragma unroll
        for (int m = 0; m < 2; ++m) { ka[s][2 * m] = pk2(phi2(lo2(acc[m][s]))); ka[s][2 * m + 1] = pk2(phi2(hi2(acc[m][s]))); }
#pragma unroll
        for (int sb = 0; sb < 3; ++sb)
          if (s > 0 || sb < 2) red[(3 * s + sb - (s > 0)) * 64] = mfma(ka[s], vb[sb], z4);
      }
      STAMP(28);
      BAR();
    }
    STAMP(4);
    f32x4 qa[2][3];
    {
      unsigned bp0 = lds0 + L_BQ + (lane >> 2) * 64 + (lane & 3) * 8 + (g & 1) * 32;
      unsigned bp1 = lds0 + L_BQ + (lane >> 2) * 64 + (lane & 3) * 8 + 32 - (g & 1) * 32;
      unsigned bp2 = lds0 + L_BQ + 2048 + (lane >> 2) * 16 + (lane & 1) * 8;
      PIN(bp0); PIN(bp1); PIN(bp2);
      kloop<2, 3, 20, 2, true>(qa,
        [&](int ks, int s, int h) {
          return s == 0 ? rdtr((LAS char*)bp0 + ks * 2560 + h * 1024) : s == 1 ? rdtr((LAS char*)bp1 + ks * 2560 + h * 1024) : rdtr((LAS char*)bp2 + ks * 2560 + h * 256);
        },
        [&](int ks, int m) { return a[ks][m]; },
        [&](int ks) {});
    }
    v4u pq0[2], pq1[2];
#pragma unroll
    for (int m = 0; m < 2; ++m) {
      pq0[m].x = pk2(phi2(lo2(qa[m][0]))); pq0[m].y = pk2(phi2(hi2(qa[m][0])));
      pq0[m].z = pk2(phi2(lo2(qa[m][1]))); pq0[m].w = pk2(phi2(hi2(qa[m][1])));
      const unsigned lo = pk2(phi2(lo2(qa[m][2]))), hi = pk2(phi2(hi2(qa[m][2])));
      pq1[m].x = g < 2 ? lo : 0u; pq1[m].y = g < 2 ? hi : 0u; pq1[m].z = 0u; pq1[m].w = 0u;
      PIN(pq0[m]); PIN(pq1[m]);
    }
    STAMP(5);
    BAR_RAW();
#pragma unroll
    for (int j = 0; j < 3; ++j)
      xr[j] = __builtin_amdgcn_raw_buffer_load_b128(rx, sel(j < 2 || g < 2, t * 1280 + g * 16), (4 * r64 + w) * 16 * 1280 + c8 * 160 + j * 64, 0);
    STAMP(17);
    {
      v4u kv[4][2];
#pragma unroll
      for (int es = 3; es >= 0; --es)
#pragma unroll
        for (int ks = 0; ks < 2; ++ks) kv[es][ks] = *(LAS v4u*)(L + L_KVF + (es * 2 + ks) * 1024 + lane * 16);
      SCHED();
      f32x4 o[2][4];
#pragma unroll
      for (int es = 3; es >= 0; --es)
#pragma unroll
        for (int m = 0; m < 2; ++m) o[m][es] = mfma(kv[es][0], pq0[m], f32x4{0.f, 0.f, 0.f, 0.f});
#pragma unroll
      for (int es = 3; es >= 0; --es)
#pragma unroll
        for (int m = 0; m < 2; ++m) o[m][es] = mfma(kv[es][1], pq1[m], o[m][es]);
#pragma unroll
      for (int m = 0; m < 2; ++m) {
        const float i0 = __builtin_amdgcn_rcpf(o[m][3][0] + 1e-6f), i1 = __builtin_amdgcn_rcpf(o[m][3][1] + 1e-6f);
        const float im = g == 0 ? i0 : i1;
        v4u ov;
        ov.x = pk2(lo2(o[m][0]) * i0); ov.y = pk2(hi2(o[m][0]) * i0);
        ov.z = pk2(lo2(o[m][1]) * im); ov.w = pk2(hi2(o[m][1]) * im);
        v2u ol; ol.x = pk2(lo2(o[m][2]) * i1); ol.y = pk2(hi2(o[m][2]) * i1);
        const int tt = 2 * grp32 + m;
        st128(ov, rs, lane * 16, WS_O + (tt * 20 + c16) * 1024);
        st64(ol, rs, g < 2 ? lane * 8 : OOB, WS_O + (tt * 20 + 16) * 1024 + c16 * 256);
      }
      STAMP(18);
      publish(FLAG(2, grp32) + c16, tag, lane);
    }
    STAMP(7);
  }

  {
    const int tt = 4 * r64 + w;
    v4u eye[2];
#pragma unroll
    for (int hf = 0; hf < 2; ++hf) {
      const int e = 16 * hf + t - 8 * g;
#pragma unroll
      for (int d = 0; d < 4; ++d) eye[hf][d] = e == 2 * d ? 0x3F80u : e == 2 * d + 1 ? 0x3F800000u : 0u;
      PIN(eye[hf]);
    }
    f32x4 acc[1][5];
#pragma unroll
    for (int s = 0; s < 5; ++s) { acc[0][s] = mfma(eye[s & 1], xr[s >> 1], f32x4{0.f, 0.f, 0.f, 0.f}); PIN(acc[0][s]); }
    wait_flag(FLAG(2, tt >> 1) + lane, 16, tag, lane);
    STAMP(8);
    if (b == 0 && tid == 0) __hip_atomic_store((gu32*)(ws + WS_EPOCH), tag, RLX_AGENT);
    v4u a[20];
#pragma unroll
    for (int ks = 0; ks < 2 * S0; ++ks) a[ks] = ld128(rs, lane * 16, WS_O + (tt * 20 + ks) * 1024);
    SCHED();
    const LBase bp(lds0 + L_WO + (lane >> 2) * 160 + (lane & 3) * 8);
    kloop<1, 5, 20, 1, true, 1>(acc, [&](int ks, int s, int h) { return bp.rd(ks * 5120 + h * 2560 + s * 32); },
      [&](int ks, int m) { return a[ks]; },
      [&](int ks) {
#pragma unroll
        for (int j = ld_hi(ks - 1, 2 * S0, 2 * AW, 20); j < ld_hi(ks, 2 * S0, 2 * AW, 20); ++j)
          a[j] = j < 16 ? ld128(rs, lane * 16, WS_O + (tt * 20 + j) * 1024) : ld_split(rs, lane, WS_O + (tt * 20 + j) * 1024);
        if (ks == 8) PROG(2);
        if (ks == 14) PROG(3);
      });
    PROG(4);
    STAMP(9);
    v2u hb[5];
    f32x2 hv[10];
#pragma unroll
    for (int s = 0; s < 5; ++s) {
      hb[s].x = pk2(lo2(acc[0][s]));
      hb[s].y = pk2(hi2(acc[0][s]));
      hv[2 * s] = bf2(hb[s].x); hv[2 * s + 1] = bf2(hb[s].y);
    }
    st128(v4u{hb[0].x, hb[0].y, hb[1].x, hb[1].y}, rs, lane * 16, WS_H + (tt * 20 + 2 * c8) * 1024);
    st128(v4u{hb[2].x, hb[2].y, hb[3].x, hb[3].y}, rs, lane * 16, WS_H + (tt * 20 + 2 * c8 + 1) * 1024);
    st64(hb[4], rs, lane * 8, WS_H + (tt * 20 + 16) * 1024 + c8 * 512);
    const f32x2 s2 = ((hv[0] + hv[1]) + (hv[2] + hv[3])) + ((hv[4] + hv[5]) + (hv[6] + hv[7])) + (hv[8] + hv[9]);
    const float mc = xsum32(xsum16(s2[0] + s2[1])) * (1.f / 80.f);
    f32x2 q2 = {0.f, 0.f}, q3 = {0.f, 0.f};
#pragma unroll
    for (int j = 0; j < 10; j += 2) { const f32x2 d0 = hv[j] - mc, d1 = hv[j + 1] - mc; q2 += d0 * d0; q3 += d1 * d1; }
    q2 += q3;
    const float m2 = xsum32(xsum16(q2[0] + q2[1]));
    st64(v2u{__builtin_bit_cast(unsigned, mc), __builtin_bit_cast(unsigned, m2)}, rs, g == 0 ? t * 8 : OOB, WS_ST + (tt * 8 + c8) * 128);
    STAMP(20);
    publish(FLAG(3, tt >> 1) + (tt & 1) * 8 + c8, tag, lane);
    STAMP(10);
  }

  {
    wait_flag(FLAG(3, grp32) + lane, 16, tag, lane);
    STAMP(11);
    v4u stt[2];
#pragma unroll
    for (int m = 0; m < 2; ++m) {
      const v2u s0 = ld64(rs, g * 256 + t * 8, WS_ST + (2 * grp32 + m) * 1024), s1 = ld64(rs, g * 256 + 128 + t * 8, WS_ST + (2 * grp32 + m) * 1024);
      stt[m] = v4u{s0.x, s0.y, s1.x, s1.y};
    }
    v4u a[20][2];
    const int abase = WS_H + 2 * grp32 * 20 * 1024;
#pragma unroll
    for (int ks = 0; ks < S0; ++ks)
#pragma unroll
      for (int m = 0; m < 2; ++m) a[ks][m] = ld128(rs, lane * 16, abase + (m * 20 + ks) * 1024);
    SCHED();
    float nm[2], rr[2];
#pragma unroll
    for (int m = 0; m < 2; ++m) {
      const f32x4 sv = __builtin_bit_cast(f32x4, stt[m]);
      const float mean = xsum32(xsum16(sv[0] + sv[2])) * 0.125f;
      const float m2 = xsum32(xsum16(sv[1] + sv[3] + 80.f * ((sv[0] - mean) * (sv[0] - mean) + (sv[2] - mean) * (sv[2] - mean))));
      const float var = m2 * (1.f / D);
      const float r1 = __builtin_amdgcn_rsqf(var + LN_EPS);
      rr[m] = r1 * __builtin_amdgcn_rsqf(var * r1 * r1 + LN_EPS);
      nm[m] = -mean * rr[m];
      PIN(rr[m]); PIN(nm[m]);
    }
    BAR_RAW();
    STAMP(22);
    const unsigned l64 = lds0 + (lane >> 2) * 64 + (lane & 3) * 8;
    const LBase bp0(l64 + (g & 1) * 32), bp1(l64 + 32 - (g & 1) * 32);
    const int h1base = WS_H1 + 2 * grp32 * 48 * 1024;
    auto finish = [&](const f32x4& c0, const f32x4& c1, int j, int m) {
      v4u o;
#pragma unroll
      for (int s = 0; s < 2; ++s) {
        const f32x4 cs = *(LAS f32x4*)(L + L_CS + ((2 * j + s) * 4 + g) * 16), bb = *(LAS f32x4*)(L + L_B1 + ((2 * j + s) * 4 + g) * 16);
        const f32x4 c = s ? c1 : c0;
        o[2 * s] = pk2(gelu2(lo2(c) * rr[m] + (lo2(cs) * nm[m] + lo2(bb))));
        o[2 * s + 1] = pk2(gelu2(hi2(c) * rr[m] + (hi2(cs) * nm[m] + hi2(bb))));
      }
      st128(o, rs, lane * 16, h1base + (m * 48 + (c16 >> 3) * 24 + (j < 2 ? 2 * (c16 & 7) + j : 16 + (c16 & 7))) * 1024);
    };
    {
      f32x4 acc[2][4];
      kloop<2, 4, 20, 1, true>(acc, [&](int ks, int s, int h) { return ((s & 1) ? bp1 : bp0).rd(L_W1 + (s >> 1) * 40960 + ks * 2048 + h * 1024); },
        [&](int ks, int m) { return a[ks][m]; },
        [&](int ks) {
#pragma unroll
          for (int j = ld_hi(ks - 1, S0, AW, 20); j < ld_hi(ks, S0, AW, 20); ++j)
#pragma unroll
            for (int m = 0; m < 2; ++m)
              a[j][m] = j < 16 ? ld128(rs, lane * 16, abase + (m * 20 + j) * 1024) : ld_split(rs, lane, abase + (m * 20 + j) * 1024);
          if (ks == 20 - AW) PROG(5);
        });
      PROG(6);
      STAMP(12);
#pragma unroll
      for (int j = 0; j < 2; ++j)
#pragma unroll
        for (int m = 0; m < 2; ++m) finish(acc[m][2 * j], acc[m][2 * j + 1], j, m);
      SCHED();
      STAMP(24);
    }
    f32x4 accb[2][2];
    const int kh = w & 1, tt = 4 * r64 + w;
    v4u hr01, hr23;
    v2u hr4;
    kloop<2, 2, 20, 3, true>(accb, [&](int ks, int s, int h) { return (s ? bp1 : bp0).rd(L_W1 + 81920 + ks * 2048 + h * 1024); },
      [&](int ks, int m) { return a[ks][m]; },
      [&](int ks) {
        if (ks == PUBA) { publish(FLAG(4, grp32) + c16, tag, lane); PROG(7); }
        if (ks == PUBA + 2) {
          hr01 = ld128(rs, lane * 16, WS_H + (tt * 20 + 2 * c8) * 1024);
          hr23 = ld128(rs, lane * 16, WS_H + (tt * 20 + 2 * c8 + 1) * 1024);
          hr4 = ld64(rs, lane * 8, WS_H + (tt * 20 + 16) * 1024 + c8 * 512);
        }
      });
    STAMP(13);
    f32x4 acc[2][5];
    {
      const unsigned hrr[10] = {hr01.x, hr01.y, hr01.z, hr01.w, hr23.x, hr23.y, hr23.z, hr23.w, hr4.x, hr4.y};
#pragma unroll
      for (int s = 0; s < 5; ++s) {
        acc[0][s] = *(LAS f32x4*)(L + L_B2 + (s * 4 + g) * 16) + f32x4{bf_lo(hrr[2 * s]), bf_hi(hrr[2 * s]), bf_lo(hrr[2 * s + 1]), bf_hi(hrr[2 * s + 1])};
        PIN(acc[0][s]);
      }
    }
    int oo[3];
    unsigned tw, tr;
    {
      int ln = lane;
      PIN(ln);
      const int qq = ln / 10, cc = ln % 10;
#pragma unroll
      for (int j = 0; j < 3; ++j) { oo[j] = sel(ln < (j < 2 ? 60 : 40), ((tt * 16 + 6 * j + qq) * D + 80 * c8) * 2 + cc * 16); PIN(oo[j]); }
      tw = lds0 + L_W2 + 122880 + w * 3072 + t * 176 + g * 8; tr = lds0 + L_W2 + 122880 + w * 3072 + qq * 176 + cc * 16;
      PIN(tw); PIN(tr);
    }
    BAR_RAW();
    STAMP(14);
    constexpr int WIN = 8;
    v4u ar[WIN][2];
    const int a4[2] = {WS_H1 + (tt * 48 + kh * 24) * 1024, WS_H1 + ((tt ^ 1) * 48 + kh * 24) * 1024};
#pragma unroll
    for (int ks = 0; ks < WIN - 1; ++ks)
#pragma unroll
      for (int m = 0; m < 2; ++m) ar[ks][m] = ld128(rs, lane * 16, a4[m] + ks * 1024);
    SCHED();
#pragma unroll
    for (int m = 0; m < 2; ++m) finish(accb[m][0], accb[m][1], 2, m);
    STAMP(29);
    const LBase bp(lds0 + L_W2 + kh * 5120 + (lane >> 2) * 160 + (lane & 3) * 8);
    kloop<2, 5, 24, 1, true, 1>(acc, [&](int ks, int s, int h) { return bp.rd((ks % 15) * 10240 + h * 2560 + s * 32); },
      [&](int ks, int m) { return ar[ks % WIN][m]; },
      [&](int ks) {
        if (ks == 5) PROG(8);
        if (ks == 9 || ks == 14 || ks == 19) BAR_RAW();
        if (ks == 3) publish(FLAG(10, grp32) + c16, tag, lane);
        if (ks >= 3) {
#pragma unroll
          for (int j = ks == 3 ? WIN - 1 : ld_hi(ks - 1, 1, WIN - 1, 24); j < ld_hi(ks, 1, WIN - 1, 24); ++j)
#pragma unroll
            for (int m = 0; m < 2; ++m) ar[j % WIN][m] = ld128(rs, lane * 16, a4[m] + j * 1024);
        }
      });
    STAMP(15);
    LAS f32x4* xch = (LAS f32x4*)(L + L_W2 + 102400);
#pragma unroll
    for (int s = 0; s < 5; ++s) xch[((w ^ 1) * 5 + s) * 64 + lane] = acc[1][s];
    BAR();
    const __amdgpu_buffer_rsrc_t rout = __builtin_amdgcn_make_buffer_rsrc((void*)out, 0, S * D * 2, 0x00020000);
    f32x4 ot[5];
#pragma unroll
    for (int s = 0; s < 5; ++s) ot[s] = xch[(w * 5 + s) * 64 + lane];
    SCHED();
#pragma unroll
    for (int s = 0; s < 5; ++s) {
      const f32x4 v = acc[0][s] + ot[s];
      *(LAS v2u*)((LAS char*)tw + s * 32) = v2u{pk2(lo2(v)), pk2(hi2(v))};
    }
    asm volatile("" ::: "memory");
    v4u ov[3];
#pragma unroll
    for (int j = 0; j < 3; ++j) ov[j] = *(LAS v4u*)((LAS char*)tr + j * 6 * 176);
#pragma unroll
    for (int j = 0; j < 3; ++j) __builtin_amdgcn_raw_buffer_store_b128(ov[j], rout, oo[j], 0, 0);
    STAMP(16);
  }
  return;
  }

  {
    const v4u srd_ws = mk_srd(ws, WS_BYTES);
    STAMP(16);
    const int q5 = lane / 5, c5 = lane % 5;
    const int f40m = c5 < 4 ? 512 : 128, f40l = c5 < 4 ? q5 * 64 + (c5 ^ (2 * ((q5 >> 2) & 1))) * 16 : 2048 + q5 * 16;
    v2u ub1, ub2;
    unsigned pf0 = 0;
    if (hw == 0) {
      const int dof = sel(lane < 40, lane * 16);
#pragma unroll
      for (int j = 0; j < 10; ++j) {
        const int k = 40 * r128 + 4 * j, R = (k & ~31) + 16 * ((k >> 2) & 1) + 4 * ((k >> 3) & 3);
        st128(tv[j], rs, dof, WS_TKV + c16 * 153600 + R * 160);
      }
      STAMP(18);
      PIN(tag); tag += 1u;
      publish(FLAG(6, c16) + r128, tag, lane);
      STAMP(13);
    } else if (hw == 1) {
      const __amdgpu_buffer_rsrc_t ro = __builtin_amdgcn_make_buffer_rsrc((void*)wo, 0, D * D * 2, 0x00020000);
      const int vq = sel(lane < 40, (8 * (q5 >> 2) + (q5 & 3)) * 3840 + c5 * 16), vo = sel(lane < 40, q10 * 1280 + c10 * 16);
#pragma unroll
      for (int j = 0; j < 5; ++j) {
        const int i8 = 5 * r128 + j, k = 32 * (i8 >> 2) + 16 * (i8 & 1) + 4 * ((i8 >> 1) & 1);
        tv[j] = __builtin_amdgcn_raw_buffer_load_b128(rq, vq, k * 3840 + c16 * 80, 0);
      }
#pragma unroll
      for (int j = 0; j < 5; ++j) tv[5 + j] = __builtin_amdgcn_raw_buffer_load_b128(ro, vo, (20 * r64 + 4 * j) * 1280 + c8 * 160, 0);
    } else if (hw == 2) {
      const GAS unsigned char* cb = (const GAS unsigned char*)(pc0 & ~127ull);
      if (lane < 11) pf0 = *(const GAS unsigned*)(cb + (lane * 32 + bi) * 128);
      if (lane < 6) {
        gu32* fp = FLAG(3 * (b >> 6) + (lane >> 1), b & 63) + (lane & 1) * 32;
        asm volatile("global_atomic_add %0, %1, off sc1" :: "v"(fp), "v"(0u) : "memory");
      }
    } else if (hw == 3) {
      const __amdgpu_buffer_rsrc_t rb1 = __builtin_amdgcn_make_buffer_rsrc((void*)b1, 0, F * 2, 0x00020000);
      const __amdgpu_buffer_rsrc_t rb2 = __builtin_amdgcn_make_buffer_rsrc((void*)b2, 0, D * 2, 0x00020000);
      ub1 = __builtin_amdgcn_raw_buffer_load_b64(rb1, lane * 8, c16 * 192, 0);
      ub2 = __builtin_amdgcn_raw_buffer_load_b64(rb2, lane * 8, c8 * 160, 0);
    }
    asm volatile("" ::: "memory");
    int red_off, kvf_off, kvp_off;
    auto items = [&]() {
      const int it = ht < 210 ? ht : 209, ie = it / 5, ic = it % 5;
      const int ihead = ie < 40 ? (ie >= 20) : ie - 40;
      const int id = 20 * ihead + 4 * ic, dg = id >> 2;
      const int sa = id >> 4, col = 40 + ie;
      const int tile = 3 * sa + (ie < 40 ? (col >> 4) - 2 : 0) - (sa > 0);
      red_off = lds0 + L_RED + (tile * 64 + (dg & 3) * 16 + (ie < 40 ? (col & 15) : 0)) * 16;
      kvf_off = lds0 + L_KVF + ((ie < 40 ? (ie >> 4) : 3) * 2 + (dg >> 3)) * 1024 + ((dg & 3) * 16 + (ie < 40 ? (ie & 15) : ihead)) * 16 + ((dg >> 2) & 1) * 8;
      kvp_off = WS_KVP + (c16 * 16 + r128) * 4096 + ht * 16;
      PIN(red_off); PIN(kvf_off); PIN(kvp_off);
    };
    if (hw != 0) items();
    int vo12, td[12];
    if (hw >= 2) {
      const int q12 = lane / 12, c12 = lane % 12;
      vo12 = hw == 2 ? sel(lane < 48, q12 * 3072 + c12 * 16) : sel(lane < 40, q10 * 1280 + c10 * 16);
#pragma unroll
      for (int j = 0; j < 12; ++j) {
        const int cc = r128 >> 1, off = 40 * (r128 & 1) + 4 * j;
        const int R = off < 64 ? 64 * cc + off : 32 * (16 + (cc >> 1)) + 16 * (cc & 1) + off - 64;
        const int d1 = WS_T1 + c16 * 122880 + (c12 >> 2) * 40960 + (R + q12) * 64 + ((c12 & 3) ^ (2 * ((R >> 2) & 1))) * 16;
        const int pc = r64 >> 1, ko = 48 * (r64 & 1) + 4 * j, jj = ko >> 5;
        const int d2 = WS_T2 + c8 * 245760 + (2 * (jj < 2 ? 2 * (pc & 7) + jj : 16 + (pc & 7)) + (pc >> 3)) * 5120 + (ko & 31) * 160 + lane * 16;
        td[j] = sel(hw == 2 ? (lane < 48 && j < 10) : lane < 40, hw == 2 ? d1 : d2);
        PIN(td[j]);
      }
      PIN(vo12);
    }
    int l16 = lane * 16, h16 = ht * 16;
    PIN(l16); PIN(h16);
    gu32* fp1 = FLAG(1, c16) + lane;
    gu32* fp78 = lane < 32 ? FLAG(7, c8) + lane : FLAG(8, c16) + t;
    gu32* fp4 = FLAG(4, 2 * r64 + (g & 1)) + t;
    gu32* fp1w = FLAG(1, c16) + r128 * 4 + hw;
    PIN(fp1); PIN(fp78); PIN(fp4); PIN(fp1w);
    ((LAS v4u*)(L + L_KVF))[ht] = v4u{0u, 0u, 0u, 0u};
    ((LAS v4u*)(L + L_KVF))[256 + ht] = v4u{0u, 0u, 0u, 0u};
    asm volatile("" ::: "memory");
    if (hw != 0) {
      if (hw == 1) {
#pragma unroll
        for (int j = 0; j < 5; ++j) {
          const int i8 = 5 * r128 + j;
          st128(tv[j], rs, sel(lane < 40, (i8 & 3) * f40m + f40l), WS_TKV + c16 * 153600 + 102400 + (i8 >> 2) * 2560);
        }
#pragma unroll
        for (int j = 0; j < 5; ++j) {
          const int pp = r64 >> 1, e = 20 * (r64 & 1) + 4 * j;
          const int R = e < 32 ? 32 * pp + e : 32 * (16 + (pp >> 2)) + 8 * (pp & 3) + e - 32;
          st128(tv[5 + j], rs, sel(lane < 40, lane * 16), WS_TO + c8 * 102400 + R * 160);
        }
      }
      if (hw == 3) {
        if (lane < 24) *(LAS f32x4*)(L + L_B1 + lane * 16) = f32x4{bf_lo(ub1.x), bf_hi(ub1.x), bf_lo(ub1.y), bf_hi(ub1.y)};
        if (lane < 20) *(LAS f32x4*)(L + L_B2 + lane * 16) = f32x4{bf_lo(ub2.x), bf_hi(ub2.x), bf_lo(ub2.y), bf_hi(ub2.y)};
      }
      asm volatile("" :: "v"(pf0));
      PIN(tag); tag += 1u;
      DRAIN();
      if (hw == 1 && lane < 2) flag_set(lane == 0 ? FLAG(8, c16) + r128 : FLAG(7, c8) + r64, tag);
    }
    if (hw == 0) {
      if (F6W) __builtin_amdgcn_s_sleep(F6W);
      wait_flag(FLAG(6, c16) + lane, 16, tag, lane);
      sig[0] = 1u;
    }
    sig_wait(sig);
    STAMP(1);
    const int kvq = WS_TKV + c16 * 153600, wos = WS_TO + c8 * 102400;
    dma_image<0, 24>(lds0 + L_BKV, l16, srd_ws, kvq, hw, 3 * (bi >> 3));
    dma_image<24, 56>(lds0 + L_BKV, l16, srd_ws, kvq, hw, 4 * (bi >> 3));
    asm volatile("s_waitcnt vmcnt(8)" ::: "memory");
    STAMP(2);
    BAR_RAW();
    dma_image<56, 100>(lds0 + L_BKV, l16, srd_ws, kvq, hw, 5 * (bi >> 3));
    asm volatile("s_waitcnt vmcnt(10)" ::: "memory");
    BAR_RAW();
    if (hw == 1) wait_flag(fp78, 48, tag, lane);
    DRAIN();
    STAMP(3);
    BAR_RAW();
    if (QPOS == 0) dma_image<100, 150>(lds0 + L_BKV, l16, srd_ws, kvq, hw, 6 * (bi >> 3));
    if (hw == 0) items();
    prog_wait(prog, 1);
    if (QPOS == 1) dma_image<100, 150>(lds0 + L_BKV, l16, srd_ws, kvq, hw, 6 * (bi >> 3));
    if (hw >= 2) {
      const __amdgpu_buffer_rsrc_t r12 = __builtin_amdgcn_make_buffer_rsrc((void*)(hw == 2 ? w1 : w2), 0, D * F * 2, 0x00020000);
      const int base = hw == 2 ? r128 * 122880 + c16 * 192 : r64 * 61440 + c8 * 160, step = hw == 2 ? 4 * 3072 : 4 * 1280;
#pragma unroll
      for (int j = 0; j < 12; ++j) tv[j] = __builtin_amdgcn_raw_buffer_load_b128(r12, vo12, (j < 10 || hw == 3) ? base + j * step : OOB, 0);
      asm volatile("s_waitcnt vmcnt(12)" ::: "memory");
    } else {
      dma_image<32, 68, 0, 2>(lds0 + L_WO, l16, srd_ws, wos, hw, bi >> 2);
      asm volatile("s_waitcnt vmcnt(18)" ::: "memory");
    }
    STAMP(4);
    BAR();
    __builtin_amdgcn_s_setprio(2);
    STAMP(11);
    {
      f32x4 pv[4];
#pragma unroll
      for (int ww = 0; ww < 4; ++ww) pv[ww] = *(LAS f32x4*)((LAS char*)red_off + ww * 8192);
      st128(__builtin_bit_cast(v4u, add4(add4(pv[0], pv[1]), add4(pv[2], pv[3]))), rs, kvp_off, 0);
      PROG(1);
      STAMP(12);
      DRAIN();
    }
    if (lane == 0) flag_set(fp1w, tag);
    if (hw == 0) {
      STAMP(10);
      __builtin_amdgcn_s_sleep(H1W);
      wait_flag(fp1, 64, tag, lane);
      sig[2] = 1u;
    } else {
      if (H1G) __builtin_amdgcn_s_sleep(H1G);
      dma_image<68, 100, 0, 3>(lds0 + L_WO, l16, srd_ws, wos, hw - 1, bi >> 2);
      prog_wait(prog + 1, 1);
      dma_image<0, 32, 0, 3>(lds0 + L_WO, l16, srd_ws, wos, hw - 1, 2 * (bi >> 2));
      if (hw >= 2) {
#pragma unroll
        for (int j = 0; j < 12; ++j) st128(tv[j], rs, td[j], 0);
      }
      STAMP(10);
      sig_wait(sig + 2);
    }
    STAMP(5);
    {
      v4u pv[16];
#pragma unroll
      for (int r = 0; r < 16; ++r) pv[r] = ld128(rs, h16, WS_KVP + (c16 * 16 + r) * 4096);
      SCHED();
      f32x4 v = __builtin_bit_cast(f32x4, pv[0]);
#pragma unroll
      for (int r = 1; r < 16; ++r) v = add4(v, __builtin_bit_cast(f32x4, pv[r]));
      v2u o; o.x = pk2(lo2(v)); o.y = pk2(hi2(v));
      *(LAS v2u*)((LAS char*)kvf_off) = o;
      if (ht >= 200) {
#pragma unroll
        for (int c = 1; c < 4; ++c) *(LAS v2u*)((LAS char*)kvf_off + c * 64) = o;
      }
    }
    DRAIN();
    STAMP(6);
    BAR();
    __builtin_amdgcn_s_setprio(0);
    if (hw >= 2 && lane == 0) flag_set(hw == 2 ? FLAG(5, c16) + r128 : FLAG(11, c8) + r64, tag);
    if (hw == 2) {
      f32x4 s0 = {0.f, 0.f, 0.f, 0.f}, s1 = {0.f, 0.f, 0.f, 0.f};
#pragma unroll
      for (int j = 0; j < 10; ++j) {
        s0 = add4(s0, f32x4{bf_lo(tv[j].x), bf_hi(tv[j].x), bf_lo(tv[j].y), bf_hi(tv[j].y)});
        s1 = add4(s1, f32x4{bf_lo(tv[j].z), bf_hi(tv[j].z), bf_lo(tv[j].w), bf_hi(tv[j].w)});
      }
#pragma unroll
      for (int d = 24; d >= 12; d -= 12) {
        f32x4 u0, u1;
#pragma unroll
        for (int e = 0; e < 4; ++e) { u0[e] = __shfl(s0[e], lane + d); u1[e] = __shfl(s1[e], lane + d); }
        s0 = add4(s0, u0); s1 = add4(s1, u1);
      }
      f32x4 sv;
#pragma unroll
      for (int e = 0; e < 4; ++e) { const float a0 = __shfl(s0[e], lane >> 1), a1 = __shfl(s1[e], lane >> 1); sv[e] = (lane & 1) ? a1 : a0; }
      st128(__builtin_bit_cast(v4u, sv), rs, sel(lane < 24, lane * 16), WS_CS + (c16 * 16 + r128) * 384);
      publish(FLAG(9, c16) + r128, tag, lane);
    }
    if (hw == 1) {
      __builtin_amdgcn_s_sleep(40);
      wait_flag(lane < 32 ? FLAG(11, c8) + lane : FLAG(5, c16) + t, 48, tag, lane);
      sig[3] = 1u;
    }
    sig_wait(sig + 3);
    prog_wait(prog, 2);
    if (hw == 0) {
      wait_flag(FLAG(9, c16) + lane, 16, tag, lane);
      v4u pv[16];
      const int co = sel(lane < 24, l16);
#pragma unroll
      for (int r = 0; r < 16; ++r) pv[r] = ld128(rs, co, WS_CS + (c16 * 16 + r) * 384);
      SCHED();
      f32x4 v = __builtin_bit_cast(f32x4, pv[0]);
#pragma unroll
      for (int r = 1; r < 16; ++r) v = add4(v, __builtin_bit_cast(f32x4, pv[r]));
      if (lane < 24) *(LAS f32x4*)(L + L_CS + lane * 16) = v;
    } else {
      dma_image<70, 120, 0, 3>(lds0 + L_W1, l16, srd_ws, WS_T1 + c16 * 122880, hw - 1, 6 * (bi >> 3));
      prog_wait(prog, 3);
      dma_image<0, 40, 0, 3>(lds0 + L_W1, l16, srd_ws, WS_T1 + c16 * 122880, hw - 1, 5 * (bi >> 3));
      STAMP(7);
      prog_wait(prog, 4);
      dma_image<40, 70, 0, 3>(lds0 + L_W1, l16, srd_ws, WS_T1 + c16 * 122880, hw - 1, 3 * (bi >> 3));
    }
    DRAIN();
    STAMP(8);
    BAR();
    const int w2s = WS_T2 + c8 * 245760;
    prog_wait(prog, 5);
    dma_image<0, 30>(lds0 + L_W2, l16, srd_ws, w2s, hw, bi >> 2);
    STAMP(14);
    prog_wait(prog, 6);
    dma_image<30, W2S>(lds0 + L_W2, l16, srd_ws, w2s, hw, bi >> 2);
    prog_wait(prog, 7);
    if (hw != 0) dma_image<W2S, 110, 0, 3>(lds0 + L_W2, l16, srd_ws, w2s, hw - 1, bi >> 2);
    DRAIN();
    STAMP(9);
    if (hw == 0) wait_flag(fp4, 32, tag, lane);
    STAMP(15);
    BAR_RAW();
    dma_image<110, 150>(lds0 + L_W2, l16, srd_ws, w2s, hw, 2 * (bi >> 2));
    prog_wait(prog, 8);
    if (hw == 0) wait_flag(fp4 + 6 * FLAG_SET / 4, 32, tag, lane);
    else dma_image<150, 198, 0, 3>(lds0 + L_W2 - 153600, l16, srd_ws, w2s, hw - 1, 3 * (bi >> 2));
    asm volatile("s_waitcnt vmcnt(16)" ::: "memory");
    STAMP(17);
    BAR_RAW();
    dma_image<198, 200>(lds0 + L_W2 - 153600, l16, srd_ws, w2s, hw, 0);
    dma_image<200, 240>(lds0 + L_W2 - 153600, l16, srd_ws, w2s, hw, 2 * (bi >> 2));
    asm volatile("s_waitcnt vmcnt(10)" ::: "memory");
    BAR_RAW();
    DRAIN();
    BAR_RAW();
  }

}
}

extern "C" void kernel_launch(void* const* d_in, const int* in_sizes, int n_in, void* d_out, int out_size, void* d_ws,
                              size_t ws_size, hipStream_t stream) {
  const unsigned short* x = (const unsigned short*)d_in[0];
  const unsigned short* wqkv = (const unsigned short*)d_in[1];
  const unsigned short* wo = (const unsigned short*)d_in[2];
  const unsigned short* w1 = (const unsigned short*)d_in[4];
  const unsigned short* b1 = (const unsigned short*)d_in[5];
  const unsigned short* w2 = (const unsigned short*)d_in[6];
  const unsigned short* b2 = (const unsigned short*)d_in[7];
  fused_block<<<256, 512, 0, stream>>>(x, wqkv, wo, w1, b1, w2, b2, (unsigned short*)d_out, (unsigned char*)d_ws);
}
```

```cpp
#include <hip/hip_runtime.h>
#include <hip/hip_bf16.h>

#define GAS __attribute__((address_space(1)))
#define LAS __attribute__((address_space(3)))
#define RLX_AGENT __ATOMIC_RELAXED, __HIP_MEMORY_SCOPE_AGENT
typedef unsigned v4u __attribute__((ext_vector_type(4)));
typedef unsigned v2u __attribute__((ext_vector_type(2)));
typedef float f32x4 __attribute__((ext_vector_type(4)));
typedef float f32x2 __attribute__((ext_vector_type(2)));
typedef __bf16 bf16x8 __attribute__((ext_vector_type(8)));
typedef __bf16 bf16x2 __attribute__((ext_vector_type(2)));
typedef short s16x4 __attribute__((ext_vector_type(4)));
typedef GAS unsigned gu32;

namespace {
constexpr int S = 2048, D = 640, F = 1536;
constexpr float LN_EPS = 1e-5f;
constexpr int AUX_SC1 = 16;
constexpr unsigned kSpinCap = 1u << 17;
constexpr int OOB = 0x7FFFFF00;

constexpr int WS_EPOCH = 0;
constexpr int WS_FLAG = 4096;
constexpr int FLAG_SET = 64 * 256;
constexpr int WS_XN = 1 << 20;
constexpr int WS_O = WS_XN + 128 * 20 * 1024;
constexpr int WS_H = WS_O + 128 * 20 * 1024;
constexpr int WS_H1 = WS_H + 128 * 20 * 1024;
constexpr int WS_KVP = WS_H1 + 128 * 48 * 1024;
constexpr int WS_ST = WS_KVP + 16 * 16 * 4096;
constexpr int WS_TKV = WS_ST + 2048 * 64;
constexpr int WS_TO = WS_TKV + 16 * 153600;
constexpr int WS_T1 = WS_TO + 8 * 102400;
constexpr int WS_T2 = WS_T1 + 16 * 122880;
constexpr int WS_CS = WS_T2 + 16 * 122880;
constexpr int WS_BYTES = WS_CS + 16 * 16 * 384;

constexpr int L_BKV = 0, L_BQ = 102400;
constexpr int L_RED = 0, L_KVF = 153600;
constexpr int L_WO = 0, L_W1 = 30720, L_W2 = 0, L_CS = 161792;
constexpr int L_B1 = L_CS + 1152, L_B2 = L_CS + 1536;
constexpr int LDS_BYTES = 163840;

__device__ __forceinline__ f32x4 mfma(v4u a, v4u b, f32x4 c) {
  return __builtin_amdgcn_mfma_f32_16x16x32_bf16(__builtin_bit_cast(bf16x8, a), __builtin_bit_cast(bf16x8, b), c, 0, 0, 0);
}
__device__ __forceinline__ unsigned pk2(f32x2 v) { return __builtin_bit_cast(unsigned, __builtin_convertvector(v, bf16x2)); }
__device__ __forceinline__ unsigned pk2(float lo, float hi) { return pk2(f32x2{lo, hi}); }
__device__ __forceinline__ float bf_lo(unsigned u) { return __builtin_bit_cast(float, u << 16); }
__device__ __forceinline__ float bf_hi(unsigned u) { return __builtin_bit_cast(float, u & 0xFFFF0000u); }
__device__ __forceinline__ f32x2 bf2(unsigned u) { return f32x2{bf_lo(u), bf_hi(u)}; }
__device__ __forceinline__ f32x2 lo2(f32x4 v) { return f32x2{v[0], v[1]}; }
__device__ __forceinline__ f32x2 hi2(f32x4 v) { return f32x2{v[2], v[3]}; }
__device__ __forceinline__ v4u mk_srd(const void* p, unsigned bytes) {
  const unsigned long long a = (unsigned long long)p;
  v4u r;
  r.x = (unsigned)a; r.y = (unsigned)(a >> 32) & 0xFFFFu; r.z = bytes; r.w = 0x00020000u;
  return r;
}
#define DRAIN() asm volatile("s_waitcnt vmcnt(0)" ::: "memory")
#define BAR() asm volatile("s_waitcnt lgkmcnt(0)\n\ts_barrier" ::: "memory")
#define BAR_RAW() asm volatile("s_barrier" ::: "memory")
#define SCHED() __builtin_amdgcn_sched_barrier(0)
#define PIN(x) asm volatile("" : "+v"(x))

__device__ __forceinline__ void dma2(unsigned lds_addr, unsigned voff, v4u srd, unsigned soff) {
  unsigned keep;
  asm volatile("s_mov_b32 %0, m0\n\ts_mov_b32 m0, %1\n\ts_nop 0\n\t"
               "buffer_load_dwordx4 %2, %3, %4 offen sc1 lds\n\t"
               "buffer_load_dwordx4 %2, %3, %4 offen offset:1024 sc1 lds\n\t"
               "s_mov_b32 m0, %0"
               : "=&s"(keep) : "s"(lds_addr), "v"(voff), "s"(srd), "s"(soff) : "memory");
}
template <int LO, int HI, int NW = 4>
__device__ __forceinline__ void dma_image(unsigned lds_base, unsigned voff, v4u srd, int ws_off, int hw, int rot) {
  constexpr int NP = (HI - LO) / 2;
#pragma unroll 1
  for (int i = hw; i < NP; i += NW) {
    int j = i + rot;
    if (j >= NP) j -= NP;
    dma2(lds_base + LO * 1024 + j * 2048, voff, srd, ws_off + LO * 1024 + j * 2048);
  }
}
__device__ __forceinline__ LAS char* lp(unsigned a) { return (LAS char*)(size_t)a; }
__device__ __forceinline__ v2u rdtr(LAS char* p) { return __builtin_bit_cast(v2u, __builtin_amdgcn_ds_read_tr16_b64_v4i16((LAS s16x4*)p)); }
struct LBase {
  unsigned a[3];
  __device__ __forceinline__ explicit LBase(unsigned v) { a[0] = v; a[1] = v + 65536; a[2] = v + 131072; PIN(a[0]); PIN(a[1]); PIN(a[2]); }
  __device__ __forceinline__ v2u rd(int off) const { return rdtr(lp(a[off >> 16]) + (off & 65535)); }
};
template <int MT, int NT, int KS, int PD, bool WA, int M0 = 0, class RB, class GA, class PRE>
__device__ __forceinline__ void kloop(f32x4 (&acc)[MT][NT], RB rb, GA ga, PRE pre) {
  v2u bl[PD + 1][NT], bh[PD + 1][NT];
#pragma unroll
  for (int m = M0; m < MT; ++m)
#pragma unroll
    for (int s = 0; s < NT; ++s) acc[m][s] = f32x4{0.f, 0.f, 0.f, 0.f};
#pragma unroll
  for (int ks = 0; ks < PD; ++ks)
#pragma unroll
    for (int s = 0; s < NT; ++s) { bl[ks][s] = rb(ks, s, 0); bh[ks][s] = rb(ks, s, 1); }
  SCHED();
#pragma unroll
  for (int ks = 0; ks < KS; ++ks) {
    pre(ks);
#pragma unroll
    for (int s = 0; s < NT; ++s)
#pragma unroll
      for (int m = 0; m < MT; ++m) {
        const v2u lo = bl[ks % (PD + 1)][s], hi = bh[ks % (PD + 1)][s];
        const v4u wv = {lo.x, lo.y, hi.x, hi.y};
        acc[m][s] = WA ? mfma(wv, ga(ks, m), acc[m][s]) : mfma(ga(ks, m), wv, acc[m][s]);
        if (ks + PD < KS) {
          if (MT == 1 || m == 0) bl[(ks + PD) % (PD + 1)][s] = rb(ks + PD, s, 0);
          if (MT == 1 || m == 1) bh[(ks + PD) % (PD + 1)][s] = rb(ks + PD, s, 1);
        }
        SCHED();
      }
  }
}
constexpr int ld_hi(int ks, int s0, int W, int KS) {
  const int a = s0 + 2 * (ks + 1), b = ks + 1 + W, c = a < b ? a : b;
  return c < KS ? c : KS;
}
__device__ __forceinline__ v4u ld128(__amdgpu_buffer_rsrc_t rs, int voff, int soff) {
  return __builtin_amdgcn_raw_buffer_load_b128(rs, voff, soff, AUX_SC1);
}
__device__ __forceinline__ v2u ld64(__amdgpu_buffer_rsrc_t rs, int voff, int soff) {
  return __builtin_amdgcn_raw_buffer_load_b64(rs, voff, soff, AUX_SC1);
}
__device__ __forceinline__ void st128(v4u v, __amdgpu_buffer_rsrc_t rs, int voff, int soff) {
  __builtin_amdgcn_raw_buffer_store_b128(v, rs, (int)((unsigned)voff + (unsigned)soff), 0, AUX_SC1);
}
__device__ __forceinline__ int sel(bool c, int a) {
  PIN(a);
  return c ? a : OOB;
}
__device__ __forceinline__ v4u ld_split(__amdgpu_buffer_rsrc_t rs, int lane, int soff) {
  const v2u a = ld64(rs, lane * 8, soff), b = ld64(rs, lane * 8 + 512, soff);
  return v4u{a.x, a.y, b.x, b.y};
}
__device__ __forceinline__ void st64(v2u v, __amdgpu_buffer_rsrc_t rs, int voff, int soff) {
  __builtin_amdgcn_raw_buffer_store_b64(v, rs, voff, soff, AUX_SC1);
}
__device__ __forceinline__ void flag_set(gu32* p, unsigned tag) {
  asm volatile("global_atomic_swap %0, %1, off sc1" :: "v"(p), "v"(tag) : "memory");
}
__device__ __forceinline__ void wait_flag(gu32* p, int n, unsigned tag, int lane) {
#pragma unroll 1
  for (unsigned spins = 0; spins < kSpinCap; ++spins) {
    unsigned v = tag;
    if (lane < n) asm volatile("global_atomic_add %0, %1, %2, off sc0 sc1\n\ts_waitcnt vmcnt(0)" : "=&v"(v) : "v"(p), "v"(0u) : "memory");
    if (__all(v == tag)) break;
    __builtin_amdgcn_s_sleep(1);
  }
  asm volatile("" ::: "memory");
}
__device__ __forceinline__ void sig_wait(volatile LAS unsigned* p) {
#pragma unroll 1
  for (unsigned spins = 0; spins < 64 * kSpinCap; ++spins) {
    if (*p != 0u) break;
    __builtin_amdgcn_s_sleep(1);
  }
  asm volatile("" ::: "memory");
}
__device__ __forceinline__ void prog_wait(volatile LAS v4u* p, unsigned stage) {
#pragma unroll 1
  for (unsigned spins = 0; spins < 64 * kSpinCap; ++spins) {
    const v4u v = *p;
    if (min(min(v.x, v.y), min(v.z, v.w)) >= stage) break;
    __builtin_amdgcn_s_sleep(1);
  }
  asm volatile("" ::: "memory");
}
__device__ __forceinline__ void publish(gu32* flag, unsigned tag, int lane) {
  DRAIN();
  if (lane == 0) flag_set(flag, tag);
}
__device__ __forceinline__ f32x4 add4(f32x4 a, f32x4 b) {
  f32x4 r;
#pragma unroll
  for (int i = 0; i < 4; ++i) { float x; asm("v_add_f32 %0, %1, %2" : "=v"(x) : "v"(a[i]), "v"(b[i])); r[i] = x; }
  return r;
}
__device__ __forceinline__ float xsum32(float x) {
  unsigned u = __builtin_bit_cast(unsigned, x), v = u;
  asm("" : "+v"(v));
  auto r = __builtin_amdgcn_permlane32_swap(u, v, false, false);
  const unsigned a = r[0], b = r[1];
  return __builtin_bit_cast(float, a) + __builtin_bit_cast(float, b);
}
__device__ __forceinline__ float xsum16(float x) {
  unsigned u = __builtin_bit_cast(unsigned, x), v = u;
  asm("" : "+v"(v));
  auto r = __builtin_amdgcn_permlane16_swap(u, v, false, false);
  const unsigned a = r[0], b = r[1];
  return __builtin_bit_cast(float, a) + __builtin_bit_cast(float, b);
}
__device__ __forceinline__ float xsum8(float x) {
  return x + __builtin_bit_cast(float, __builtin_amdgcn_update_dpp(0, __builtin_bit_cast(int, x), 0x128, 0xf, 0xf, false));
}
__device__ __forceinline__ f32x2 phi2(f32x2 t) {
  const f32x2 u = t * 1.4426950408889634f, v = t + 1e-6f;
  const f32x2 e = {__builtin_amdgcn_fmed3f(__builtin_amdgcn_exp2f(u[0]), 0.f, 1.f), __builtin_amdgcn_fmed3f(__builtin_amdgcn_exp2f(u[1]), 0.f, 1.f)};
  return f32x2{fmaxf(v[0], 1e-6f), fmaxf(v[1], 1e-6f)} + e;
}
__device__ __forceinline__ f32x2 gelu2(f32x2 x) {
  const f32x2 u = x * (x * x * -0.1029432397f + -2.302208198f);
  const f32x2 d = f32x2{__builtin_amdgcn_exp2f(u[0]), __builtin_amdgcn_exp2f(u[1])} + 1.f;
  return x * f32x2{__builtin_amdgcn_rcpf(d[0]), __builtin_amdgcn_rcpf(d[1])};
}

__global__ void __launch_bounds__(512, 2)
fused_block(const unsigned short* __restrict__ x, const unsigned short* __restrict__ wqkv, const unsigned short* __restrict__ wo,
            const unsigned short* __restrict__ w1, const unsigned short* __restrict__ b1, const unsigned short* __restrict__ w2,
            const unsigned short* __restrict__ b2, unsigned short* __restrict__ out, unsigned char* ws) {
  __shared__ __attribute__((aligned(16))) char smem[LDS_BYTES];
  unsigned long long pc0;
  asm volatile("s_getpc_b64 %0" : "=s"(pc0));
  LAS char* L = (LAS char*)smem;
  const unsigned lds0 = (unsigned)(size_t)L;
  const int tid = threadIdx.x, lane = tid & 63, w = __builtin_amdgcn_readfirstlane(tid >> 6);
  const int b = blockIdx.x, xcd = b & 7, bi = b >> 3;
  const int g = lane >> 4, t = lane & 15;
  const __amdgpu_buffer_rsrc_t rs = __builtin_amdgcn_make_buffer_rsrc((void*)ws, 0, WS_BYTES, 0x00020000);
  gu32* flags = (gu32*)(ws + WS_FLAG);
#define FLAG(set, grp) (flags + ((set) * FLAG_SET + (grp) * 256) / 4)
  asm volatile("" :: "s"(x), "s"(wqkv), "s"(wo), "s"(w1), "s"(b1), "s"(w2), "s"(b2), "s"(out), "s"(ws));
  const int r128 = 4 * (xcd >> 1) + (bi >> 3), c16 = 8 * (xcd & 1) + (bi & 7);
  const int r64 = 8 * (xcd >> 1) + (bi >> 2), c8 = 4 * (xcd & 1) + (bi & 3);

  const __amdgpu_buffer_rsrc_t rx = __builtin_amdgcn_make_buffer_rsrc((void*)x, 0, S * D * 2, 0x00020000);
  const int hw = w - 4, ht = tid - 256;
  volatile LAS unsigned* sig = (volatile LAS unsigned*)(L + L_CS + 1024);
  volatile LAS v4u* prog = (volatile LAS v4u*)(L + L_CS + 1088);
#define PROG(n) do { asm volatile("" ::: "memory"); sig[16 + w] = (n); } while (0)

  unsigned tag = __hip_atomic_load((gu32*)(ws + WS_EPOCH), RLX_AGENT);
  const __amdgpu_buffer_rsrc_t rq = __builtin_amdgcn_make_buffer_rsrc((void*)wqkv, 0, D * 3 * D * 2, 0x00020000);
  v4u tv[12];
  const int q10 = lane / 10, c10 = lane % 10;
  if (w < 4) {
#pragma unroll
    for (int j = 0; j < 3; ++j)
      tv[j] = __builtin_amdgcn_raw_buffer_load_b128(rx, (lane & 7) * 1280 + (lane >> 3) * 16, (j < 2 || w < 2) ? 8 * b * 1280 + (w + 4 * j) * 128 : OOB, 0);
  } else if (w == 4) {
    const int vo = sel(lane < 40, q10 * 3840 + (c10 < 5 ? 1280 : 2480) + c10 * 16);
#pragma unroll
    for (int j = 0; j < 10; ++j) tv[j] = __builtin_amdgcn_raw_buffer_load_b128(rq, vo, (40 * r128 + 4 * j) * 3840 + c16 * 80, 0);
  } else if (w == 7) {
    *(LAS v4u*)(L + L_CS + 512 + lane * 16) = v4u{0u, 0u, 0u, 0u};
  }
  BAR();

  if (w < 4) {
  const int grp32 = 4 * r128 + w;
  constexpr int AW = 6;

  {
    const int tok = 8 * b + (lane & 7), cl = lane >> 3;
    const bool has3 = w < 2;
    int xd = ((tok >> 4) * 20 + 2 * w + (cl >> 2)) * 1024 + ((cl & 3) * 16 + (tok & 15)) * 16;
    PIN(xd);
    SCHED();
    f32x2 f[12];
#pragma unroll
    for (int j = 0; j < 3; ++j)
#pragma unroll
      for (int e = 0; e < 4; ++e) f[j * 4 + e] = bf2(tv[j][e]);
    f32x2 s2 = (f[0] + f[1]) + (f[2] + f[3]) + ((f[4] + f[5]) + (f[6] + f[7])) + ((f[8] + f[9]) + (f[10] + f[11]));
    const float mloc = xsum32(xsum16(xsum8(s2[0] + s2[1]))) * (has3 ? 1.f / 192.f : 1.f / 128.f);
    const float m3 = has3 ? mloc : 0.f;
    f32x2 q2 = {0.f, 0.f}, q3 = {0.f, 0.f};
#pragma unroll
    for (int j = 0; j < 12; j += 2) {
      const f32x2 d0 = f[j] - (j < 8 ? mloc : m3), d1 = f[j + 1] - (j < 8 ? mloc : m3);
      q2 += d0 * d0; q3 += d1 * d1;
    }
    q2 += q3;
    const float m2loc = xsum32(xsum16(xsum8(q2[0] + q2[1])));
    if (lane < 8) *(volatile LAS f32x4*)(L + L_CS + 512 + (lane * 4 + w) * 16) = f32x4{mloc, m2loc, 1.f, 1.f};
    f32x4 pr[4];
#pragma unroll 1
    for (unsigned spins = 0; spins < 64 * kSpinCap; ++spins) {
      bool ok = true;
#pragma unroll
      for (int ww = 0; ww < 4; ++ww) {
        pr[ww] = *(volatile LAS f32x4*)(L + L_CS + 512 + ((lane & 7) * 4 + ww) * 16);
        ok = ok && pr[ww][2] == 1.f;
      }
      if (__all(ok)) break;
    }
    const f32x4 p01 = {pr[0][0], pr[0][1], pr[1][0], pr[1][1]}, p23 = {pr[2][0], pr[2][1], pr[3][0], pr[3][1]};
    const float mean = (p01[0] + p01[2]) * 0.3f + (p23[0] + p23[2]) * 0.2f;
    const float e0 = p01[0] - mean, e1 = p01[2] - mean, e2 = p23[0] - mean, e3 = p23[2] - mean;
    const float m2 = (p01[1] + p01[3]) + (p23[1] + p23[3]) + 192.f * (e0 * e0 + e1 * e1) + 128.f * (e2 * e2 + e3 * e3);
    const float rstd = __builtin_amdgcn_rsqf(m2 * (1.f / D) + LN_EPS);
#pragma unroll
    for (int j = 0; j < 3; ++j) {
      v4u o;
#pragma unroll
      for (int e = 0; e < 4; ++e) o[e] = pk2((f[j * 4 + e] - mean) * rstd);
      st128(o, rs, (j < 2 || has3) ? xd : OOB, WS_XN + j * 8192);
    }
    PIN(tag); tag += 1u;
    publish(FLAG(0, b >> 2) + (b & 3) * 4 + w, tag, lane);
  }

  v4u xr[3];
  {
    wait_flag(FLAG(0, grp32) + lane, 16, tag, lane);
    v4u a[20][2];
    const int abase = WS_XN + 2 * grp32 * 20 * 1024;
#pragma unroll
    for (int ks = 0; ks < AW; ++ks)
#pragma unroll
      for (int m = 0; m < 2; ++m) a[ks][m] = ld128(rs, lane * 16, abase + (m * 20 + ks) * 1024);
    SCHED();
    BAR_RAW();
    {
      f32x4 acc[2][5];
      const LBase bp(lds0 + L_BKV + (lane >> 2) * 160 + (lane & 3) * 8);
      kloop<2, 5, 20, 1, false>(acc, [&](int ks, int s, int h) { return bp.rd(ks * 5120 + h * 2560 + s * 32); },
        [&](int ks, int m) { return a[ks][m]; },
        [&](int ks) {
#pragma unroll
          for (int j = ld_hi(ks - 1, AW, AW, 20); j < ld_hi(ks, AW, AW, 20); ++j)
#pragma unroll
            for (int m = 0; m < 2; ++m) a[j][m] = ld128(rs, lane * 16, abase + (m * 20 + j) * 1024);
          if (ks == 3 || ks == 10) BAR_RAW();
          if (ks == 20 - AW) PROG(1);
        });
      v4u ka[3], vb[3];
#pragma unroll
      for (int s = 0; s < 3; ++s)
#pragma unroll
        for (int m = 0; m < 2; ++m) {
          vb[s][2 * m] = pk2(lo2(acc[m][s + 2])); vb[s][2 * m + 1] = pk2(hi2(acc[m][s + 2]));
        }
#pragma unroll
      for (int e = 0; e < 4; ++e) vb[0][e] = t < 8 ? 0x3F803F80u : vb[0][e];
      const f32x4 z4 = {0.f, 0.f, 0.f, 0.f};
      LAS f32x4* red = (LAS f32x4*)(L + L_RED) + w * 512 + lane;
#pragma unroll
      for (int s = 0; s < 3; ++s) {
#pragma unroll
        for (int m = 0; m < 2; ++m) { ka[s][2 * m] = pk2(phi2(lo2(acc[m][s]))); ka[s][2 * m + 1] = pk2(phi2(hi2(acc[m][s]))); }
#pragma unroll
        for (int sb = 0; sb < 3; ++sb)
          if (s > 0 || sb < 2) red[(3 * s + sb - (s > 0)) * 64] = mfma(ka[s], vb[sb], z4);
      }
      BAR();
    }
    f32x4 qa[2][3];
    {
      unsigned bp0 = lds0 + L_BQ + (lane >> 2) * 64 + (lane & 3) * 8 + (g & 1) * 32;
      unsigned bp1 = lds0 + L_BQ + (lane >> 2) * 64 + (lane & 3) * 8 + 32 - (g & 1) * 32;
      unsigned bp2 = lds0 + L_BQ + 2048 + (lane >> 2) * 16 + (lane & 1) * 8;
      PIN(bp0); PIN(bp1); PIN(bp2);
      kloop<2, 3, 20, 2, true>(qa,
        [&](int ks, int s, int h) {
          return s == 0 ? rdtr(lp(bp0) + ks * 2560 + h * 1024) : s == 1 ? rdtr(lp(bp1) + ks * 2560 + h * 1024) : rdtr(lp(bp2) + ks * 2560 + h * 256);
        },
        [&](int ks, int m) { return a[ks][m]; },
        [&](int ks) {});
    }
    v4u pq0[2], pq1[2];
#pragma unroll
    for (int m = 0; m < 2; ++m) {
      pq0[m].x = pk2(phi2(lo2(qa[m][0]))); pq0[m].y = pk2(phi2(hi2(qa[m][0])));
      pq0[m].z = pk2(phi2(lo2(qa[m][1]))); pq0[m].w = pk2(phi2(hi2(qa[m][1])));
      const unsigned lo = pk2(phi2(lo2(qa[m][2]))), hi = pk2(phi2(hi2(qa[m][2])));
      pq1[m].x = g < 2 ? lo : 0u; pq1[m].y = g < 2 ? hi : 0u; pq1[m].z = 0u; pq1[m].w = 0u;
      PIN(pq0[m]); PIN(pq1[m]);
    }
    BAR_RAW();
#pragma unroll
    for (int j = 0; j < 3; ++j)
      xr[j] = __builtin_amdgcn_raw_buffer_load_b128(rx, sel(j < 2 || g < 2, t * 1280 + g * 16), (4 * r64 + w) * 16 * 1280 + c8 * 160 + j * 64, 0);
    {
      v4u kv[4][2];
#pragma unroll
      for (int es = 3; es >= 0; --es)
#pragma unroll
        for (int ks = 0; ks < 2; ++ks) kv[es][ks] = *(LAS v4u*)(L + L_KVF + (es * 2 + ks) * 1024 + lane * 16);
      SCHED();
      f32x4 o[2][4];
#pragma unroll
      for (int es = 3; es >= 0; --es)
#pragma unroll
        for (int m = 0; m < 2; ++m) o[m][es] = mfma(kv[es][0], pq0[m], f32x4{0.f, 0.f, 0.f, 0.f});
#pragma unroll
      for (int es = 3; es >= 0; --es)
#pragma unroll
        for (int m = 0; m < 2; ++m) o[m][es] = mfma(kv[es][1], pq1[m], o[m][es]);
#pragma unroll
      for (int m = 0; m < 2; ++m) {
        const float i0 = __builtin_amdgcn_rcpf(o[m][3][0] + 1e-6f), i1 = __builtin_amdgcn_rcpf(o[m][3][1] + 1e-6f);
        const float im = g == 0 ? i0 : i1;
        v4u ov;
        ov.x = pk2(lo2(o[m][0]) * i0); ov.y = pk2(hi2(o[m][0]) * i0);
        ov.z = pk2(lo2(o[m][1]) * im); ov.w = pk2(hi2(o[m][1]) * im);
        v2u ol; ol.x = pk2(lo2(o[m][2]) * i1); ol.y = pk2(hi2(o[m][2]) * i1);
        const int tt = 2 * grp32 + m;
        st128(ov, rs, lane * 16, WS_O + (tt * 20 + c16) * 1024);
        st64(ol, rs, g < 2 ? lane * 8 : OOB, WS_O + (tt * 20 + 16) * 1024 + c16 * 256);
      }
      publish(FLAG(2, grp32) + c16, tag, lane);
    }
  }

  {
    const int tt = 4 * r64 + w;
    v4u eye[2];
#pragma unroll
    for (int hf = 0; hf < 2; ++hf) {
      const int e = 16 * hf + t - 8 * g;
#pragma unroll
      for (int d = 0; d < 4; ++d) eye[hf][d] = e == 2 * d ? 0x3F80u : e == 2 * d + 1 ? 0x3F800000u : 0u;
      PIN(eye[hf]);
    }
    f32x4 acc[1][5];
#pragma unroll
    for (int s = 0; s < 5; ++s) { acc[0][s] = mfma(eye[s & 1], xr[s >> 1], f32x4{0.f, 0.f, 0.f, 0.f}); PIN(acc[0][s]); }
    wait_flag(FLAG(2, tt >> 1) + lane, 16, tag, lane);
    if (b == 0 && tid == 0) __hip_atomic_store((gu32*)(ws + WS_EPOCH), tag, RLX_AGENT);
    v4u a[20];
#pragma unroll
    for (int ks = 0; ks < 2 * AW; ++ks) a[ks] = ld128(rs, lane * 16, WS_O + (tt * 20 + ks) * 1024);
    SCHED();
    const LBase bp(lds0 + L_WO + (lane >> 2) * 160 + (lane & 3) * 8);
    kloop<1, 5, 20, 1, true, 1>(acc, [&](int ks, int s, int h) { return bp.rd(ks * 5120 + h * 2560 + s * 32); },
      [&](int ks, int m) { return a[ks]; },
      [&](int ks) {
#pragma unroll
        for (int j = ld_hi(ks - 1, 2 * AW, 2 * AW, 20); j < ld_hi(ks, 2 * AW, 2 * AW, 20); ++j)
          a[j] = j < 16 ? ld128(rs, lane * 16, WS_O + (tt * 20 + j) * 1024) : ld_split(rs, lane, WS_O + (tt * 20 + j) * 1024);
        if (ks == 8) PROG(2);
        if (ks == 14) PROG(3);
      });
    PROG(4);
    v2u hb[5];
    f32x2 hv[10];
#pragma unroll
    for (int s = 0; s < 5; ++s) {
      hb[s].x = pk2(lo2(acc[0][s]));
      hb[s].y = pk2(hi2(acc[0][s]));
      hv[2 * s] = bf2(hb[s].x); hv[2 * s + 1] = bf2(hb[s].y);
    }
    st128(v4u{hb[0].x, hb[0].y, hb[1].x, hb[1].y}, rs, lane * 16, WS_H + (tt * 20 + 2 * c8) * 1024);
    st128(v4u{hb[2].x, hb[2].y, hb[3].x, hb[3].y}, rs, lane * 16, WS_H + (tt * 20 + 2 * c8 + 1) * 1024);
    st64(hb[4], rs, lane * 8, WS_H + (tt * 20 + 16) * 1024 + c8 * 512);
    const f32x2 s2 = ((hv[0] + hv[1]) + (hv[2] + hv[3])) + ((hv[4] + hv[5]) + (hv[6] + hv[7])) + (hv[8] + hv[9]);
    const float mc = xsum32(xsum16(s2[0] + s2[1])) * (1.f / 80.f);
    f32x2 q2 = {0.f, 0.f}, q3 = {0.f, 0.f};
#pragma unroll
    for (int j = 0; j < 10; j += 2) { const f32x2 d0 = hv[j] - mc, d1 = hv[j + 1] - mc; q2 += d0 * d0; q3 += d1 * d1; }
    q2 += q3;
    const float m2 = xsum32(xsum16(q2[0] + q2[1]));
    st64(v2u{__builtin_bit_cast(unsigned, mc), __builtin_bit_cast(unsigned, m2)}, rs, g == 0 ? t * 8 : OOB, WS_ST + (tt * 8 + c8) * 128);
    publish(FLAG(3, tt >> 1) + (tt & 1) * 8 + c8, tag, lane);
  }

  {
    wait_flag(FLAG(3, grp32) + lane, 16, tag, lane);
    v4u stt[2];
#pragma unroll
    for (int m = 0; m < 2; ++m) {
      const v2u s0 = ld64(rs, g * 256 + t * 8, WS_ST + (2 * grp32 + m) * 1024), s1 = ld64(rs, g * 256 + 128 + t * 8, WS_ST + (2 * grp32 + m) * 1024);
      stt[m] = v4u{s0.x, s0.y, s1.x, s1.y};
    }
    v4u a[20][2];
    const int abase = WS_H + 2 * grp32 * 20 * 1024;
#pragma unroll
    for (int ks = 0; ks < AW; ++ks)
#pragma unroll
      for (int m = 0; m < 2; ++m) a[ks][m] = ld128(rs, lane * 16, abase + (m * 20 + ks) * 1024);
    SCHED();
    float nm[2], rr[2];
#pragma unroll
    for (int m = 0; m < 2; ++m) {
      const f32x4 sv = __builtin_bit_cast(f32x4, stt[m]);
      const float mean = xsum32(xsum16(sv[0] + sv[2])) * 0.125f;
      const float m2 = xsum32(xsum16(sv[1] + sv[3] + 80.f * ((sv[0] - mean) * (sv[0] - mean) + (sv[2] - mean) * (sv[2] - mean))));
      const float var = m2 * (1.f / D);
      const float r1 = __builtin_amdgcn_rsqf(var + LN_EPS);
      rr[m] = r1 * __builtin_amdgcn_rsqf(var * r1 * r1 + LN_EPS);
      nm[m] = -mean * rr[m];
      PIN(rr[m]); PIN(nm[m]);
    }
    BAR_RAW();
    const unsigned l64 = lds0 + (lane >> 2) * 64 + (lane & 3) * 8;
    const LBase bp0(l64 + (g & 1) * 32), bp1(l64 + 32 - (g & 1) * 32);
    const int h1base = WS_H1 + 2 * grp32 * 48 * 1024;
    auto finish = [&](const f32x4& c0, const f32x4& c1, int j, int m) {
      v4u o;
#pragma unroll
      for (int s = 0; s < 2; ++s) {
        const f32x4 cs = *(LAS f32x4*)(L + L_CS + ((2 * j + s) * 4 + g) * 16), bb = *(LAS f32x4*)(L + L_B1 + ((2 * j + s) * 4 + g) * 16);
        const f32x4 c = s ? c1 : c0;
        o[2 * s] = pk2(gelu2(lo2(c) * rr[m] + (lo2(cs) * nm[m] + lo2(bb))));
        o[2 * s + 1] = pk2(gelu2(hi2(c) * rr[m] + (hi2(cs) * nm[m] + hi2(bb))));
      }
      st128(o, rs, lane * 16, h1base + (m * 48 + (c16 >> 3) * 24 + (j < 2 ? 2 * (c16 & 7) + j : 16 + (c16 & 7))) * 1024);
    };
    {
      f32x4 acc[2][4];
      kloop<2, 4, 20, 1, true>(acc, [&](int ks, int s, int h) { return ((s & 1) ? bp1 : bp0).rd(L_W1 + (s >> 1) * 40960 + ks * 2048 + h * 1024); },
        [&](int ks, int m) { return a[ks][m]; },
        [&](int ks) {
#pragma unroll
          for (int j = ld_hi(ks - 1, AW, AW, 20); j < ld_hi(ks, AW, AW, 20); ++j)
#pragma unroll
            for (int m = 0; m < 2; ++m)
              a[j][m] = j < 16 ? ld128(rs, lane * 16, abase + (m * 20 + j) * 1024) : ld_split(rs, lane, abase + (m * 20 + j) * 1024);
          if (ks == 20 - AW) PROG(5);
        });
      PROG(6);
#pragma unroll
      for (int j = 0; j < 2; ++j)
#pragma unroll
        for (int m = 0; m < 2; ++m) finish(acc[m][2 * j], acc[m][2 * j + 1], j, m);
      SCHED();
    }
    f32x4 accb[2][2];
    const int kh = w & 1, tt = 4 * r64 + w;
    v4u hr01, hr23;
    v2u hr4;
    kloop<2, 2, 20, 3, true>(accb, [&](int ks, int s, int h) { return (s ? bp1 : bp0).rd(L_W1 + 81920 + ks * 2048 + h * 1024); },
      [&](int ks, int m) { return a[ks][m]; },
      [&](int ks) {
        if (ks == 8) { publish(FLAG(4, grp32) + c16, tag, lane); PROG(7); }
        if (ks == 10) {
          hr01 = ld128(rs, lane * 16, WS_H + (tt * 20 + 2 * c8) * 1024);
          hr23 = ld128(rs, lane * 16, WS_H + (tt * 20 + 2 * c8 + 1) * 1024);
          hr4 = ld64(rs, lane * 8, WS_H + (tt * 20 + 16) * 1024 + c8 * 512);
        }
      });
    f32x4 acc[2][5];
    {
      const unsigned hrr[10] = {hr01.x, hr01.y, hr01.z, hr01.w, hr23.x, hr23.y, hr23.z, hr23.w, hr4.x, hr4.y};
#pragma unroll
      for (int s = 0; s < 5; ++s) {
        acc[0][s] = *(LAS f32x4*)(L + L_B2 + (s * 4 + g) * 16) + f32x4{bf_lo(hrr[2 * s]), bf_hi(hrr[2 * s]), bf_lo(hrr[2 * s + 1]), bf_hi(hrr[2 * s + 1])};
        PIN(acc[0][s]);
      }
    }
    int oo[3];
    unsigned tw, tr;
    {
      int ln = lane;
      PIN(ln);
      const int qq = ln / 10, cc = ln % 10;
#pragma unroll
      for (int j = 0; j < 3; ++j) { oo[j] = sel(ln < (j < 2 ? 60 : 40), ((tt * 16 + 6 * j + qq) * D + 80 * c8) * 2 + cc * 16); PIN(oo[j]); }
      tw = lds0 + L_W2 + 122880 + w * 3072 + t * 176 + g * 8; tr = lds0 + L_W2 + 122880 + w * 3072 + qq * 176 + cc * 16;
      PIN(tw); PIN(tr);
    }
    BAR_RAW();
    constexpr int WIN = 8;
    v4u ar[WIN][2];
    const int a4[2] = {WS_H1 + (tt * 48 + kh * 24) * 1024, WS_H1 + ((tt ^ 1) * 48 + kh * 24) * 1024};
#pragma unroll
    for (int ks = 0; ks < WIN - 1; ++ks)
#pragma unroll
      for (int m = 0; m < 2; ++m) ar[ks][m] = ld128(rs, lane * 16, a4[m] + ks * 1024);
    SCHED();
#pragma unroll
    for (int m = 0; m < 2; ++m) finish(accb[m][0], accb[m][1], 2, m);
    const LBase bp(lds0 + L_W2 + kh * 5120 + (lane >> 2) * 160 + (lane & 3) * 8);
    kloop<2, 5, 24, 1, true, 1>(acc, [&](int ks, int s, int h) { return bp.rd((ks % 15) * 10240 + h * 2560 + s * 32); },
      [&](int ks, int m) { return ar[ks % WIN][m]; },
      [&](int ks) {
        if (ks == 5) PROG(8);
        if (ks == 9 || ks == 14 || ks == 19) BAR_RAW();
        if (ks == 3) publish(FLAG(10, grp32) + c16, tag, lane);
        if (ks >= 3) {
#pragma unroll
          for (int j = ks == 3 ? WIN - 1 : ld_hi(ks - 1, 1, WIN - 1, 24); j < ld_hi(ks, 1, WIN - 1, 24); ++j)
#pragma unroll
            for (int m = 0; m < 2; ++m) ar[j % WIN][m] = ld128(rs, lane * 16, a4[m] + j * 1024);
        }
      });
    LAS f32x4* xch = (LAS f32x4*)(L + L_W2 + 102400);
#pragma unroll
    for (int s = 0; s < 5; ++s) xch[((w ^ 1) * 5 + s) * 64 + lane] = acc[1][s];
    BAR();
    const __amdgpu_buffer_rsrc_t rout = __builtin_amdgcn_make_buffer_rsrc((void*)out, 0, S * D * 2, 0x00020000);
    f32x4 ot[5];
#pragma unroll
    for (int s = 0; s < 5; ++s) ot[s] = xch[(w * 5 + s) * 64 + lane];
    SCHED();
#pragma unroll
    for (int s = 0; s < 5; ++s) {
      const f32x4 v = acc[0][s] + ot[s];
      *(LAS v2u*)(lp(tw) + s * 32) = v2u{pk2(lo2(v)), pk2(hi2(v))};
    }
    asm volatile("" ::: "memory");
    v4u ov[3];
#pragma unroll
    for (int j = 0; j < 3; ++j) ov[j] = *(LAS v4u*)(lp(tr) + j * 6 * 176);
#pragma unroll
    for (int j = 0; j < 3; ++j) __builtin_amdgcn_raw_buffer_store_b128(ov[j], rout, oo[j], 0, 0);
  }
  return;
  }

  {
    const v4u srd_ws = mk_srd(ws, WS_BYTES);
    const int q5 = lane / 5, c5 = lane % 5;
    const int f40m = c5 < 4 ? 512 : 128, f40l = c5 < 4 ? q5 * 64 + (c5 ^ (2 * ((q5 >> 2) & 1))) * 16 : 2048 + q5 * 16;
    v2u ub1, ub2;
    unsigned pf0 = 0;
    if (hw == 0) {
      const int dof = sel(lane < 40, lane * 16);
#pragma unroll
      for (int j = 0; j < 10; ++j) {
        const int k = 40 * r128 + 4 * j, R = (k & ~31) + 16 * ((k >> 2) & 1) + 4 * ((k >> 3) & 3);
        st128(tv[j], rs, dof, WS_TKV + c16 * 153600 + R * 160);
      }
      PIN(tag); tag += 1u;
      publish(FLAG(6, c16) + r128, tag, lane);
    } else if (hw == 1) {
      const __amdgpu_buffer_rsrc_t ro = __builtin_amdgcn_make_buffer_rsrc((void*)wo, 0, D * D * 2, 0x00020000);
      const int vq = sel(lane < 40, (8 * (q5 >> 2) + (q5 & 3)) * 3840 + c5 * 16), vo = sel(lane < 40, q10 * 1280 + c10 * 16);
#pragma unroll
      for (int j = 0; j < 5; ++j) {
        const int i8 = 5 * r128 + j, k = 32 * (i8 >> 2) + 16 * (i8 & 1) + 4 * ((i8 >> 1) & 1);
        tv[j] = __builtin_amdgcn_raw_buffer_load_b128(rq, vq, k * 3840 + c16 * 80, 0);
      }
#pragma unroll
      for (int j = 0; j < 5; ++j) tv[5 + j] = __builtin_amdgcn_raw_buffer_load_b128(ro, vo, (20 * r64 + 4 * j) * 1280 + c8 * 160, 0);
    } else if (hw == 2) {
      const GAS unsigned char* cb = (const GAS unsigned char*)(pc0 & ~127ull);
      if (lane < 11) pf0 = *(const GAS unsigned*)(cb + (lane * 32 + bi) * 128);
      if (lane < 6) {
        gu32* fp = FLAG(3 * (b >> 6) + (lane >> 1), b & 63) + (lane & 1) * 32;
        asm volatile("global_atomic_add %0, %1, off sc1" :: "v"(fp), "v"(0u) : "memory");
      }
    } else if (hw == 3) {
      const __amdgpu_buffer_rsrc_t rb1 = __builtin_amdgcn_make_buffer_rsrc((void*)b1, 0, F * 2, 0x00020000);
      const __amdgpu_buffer_rsrc_t rb2 = __builtin_amdgcn_make_buffer_rsrc((void*)b2, 0, D * 2, 0x00020000);
      ub1 = __builtin_amdgcn_raw_buffer_load_b64(rb1, lane * 8, c16 * 192, 0);
      ub2 = __builtin_amdgcn_raw_buffer_load_b64(rb2, lane * 8, c8 * 160, 0);
    }
    asm volatile("" ::: "memory");
    int red_off, kvf_off, kvp_off;
    auto items = [&]() {
      const int it = ht < 210 ? ht : 209, ie = it / 5, ic = it % 5;
      const int ihead = ie < 40 ? (ie >= 20) : ie - 40;
      const int id = 20 * ihead + 4 * ic, dg = id >> 2;
      const int sa = id >> 4, col = 40 + ie;
      const int tile = 3 * sa + (ie < 40 ? (col >> 4) - 2 : 0) - (sa > 0);
      red_off = lds0 + L_RED + (tile * 64 + (dg & 3) * 16 + (ie < 40 ? (col & 15) : 0)) * 16;
      kvf_off = lds0 + L_KVF + ((ie < 40 ? (ie >> 4) : 3) * 2 + (dg >> 3)) * 1024 + ((dg & 3) * 16 + (ie < 40 ? (ie & 15) : ihead)) * 16 + ((dg >> 2) & 1) * 8;
      kvp_off = WS_KVP + (c16 * 16 + r128) * 4096 + ht * 16;
      PIN(red_off); PIN(kvf_off); PIN(kvp_off);
    };
    if (hw != 0) items();
    int vo12, td[12];
    if (hw >= 2) {
      const int q12 = lane / 12, c12 = lane % 12;
      vo12 = hw == 2 ? sel(lane < 48, q12 * 3072 + c12 * 16) : sel(lane < 40, q10 * 1280 + c10 * 16);
#pragma unroll
      for (int j = 0; j < 12; ++j) {
        const int cc = r128 >> 1, off = 40 * (r128 & 1) + 4 * j;
        const int R = off < 64 ? 64 * cc + off : 32 * (16 + (cc >> 1)) + 16 * (cc & 1) + off - 64;
        const int d1 = WS_T1 + c16 * 122880 + (c12 >> 2) * 40960 + (R + q12) * 64 + ((c12 & 3) ^ (2 * ((R >> 2) & 1))) * 16;
        const int pc = r64 >> 1, ko = 48 * (r64 & 1) + 4 * j, jj = ko >> 5;
        const int d2 = WS_T2 + c8 * 245760 + (2 * (jj < 2 ? 2 * (pc & 7) + jj : 16 + (pc & 7)) + (pc >> 3)) * 5120 + (ko & 31) * 160 + lane * 16;
        td[j] = sel(hw == 2 ? (lane < 48 && j < 10) : lane < 40, hw == 2 ? d1 : d2);
        PIN(td[j]);
      }
      PIN(vo12);
    }
    int l16 = lane * 16, h16 = sel(ht < 210, ht * 16);
    PIN(l16); PIN(h16);
    gu32* fp1 = FLAG(1, c16) + lane;
    gu32* fp78 = lane < 32 ? FLAG(7, c8) + lane : FLAG(8, c16) + t;
    gu32* fp4 = FLAG(4, 2 * r64 + (g & 1)) + t;
    gu32* fp1w = FLAG(1, c16) + r128 * 4 + hw;
    PIN(fp1); PIN(fp78); PIN(fp4); PIN(fp1w);
    ((LAS v4u*)(L + L_KVF))[ht] = v4u{0u, 0u, 0u, 0u};
    ((LAS v4u*)(L + L_KVF))[256 + ht] = v4u{0u, 0u, 0u, 0u};
    asm volatile("" ::: "memory");
    if (hw != 0) {
      if (hw == 1) {
#pragma unroll
        for (int j = 0; j < 5; ++j) {
          const int i8 = 5 * r128 + j;
          st128(tv[j], rs, sel(lane < 40, (i8 & 3) * f40m + f40l), WS_TKV + c16 * 153600 + 102400 + (i8 >> 2) * 2560);
        }
#pragma unroll
        for (int j = 0; j < 5; ++j) {
          const int pp = r64 >> 1, e = 20 * (r64 & 1) + 4 * j;
          const int R = e < 32 ? 32 * pp + e : 32 * (16 + (pp >> 2)) + 8 * (pp & 3) + e - 32;
          st128(tv[5 + j], rs, sel(lane < 40, lane * 16), WS_TO + c8 * 102400 + R * 160);
        }
      }
      if (hw == 3) {
        if (lane < 24) *(LAS f32x4*)(L + L_B1 + lane * 16) = f32x4{bf_lo(ub1.x), bf_hi(ub1.x), bf_lo(ub1.y), bf_hi(ub1.y)};
        if (lane < 20) *(LAS f32x4*)(L + L_B2 + lane * 16) = f32x4{bf_lo(ub2.x), bf_hi(ub2.x), bf_lo(ub2.y), bf_hi(ub2.y)};
      }
      asm volatile("" :: "v"(pf0));
      PIN(tag); tag += 1u;
      DRAIN();
      if (hw == 1 && lane < 2) flag_set(lane == 0 ? FLAG(8, c16) + r128 : FLAG(7, c8) + r64, tag);
    }
    if (hw == 0) {
      wait_flag(FLAG(6, c16) + lane, 16, tag, lane);
      sig[0] = 1u;
    }
    sig_wait(sig);
    const int kvq = WS_TKV + c16 * 153600, wos = WS_TO + c8 * 102400;
    dma_image<0, 24>(lds0 + L_BKV, l16, srd_ws, kvq, hw, 3 * (bi >> 3));
    dma_image<24, 56>(lds0 + L_BKV, l16, srd_ws, kvq, hw, 4 * (bi >> 3));
    asm volatile("s_waitcnt vmcnt(8)" ::: "memory");
    BAR_RAW();
    dma_image<56, 100>(lds0 + L_BKV, l16, srd_ws, kvq, hw, 5 * (bi >> 3));
    asm volatile("s_waitcnt vmcnt(10)" ::: "memory");
    BAR_RAW();
    if (hw == 1) wait_flag(fp78, 48, tag, lane);
    DRAIN();
    BAR_RAW();
    dma_image<100, 150>(lds0 + L_BKV, l16, srd_ws, kvq, hw, 6 * (bi >> 3));
    if (hw == 0) items();
    prog_wait(prog, 1);
    if (hw >= 2) {
      const __amdgpu_buffer_rsrc_t r12 = __builtin_amdgcn_make_buffer_rsrc((void*)(hw == 2 ? w1 : w2), 0, D * F * 2, 0x00020000);
      const int base = hw == 2 ? r128 * 122880 + c16 * 192 : r64 * 61440 + c8 * 160, step = hw == 2 ? 4 * 3072 : 4 * 1280;
#pragma unroll
      for (int j = 0; j < 12; ++j) tv[j] = __builtin_amdgcn_raw_buffer_load_b128(r12, vo12, (j < 10 || hw == 3) ? base + j * step : OOB, 0);
      asm volatile("s_waitcnt vmcnt(12)" ::: "memory");
    } else {
      dma_image<32, 68, 2>(lds0 + L_WO, l16, srd_ws, wos, hw, bi >> 2);
      asm volatile("s_waitcnt vmcnt(18)" ::: "memory");
    }
    BAR();
    __builtin_amdgcn_s_setprio(2);
    {
      f32x4 pv[4];
#pragma unroll
      for (int ww = 0; ww < 4; ++ww) pv[ww] = *(LAS f32x4*)(lp(red_off) + ww * 8192);
      st128(__builtin_bit_cast(v4u, add4(add4(pv[0], pv[1]), add4(pv[2], pv[3]))), rs, kvp_off, 0);
      PROG(1);
      DRAIN();
    }
    if (lane == 0) flag_set(fp1w, tag);
    if (hw == 0) {
      __builtin_amdgcn_s_sleep(6);
      wait_flag(fp1, 64, tag, lane);
      sig[2] = 1u;
    } else {
      __builtin_amdgcn_s_sleep(2);
      dma_image<68, 100, 3>(lds0 + L_WO, l16, srd_ws, wos, hw - 1, bi >> 2);
      prog_wait(prog + 1, 1);
      dma_image<0, 32, 3>(lds0 + L_WO, l16, srd_ws, wos, hw - 1, 2 * (bi >> 2));
      if (hw >= 2) {
#pragma unroll
        for (int j = 0; j < 12; ++j) st128(tv[j], rs, td[j], 0);
      }
      sig_wait(sig + 2);
    }
    {
      v4u pv[16];
#pragma unroll
      for (int r = 0; r < 16; ++r) pv[r] = ld128(rs, h16, WS_KVP + (c16 * 16 + r) * 4096);
      SCHED();
      f32x4 v = __builtin_bit_cast(f32x4, pv[0]);
#pragma unroll
      for (int r = 1; r < 16; ++r) v = add4(v, __builtin_bit_cast(f32x4, pv[r]));
      v2u o; o.x = pk2(lo2(v)); o.y = pk2(hi2(v));
      if (ht < 210) *(LAS v2u*)(lp(kvf_off)) = o;
      if (ht >= 200 && ht < 210) {
#pragma unroll
        for (int c = 1; c < 4; ++c) *(LAS v2u*)(lp(kvf_off) + c * 64) = o;
      }
    }
    DRAIN();
    BAR();
    __builtin_amdgcn_s_setprio(0);
    if (hw >= 2 && lane == 0) flag_set(hw == 2 ? FLAG(5, c16) + r128 : FLAG(11, c8) + r64, tag);
    if (hw == 2) {
      f32x4 s0 = {0.f, 0.f, 0.f, 0.f}, s1 = {0.f, 0.f, 0.f, 0.f};
#pragma unroll
      for (int j = 0; j < 10; ++j) {
        s0 = add4(s0, f32x4{bf_lo(tv[j].x), bf_hi(tv[j].x), bf_lo(tv[j].y), bf_hi(tv[j].y)});
        s1 = add4(s1, f32x4{bf_lo(tv[j].z), bf_hi(tv[j].z), bf_lo(tv[j].w), bf_hi(tv[j].w)});
      }
#pragma unroll
      for (int d = 24; d >= 12; d -= 12) {
        f32x4 u0, u1;
#pragma unroll
        for (int e = 0; e < 4; ++e) { u0[e] = __shfl(s0[e], lane + d); u1[e] = __shfl(s1[e], lane + d); }
        s0 = add4(s0, u0); s1 = add4(s1, u1);
      }
      f32x4 sv;
#pragma unroll
      for (int e = 0; e < 4; ++e) { const float a0 = __shfl(s0[e], lane >> 1), a1 = __shfl(s1[e], lane >> 1); sv[e] = (lane & 1) ? a1 : a0; }
      st128(__builtin_bit_cast(v4u, sv), rs, sel(lane < 24, lane * 16), WS_CS + (c16 * 16 + r128) * 384);
      publish(FLAG(9, c16) + r128, tag, lane);
    }
    if (hw == 1) {
      __builtin_amdgcn_s_sleep(40);
      wait_flag(lane < 32 ? FLAG(11, c8) + lane : FLAG(5, c16) + t, 48, tag, lane);
      sig[3] = 1u;
    }
    sig_wait(sig + 3);
    prog_wait(prog, 2);
    if (hw == 0) {
      wait_flag(FLAG(9, c16) + lane, 16, tag, lane);
      v4u pv[16];
      const int co = sel(lane < 24, l16);
#pragma unroll
      for (int r = 0; r < 16; ++r) pv[r] = ld128(rs, co, WS_CS + (c16 * 16 + r) * 384);
      SCHED();
      f32x4 v = __builtin_bit_cast(f32x4, pv[0]);
#pragma unroll
      for (int r = 1; r < 16; ++r) v = add4(v, __builtin_bit_cast(f32x4, pv[r]));
      if (lane < 24) *(LAS f32x4*)(L + L_CS + lane * 16) = v;
    } else {
      dma_image<70, 120, 3>(lds0 + L_W1, l16, srd_ws, WS_T1 + c16 * 122880, hw - 1, 6 * (bi >> 3));
      prog_wait(prog, 3);
      dma_image<0, 40, 3>(lds0 + L_W1, l16, srd_ws, WS_T1 + c16 * 122880, hw - 1, 5 * (bi >> 3));
      prog_wait(prog, 4);
      dma_image<40, 70, 3>(lds0 + L_W1, l16, srd_ws, WS_T1 + c16 * 122880, hw - 1, 3 * (bi >> 3));
    }
    DRAIN();
    BAR();
    const int w2s = WS_T2 + c8 * 245760;
    prog_wait(prog, 5);
    dma_image<0, 30>(lds0 + L_W2, l16, srd_ws, w2s, hw, bi >> 2);
    prog_wait(prog, 6);
    dma_image<30, 70>(lds0 + L_W2, l16, srd_ws, w2s, hw, bi >> 2);
    prog_wait(prog, 7);
    if (hw != 0) dma_image<70, 110, 3>(lds0 + L_W2, l16, srd_ws, w2s, hw - 1, bi >> 2);
    DRAIN();
    if (hw == 0) wait_flag(fp4, 32, tag, lane);
    BAR_RAW();
    dma_image<110, 150>(lds0 + L_W2, l16, srd_ws, w2s, hw, 2 * (bi >> 2));
    prog_wait(prog, 8);
    if (hw == 0) wait_flag(fp4 + 6 * FLAG_SET / 4, 32, tag, lane);
    else dma_image<150, 198, 3>(lds0 + L_W2 - 153600, l16, srd_ws, w2s, hw - 1, 3 * (bi >> 2));
    asm volatile("s_waitcnt vmcnt(16)" ::: "memory");
    BAR_RAW();
    dma_image<198, 200>(lds0 + L_W2 - 153600, l16, srd_ws, w2s, hw, 0);
    dma_image<200, 240>(lds0 + L_W2 - 153600, l16, srd_ws, w2s, hw, 2 * (bi >> 2));
    asm volatile("s_waitcnt vmcnt(10)" ::: "memory");
    BAR_RAW();
    DRAIN();
    BAR_RAW();
  }

}
}

extern "C" void kernel_launch(void* const* d_in, const int* in_sizes, int n_in, void* d_out, int out_size, void* d_ws,
                              size_t ws_size, hipStream_t stream) {
  const unsigned short* x = (const unsigned short*)d_in[0];
  const unsigned short* wqkv = (const unsigned short*)d_in[1];
  const unsigned short* wo = (const unsigned short*)d_in[2];
  const unsigned short* w1 = (const unsigned short*)d_in[4];
  const unsigned short* b1 = (const unsigned short*)d_in[5];
  const unsigned short* w2 = (const unsigned short*)d_in[6];
  const unsigned short* b2 = (const unsigned short*)d_in[7];
  fused_block<<<256, 512, 0, stream>>>(x, wqkv, wo, w1, b1, w2, b2, (unsigned short*)d_out, (unsigned char*)d_ws);
}
```

```cpp
#include <hip/hip_runtime.h>
#include <hip/hip_bf16.h>

#define STAMPS 0
#define THR 0
#define TOUCH 0
#define CODEPF 1
#define FLAGPF 1
#define OUTAUX 0
#define OUTPF 0
#define S0 6
#define XRPOS 0
#define QPOS 0
#define H1W 8
#define H1G 2
#define W2S 70
#define F6W 0
#define PUBA 8
#define NCDELAY 0
#define HPRIO 0
#define WOLATE 0
#define SLEEP 1

#define GAS __attribute__((address_space(1)))
#define LAS __attribute__((address_space(3)))
#define RLX_AGENT __ATOMIC_RELAXED, __HIP_MEMORY_SCOPE_AGENT
typedef unsigned v4u __attribute__((ext_vector_type(4)));
typedef unsigned v2u __attribute__((ext_vector_type(2)));
typedef float f32x4 __attribute__((ext_vector_type(4)));
typedef float f32x2 __attribute__((ext_vector_type(2)));
typedef __bf16 bf16x8 __attribute__((ext_vector_type(8)));
typedef __bf16 bf16x2 __attribute__((ext_vector_type(2)));
typedef short s16x4 __attribute__((ext_vector_type(4)));
typedef GAS unsigned gu32;

namespace {
constexpr int S = 2048, D = 640, F = 1536;
constexpr float LN_EPS = 1e-5f;
constexpr int AUX_SC1 = 16;
constexpr unsigned kSpinCap = 1u << 17;
constexpr int OOB = 0x7FFFFF00;

constexpr int WS_EPOCH = 0;
constexpr int WS_FLAG = 4096;
constexpr int FLAG_SET = 64 * 256;
constexpr int WS_XN = 1 << 20;
constexpr int WS_O = WS_XN + 128 * 20 * 1024;
constexpr int WS_H = WS_O + 128 * 20 * 1024;
constexpr int WS_H1 = WS_H + 128 * 20 * 1024;
constexpr int WS_KVP = WS_H1 + 128 * 48 * 1024;
constexpr int WS_ST = WS_KVP + 16 * 16 * 4096;
constexpr int WS_DBG = WS_ST + 2048 * 64;
constexpr int WS_TKV = WS_DBG + 256 * 8 * 32 * 8;
constexpr int WS_TO = WS_TKV + 16 * 153600;
constexpr int WS_T1 = WS_TO + 8 * 102400;
constexpr int WS_T2 = WS_T1 + 16 * 122880;
constexpr int WS_CS = WS_T2 + 16 * 122880;
constexpr int WS_BYTES = WS_CS + 16 * 16 * 384;

constexpr int L_BKV = 0, L_BQ = 102400;
constexpr int L_RED = 0, L_KVF = 153600;
constexpr int L_WO = 0, L_W1 = 30720, L_W2 = 0, L_CS = 161792;
constexpr int L_B1 = L_CS + 1152, L_B2 = L_CS + 1536;
constexpr int LDS_BYTES = 163840;

__device__ __forceinline__ f32x4 mfma(v4u a, v4u b, f32x4 c) {
  return __builtin_amdgcn_mfma_f32_16x16x32_bf16(__builtin_bit_cast(bf16x8, a), __builtin_bit_cast(bf16x8, b), c, 0, 0, 0);
}
__device__ __forceinline__ unsigned pk2(f32x2 v) { return __builtin_bit_cast(unsigned, __builtin_convertvector(v, bf16x2)); }
__device__ __forceinline__ unsigned pk2(float lo, float hi) { return pk2(f32x2{lo, hi}); }
__device__ __forceinline__ float bf_lo(unsigned u) { return __builtin_bit_cast(float, u << 16); }
__device__ __forceinline__ float bf_hi(unsigned u) { return __builtin_bit_cast(float, u & 0xFFFF0000u); }
__device__ __forceinline__ f32x2 bf2(unsigned u) { return f32x2{bf_lo(u), bf_hi(u)}; }
__device__ __forceinline__ f32x2 lo2(f32x4 v) { return f32x2{v[0], v[1]}; }
__device__ __forceinline__ f32x2 hi2(f32x4 v) { return f32x2{v[2], v[3]}; }
__device__ __forceinline__ v4u mk_srd(const void* p, unsigned bytes) {
  const unsigned long long a = (unsigned long long)p;
  v4u r;
  r.x = (unsigned)a; r.y = (unsigned)(a >> 32) & 0xFFFFu; r.z = bytes; r.w = 0x00020000u;
  return r;
}
#define DRAIN() asm volatile("s_waitcnt vmcnt(0)" ::: "memory")
#define BAR() asm volatile("s_waitcnt lgkmcnt(0)\n\ts_barrier" ::: "memory")
#define BAR_RAW() asm volatile("s_barrier" ::: "memory")
#define SCHED() __builtin_amdgcn_sched_barrier(0)
#define PIN(x) asm volatile("" : "+v"(x))

__device__ __forceinline__ void dma2(unsigned lds_addr, unsigned voff, v4u srd, unsigned soff) {
  unsigned keep;
  asm volatile("s_mov_b32 %0, m0\n\ts_mov_b32 m0, %1\n\ts_nop 0\n\t"
               "buffer_load_dwordx4 %2, %3, %4 offen sc1 lds\n\t"
               "buffer_load_dwordx4 %2, %3, %4 offen offset:1024 sc1 lds\n\t"
               "s_mov_b32 m0, %0"
               : "=&s"(keep) : "s"(lds_addr), "v"(voff), "s"(srd), "s"(soff) : "memory");
}
template <int LO, int HI, int TH = 0, int NW = 4>
__device__ __forceinline__ void dma_image(unsigned lds_base, unsigned voff, v4u srd, int ws_off, int hw, int rot) {
  constexpr int NP = (HI - LO) / 2;
#pragma unroll 1
  for (int i = hw; i < NP; i += NW) {
    int j = i + rot;
    if (j >= NP) j -= NP;
    dma2(lds_base + LO * 1024 + j * 2048, voff, srd, ws_off + LO * 1024 + j * 2048);
    if (TH) asm volatile("s_waitcnt vmcnt(%0)" :: "i"(TH - 2) : "memory");
  }
}
__device__ __forceinline__ v2u rdtr(LAS char* p) { return __builtin_bit_cast(v2u, __builtin_amdgcn_ds_read_tr16_b64_v4i16((LAS s16x4*)p)); }
struct LBase {
  unsigned a[3];
  __device__ __forceinline__ explicit LBase(unsigned v) { a[0] = v; a[1] = v + 65536; a[2] = v + 131072; PIN(a[0]); PIN(a[1]); PIN(a[2]); }
  __device__ __forceinline__ v2u rd(int off) const { return rdtr((LAS char*)(a[off >> 16]) + (off & 65535)); }
};
template <int MT, int NT, int KS, int PD, bool WA, int M0 = 0, class RB, class GA, class PRE>
__device__ __forceinline__ void kloop(f32x4 (&acc)[MT][NT], RB rb, GA ga, PRE pre) {
  v2u bl[PD + 1][NT], bh[PD + 1][NT];
#pragma unroll
  for (int m = M0; m < MT; ++m)
#pragma unroll
    for (int s = 0; s < NT; ++s) acc[m][s] = f32x4{0.f, 0.f, 0.f, 0.f};
#pragma unroll
  for (int ks = 0; ks < PD; ++ks)
#pragma unroll
    for (int s = 0; s < NT; ++s) { bl[ks][s] = rb(ks, s, 0); bh[ks][s] = rb(ks, s, 1); }
  SCHED();
#pragma unroll
  for (int ks = 0; ks < KS; ++ks) {
    pre(ks);
#pragma unroll
    for (int s = 0; s < NT; ++s)
#pragma unroll
      for (int m = 0; m < MT; ++m) {
        const v2u lo = bl[ks % (PD + 1)][s], hi = bh[ks % (PD + 1)][s];
        const v4u wv = {lo.x, lo.y, hi.x, hi.y};
        acc[m][s] = WA ? mfma(wv, ga(ks, m), acc[m][s]) : mfma(ga(ks, m), wv, acc[m][s]);
        if (ks + PD < KS) {
          if (MT == 1 || m == 0) bl[(ks + PD) % (PD + 1)][s] = rb(ks + PD, s, 0);
          if (MT == 1 || m == 1) bh[(ks + PD) % (PD + 1)][s] = rb(ks + PD, s, 1);
        }
        SCHED();
      }
  }
}
constexpr int ld_hi(int ks, int s0, int W, int KS) {
  const int a = s0 + 2 * (ks + 1), b = ks + 1 + W, c = a < b ? a : b;
  return c < KS ? c : KS;
}
__device__ __forceinline__ v4u ld128(__amdgpu_buffer_rsrc_t rs, int voff, int soff) {
  return __builtin_amdgcn_raw_buffer_load_b128(rs, voff, soff, AUX_SC1);
}
__device__ __forceinline__ v2u ld64(__amdgpu_buffer_rsrc_t rs, int voff, int soff) {
  return __builtin_amdgcn_raw_buffer_load_b64(rs, voff, soff, AUX_SC1);
}
__device__ __forceinline__ void st128(v4u v, __amdgpu_buffer_rsrc_t rs, int voff, int soff) {
  __builtin_amdgcn_raw_buffer_store_b128(v, rs, (int)((unsigned)voff + (unsigned)soff), 0, AUX_SC1);
}
__device__ __forceinline__ int sel(bool c, int a) {
  PIN(a);
  return c ? a : OOB;
}
__device__ __forceinline__ v4u ld_split(__amdgpu_buffer_rsrc_t rs, int lane, int soff) {
  const v2u a = ld64(rs, lane * 8, soff), b = ld64(rs, lane * 8 + 512, soff);
  return v4u{a.x, a.y, b.x, b.y};
}
__device__ __forceinline__ void st64(v2u v, __amdgpu_buffer_rsrc_t rs, int voff, int soff) {
  __builtin_amdgcn_raw_buffer_store_b64(v, rs, voff, soff, AUX_SC1);
}
__device__ __forceinline__ void flag_set(gu32* p, unsigned tag) {
  asm volatile("global_atomic_swap %0, %1, off sc1" :: "v"(p), "v"(tag) : "memory");
}
__device__ __forceinline__ void wait_flag(gu32* p, int n, unsigned tag, int lane) {
#pragma unroll 1
  for (unsigned spins = 0; spins < kSpinCap; ++spins) {
    unsigned v = tag;
    if (lane < n) asm volatile("global_atomic_add %0, %1, %2, off sc0 sc1\n\ts_waitcnt vmcnt(0)" : "=&v"(v) : "v"(p), "v"(0u) : "memory");
    if (__all(v == tag)) break;
    if (SLEEP) __builtin_amdgcn_s_sleep(SLEEP);
  }
  asm volatile("" ::: "memory");
}
__device__ __forceinline__ void sig_wait(volatile LAS unsigned* p) {
#pragma unroll 1
  for (unsigned spins = 0; spins < 64 * kSpinCap; ++spins) {
    if (*p != 0u) break;
    __builtin_amdgcn_s_sleep(1);
  }
  asm volatile("" ::: "memory");
}
__device__ __forceinline__ void prog_wait(volatile LAS v4u* p, unsigned stage) {
#pragma unroll 1
  for (unsigned spins = 0; spins < 64 * kSpinCap; ++spins) {
    const v4u v = *p;
    if (min(min(v.x, v.y), min(v.z, v.w)) >= stage) break;
    __builtin_amdgcn_s_sleep(1);
  }
  asm volatile("" ::: "memory");
}
__device__ __forceinline__ void publish(gu32* flag, unsigned tag, int lane) {
  DRAIN();
  if (lane == 0) flag_set(flag, tag);
}
__device__ __forceinline__ f32x4 add4(f32x4 a, f32x4 b) {
  f32x4 r;
#pragma unroll
  for (int i = 0; i < 4; ++i) { float x; asm("v_add_f32 %0, %1, %2" : "=v"(x) : "v"(a[i]), "v"(b[i])); r[i] = x; }
  return r;
}
__device__ __forceinline__ float xsum32(float x) {
  unsigned u = __builtin_bit_cast(unsigned, x), v = u;
  asm("" : "+v"(v));
  auto r = __builtin_amdgcn_permlane32_swap(u, v, false, false);
  const unsigned a = r[0], b = r[1];
  return __builtin_bit_cast(float, a) + __builtin_bit_cast(float, b);
}
__device__ __forceinline__ float xsum16(float x) {
  unsigned u = __builtin_bit_cast(unsigned, x), v = u;
  asm("" : "+v"(v));
  auto r = __builtin_amdgcn_permlane16_swap(u, v, false, false);
  const unsigned a = r[0], b = r[1];
  return __builtin_bit_cast(float, a) + __builtin_bit_cast(float, b);
}
__device__ __forceinline__ float xsum8(float x) {
  return x + __builtin_bit_cast(float, __builtin_amdgcn_update_dpp(0, __builtin_bit_cast(int, x), 0x128, 0xf, 0xf, false));
}
__device__ __forceinline__ f32x2 phi2(f32x2 t) {
  const f32x2 u = t * 1.4426950408889634f, v = t + 1e-6f;
  const f32x2 e = {__builtin_amdgcn_fmed3f(__builtin_amdgcn_exp2f(u[0]), 0.f, 1.f), __builtin_amdgcn_fmed3f(__builtin_amdgcn_exp2f(u[1]), 0.f, 1.f)};
  return f32x2{fmaxf(v[0], 1e-6f), fmaxf(v[1], 1e-6f)} + e;
}
__device__ __forceinline__ f32x2 gelu2(f32x2 x) {
  const f32x2 u = x * (x * x * -0.1029432397f + -2.302208198f);
  const f32x2 d = f32x2{__builtin_amdgcn_exp2f(u[0]), __builtin_amdgcn_exp2f(u[1])} + 1.f;
  return x * f32x2{__builtin_amdgcn_rcpf(d[0]), __builtin_amdgcn_rcpf(d[1])};
}

#define STAMP(i) do { } while (0)

__global__ void __launch_bounds__(512, 2)
fused_block(const unsigned short* __restrict__ x, const unsigned short* __restrict__ wqkv, const unsigned short* __restrict__ wo,
            const unsigned short* __restrict__ w1, const unsigned short* __restrict__ b1, const unsigned short* __restrict__ w2,
            const unsigned short* __restrict__ b2, unsigned short* __restrict__ out, unsigned char* ws) {
  __shared__ __attribute__((aligned(16))) char smem[LDS_BYTES];
  unsigned long long pc0;
  asm volatile("s_getpc_b64 %0" : "=s"(pc0));
  LAS char* L = (LAS char*)smem;
  const unsigned lds0 = (unsigned)(size_t)L;
  const int tid = threadIdx.x, lane = tid & 63, w = __builtin_amdgcn_readfirstlane(tid >> 6);
  const int b = blockIdx.x, xcd = b & 7, bi = b >> 3;
  const int g = lane >> 4, t = lane & 15;
  const __amdgpu_buffer_rsrc_t rs = __builtin_amdgcn_make_buffer_rsrc((void*)ws, 0, WS_BYTES, 0x00020000);
  gu32* flags = (gu32*)(ws + WS_FLAG);
#define FLAG(set, grp) (flags + ((set) * FLAG_SET + (grp) * 256) / 4)
  asm volatile("" :: "s"(x), "s"(wqkv), "s"(wo), "s"(w1), "s"(b1), "s"(w2), "s"(b2), "s"(out), "s"(ws));
  STAMP(0);
  const int r128 = 4 * (xcd >> 1) + (bi >> 3), c16 = 8 * (xcd & 1) + (bi & 7);
  const int r64 = 8 * (xcd >> 1) + (bi >> 2), c8 = 4 * (xcd & 1) + (bi & 3);

  const __amdgpu_buffer_rsrc_t rx = __builtin_amdgcn_make_buffer_rsrc((void*)x, 0, S * D * 2, 0x00020000);
  const int hw = w - 4, ht = tid - 256;
  volatile LAS unsigned* sig = (volatile LAS unsigned*)(L + L_CS + 1024);
  volatile LAS v4u* prog = (volatile LAS v4u*)(L + L_CS + 1088);
#define PROG(n) do { asm volatile("" ::: "memory"); sig[16 + w] = (n); } while (0)

  unsigned tag = __hip_atomic_load((gu32*)(ws + WS_EPOCH), RLX_AGENT);
  const __amdgpu_buffer_rsrc_t rq = __builtin_amdgcn_make_buffer_rsrc((void*)wqkv, 0, D * 3 * D * 2, 0x00020000);
  v4u tv[12];
  const int q10 = lane / 10, c10 = lane % 10;
  if (w < 4) {
#pragma unroll
    for (int j = 0; j < 3; ++j)
      tv[j] = __builtin_amdgcn_raw_buffer_load_b128(rx, (lane & 7) * 1280 + (lane >> 3) * 16, (j < 2 || w < 2) ? 8 * b * 1280 + (w + 4 * j) * 128 : OOB, 0);
  } else if (w == 4) {
    const int vo = sel(lane < 40, q10 * 3840 + (c10 < 5 ? 1280 : 2480) + c10 * 16);
#pragma unroll
    for (int j = 0; j < 10; ++j) tv[j] = __builtin_amdgcn_raw_buffer_load_b128(rq, vo, (40 * r128 + 4 * j) * 3840 + c16 * 80, 0);
  } else if (w == 7) {
    *(LAS v4u*)(L + L_CS + 512 + lane * 16) = v4u{0u, 0u, 0u, 0u};
  }
  BAR();

  if (w < 4) {
  const int grp32 = 4 * r128 + w;
  constexpr int AW = 6;

  {
    const int tok = 8 * b + (lane & 7), cl = lane >> 3;
    const bool has3 = w < 2;
    int xd = ((tok >> 4) * 20 + 2 * w + (cl >> 2)) * 1024 + ((cl & 3) * 16 + (tok & 15)) * 16;
    PIN(xd);
    SCHED();
    f32x2 f[12];
#pragma unroll
    for (int j = 0; j < 3; ++j)
#pragma unroll
      for (int e = 0; e < 4; ++e) f[j * 4 + e] = bf2(tv[j][e]);
    f32x2 s2 = (f[0] + f[1]) + (f[2] + f[3]) + ((f[4] + f[5]) + (f[6] + f[7])) + ((f[8] + f[9]) + (f[10] + f[11]));
    const float mloc = xsum32(xsum16(xsum8(s2[0] + s2[1]))) * (has3 ? 1.f / 192.f : 1.f / 128.f);
    const float m3 = has3 ? mloc : 0.f;
    f32x2 q2 = {0.f, 0.f}, q3 = {0.f, 0.f};
#pragma unroll
    for (int j = 0; j < 12; j += 2) {
      const f32x2 d0 = f[j] - (j < 8 ? mloc : m3), d1 = f[j + 1] - (j < 8 ? mloc : m3);
      q2 += d0 * d0; q3 += d1 * d1;
    }
    q2 += q3;
    const float m2loc = xsum32(xsum16(xsum8(q2[0] + q2[1])));
    if (lane < 8) *(volatile LAS f32x4*)(L + L_CS + 512 + (lane * 4 + w) * 16) = f32x4{mloc, m2loc, 1.f, 1.f};
    STAMP(25);
    f32x4 pr[4];
#pragma unroll 1
    for (unsigned spins = 0; spins < 64 * kSpinCap; ++spins) {
      bool ok = true;
#pragma unroll
      for (int ww = 0; ww < 4; ++ww) {
        pr[ww] = *(volatile LAS f32x4*)(L + L_CS + 512 + ((lane & 7) * 4 + ww) * 16);
        ok = ok && pr[ww][2] == 1.f;
      }
      if (__all(ok)) break;
    }
    const f32x4 p01 = {pr[0][0], pr[0][1], pr[1][0], pr[1][1]}, p23 = {pr[2][0], pr[2][1], pr[3][0], pr[3][1]};
    const float mean = (p01[0] + p01[2]) * 0.3f + (p23[0] + p23[2]) * 0.2f;
    const float e0 = p01[0] - mean, e1 = p01[2] - mean, e2 = p23[0] - mean, e3 = p23[2] - mean;
    const float m2 = (p01[1] + p01[3]) + (p23[1] + p23[3]) + 192.f * (e0 * e0 + e1 * e1) + 128.f * (e2 * e2 + e3 * e3);
    const float rstd = __builtin_amdgcn_rsqf(m2 * (1.f / D) + LN_EPS);
#pragma unroll
    for (int j = 0; j < 3; ++j) {
      v4u o;
#pragma unroll
      for (int e = 0; e < 4; ++e) o[e] = pk2((f[j * 4 + e] - mean) * rstd);
      st128(o, rs, (j < 2 || has3) ? xd : OOB, WS_XN + j * 8192);
    }
    STAMP(26);
    PIN(tag); tag += 1u;
    publish(FLAG(0, b >> 2) + (b & 3) * 4 + w, tag, lane);
  }

  STAMP(1);

  v4u xr[3];
  {
    wait_flag(FLAG(0, grp32) + lane, 16, tag, lane);
    STAMP(2);
    v4u a[20][2];
    const int abase = WS_XN + 2 * grp32 * 20 * 1024;
#pragma unroll
    for (int ks = 0; ks < S0; ++ks)
#pragma unroll
      for (int m = 0; m < 2; ++m) a[ks][m] = ld128(rs, lane * 16, abase + (m * 20 + ks) * 1024);
    SCHED();
    BAR_RAW();
    STAMP(27);
    {
      f32x4 acc[2][5];
      const LBase bp(lds0 + L_BKV + (lane >> 2) * 160 + (lane & 3) * 8);
      kloop<2, 5, 20, 1, false>(acc, [&](int ks, int s, int h) { return bp.rd(ks * 5120 + h * 2560 + s * 32); },
        [&](int ks, int m) { return a[ks][m]; },
        [&](int ks) {
#pragma unroll
          for (int j = ld_hi(ks - 1, S0, AW, 20); j < ld_hi(ks, S0, AW, 20); ++j)
#pragma unroll
            for (int m = 0; m < 2; ++m) a[j][m] = ld128(rs, lane * 16, abase + (m * 20 + j) * 1024);
          if (ks == 3 || ks == 10) BAR_RAW();
          if (ks == 20 - AW) PROG(1);
        });
      STAMP(3);
      v4u ka[3], vb[3];
#pragma unroll
      for (int s = 0; s < 3; ++s)
#pragma unroll
        for (int m = 0; m < 2; ++m) {
          vb[s][2 * m] = pk2(lo2(acc[m][s + 2])); vb[s][2 * m + 1] = pk2(hi2(acc[m][s + 2]));
        }
#pragma unroll
      for (int e = 0; e < 4; ++e) vb[0][e] = t < 8 ? 0x3F803F80u : vb[0][e];
      const f32x4 z4 = {0.f, 0.f, 0.f, 0.f};
      LAS f32x4* red = (LAS f32x4*)(L + L_RED) + w * 512 + lane;
#pragma unroll
      for (int s = 0; s < 3; ++s) {
#pragma unroll
        for (int m = 0; m < 2; ++m) { ka[s][2 * m] = pk2(phi2(lo2(acc[m][s]))); ka[s][2 * m + 1] = pk2(phi2(hi2(acc[m][s]))); }
#pragma unroll
        for (int sb = 0; sb < 3; ++sb)
          if (s > 0 || sb < 2) red[(3 * s + sb - (s > 0)) * 64] = mfma(ka[s], vb[sb], z4);
      }
      STAMP(28);
      BAR();
    }
    STAMP(4);
    f32x4 qa[2][3];
    {
      unsigned bp0 = lds0 + L_BQ + (lane >> 2) * 64 + (lane & 3) * 8 + (g & 1) * 32;
      unsigned bp1 = lds0 + L_BQ + (lane >> 2) * 64 + (lane & 3) * 8 + 32 - (g & 1) * 32;
      unsigned bp2 = lds0 + L_BQ + 2048 + (lane >> 2) * 16 + (lane & 1) * 8;
      PIN(bp0); PIN(bp1); PIN(bp2);
      kloop<2, 3, 20, 2, true>(qa,
        [&](int ks, int s, int h) {
          return s == 0 ? rdtr((LAS char*)bp0 + ks * 2560 + h * 1024) : s == 1 ? rdtr((LAS char*)bp1 + ks * 2560 + h * 1024) : rdtr((LAS char*)bp2 + ks * 2560 + h * 256);
        },
        [&](int ks, int m) { return a[ks][m]; },
        [&](int ks) {});
    }
    v4u pq0[2], pq1[2];
#pragma unroll
    for (int m = 0; m < 2; ++m) {
      pq0[m].x = pk2(phi2(lo2(qa[m][0]))); pq0[m].y = pk2(phi2(hi2(qa[m][0])));
      pq0[m].z = pk2(phi2(lo2(qa[m][1]))); pq0[m].w = pk2(phi2(hi2(qa[m][1])));
      const unsigned lo = pk2(phi2(lo2(qa[m][2]))), hi = pk2(phi2(hi2(qa[m][2])));
      pq1[m].x = g < 2 ? lo : 0u; pq1[m].y = g < 2 ? hi : 0u; pq1[m].z = 0u; pq1[m].w = 0u;
      PIN(pq0[m]); PIN(pq1[m]);
    }
    STAMP(5);
    BAR_RAW();
#pragma unroll
    for (int j = 0; j < 3; ++j)
      xr[j] = __builtin_amdgcn_raw_buffer_load_b128(rx, sel(j < 2 || g < 2, t * 1280 + g * 16), (4 * r64 + w) * 16 * 1280 + c8 * 160 + j * 64, 0);
    STAMP(17);
    {
      v4u kv[4][2];
#pragma unroll
      for (int es = 3; es >= 0; --es)
#pragma unroll
        for (int ks = 0; ks < 2; ++ks) kv[es][ks] = *(LAS v4u*)(L + L_KVF + (es * 2 + ks) * 1024 + lane * 16);
      SCHED();
      f32x4 o[2][4];
#pragma unroll
      for (int es = 3; es >= 0; --es)
#pragma unroll
        for (int m = 0; m < 2; ++m) o[m][es] = mfma(kv[es][0], pq0[m], f32x4{0.f, 0.f, 0.f, 0.f});
#pragma unroll
      for (int es = 3; es >= 0; --es)
#pragma unroll
        for (int m = 0; m < 2; ++m) o[m][es] = mfma(kv[es][1], pq1[m], o[m][es]);
#pragma unroll
      for (int m = 0; m < 2; ++m) {
        const float i0 = __builtin_amdgcn_rcpf(o[m][3][0] + 1e-6f), i1 = __builtin_amdgcn_rcpf(o[m][3][1] + 1e-6f);
        const float im = g == 0 ? i0 : i1;
        v4u ov;
        ov.x = pk2(lo2(o[m][0]) * i0); ov.y = pk2(hi2(o[m][0]) * i0);
        ov.z = pk2(lo2(o[m][1]) * im); ov.w = pk2(hi2(o[m][1]) * im);
        v2u ol; ol.x = pk2(lo2(o[m][2]) * i1); ol.y = pk2(hi2(o[m][2]) * i1);
        const int tt = 2 * grp32 + m;
        st128(ov, rs, lane * 16, WS_O + (tt * 20 + c16) * 1024);
        st64(ol, rs, g < 2 ? lane * 8 : OOB, WS_O + (tt * 20 + 16) * 1024 + c16 * 256);
      }
      STAMP(18);
      publish(FLAG(2, grp32) + c16, tag, lane);
    }
    STAMP(7);
  }

  {
    const int tt = 4 * r64 + w;
    v4u eye[2];
#pragma unroll
    for (int hf = 0; hf < 2; ++hf) {
      const int e = 16 * hf + t - 8 * g;
#pragma unroll
      for (int d = 0; d < 4; ++d) eye[hf][d] = e == 2 * d ? 0x3F80u : e == 2 * d + 1 ? 0x3F800000u : 0u;
      PIN(eye[hf]);
    }
    f32x4 acc[1][5];
#pragma unroll
    for (int s = 0; s < 5; ++s) { acc[0][s] = mfma(eye[s & 1], xr[s >> 1], f32x4{0.f, 0.f, 0.f, 0.f}); PIN(acc[0][s]); }
    wait_flag(FLAG(2, tt >> 1) + lane, 16, tag, lane);
    STAMP(8);
    if (b == 0 && tid == 0) __hip_atomic_store((gu32*)(ws + WS_EPOCH), tag, RLX_AGENT);
    v4u a[20];
#pragma unroll
    for (int ks = 0; ks < 2 * S0; ++ks) a[ks] = ld128(rs, lane * 16, WS_O + (tt * 20 + ks) * 1024);
    SCHED();
    const LBase bp(lds0 + L_WO + (lane >> 2) * 160 + (lane & 3) * 8);
    kloop<1, 5, 20, 1, true, 1>(acc, [&](int ks, int s, int h) { return bp.rd(ks * 5120 + h * 2560 + s * 32); },
      [&](int ks, int m) { return a[ks]; },
      [&](int ks) {
#pragma unroll
        for (int j = ld_hi(ks - 1, 2 * S0, 2 * AW, 20); j < ld_hi(ks, 2 * S0, 2 * AW, 20); ++j)
          a[j] = j < 16 ? ld128(rs, lane * 16, WS_O + (tt * 20 + j) * 1024) : ld_split(rs, lane, WS_O + (tt * 20 + j) * 1024);
        if (ks == 8) PROG(2);
        if (ks == 14) PROG(3);
      });
    PROG(4);
    STAMP(9);
    v2u hb[5];
    f32x2 hv[10];
#pragma unroll
    for (int s = 0; s < 5; ++s) {
      hb[s].x = pk2(lo2(acc[0][s]));
      hb[s].y = pk2(hi2(acc[0][s]));
      hv[2 * s] = bf2(hb[s].x); hv[2 * s + 1] = bf2(hb[s].y);
    }
    st128(v4u{hb[0].x, hb[0].y, hb[1].x, hb[1].y}, rs, lane * 16, WS_H + (tt * 20 + 2 * c8) * 1024);
    st128(v4u{hb[2].x, hb[2].y, hb[3].x, hb[3].y}, rs, lane * 16, WS_H + (tt * 20 + 2 * c8 + 1) * 1024);
    st64(hb[4], rs, lane * 8, WS_H + (tt * 20 + 16) * 1024 + c8 * 512);
    const f32x2 s2 = ((hv[0] + hv[1]) + (hv[2] + hv[3])) + ((hv[4] + hv[5]) + (hv[6] + hv[7])) + (hv[8] + hv[9]);
    const float mc = xsum32(xsum16(s2[0] + s2[1])) * (1.f / 80.f);
    f32x2 q2 = {0.f, 0.f}, q3 = {0.f, 0.f};
#pragma unroll
    for (int j = 0; j < 10; j += 2) { const f32x2 d0 = hv[j] - mc, d1 = hv[j + 1] - mc; q2 += d0 * d0; q3 += d1 * d1; }
    q2 += q3;
    const float m2 = xsum32(xsum16(q2[0] + q2[1]));
    st64(v2u{__builtin_bit_cast(unsigned, mc), __builtin_bit_cast(unsigned, m2)}, rs, g == 0 ? t * 8 : OOB, WS_ST + (tt * 8 + c8) * 128);
    STAMP(20);
    publish(FLAG(3, tt >> 1) + (tt & 1) * 8 + c8, tag, lane);
    STAMP(10);
  }

  {
    wait_flag(FLAG(3, grp32) + lane, 16, tag, lane);
    STAMP(11);
    v4u stt[2];
#pragma unroll
    for (int m = 0; m < 2; ++m) {
      const v2u s0 = ld64(rs, g * 256 + t * 8, WS_ST + (2 * grp32 + m) * 1024), s1 = ld64(rs, g * 256 + 128 + t * 8, WS_ST + (2 * grp32 + m) * 1024);
      stt[m] = v4u{s0.x, s0.y, s1.x, s1.y};
    }
    v4u a[20][2];
    const int abase = WS_H + 2 * grp32 * 20 * 1024;
#pragma unroll
    for (int ks = 0; ks < S0; ++ks)
#pragma unroll
      for (int m = 0; m < 2; ++m) a[ks][m] = ld128(rs, lane * 16, abase + (m * 20 + ks) * 1024);
    SCHED();
    float nm[2], rr[2];
#pragma unroll
    for (int m = 0; m < 2; ++m) {
      const f32x4 sv = __builtin_bit_cast(f32x4, stt[m]);
      const float mean = xsum32(xsum16(sv[0] + sv[2])) * 0.125f;
      const float m2 = xsum32(xsum16(sv[1] + sv[3] + 80.f * ((sv[0] - mean) * (sv[0] - mean) + (sv[2] - mean) * (sv[2] - mean))));
      const float var = m2 * (1.f / D);
      const float r1 = __builtin_amdgcn_rsqf(var + LN_EPS);
      rr[m] = r1 * __builtin_amdgcn_rsqf(var * r1 * r1 + LN_EPS);
      nm[m] = -mean * rr[m];
      PIN(rr[m]); PIN(nm[m]);
    }
    BAR_RAW();
    STAMP(22);
    const unsigned l64 = lds0 + (lane >> 2) * 64 + (lane & 3) * 8;
    const LBase bp0(l64 + (g & 1) * 32), bp1(l64 + 32 - (g & 1) * 32);
    const int h1base = WS_H1 + 2 * grp32 * 48 * 1024;
    auto finish = [&](const f32x4& c0, const f32x4& c1, int j, int m) {
      v4u o;
#pragma unroll
      for (int s = 0; s < 2; ++s) {
        const f32x4 cs = *(LAS f32x4*)(L + L_CS + ((2 * j + s) * 4 + g) * 16), bb = *(LAS f32x4*)(L + L_B1 + ((2 * j + s) * 4 + g) * 16);
        const f32x4 c = s ? c1 : c0;
        o[2 * s] = pk2(gelu2(lo2(c) * rr[m] + (lo2(cs) * nm[m] + lo2(bb))));
        o[2 * s + 1] = pk2(gelu2(hi2(c) * rr[m] + (hi2(cs) * nm[m] + hi2(bb))));
      }
      st128(o, rs, lane * 16, h1base + (m * 48 + (c16 >> 3) * 24 + (j < 2 ? 2 * (c16 & 7) + j : 16 + (c16 & 7))) * 1024);
    };
    {
      f32x4 acc[2][4];
      kloop<2, 4, 20, 1, true>(acc, [&](int ks, int s, int h) { return ((s & 1) ? bp1 : bp0).rd(L_W1 + (s >> 1) * 40960 + ks * 2048 + h * 1024); },
        [&](int ks, int m) { return a[ks][m]; },
        [&](int ks) {
#pragma unroll
          for (int j = ld_hi(ks - 1, S0, AW, 20); j < ld_hi(ks, S0, AW, 20); ++j)
#pragma unroll
            for (int m = 0; m < 2; ++m)
              a[j][m] = j < 16 ? ld128(rs, lane * 16, abase + (m * 20 + j) * 1024) : ld_split(rs, lane, abase + (m * 20 + j) * 1024);
          if (ks == 20 - AW) PROG(5);
        });
      PROG(6);
      STAMP(12);
#pragma unroll
      for (int j = 0; j < 2; ++j)
#pragma unroll
        for (int m = 0; m < 2; ++m) finish(acc[m][2 * j], acc[m][2 * j + 1], j, m);
      SCHED();
      STAMP(24);
    }
    f32x4 accb[2][2];
    const int kh = w & 1, tt = 4 * r64 + w;
    v4u hr01, hr23;
    v2u hr4;
    kloop<2, 2, 20, 3, true>(accb, [&](int ks, int s, int h) { return (s ? bp1 : bp0).rd(L_W1 + 81920 + ks * 2048 + h * 1024); },
      [&](int ks, int m) { return a[ks][m]; },
      [&](int ks) {
        if (ks == PUBA) { publish(FLAG(4, grp32) + c16, tag, lane); PROG(7); }
        if (ks == PUBA + 2) {
          hr01 = ld128(rs, lane * 16, WS_H + (tt * 20 + 2 * c8) * 1024);
          hr23 = ld128(rs, lane * 16, WS_H + (tt * 20 + 2 * c8 + 1) * 1024);
          hr4 = ld64(rs, lane * 8, WS_H + (tt * 20 + 16) * 1024 + c8 * 512);
        }
      });
    STAMP(13);
    f32x4 acc[2][5];
    {
      const unsigned hrr[10] = {hr01.x, hr01.y, hr01.z, hr01.w, hr23.x, hr23.y, hr23.z, hr23.w, hr4.x, hr4.y};
#pragma unroll
      for (int s = 0; s < 5; ++s) {
        acc[0][s] = *(LAS f32x4*)(L + L_B2 + (s * 4 + g) * 16) + f32x4{bf_lo(hrr[2 * s]), bf_hi(hrr[2 * s]), bf_lo(hrr[2 * s + 1]), bf_hi(hrr[2 * s + 1])};
        PIN(acc[0][s]);
      }
    }
    int oo[3];
    unsigned tw, tr;
    {
      int ln = lane;
      PIN(ln);
      const int qq = ln / 10, cc = ln % 10;
#pragma unroll
      for (int j = 0; j < 3; ++j) { oo[j] = sel(ln < (j < 2 ? 60 : 40), ((tt * 16 + 6 * j + qq) * D + 80 * c8) * 2 + cc * 16); PIN(oo[j]); }
      tw = lds0 + L_W2 + 122880 + w * 3072 + t * 176 + g * 8; tr = lds0 + L_W2 + 122880 + w * 3072 + qq * 176 + cc * 16;
      PIN(tw); PIN(tr);
    }
    BAR_RAW();
    STAMP(14);
    constexpr int WIN = 8;
    v4u ar[WIN][2];
    const int a4[2] = {WS_H1 + (tt * 48 + kh * 24) * 1024, WS_H1 + ((tt ^ 1) * 48 + kh * 24) * 1024};
#pragma unroll
    for (int ks = 0; ks < WIN - 1; ++ks)
#pragma unroll
      for (int m = 0; m < 2; ++m) ar[ks][m] = ld128(rs, lane * 16, a4[m] + ks * 1024);
    SCHED();
#pragma unroll
    for (int m = 0; m < 2; ++m) finish(accb[m][0], accb[m][1], 2, m);
    STAMP(29);
    const LBase bp(lds0 + L_W2 + kh * 5120 + (lane >> 2) * 160 + (lane & 3) * 8);
    kloop<2, 5, 24, 1, true, 1>(acc, [&](int ks, int s, int h) { return bp.rd((ks % 15) * 10240 + h * 2560 + s * 32); },
      [&](int ks, int m) { return ar[ks % WIN][m]; },
      [&](int ks) {
        if (ks == 5) PROG(8);
        if (ks == 9 || ks == 14 || ks == 19) BAR_RAW();
        if (ks == 3) publish(FLAG(10, grp32) + c16, tag, lane);
        if (ks >= 3) {
#pragma unroll
          for (int j = ks == 3 ? WIN - 1 : ld_hi(ks - 1, 1, WIN - 1, 24); j < ld_hi(ks, 1, WIN - 1, 24); ++j)
#pragma unroll
            for (int m = 0; m < 2; ++m) ar[j % WIN][m] = ld128(rs, lane * 16, a4[m] + j * 1024);
        }
      });
    STAMP(15);
    LAS f32x4* xch = (LAS f32x4*)(L + L_W2 + 102400);
#pragma unroll
    for (int s = 0; s < 5; ++s) xch[((w ^ 1) * 5 + s) * 64 + lane] = acc[1][s];
    BAR();
    const __amdgpu_buffer_rsrc_t rout = __builtin_amdgcn_make_buffer_rsrc((void*)out, 0, S * D * 2, 0x00020000);
    f32x4 ot[5];
#pragma unroll
    for (int s = 0; s < 5; ++s) ot[s] = xch[(w * 5 + s) * 64 + lane];
    SCHED();
#pragma unroll
    for (int s = 0; s < 5; ++s) {
      const f32x4 v = acc[0][s] + ot[s];
      *(LAS v2u*)((LAS char*)tw + s * 32) = v2u{pk2(lo2(v)), pk2(hi2(v))};
    }
    asm volatile("" ::: "memory");
    v4u ov[3];
#pragma unroll
    for (int j = 0; j < 3; ++j) ov[j] = *(LAS v4u*)((LAS char*)tr + j * 6 * 176);
#pragma unroll
    for (int j = 0; j < 3; ++j) __builtin_amdgcn_raw_buffer_store_b128(ov[j], rout, oo[j], 0, 0);
    STAMP(16);
  }
  return;
  }

  {
    const v4u srd_ws = mk_srd(ws, WS_BYTES);
    STAMP(16);
    const int q5 = lane / 5, c5 = lane % 5;
    const int f40m = c5 < 4 ? 512 : 128, f40l = c5 < 4 ? q5 * 64 + (c5 ^ (2 * ((q5 >> 2) & 1))) * 16 : 2048 + q5 * 16;
    v2u ub1, ub2;
    unsigned pf0 = 0;
    if (hw == 0) {
      const int dof = sel(lane < 40, lane * 16);
#pragma unroll
      for (int j = 0; j < 10; ++j) {
        const int k = 40 * r128 + 4 * j, R = (k & ~31) + 16 * ((k >> 2) & 1) + 4 * ((k >> 3) & 3);
        st128(tv[j], rs, dof, WS_TKV + c16 * 153600 + R * 160);
      }
      STAMP(18);
      PIN(tag); tag += 1u;
      publish(FLAG(6, c16) + r128, tag, lane);
      STAMP(13);
    } else if (hw == 1) {
      const __amdgpu_buffer_rsrc_t ro = __builtin_amdgcn_make_buffer_rsrc((void*)wo, 0, D * D * 2, 0x00020000);
      const int vq = sel(lane < 40, (8 * (q5 >> 2) + (q5 & 3)) * 3840 + c5 * 16), vo = sel(lane < 40, q10 * 1280 + c10 * 16);
#pragma unroll
      for (int j = 0; j < 5; ++j) {
        const int i8 = 5 * r128 + j, k = 32 * (i8 >> 2) + 16 * (i8 & 1) + 4 * ((i8 >> 1) & 1);
        tv[j] = __builtin_amdgcn_raw_buffer_load_b128(rq, vq, k * 3840 + c16 * 80, 0);
      }
#pragma unroll
      for (int j = 0; j < 5; ++j) tv[5 + j] = __builtin_amdgcn_raw_buffer_load_b128(ro, vo, (20 * r64 + 4 * j) * 1280 + c8 * 160, 0);
    } else if (hw == 2) {
      const GAS unsigned char* cb = (const GAS unsigned char*)(pc0 & ~127ull);
      if (lane < 11) pf0 = *(const GAS unsigned*)(cb + (lane * 32 + bi) * 128);
      if (lane < 6) {
        gu32* fp = FLAG(3 * (b >> 6) + (lane >> 1), b & 63) + (lane & 1) * 32;
        asm volatile("global_atomic_add %0, %1, off sc1" :: "v"(fp), "v"(0u) : "memory");
      }
    } else if (hw == 3) {
      const __amdgpu_buffer_rsrc_t rb1 = __builtin_amdgcn_make_buffer_rsrc((void*)b1, 0, F * 2, 0x00020000);
      const __amdgpu_buffer_rsrc_t rb2 = __builtin_amdgcn_make_buffer_rsrc((void*)b2, 0, D * 2, 0x00020000);
      ub1 = __builtin_amdgcn_raw_buffer_load_b64(rb1, lane * 8, c16 * 192, 0);
      ub2 = __builtin_amdgcn_raw_buffer_load_b64(rb2, lane * 8, c8 * 160, 0);
    }
    asm volatile("" ::: "memory");
    int red_off, kvf_off, kvp_off;
    auto items = [&]() {
      const int it = ht < 210 ? ht : 209, ie = it / 5, ic = it % 5;
      const int ihead = ie < 40 ? (ie >= 20) : ie - 40;
      const int id = 20 * ihead + 4 * ic, dg = id >> 2;
      const int sa = id >> 4, col = 40 + ie;
      const int tile = 3 * sa + (ie < 40 ? (col >> 4) - 2 : 0) - (sa > 0);
      red_off = lds0 + L_RED + (tile * 64 + (dg & 3) * 16 + (ie < 40 ? (col & 15) : 0)) * 16;
      kvf_off = lds0 + L_KVF + ((ie < 40 ? (ie >> 4) : 3) * 2 + (dg >> 3)) * 1024 + ((dg & 3) * 16 + (ie < 40 ? (ie & 15) : ihead)) * 16 + ((dg >> 2) & 1) * 8;
      kvp_off = WS_KVP + (c16 * 16 + r128) * 4096 + ht * 16;
      PIN(red_off); PIN(kvf_off); PIN(kvp_off);
    };
    if (hw != 0) items();
    int vo12, td[12];
    if (hw >= 2) {
      const int q12 = lane / 12, c12 = lane % 12;
      vo12 = hw == 2 ? sel(lane < 48, q12 * 3072 + c12 * 16) : sel(lane < 40, q10 * 1280 + c10 * 16);
#pragma unroll
      for (int j = 0; j < 12; ++j) {
        const int cc = r128 >> 1, off = 40 * (r128 & 1) + 4 * j;
        const int R = off < 64 ? 64 * cc + off : 32 * (16 + (cc >> 1)) + 16 * (cc & 1) + off - 64;
        const int d1 = WS_T1 + c16 * 122880 + (c12 >> 2) * 40960 + (R + q12) * 64 + ((c12 & 3) ^ (2 * ((R >> 2) & 1))) * 16;
        const int pc = r64 >> 1, ko = 48 * (r64 & 1) + 4 * j, jj = ko >> 5;
        const int d2 = WS_T2 + c8 * 245760 + (2 * (jj < 2 ? 2 * (pc & 7) + jj : 16 + (pc & 7)) + (pc >> 3)) * 5120 + (ko & 31) * 160 + lane * 16;
        td[j] = sel(hw == 2 ? (lane < 48 && j < 10) : lane < 40, hw == 2 ? d1 : d2);
        PIN(td[j]);
      }
      PIN(vo12);
    }
    int l16 = lane * 16, h16 = ht * 16;
    PIN(l16); PIN(h16);
    gu32* fp1 = FLAG(1, c16) + lane;
    gu32* fp78 = lane < 32 ? FLAG(7, c8) + lane : FLAG(8, c16) + t;
    gu32* fp4 = FLAG(4, 2 * r64 + (g & 1)) + t;
    gu32* fp1w = FLAG(1, c16) + r128 * 4 + hw;
    PIN(fp1); PIN(fp78); PIN(fp4); PIN(fp1w);
    ((LAS v4u*)(L + L_KVF))[ht] = v4u{0u, 0u, 0u, 0u};
    ((LAS v4u*)(L + L_KVF))[256 + ht] = v4u{0u, 0u, 0u, 0u};
    asm volatile("" ::: "memory");
    if (hw != 0) {
      if (hw == 1) {
#pragma unroll
        for (int j = 0; j < 5; ++j) {
          const int i8 = 5 * r128 + j;
          st128(tv[j], rs, sel(lane < 40, (i8 & 3) * f40m + f40l), WS_TKV + c16 * 153600 + 102400 + (i8 >> 2) * 2560);
        }
#pragma unroll
        for (int j = 0; j < 5; ++j) {
          const int pp = r64 >> 1, e = 20 * (r64 & 1) + 4 * j;
          const int R = e < 32 ? 32 * pp + e : 32 * (16 + (pp >> 2)) + 8 * (pp & 3) + e - 32;
          st128(tv[5 + j], rs, sel(lane < 40, lane * 16), WS_TO + c8 * 102400 + R * 160);
        }
      }
      if (hw == 3) {
        if (lane < 24) *(LAS f32x4*)(L + L_B1 + lane * 16) = f32x4{bf_lo(ub1.x), bf_hi(ub1.x), bf_lo(ub1.y), bf_hi(ub1.y)};
        if (lane < 20) *(LAS f32x4*)(L + L_B2 + lane * 16) = f32x4{bf_lo(ub2.x), bf_hi(ub2.x), bf_lo(ub2.y), bf_hi(ub2.y)};
      }
      asm volatile("" :: "v"(pf0));
      PIN(tag); tag += 1u;
      DRAIN();
      if (hw == 1 && lane < 2) flag_set(lane == 0 ? FLAG(8, c16) + r128 : FLAG(7, c8) + r64, tag);
    }
    if (hw == 0) {
      if (F6W) __builtin_amdgcn_s_sleep(F6W);
      wait_flag(FLAG(6, c16) + lane, 16, tag, lane);
      sig[0] = 1u;
    }
    sig_wait(sig);
    STAMP(1);
    const int kvq = WS_TKV + c16 * 153600, wos = WS_TO + c8 * 102400;
    dma_image<0, 24>(lds0 + L_BKV, l16, srd_ws, kvq, hw, 3 * (bi >> 3));
    dma_image<24, 56>(lds0 + L_BKV, l16, srd_ws, kvq, hw, 4 * (bi >> 3));
    asm volatile("s_waitcnt vmcnt(8)" ::: "memory");
    STAMP(2);
    BAR_RAW();
    dma_image<56, 100>(lds0 + L_BKV, l16, srd_ws, kvq, hw, 5 * (bi >> 3));
    asm volatile("s_waitcnt vmcnt(10)" ::: "memory");
    BAR_RAW();
    if (hw == 1) wait_flag(fp78, 48, tag, lane);
    DRAIN();
    STAMP(3);
    BAR_RAW();
    if (QPOS == 0) dma_image<100, 150>(lds0 + L_BKV, l16, srd_ws, kvq, hw, 6 * (bi >> 3));
    if (hw == 0) items();
    prog_wait(prog, 1);
    if (QPOS == 1) dma_image<100, 150>(lds0 + L_BKV, l16, srd_ws, kvq, hw, 6 * (bi >> 3));
    if (hw >= 2) {
      const __amdgpu_buffer_rsrc_t r12 = __builtin_amdgcn_make_buffer_rsrc((void*)(hw == 2 ? w1 : w2), 0, D * F * 2, 0x00020000);
      const int base = hw == 2 ? r128 * 122880 + c16 * 192 : r64 * 61440 + c8 * 160, step = hw == 2 ? 4 * 3072 : 4 * 1280;
#pragma unroll
      for (int j = 0; j < 12; ++j) tv[j] = __builtin_amdgcn_raw_buffer_load_b128(r12, vo12, (j < 10 || hw == 3) ? base + j * step : OOB, 0);
      asm volatile("s_waitcnt vmcnt(12)" ::: "memory");
    } else {
      dma_image<32, 68, 0, 2>(lds0 + L_WO, l16, srd_ws, wos, hw, bi >> 2);
      asm volatile("s_waitcnt vmcnt(18)" ::: "memory");
    }
    STAMP(4);
    BAR();
    __builtin_amdgcn_s_setprio(2);
    STAMP(11);
    {
      f32x4 pv[4];
#pragma unroll
      for (int ww = 0; ww < 4; ++ww) pv[ww] = *(LAS f32x4*)((LAS char*)red_off + ww * 8192);
      st128(__builtin_bit_cast(v4u, add4(add4(pv[0], pv[1]), add4(pv[2], pv[3]))), rs, kvp_off, 0);
      PROG(1);
      STAMP(12);
      DRAIN();
    }
    if (lane == 0) flag_set(fp1w, tag);
    if (hw == 0) {
      STAMP(10);
      __builtin_amdgcn_s_sleep(H1W);
      wait_flag(fp1, 64, tag, lane);
      sig[2] = 1u;
    } else {
      if (H1G) __builtin_amdgcn_s_sleep(H1G);
      dma_image<68, 100, 0, 3>(lds0 + L_WO, l16, srd_ws, wos, hw - 1, bi >> 2);
      prog_wait(prog + 1, 1);
      dma_image<0, 32, 0, 3>(lds0 + L_WO, l16, srd_ws, wos, hw - 1, 2 * (bi >> 2));
      if (hw >= 2) {
#pragma unroll
        for (int j = 0; j < 12; ++j) st128(tv[j], rs, td[j], 0);
      }
      STAMP(10);
      sig_wait(sig + 2);
    }
    STAMP(5);
    {
      v4u pv[16];
#pragma unroll
      for (int r = 0; r < 16; ++r) pv[r] = ld128(rs, h16, WS_KVP + (c16 * 16 + r) * 4096);
      SCHED();
      f32x4 v = __builtin_bit_cast(f32x4, pv[0]);
#pragma unroll
      for (int r = 1; r < 16; ++r) v = add4(v, __builtin_bit_cast(f32x4, pv[r]));
      v2u o; o.x = pk2(lo2(v)); o.y = pk2(hi2(v));
      *(LAS v2u*)((LAS char*)kvf_off) = o;
      if (ht >= 200) {
#pragma unroll
        for (int c = 1; c < 4; ++c) *(LAS v2u*)((LAS char*)kvf_off + c * 64) = o;
      }
    }
    DRAIN();
    STAMP(6);
    BAR();
    __builtin_amdgcn_s_setprio(0);
    if (hw >= 2 && lane == 0) flag_set(hw == 2 ? FLAG(5, c16) + r128 : FLAG(11, c8) + r64, tag);
    if (hw == 2) {
      f32x4 s0 = {0.f, 0.f, 0.f, 0.f}, s1 = {0.f, 0.f, 0.f, 0.f};
#pragma unroll
      for (int j = 0; j < 10; ++j) {
        s0 = add4(s0, f32x4{bf_lo(tv[j].x), bf_hi(tv[j].x), bf_lo(tv[j].y), bf_hi(tv[j].y)});
        s1 = add4(s1, f32x4{bf_lo(tv[j].z), bf_hi(tv[j].z), bf_lo(tv[j].w), bf_hi(tv[j].w)});
      }
#pragma unroll
      for (int d = 24; d >= 12; d -= 12) {
        f32x4 u0, u1;
#pragma unroll
        for (int e = 0; e < 4; ++e) { u0[e] = __shfl(s0[e], lane + d); u1[e] = __shfl(s1[e], lane + d); }
        s0 = add4(s0, u0); s1 = add4(s1, u1);
      }
      f32x4 sv;
#pragma unroll
      for (int e = 0; e < 4; ++e) { const float a0 = __shfl(s0[e], lane >> 1), a1 = __shfl(s1[e], lane >> 1); sv[e] = (lane & 1) ? a1 : a0; }
      st128(__builtin_bit_cast(v4u, sv), rs, sel(lane < 24, lane * 16), WS_CS + (c16 * 16 + r128) * 384);
      publish(FLAG(9, c16) + r128, tag, lane);
    }
    if (hw == 1) {
      __builtin_amdgcn_s_sleep(40);
      wait_flag(lane < 32 ? FLAG(11, c8) + lane : FLAG(5, c16) + t, 48, tag, lane);
      sig[3] = 1u;
    }
    sig_wait(sig + 3);
    prog_wait(prog, 2);
    if (hw == 0) {
      wait_flag(FLAG(9, c16) + lane, 16, tag, lane);
      v4u pv[16];
      const int co = sel(lane < 24, l16);
#pragma unroll
      for (int r = 0; r < 16; ++r) pv[r] = ld128(rs, co, WS_CS + (c16 * 16 + r) * 384);
      SCHED();
      f32x4 v = __builtin_bit_cast(f32x4, pv[0]);
#pragma unroll
      for (int r = 1; r < 16; ++r) v = add4(v, __builtin_bit_cast(f32x4, pv[r]));
      if (lane < 24) *(LAS f32x4*)(L + L_CS + lane * 16) = v;
    } else {
      dma_image<70, 120, 0, 3>(lds0 + L_W1, l16, srd_ws, WS_T1 + c16 * 122880, hw - 1, 6 * (bi >> 3));
      prog_wait(prog, 3);
      dma_image<0, 40, 0, 3>(lds0 + L_W1, l16, srd_ws, WS_T1 + c16 * 122880, hw - 1, 5 * (bi >> 3));
      STAMP(7);
      prog_wait(prog, 4);
      dma_image<40, 70, 0, 3>(lds0 + L_W1, l16, srd_ws, WS_T1 + c16 * 122880, hw - 1, 3 * (bi >> 3));
    }
    DRAIN();
    STAMP(8);
    BAR();
    const int w2s = WS_T2 + c8 * 245760;
    prog_wait(prog, 5);
    dma_image<0, 30>(lds0 + L_W2, l16, srd_ws, w2s, hw, bi >> 2);
    STAMP(14);
    prog_wait(prog, 6);
    dma_image<30, W2S>(lds0 + L_W2, l16, srd_ws, w2s, hw, bi >> 2);
    prog_wait(prog, 7);
    if (hw != 0) dma_image<W2S, 110, 0, 3>(lds0 + L_W2, l16, srd_ws, w2s, hw - 1, bi >> 2);
    DRAIN();
    STAMP(9);
    if (hw == 0) wait_flag(fp4, 32, tag, lane);
    STAMP(15);
    BAR_RAW();
    dma_image<110, 150>(lds0 + L_W2, l16, srd_ws, w2s, hw, 2 * (bi >> 2));
    prog_wait(prog, 8);
    if (hw == 0) wait_flag(fp4 + 6 * FLAG_SET / 4, 32, tag, lane);
    else dma_image<150, 198, 0, 3>(lds0 + L_W2 - 153600, l16, srd_ws, w2s, hw - 1, 3 * (bi >> 2));
    asm volatile("s_waitcnt vmcnt(16)" ::: "memory");
    STAMP(17);
    BAR_RAW();
    dma_image<198, 200>(lds0 + L_W2 - 153600, l16, srd_ws, w2s, hw, 0);
    dma_image<200, 240>(lds0 + L_W2 - 153600, l16, srd_ws, w2s, hw, 2 * (bi >> 2));
    asm volatile("s_waitcnt vmcnt(10)" ::: "memory");
    BAR_RAW();
    DRAIN();
    BAR_RAW();
  }

}
}

extern "C" void kernel_launch(void* const* d_in, const int* in_sizes, int n_in, void* d_out, int out_size, void* d_ws,
                              size_t ws_size, hipStream_t stream) {
  const unsigned short* x = (const unsigned short*)d_in[0];
  const unsigned short* wqkv = (const unsigned short*)d_in[1];
  const unsigned short* wo = (const unsigned short*)d_in[2];
  const unsigned short* w1 = (const unsigned short*)d_in[4];
  const unsigned short* b1 = (const unsigned short*)d_in[5];
  const unsigned short* w2 = (const unsigned short*)d_in[6];
  const unsigned short* b2 = (const unsigned short*)d_in[7];
  fused_block<<<256, 512, 0, stream>>>(x, wqkv, wo, w1, b1, w2, b2, (unsigned short*)d_out, (unsigned char*)d_ws);
}
```

```cpp
#include <hip/hip_runtime.h>
#include <hip/hip_bf16.h>

#define STAMPS 0
#define THR 0
#define TOUCH 0
#define CODEPF 1
#define FLAGPF 1
#define OUTAUX 0
#define OUTPF 0
#define S0 6
#define XRPOS 0
#define QPOS 0
#define H1W 8
#define H1G 2
#define W2S 70
#define F6W 0
#define PUBA 8
#define NCDELAY 0
#define HPRIO 0
#define WOLATE 0
#define SLEEP 1

#define GAS __attribute__((address_space(1)))
#define LAS __attribute__((address_space(3)))
#define RLX_AGENT __ATOMIC_RELAXED, __HIP_MEMORY_SCOPE_AGENT
typedef unsigned v4u __attribute__((ext_vector_type(4)));
typedef unsigned v2u __attribute__((ext_vector_type(2)));
typedef float f32x4 __attribute__((ext_vector_type(4)));
typedef float f32x2 __attribute__((ext_vector_type(2)));
typedef __bf16 bf16x8 __attribute__((ext_vector_type(8)));
typedef __bf16 bf16x2 __attribute__((ext_vector_type(2)));
typedef short s16x4 __attribute__((ext_vector_type(4)));
typedef GAS unsigned gu32;

namespace {
constexpr int S = 2048, D = 640, F = 1536;
constexpr float LN_EPS = 1e-5f;
constexpr int AUX_SC1 = 16;
constexpr unsigned kSpinCap = 1u << 17;
constexpr int OOB = 0x7FFFFF00;

constexpr int WS_EPOCH = 0;
constexpr int WS_FLAG = 4096;
constexpr int FLAG_SET = 64 * 256;
constexpr int WS_XN = 1 << 20;
constexpr int WS_O = WS_XN + 128 * 20 * 1024;
constexpr int WS_H = WS_O + 128 * 20 * 1024;
constexpr int WS_H1 = WS_H + 128 * 20 * 1024;
constexpr int WS_KVP = WS_H1 + 128 * 48 * 1024;
constexpr int WS_ST = WS_KVP + 16 * 16 * 4096;
constexpr int WS_DBG = WS_ST + 2048 * 64;
constexpr int WS_TKV = WS_DBG + 256 * 8 * 32 * 8;
constexpr int WS_TO = WS_TKV + 16 * 153600;
constexpr int WS_T1 = WS_TO + 8 * 102400;
constexpr int WS_T2 = WS_T1 + 16 * 122880;
constexpr int WS_CS = WS_T2 + 16 * 122880;
constexpr int WS_BYTES = WS_CS + 16 * 16 * 384;

constexpr int L_BKV = 0, L_BQ = 102400;
constexpr int L_RED = 0, L_KVF = 153600;
constexpr int L_WO = 0, L_W1 = 30720, L_W2 = 0, L_CS = 161792;
constexpr int L_B1 = L_CS + 1152, L_B2 = L_CS + 1536;
constexpr int LDS_BYTES = 163840;

__device__ __forceinline__ f32x4 mfma(v4u a, v4u b, f32x4 c) {
  return __builtin_amdgcn_mfma_f32_16x16x32_bf16(__builtin_bit_cast(bf16x8, a), __builtin_bit_cast(bf16x8, b), c, 0, 0, 0);
}
__device__ __forceinline__ unsigned pk2(f32x2 v) { return __builtin_bit_cast(unsigned, __builtin_convertvector(v, bf16x2)); }
__device__ __forceinline__ unsigned pk2(float lo, float hi) { return pk2(f32x2{lo, hi}); }
__device__ __forceinline__ float bf_lo(unsigned u) { return __builtin_bit_cast(float, u << 16); }
__device__ __forceinline__ float bf_hi(unsigned u) { return __builtin_bit_cast(float, u & 0xFFFF0000u); }
__device__ __forceinline__ f32x2 bf2(unsigned u) { return f32x2{bf_lo(u), bf_hi(u)}; }
__device__ __forceinline__ f32x2 lo2(f32x4 v) { return f32x2{v[0], v[1]}; }
__device__ __forceinline__ f32x2 hi2(f32x4 v) { return f32x2{v[2], v[3]}; }
__device__ __forceinline__ v4u mk_srd(const void* p, unsigned bytes) {
  const unsigned long long a = (unsigned long long)p;
  v4u r;
  r.x = (unsigned)a; r.y = (unsigned)(a >> 32) & 0xFFFFu; r.z = bytes; r.w = 0x00020000u;
  return r;
}
#define DRAIN() asm volatile("s_waitcnt vmcnt(0)" ::: "memory")
#define BAR() asm volatile("s_waitcnt lgkmcnt(0)\n\ts_barrier" ::: "memory")
#define BAR_RAW() asm volatile("s_barrier" ::: "memory")
#define SCHED() __builtin_amdgcn_sched_barrier(0)
#define PIN(x) asm volatile("" : "+v"(x))

__device__ __forceinline__ void dma2(unsigned lds_addr, unsigned voff, v4u srd, unsigned soff) {
  unsigned keep;
  asm volatile("s_mov_b32 %0, m0\n\ts_mov_b32 m0, %1\n\ts_nop 0\n\t"
               "buffer_load_dwordx4 %2, %3, %4 offen sc1 lds\n\t"
               "buffer_load_dwordx4 %2, %3, %4 offen offset:1024 sc1 lds\n\t"
               "s_mov_b32 m0, %0"
               : "=&s"(keep) : "s"(lds_addr), "v"(voff), "s"(srd), "s"(soff) : "memory");
}
template <int LO, int HI, int TH = 0, int NW = 4>
__device__ __forceinline__ void dma_image(unsigned lds_base, unsigned voff, v4u srd, int ws_off, int hw, int rot) {
  constexpr int NP = (HI - LO) / 2;
#pragma unroll 1
  for (int i = hw; i < NP; i += NW) {
    int j = i + rot;
    if (j >= NP) j -= NP;
    dma2(lds_base + LO * 1024 + j * 2048, voff, srd, ws_off + LO * 1024 + j * 2048);
    if (TH) asm volatile("s_waitcnt vmcnt(%0)" :: "i"(TH - 2) : "memory");
  }
}
__device__ __forceinline__ v2u rdtr(LAS char* p) { return __builtin_bit_cast(v2u, __builtin_amdgcn_ds_read_tr16_b64_v4i16((LAS s16x4*)p)); }
struct LBase {
  unsigned a[3];
  __device__ __forceinline__ explicit LBase(unsigned v) { a[0] = v; a[1] = v + 65536; a[2] = v + 131072; PIN(a[0]); PIN(a[1]); PIN(a[2]); }
  __device__ __forceinline__ v2u rd(int off) const { return rdtr((LAS char*)(a[off >> 16]) + (off & 65535)); }
};
template <int MT, int NT, int KS, int PD, bool WA, int M0 = 0, class RB, class GA, class PRE>
__device__ __forceinline__ void kloop(f32x4 (&acc)[MT][NT], RB rb, GA ga, PRE pre) {
  v2u bl[PD + 1][NT], bh[PD + 1][NT];
#pragma unroll
  for (int m = M0; m < MT; ++m)
#pragma unroll
    for (int s = 0; s < NT; ++s) acc[m][s] = f32x4{0.f, 0.f, 0.f, 0.f};
#pragma unroll
  for (int ks = 0; ks < PD; ++ks)
#pragma unroll
    for (int s = 0; s < NT; ++s) { bl[ks][s] = rb(ks, s, 0); bh[ks][s] = rb(ks, s, 1); }
  SCHED();
#pragma unroll
  for (int ks = 0; ks < KS; ++ks) {
    pre(ks);
#pragma unroll
    for (int s = 0; s < NT; ++s)
#pragma unroll
      for (int m = 0; m < MT; ++m) {
        const v2u lo = bl[ks % (PD + 1)][s], hi = bh[ks % (PD + 1)][s];
        const v4u wv = {lo.x, lo.y, hi.x, hi.y};
        acc[m][s] = WA ? mfma(wv, ga(ks, m), acc[m][s]) : mfma(ga(ks, m), wv, acc[m][s]);
        if (ks + PD < KS) {
          if (MT == 1 || m == 0) bl[(ks + PD) % (PD + 1)][s] = rb(ks + PD, s, 0);
          if (MT == 1 || m == 1) bh[(ks + PD) % (PD + 1)][s] = rb(ks + PD, s, 1);
        }
        SCHED();
      }
  }
}
constexpr int ld_hi(int ks, int s0, int W, int KS) {
  const int a = s0 + 2 * (ks + 1), b = ks + 1 + W, c = a < b ? a : b;
  return c < KS ? c : KS;
}
__device__ __forceinline__ v4u ld128(__amdgpu_buffer_rsrc_t rs, int voff, int soff) {
  return __builtin_amdgcn_raw_buffer_load_b128(rs, voff, soff, AUX_SC1);
}
__device__ __forceinline__ v2u ld64(__amdgpu_buffer_rsrc_t rs, int voff, int soff) {
  return __builtin_amdgcn_raw_buffer_load_b64(rs, voff, soff, AUX_SC1);
}
__device__ __forceinline__ void st128(v4u v, __amdgpu_buffer_rsrc_t rs, int voff, int soff) {
  __builtin_amdgcn_raw_buffer_store_b128(v, rs, (int)((unsigned)voff + (unsigned)soff), 0, AUX_SC1);
}
__device__ __forceinline__ int sel(bool c, int a) {
  PIN(a);
  return c ? a : OOB;
}
__device__ __forceinline__ v4u ld_split(__amdgpu_buffer_rsrc_t rs, int lane, int soff) {
  const v2u a = ld64(rs, lane * 8, soff), b = ld64(rs, lane * 8 + 512, soff);
  return v4u{a.x, a.y, b.x, b.y};
}
__device__ __forceinline__ void st64(v2u v, __amdgpu_buffer_rsrc_t rs, int voff, int soff) {
  __builtin_amdgcn_raw_buffer_store_b64(v, rs, voff, soff, AUX_SC1);
}
__device__ __forceinline__ void flag_set(gu32* p, unsigned tag) {
  asm volatile("global_atomic_swap %0, %1, off sc1" :: "v"(p), "v"(tag) : "memory");
}
__device__ __forceinline__ void wait_flag(gu32* p, int n, unsigned tag, int lane) {
#pragma unroll 1
  for (unsigned spins = 0; spins < kSpinCap; ++spins) {
    unsigned v = tag;
    if (lane < n) asm volatile("global_atomic_add %0, %1, %2, off sc0 sc1\n\ts_waitcnt vmcnt(0)" : "=&v"(v) : "v"(p), "v"(0u) : "memory");
    if (__all(v == tag)) break;
    if (SLEEP) __builtin_amdgcn_s_sleep(SLEEP);
  }
  asm volatile("" ::: "memory");
}
__device__ __forceinline__ void sig_wait(volatile LAS unsigned* p) {
#pragma unroll 1
  for (unsigned spins = 0; spins < 64 * kSpinCap; ++spins) {
    if (*p != 0u) break;
    __builtin_amdgcn_s_sleep(1);
  }
  asm volatile("" ::: "memory");
}
__device__ __forceinline__ void prog_wait(volatile LAS v4u* p, unsigned stage) {
#pragma unroll 1
  for (unsigned spins = 0; spins < 64 * kSpinCap; ++spins) {
    const v4u v = *p;
    if (min(min(v.x, v.y), min(v.z, v.w)) >= stage) break;
    __builtin_amdgcn_s_sleep(1);
  }
  asm volatile("" ::: "memory");
}
__device__ __forceinline__ void publish(gu32* flag, unsigned tag, int lane) {
  DRAIN();
  if (lane == 0) flag_set(flag, tag);
}
__device__ __forceinline__ f32x4 add4(f32x4 a, f32x4 b) {
  f32x4 r;
#pragma unroll
  for (int i = 0; i < 4; ++i) { float x; asm("v_add_f32 %0, %1, %2" : "=v"(x) : "v"(a[i]), "v"(b[i])); r[i] = x; }
  return r;
}
__device__ __forceinline__ float xsum32(float x) {
  unsigned u = __builtin_bit_cast(unsigned, x), v = u;
  asm("" : "+v"(v));
  auto r = __builtin_amdgcn_permlane32_swap(u, v, false, false);
  const unsigned a = r[0], b = r[1];
  return __builtin_bit_cast(float, a) + __builtin_bit_cast(float, b);
}
__device__ __forceinline__ float xsum16(float x) {
  unsigned u = __builtin_bit_cast(unsigned, x), v = u;
  asm("" : "+v"(v));
  auto r = __builtin_amdgcn_permlane16_swap(u, v, false, false);
  const unsigned a = r[0], b = r[1];
  return __builtin_bit_cast(float, a) + __builtin_bit_cast(float, b);
}
__device__ __forceinline__ float xsum8(float x) {
  return x + __builtin_bit_cast(float, __builtin_amdgcn_update_dpp(0, __builtin_bit_cast(int, x), 0x128, 0xf, 0xf, false));
}
__device__ __forceinline__ f32x2 phi2(f32x2 t) {
  const f32x2 u = t * 1.4426950408889634f, v = t + 1e-6f;
  const f32x2 e = {__builtin_amdgcn_fmed3f(__builtin_amdgcn_exp2f(u[0]), 0.f, 1.f), __builtin_amdgcn_fmed3f(__builtin_amdgcn_exp2f(u[1]), 0.f, 1.f)};
  return f32x2{fmaxf(v[0], 1e-6f), fmaxf(v[1], 1e-6f)} + e;
}
__device__ __forceinline__ f32x2 gelu2(f32x2 x) {
  const f32x2 u = x * (x * x * -0.1029432397f + -2.302208198f);
  const f32x2 d = f32x2{__builtin_amdgcn_exp2f(u[0]), __builtin_amdgcn_exp2f(u[1])} + 1.f;
  return x * f32x2{__builtin_amdgcn_rcpf(d[0]), __builtin_amdgcn_rcpf(d[1])};
}

#define STAMP(i) do { } while (0)

__global__ void __launch_bounds__(512, 2)
fused_block(const unsigned short* __restrict__ x, const unsigned short* __restrict__ wqkv, const unsigned short* __restrict__ wo,
            const unsigned short* __restrict__ w1, const unsigned short* __restrict__ b1, const unsigned short* __restrict__ w2,
            const unsigned short* __restrict__ b2, unsigned short* __restrict__ out, unsigned char* ws) {
  __shared__ __attribute__((aligned(16))) char smem[LDS_BYTES];
  unsigned long long pc0;
  asm volatile("s_getpc_b64 %0" : "=s"(pc0));
  LAS char* L = (LAS char*)smem;
  const unsigned lds0 = (unsigned)(size_t)L;
  const int tid = threadIdx.x, lane = tid & 63, w = __builtin_amdgcn_readfirstlane(tid >> 6);
  const int b = blockIdx.x, xcd = b & 7, bi = b >> 3;
  const int g = lane >> 4, t = lane & 15;
  const __amdgpu_buffer_rsrc_t rs = __builtin_amdgcn_make_buffer_rsrc((void*)ws, 0, WS_BYTES, 0x00020000);
  gu32* flags = (gu32*)(ws + WS_FLAG);
#define FLAG(set, grp) (flags + ((set) * FLAG_SET + (grp) * 256) / 4)
  asm volatile("" :: "s"(x), "s"(wqkv), "s"(wo), "s"(w1), "s"(b1), "s"(w2), "s"(b2), "s"(out), "s"(ws));
  STAMP(0);
  const int r128 = 4 * (xcd >> 1) + (bi >> 3), c16 = 8 * (xcd & 1) + (bi & 7);
  const int r64 = 8 * (xcd >> 1) + (bi >> 2), c8 = 4 * (xcd & 1) + (bi & 3);

  const __amdgpu_buffer_rsrc_t rx = __builtin_amdgcn_make_buffer_rsrc((void*)x, 0, S * D * 2, 0x00020000);
  const int hw = w - 4, ht = tid - 256;
  volatile LAS unsigned* sig = (volatile LAS unsigned*)(L + L_CS + 1024);
  volatile LAS v4u* prog = (volatile LAS v4u*)(L + L_CS + 1088);
#define PROG(n) do { asm volatile("" ::: "memory"); sig[16 + w] = (n); } while (0)

  const __amdgpu_buffer_rsrc_t rq = __builtin_amdgcn_make_buffer_rsrc((void*)wqkv, 0, D * 3 * D * 2, 0x00020000);
  v4u tv[12];
  unsigned ep;
  const int q10 = lane / 10, c10 = lane % 10;
  if (w < 4) {
#pragma unroll
    for (int j = 0; j < 3; ++j)
      tv[j] = __builtin_amdgcn_raw_buffer_load_b128(rx, (lane & 7) * 1280 + (lane >> 3) * 16, (j < 2 || w < 2) ? 8 * b * 1280 + (w + 4 * j) * 128 : OOB, 0);
    ep = __hip_atomic_load((gu32*)(ws + WS_EPOCH), RLX_AGENT);
  } else if (w == 4) {
    const int vo = sel(lane < 40, q10 * 3840 + (c10 < 5 ? 1280 : 2480) + c10 * 16);
#pragma unroll
    for (int j = 0; j < 10; ++j) tv[j] = __builtin_amdgcn_raw_buffer_load_b128(rq, vo, (40 * r128 + 4 * j) * 3840 + c16 * 80, 0);
  } else if (w == 7) {
    *(LAS v4u*)(L + L_CS + 512 + lane * 16) = v4u{0u, 0u, 0u, 0u};
  }
  BAR();

  if (w < 4) {
  unsigned tag = ep;
  const int grp32 = 4 * r128 + w;
  constexpr int AW = 6;

  {
    const int tok = 8 * b + (lane & 7), cl = lane >> 3;
    const bool has3 = w < 2;
    int xd = ((tok >> 4) * 20 + 2 * w + (cl >> 2)) * 1024 + ((cl & 3) * 16 + (tok & 15)) * 16;
    PIN(xd);
    SCHED();
    f32x2 f[12];
#pragma unroll
    for (int j = 0; j < 3; ++j)
#pragma unroll
      for (int e = 0; e < 4; ++e) f[j * 4 + e] = bf2(tv[j][e]);
    f32x2 s2 = (f[0] + f[1]) + (f[2] + f[3]) + ((f[4] + f[5]) + (f[6] + f[7])) + ((f[8] + f[9]) + (f[10] + f[11]));
    const float mloc = xsum32(xsum16(xsum8(s2[0] + s2[1]))) * (has3 ? 1.f / 192.f : 1.f / 128.f);
    const float m3 = has3 ? mloc : 0.f;
    f32x2 q2 = {0.f, 0.f}, q3 = {0.f, 0.f};
#pragma unroll
    for (int j = 0; j < 12; j += 2) {
      const f32x2 d0 = f[j] - (j < 8 ? mloc : m3), d1 = f[j + 1] - (j < 8 ? mloc : m3);
      q2 += d0 * d0; q3 += d1 * d1;
    }
    q2 += q3;
    const float m2loc = xsum32(xsum16(xsum8(q2[0] + q2[1])));
    if (lane < 8) *(volatile LAS f32x4*)(L + L_CS + 512 + (lane * 4 + w) * 16) = f32x4{mloc, m2loc, 1.f, 1.f};
    STAMP(25);
    f32x4 pr[4];
#pragma unroll 1
    for (unsigned spins = 0; spins < 64 * kSpinCap; ++spins) {
      bool ok = true;
#pragma unroll
      for (int ww = 0; ww < 4; ++ww) {
        pr[ww] = *(volatile LAS f32x4*)(L + L_CS + 512 + ((lane & 7) * 4 + ww) * 16);
        ok = ok && pr[ww][2] == 1.f;
      }
      if (__all(ok)) break;
    }
    const f32x4 p01 = {pr[0][0], pr[0][1], pr[1][0], pr[1][1]}, p23 = {pr[2][0], pr[2][1], pr[3][0], pr[3][1]};
    const float mean = (p01[0] + p01[2]) * 0.3f + (p23[0] + p23[2]) * 0.2f;
    const float e0 = p01[0] - mean, e1 = p01[2] - mean, e2 = p23[0] - mean, e3 = p23[2] - mean;
    const float m2 = (p01[1] + p01[3]) + (p23[1] + p23[3]) + 192.f * (e0 * e0 + e1 * e1) + 128.f * (e2 * e2 + e3 * e3);
    const float rstd = __builtin_amdgcn_rsqf(m2 * (1.f / D) + LN_EPS);
#pragma unroll
    for (int j = 0; j < 3; ++j) {
      v4u o;
#pragma unroll
      for (int e = 0; e < 4; ++e) o[e] = pk2((f[j * 4 + e] - mean) * rstd);
      st128(o, rs, (j < 2 || has3) ? xd : OOB, WS_XN + j * 8192);
    }
    STAMP(26);
    PIN(tag); tag += 1u;
    publish(FLAG(0, b >> 2) + (b & 3) * 4 + w, tag, lane);
    if (w == 0) sig[6] = 1u;
  }

  STAMP(1);

  v4u xr[3];
  {
    wait_flag(FLAG(0, grp32) + lane, 16, tag, lane);
    STAMP(2);
    v4u a[20][2];
    const int abase = WS_XN + 2 * grp32 * 20 * 1024;
#pragma unroll
    for (int ks = 0; ks < S0; ++ks)
#pragma unroll
      for (int m = 0; m < 2; ++m) a[ks][m] = ld128(rs, lane * 16, abase + (m * 20 + ks) * 1024);
    SCHED();
    BAR_RAW();
    STAMP(27);
    {
      f32x4 acc[2][5];
      const LBase bp(lds0 + L_BKV + (lane >> 2) * 160 + (lane & 3) * 8);
      kloop<2, 5, 20, 1, false>(acc, [&](int ks, int s, int h) { return bp.rd(ks * 5120 + h * 2560 + s * 32); },
        [&](int ks, int m) { return a[ks][m]; },
        [&](int ks) {
#pragma unroll
          for (int j = ld_hi(ks - 1, S0, AW, 20); j < ld_hi(ks, S0, AW, 20); ++j)
#pragma unroll
            for (int m = 0; m < 2; ++m) a[j][m] = ld128(rs, lane * 16, abase + (m * 20 + j) * 1024);
          if (ks == 3 || ks == 10) BAR_RAW();
          if (ks == 20 - AW) PROG(1);
        });
      STAMP(3);
      v4u ka[3], vb[3];
#pragma unroll
      for (int s = 0; s < 3; ++s)
#pragma unroll
        for (int m = 0; m < 2; ++m) {
          vb[s][2 * m] = pk2(lo2(acc[m][s + 2])); vb[s][2 * m + 1] = pk2(hi2(acc[m][s + 2]));
        }
#pragma unroll
      for (int e = 0; e < 4; ++e) vb[0][e] = t < 8 ? 0x3F803F80u : vb[0][e];
      const f32x4 z4 = {0.f, 0.f, 0.f, 0.f};
      LAS f32x4* red = (LAS f32x4*)(L + L_RED) + w * 512 + lane;
#pragma unroll
      for (int s = 0; s < 3; ++s) {
#pragma unroll
        for (int m = 0; m < 2; ++m) { ka[s][2 * m] = pk2(phi2(lo2(acc[m][s]))); ka[s][2 * m + 1] = pk2(phi2(hi2(acc[m][s]))); }
#pragma unroll
        for (int sb = 0; sb < 3; ++sb)
          if (s > 0 || sb < 2) red[(3 * s + sb - (s > 0)) * 64] = mfma(ka[s], vb[sb], z4);
      }
      STAMP(28);
      BAR();
    }
    STAMP(4);
    f32x4 qa[2][3];
    {
      unsigned bp0 = lds0 + L_BQ + (lane >> 2) * 64 + (lane & 3) * 8 + (g & 1) * 32;
      unsigned bp1 = lds0 + L_BQ + (lane >> 2) * 64 + (lane & 3) * 8 + 32 - (g & 1) * 32;
      unsigned bp2 = lds0 + L_BQ + 2048 + (lane >> 2) * 16 + (lane & 1) * 8;
      PIN(bp0); PIN(bp1); PIN(bp2);
      kloop<2, 3, 20, 2, true>(qa,
        [&](int ks, int s, int h) {
          return s == 0 ? rdtr((LAS char*)bp0 + ks * 2560 + h * 1024) : s == 1 ? rdtr((LAS char*)bp1 + ks * 2560 + h * 1024) : rdtr((LAS char*)bp2 + ks * 2560 + h * 256);
        },
        [&](int ks, int m) { return a[ks][m]; },
        [&](int ks) {});
    }
    v4u pq0[2], pq1[2];
#pragma unroll
    for (int m = 0; m < 2; ++m) {
      pq0[m].x = pk2(phi2(lo2(qa[m][0]))); pq0[m].y = pk2(phi2(hi2(qa[m][0])));
      pq0[m].z = pk2(phi2(lo2(qa[m][1]))); pq0[m].w = pk2(phi2(hi2(qa[m][1])));
      const unsigned lo = pk2(phi2(lo2(qa[m][2]))), hi = pk2(phi2(hi2(qa[m][2])));
      pq1[m].x = g < 2 ? lo : 0u; pq1[m].y = g < 2 ? hi : 0u; pq1[m].z = 0u; pq1[m].w = 0u;
      PIN(pq0[m]); PIN(pq1[m]);
    }
    STAMP(5);
    BAR_RAW();
#pragma unroll
    for (int j = 0; j < 3; ++j)
      xr[j] = __builtin_amdgcn_raw_buffer_load_b128(rx, sel(j < 2 || g < 2, t * 1280 + g * 16), (4 * r64 + w) * 16 * 1280 + c8 * 160 + j * 64, 0);
    STAMP(17);
    {
      v4u kv[4][2];
#pragma unroll
      for (int es = 3; es >= 0; --es)
#pragma unroll
        for (int ks = 0; ks < 2; ++ks) kv[es][ks] = *(LAS v4u*)(L + L_KVF + (es * 2 + ks) * 1024 + lane * 16);
      SCHED();
      f32x4 o[2][4];
#pragma unroll
      for (int es = 3; es >= 0; --es)
#pragma unroll
        for (int m = 0; m < 2; ++m) o[m][es] = mfma(kv[es][0], pq0[m], f32x4{0.f, 0.f, 0.f, 0.f});
#pragma unroll
      for (int es = 3; es >= 0; --es)
#pragma unroll
        for (int m = 0; m < 2; ++m) o[m][es] = mfma(kv[es][1], pq1[m], o[m][es]);
#pragma unroll
      for (int m = 0; m < 2; ++m) {
        const float i0 = __builtin_amdgcn_rcpf(o[m][3][0] + 1e-6f), i1 = __builtin_amdgcn_rcpf(o[m][3][1] + 1e-6f);
        const float im = g == 0 ? i0 : i1;
        v4u ov;
        ov.x = pk2(lo2(o[m][0]) * i0); ov.y = pk2(hi2(o[m][0]) * i0);
        ov.z = pk2(lo2(o[m][1]) * im); ov.w = pk2(hi2(o[m][1]) * im);
        v2u ol; ol.x = pk2(lo2(o[m][2]) * i1); ol.y = pk2(hi2(o[m][2]) * i1);
        const int tt = 2 * grp32 + m;
        st128(ov, rs, lane * 16, WS_O + (tt * 20 + c16) * 1024);
        st64(ol, rs, g < 2 ? lane * 8 : OOB, WS_O + (tt * 20 + 16) * 1024 + c16 * 256);
      }
      STAMP(18);
      publish(FLAG(2, grp32) + c16, tag, lane);
    }
    STAMP(7);
  }

  {
    const int tt = 4 * r64 + w;
    v4u eye[2];
#pragma unroll
    for (int hf = 0; hf < 2; ++hf) {
      const int e = 16 * hf + t - 8 * g;
#pragma unroll
      for (int d = 0; d < 4; ++d) eye[hf][d] = e == 2 * d ? 0x3F80u : e == 2 * d + 1 ? 0x3F800000u : 0u;
      PIN(eye[hf]);
    }
    f32x4 acc[1][5];
#pragma unroll
    for (int s = 0; s < 5; ++s) { acc[0][s] = mfma(eye[s & 1], xr[s >> 1], f32x4{0.f, 0.f, 0.f, 0.f}); PIN(acc[0][s]); }
    wait_flag(FLAG(2, tt >> 1) + lane, 16, tag, lane);
    STAMP(8);
    if (b == 0 && tid == 0) __hip_atomic_store((gu32*)(ws + WS_EPOCH), tag, RLX_AGENT);
    v4u a[20];
#pragma unroll
    for (int ks = 0; ks < 2 * S0; ++ks) a[ks] = ld128(rs, lane * 16, WS_O + (tt * 20 + ks) * 1024);
    SCHED();
    const LBase bp(lds0 + L_WO + (lane >> 2) * 160 + (lane & 3) * 8);
    kloop<1, 5, 20, 1, true, 1>(acc, [&](int ks, int s, int h) { return bp.rd(ks * 5120 + h * 2560 + s * 32); },
      [&](int ks, int m) { return a[ks]; },
      [&](int ks) {
#pragma unroll
        for (int j = ld_hi(ks - 1, 2 * S0, 2 * AW, 20); j < ld_hi(ks, 2 * S0, 2 * AW, 20); ++j)
          a[j] = j < 16 ? ld128(rs, lane * 16, WS_O + (tt * 20 + j) * 1024) : ld_split(rs, lane, WS_O + (tt * 20 + j) * 1024);
        if (ks == 8) PROG(2);
        if (ks == 14) PROG(3);
      });
    PROG(4);
    STAMP(9);
    v2u hb[5];
    f32x2 hv[10];
#pragma unroll
    for (int s = 0; s < 5; ++s) {
      hb[s].x = pk2(lo2(acc[0][s]));
      hb[s].y = pk2(hi2(acc[0][s]));
      hv[2 * s] = bf2(hb[s].x); hv[2 * s + 1] = bf2(hb[s].y);
    }
    st128(v4u{hb[0].x, hb[0].y, hb[1].x, hb[1].y}, rs, lane * 16, WS_H + (tt * 20 + 2 * c8) * 1024);
    st128(v4u{hb[2].x, hb[2].y, hb[3].x, hb[3].y}, rs, lane * 16, WS_H + (tt * 20 + 2 * c8 + 1) * 1024);
    st64(hb[4], rs, lane * 8, WS_H + (tt * 20 + 16) * 1024 + c8 * 512);
    const f32x2 s2 = ((hv[0] + hv[1]) + (hv[2] + hv[3])) + ((hv[4] + hv[5]) + (hv[6] + hv[7])) + (hv[8] + hv[9]);
    const float mc = xsum32(xsum16(s2[0] + s2[1])) * (1.f / 80.f);
    f32x2 q2 = {0.f, 0.f}, q3 = {0.f, 0.f};
#pragma unroll
    for (int j = 0; j < 10; j += 2) { const f32x2 d0 = hv[j] - mc, d1 = hv[j + 1] - mc; q2 += d0 * d0; q3 += d1 * d1; }
    q2 += q3;
    const float m2 = xsum32(xsum16(q2[0] + q2[1]));
    st64(v2u{__builtin_bit_cast(unsigned, mc), __builtin_bit_cast(unsigned, m2)}, rs, g == 0 ? t * 8 : OOB, WS_ST + (tt * 8 + c8) * 128);
    STAMP(20);
    publish(FLAG(3, tt >> 1) + (tt & 1) * 8 + c8, tag, lane);
    STAMP(10);
  }

  {
    wait_flag(FLAG(3, grp32) + lane, 16, tag, lane);
    STAMP(11);
    v4u stt[2];
#pragma unroll
    for (int m = 0; m < 2; ++m) {
      const v2u s0 = ld64(rs, g * 256 + t * 8, WS_ST + (2 * grp32 + m) * 1024), s1 = ld64(rs, g * 256 + 128 + t * 8, WS_ST + (2 * grp32 + m) * 1024);
      stt[m] = v4u{s0.x, s0.y, s1.x, s1.y};
    }
    v4u a[20][2];
    const int abase = WS_H + 2 * grp32 * 20 * 1024;
#pragma unroll
    for (int ks = 0; ks < S0; ++ks)
#pragma unroll
      for (int m = 0; m < 2; ++m) a[ks][m] = ld128(rs, lane * 16, abase + (m * 20 + ks) * 1024);
    SCHED();
    float nm[2], rr[2];
#pragma unroll
    for (int m = 0; m < 2; ++m) {
      const f32x4 sv = __builtin_bit_cast(f32x4, stt[m]);
      const float mean = xsum32(xsum16(sv[0] + sv[2])) * 0.125f;
      const float m2 = xsum32(xsum16(sv[1] + sv[3] + 80.f * ((sv[0] - mean) * (sv[0] - mean) + (sv[2] - mean) * (sv[2] - mean))));
      const float var = m2 * (1.f / D);
      const float r1 = __builtin_amdgcn_rsqf(var + LN_EPS);
      rr[m] = r1 * __builtin_amdgcn_rsqf(var * r1 * r1 + LN_EPS);
      nm[m] = -mean * rr[m];
      PIN(rr[m]); PIN(nm[m]);
    }
    BAR_RAW();
    STAMP(22);
    const unsigned l64 = lds0 + (lane >> 2) * 64 + (lane & 3) * 8;
    const LBase bp0(l64 + (g & 1) * 32), bp1(l64 + 32 - (g & 1) * 32);
    const int h1base = WS_H1 + 2 * grp32 * 48 * 1024;
    auto finish = [&](const f32x4& c0, const f32x4& c1, int j, int m) {
      v4u o;
#pragma unroll
      for (int s = 0; s < 2; ++s) {
        const f32x4 cs = *(LAS f32x4*)(L + L_CS + ((2 * j + s) * 4 + g) * 16), bb = *(LAS f32x4*)(L + L_B1 + ((2 * j + s) * 4 + g) * 16);
        const f32x4 c = s ? c1 : c0;
        o[2 * s] = pk2(gelu2(lo2(c) * rr[m] + (lo2(cs) * nm[m] + lo2(bb))));
        o[2 * s + 1] = pk2(gelu2(hi2(c) * rr[m] + (hi2(cs) * nm[m] + hi2(bb))));
      }
      st128(o, rs, lane * 16, h1base + (m * 48 + (c16 >> 3) * 24 + (j < 2 ? 2 * (c16 & 7) + j : 16 + (c16 & 7))) * 1024);
    };
    {
      f32x4 acc[2][4];
      kloop<2, 4, 20, 1, true>(acc, [&](int ks, int s, int h) { return ((s & 1) ? bp1 : bp0).rd(L_W1 + (s >> 1) * 40960 + ks * 2048 + h * 1024); },
        [&](int ks, int m) { return a[ks][m]; },
        [&](int ks) {
#pragma unroll
          for (int j = ld_hi(ks - 1, S0, AW, 20); j < ld_hi(ks, S0, AW, 20); ++j)
#pragma unroll
            for (int m = 0; m < 2; ++m)
              a[j][m] = j < 16 ? ld128(rs, lane * 16, abase + (m * 20 + j) * 1024) : ld_split(rs, lane, abase + (m * 20 + j) * 1024);
          if (ks == 20 - AW) PROG(5);
        });
      PROG(6);
      STAMP(12);
#pragma unroll
      for (int j = 0; j < 2; ++j)
#pragma unroll
        for (int m = 0; m < 2; ++m) finish(acc[m][2 * j], acc[m][2 * j + 1], j, m);
      SCHED();
      STAMP(24);
    }
    f32x4 accb[2][2];
    const int kh = w & 1, tt = 4 * r64 + w;
    v4u hr01, hr23;
    v2u hr4;
    kloop<2, 2, 20, 3, true>(accb, [&](int ks, int s, int h) { return (s ? bp1 : bp0).rd(L_W1 + 81920 + ks * 2048 + h * 1024); },
      [&](int ks, int m) { return a[ks][m]; },
      [&](int ks) {
        if (ks == PUBA) { publish(FLAG(4, grp32) + c16, tag, lane); PROG(7); }
        if (ks == PUBA + 2) {
          hr01 = ld128(rs, lane * 16, WS_H + (tt * 20 + 2 * c8) * 1024);
          hr23 = ld128(rs, lane * 16, WS_H + (tt * 20 + 2 * c8 + 1) * 1024);
          hr4 = ld64(rs, lane * 8, WS_H + (tt * 20 + 16) * 1024 + c8 * 512);
        }
      });
    STAMP(13);
    f32x4 acc[2][5];
    {
      const unsigned hrr[10] = {hr01.x, hr01.y, hr01.z, hr01.w, hr23.x, hr23.y, hr23.z, hr23.w, hr4.x, hr4.y};
#pragma unroll
      for (int s = 0; s < 5; ++s) {
        acc[0][s] = *(LAS f32x4*)(L + L_B2 + (s * 4 + g) * 16) + f32x4{bf_lo(hrr[2 * s]), bf_hi(hrr[2 * s]), bf_lo(hrr[2 * s + 1]), bf_hi(hrr[2 * s + 1])};
        PIN(acc[0][s]);
      }
    }
    int oo[3];
    unsigned tw, tr;
    {
      int ln = lane;
      PIN(ln);
      const int qq = ln / 10, cc = ln % 10;
#pragma unroll
      for (int j = 0; j < 3; ++j) { oo[j] = sel(ln < (j < 2 ? 60 : 40), ((tt * 16 + 6 * j + qq) * D + 80 * c8) * 2 + cc * 16); PIN(oo[j]); }
      tw = lds0 + L_W2 + 122880 + w * 3072 + t * 176 + g * 8; tr = lds0 + L_W2 + 122880 + w * 3072 + qq * 176 + cc * 16;
      PIN(tw); PIN(tr);
    }
    BAR_RAW();
    STAMP(14);
    constexpr int WIN = 8;
    v4u ar[WIN][2];
    const int a4[2] = {WS_H1 + (tt * 48 + kh * 24) * 1024, WS_H1 + ((tt ^ 1) * 48 + kh * 24) * 1024};
#pragma unroll
    for (int ks = 0; ks < WIN - 1; ++ks)
#pragma unroll
      for (int m = 0; m < 2; ++m) ar[ks][m] = ld128(rs, lane * 16, a4[m] + ks * 1024);
    SCHED();
#pragma unroll
    for (int m = 0; m < 2; ++m) finish(accb[m][0], accb[m][1], 2, m);
    STAMP(29);
    const LBase bp(lds0 + L_W2 + kh * 5120 + (lane >> 2) * 160 + (lane & 3) * 8);
    kloop<2, 5, 24, 1, true, 1>(acc, [&](int ks, int s, int h) { return bp.rd((ks % 15) * 10240 + h * 2560 + s * 32); },
      [&](int ks, int m) { return ar[ks % WIN][m]; },
      [&](int ks) {
        if (ks == 5) PROG(8);
        if (ks == 9 || ks == 14 || ks == 19) BAR_RAW();
        if (ks == 3) publish(FLAG(10, grp32) + c16, tag, lane);
        if (ks >= 3) {
#pragma unroll
          for (int j = ks == 3 ? WIN - 1 : ld_hi(ks - 1, 1, WIN - 1, 24); j < ld_hi(ks, 1, WIN - 1, 24); ++j)
#pragma unroll
            for (int m = 0; m < 2; ++m) ar[j % WIN][m] = ld128(rs, lane * 16, a4[m] + j * 1024);
        }
      });
    STAMP(15);
    LAS f32x4* xch = (LAS f32x4*)(L + L_W2 + 102400);
#pragma unroll
    for (int s = 0; s < 5; ++s) xch[((w ^ 1) * 5 + s) * 64 + lane] = acc[1][s];
    BAR();
    const __amdgpu_buffer_rsrc_t rout = __builtin_amdgcn_make_buffer_rsrc((void*)out, 0, S * D * 2, 0x00020000);
    f32x4 ot[5];
#pragma unroll
    for (int s = 0; s < 5; ++s) ot[s] = xch[(w * 5 + s) * 64 + lane];
    SCHED();
#pragma unroll
    for (int s = 0; s < 5; ++s) {
      const f32x4 v = acc[0][s] + ot[s];
      *(LAS v2u*)((LAS char*)tw + s * 32) = v2u{pk2(lo2(v)), pk2(hi2(v))};
    }
    asm volatile("" ::: "memory");
    v4u ov[3];
#pragma unroll
    for (int j = 0; j < 3; ++j) ov[j] = *(LAS v4u*)((LAS char*)tr + j * 6 * 176);
#pragma unroll
    for (int j = 0; j < 3; ++j) __builtin_amdgcn_raw_buffer_store_b128(ov[j], rout, oo[j], 0, 0);
    STAMP(16);
  }
  return;
  }

  {
    const v4u srd_ws = mk_srd(ws, WS_BYTES);
    STAMP(16);
    const int q5 = lane / 5, c5 = lane % 5;
    const int f40m = c5 < 4 ? 512 : 128, f40l = c5 < 4 ? q5 * 64 + (c5 ^ (2 * ((q5 >> 2) & 1))) * 16 : 2048 + q5 * 16;
    v2u ub1, ub2;
    unsigned pf0;
    v4u tq[10];
    if (hw == 1) {
      const __amdgpu_buffer_rsrc_t ro = __builtin_amdgcn_make_buffer_rsrc((void*)wo, 0, D * D * 2, 0x00020000);
      const int vq = sel(lane < 40, (8 * (q5 >> 2) + (q5 & 3)) * 3840 + c5 * 16), vo = sel(lane < 40, q10 * 1280 + c10 * 16);
#pragma unroll
      for (int j = 0; j < 5; ++j) {
        const int i8 = 5 * r128 + j, k = 32 * (i8 >> 2) + 16 * (i8 & 1) + 4 * ((i8 >> 1) & 1);
        tq[j] = __builtin_amdgcn_raw_buffer_load_b128(rq, vq, k * 3840 + c16 * 80, 0);
      }
#pragma unroll
      for (int j = 0; j < 5; ++j) tq[5 + j] = __builtin_amdgcn_raw_buffer_load_b128(ro, vo, (20 * r64 + 4 * j) * 1280 + c8 * 160, 0);
    } else if (hw == 2) {
      const GAS unsigned char* cb = (const GAS unsigned char*)(pc0 & ~127ull);
      pf0 = *(const GAS unsigned*)(cb + (min(lane, 10) * 32 + bi) * 128);
      if (lane < 6) {
        gu32* fp = FLAG(3 * (b >> 6) + (lane >> 1), b & 63) + (lane & 1) * 32;
        asm volatile("global_atomic_add %0, %1, off sc1" :: "v"(fp), "v"(0u) : "memory");
      }
    } else if (hw == 3) {
      const __amdgpu_buffer_rsrc_t rb1 = __builtin_amdgcn_make_buffer_rsrc((void*)b1, 0, F * 2, 0x00020000);
      const __amdgpu_buffer_rsrc_t rb2 = __builtin_amdgcn_make_buffer_rsrc((void*)b2, 0, D * 2, 0x00020000);
      ub1 = __builtin_amdgcn_raw_buffer_load_b64(rb1, lane * 8, c16 * 192, 0);
      ub2 = __builtin_amdgcn_raw_buffer_load_b64(rb2, lane * 8, c8 * 160, 0);
    }
    unsigned tag = __hip_atomic_load((gu32*)(ws + WS_EPOCH), RLX_AGENT);
    if (hw == 0) {
      const int dof = sel(lane < 40, lane * 16);
#pragma unroll
      for (int j = 0; j < 10; ++j) {
        const int k = 40 * r128 + 4 * j, R = (k & ~31) + 16 * ((k >> 2) & 1) + 4 * ((k >> 3) & 3);
        st128(tv[j], rs, dof, WS_TKV + c16 * 153600 + R * 160);
      }
      STAMP(18);
      PIN(tag); tag += 1u;
      publish(FLAG(6, c16) + r128, tag, lane);
      STAMP(13);
    }
    asm volatile("" ::: "memory");
    int red_off, kvf_off, kvp_off;
    auto items = [&]() {
      const int it = ht < 210 ? ht : 209, ie = it / 5, ic = it % 5;
      const int ihead = ie < 40 ? (ie >= 20) : ie - 40;
      const int id = 20 * ihead + 4 * ic, dg = id >> 2;
      const int sa = id >> 4, col = 40 + ie;
      const int tile = 3 * sa + (ie < 40 ? (col >> 4) - 2 : 0) - (sa > 0);
      red_off = lds0 + L_RED + (tile * 64 + (dg & 3) * 16 + (ie < 40 ? (col & 15) : 0)) * 16;
      kvf_off = lds0 + L_KVF + ((ie < 40 ? (ie >> 4) : 3) * 2 + (dg >> 3)) * 1024 + ((dg & 3) * 16 + (ie < 40 ? (ie & 15) : ihead)) * 16 + ((dg >> 2) & 1) * 8;
      kvp_off = WS_KVP + (c16 * 16 + r128) * 4096 + ht * 16;
      PIN(red_off); PIN(kvf_off); PIN(kvp_off);
    };
    if (hw != 0) items();
    int vo12, td[12];
    if (hw >= 2) {
      const int q12 = lane / 12, c12 = lane % 12;
      vo12 = hw == 2 ? sel(lane < 48, q12 * 3072 + c12 * 16) : sel(lane < 40, q10 * 1280 + c10 * 16);
#pragma unroll
      for (int j = 0; j < 12; ++j) {
        const int cc = r128 >> 1, off = 40 * (r128 & 1) + 4 * j;
        const int R = off < 64 ? 64 * cc + off : 32 * (16 + (cc >> 1)) + 16 * (cc & 1) + off - 64;
        const int d1 = WS_T1 + c16 * 122880 + (c12 >> 2) * 40960 + (R + q12) * 64 + ((c12 & 3) ^ (2 * ((R >> 2) & 1))) * 16;
        const int pc = r64 >> 1, ko = 48 * (r64 & 1) + 4 * j, jj = ko >> 5;
        const int d2 = WS_T2 + c8 * 245760 + (2 * (jj < 2 ? 2 * (pc & 7) + jj : 16 + (pc & 7)) + (pc >> 3)) * 5120 + (ko & 31) * 160 + lane * 16;
        td[j] = sel(hw == 2 ? (lane < 48 && j < 10) : lane < 40, hw == 2 ? d1 : d2);
        PIN(td[j]);
      }
      PIN(vo12);
    }
    int l16 = lane * 16, h16 = sel(ht < 210, ht * 16);
    PIN(l16); PIN(h16);
    gu32* fp1 = FLAG(1, c16) + lane;
    gu32* fp78 = lane < 32 ? FLAG(7, c8) + lane : FLAG(8, c16) + t;
    gu32* fp4 = FLAG(4, 2 * r64 + (g & 1)) + t;
    gu32* fp1w = FLAG(1, c16) + r128 * 4 + hw;
    PIN(fp1); PIN(fp78); PIN(fp4); PIN(fp1w);
    ((LAS v4u*)(L + L_KVF))[ht] = v4u{0u, 0u, 0u, 0u};
    ((LAS v4u*)(L + L_KVF))[256 + ht] = v4u{0u, 0u, 0u, 0u};
    asm volatile("" ::: "memory");
    if (hw != 0) {
      if (hw == 1) {
        sig_wait(sig + 6);
#pragma unroll
        for (int j = 0; j < 5; ++j) {
          const int i8 = 5 * r128 + j;
          st128(tq[j], rs, sel(lane < 40, (i8 & 3) * f40m + f40l), WS_TKV + c16 * 153600 + 102400 + (i8 >> 2) * 2560);
        }
#pragma unroll
        for (int j = 0; j < 5; ++j) {
          const int pp = r64 >> 1, e = 20 * (r64 & 1) + 4 * j;
          const int R = e < 32 ? 32 * pp + e : 32 * (16 + (pp >> 2)) + 8 * (pp & 3) + e - 32;
          st128(tq[5 + j], rs, sel(lane < 40, lane * 16), WS_TO + c8 * 102400 + R * 160);
        }
      }
      if (hw == 3) {
        if (lane < 24) *(LAS f32x4*)(L + L_B1 + lane * 16) = f32x4{bf_lo(ub1.x), bf_hi(ub1.x), bf_lo(ub1.y), bf_hi(ub1.y)};
        if (lane < 20) *(LAS f32x4*)(L + L_B2 + lane * 16) = f32x4{bf_lo(ub2.x), bf_hi(ub2.x), bf_lo(ub2.y), bf_hi(ub2.y)};
      }
      if (hw == 2) asm volatile("" :: "v"(pf0));
      PIN(tag); tag += 1u;
      DRAIN();
      if (hw == 1 && lane < 2) flag_set(lane == 0 ? FLAG(8, c16) + r128 : FLAG(7, c8) + r64, tag);
    }
    if (hw == 0) {
      if (F6W) __builtin_amdgcn_s_sleep(F6W);
      wait_flag(FLAG(6, c16) + lane, 16, tag, lane);
      sig[0] = 1u;
    }
    sig_wait(sig);
    STAMP(1);
    const int kvq = WS_TKV + c16 * 153600, wos = WS_TO + c8 * 102400;
    dma_image<0, 24>(lds0 + L_BKV, l16, srd_ws, kvq, hw, 3 * (bi >> 3));
    dma_image<24, 56>(lds0 + L_BKV, l16, srd_ws, kvq, hw, 4 * (bi >> 3));
    asm volatile("s_waitcnt vmcnt(8)" ::: "memory");
    STAMP(2);
    BAR_RAW();
    dma_image<56, 100>(lds0 + L_BKV, l16, srd_ws, kvq, hw, 5 * (bi >> 3));
    asm volatile("s_waitcnt vmcnt(10)" ::: "memory");
    BAR_RAW();
    if (hw == 1) wait_flag(fp78, 48, tag, lane);
    DRAIN();
    STAMP(3);
    BAR_RAW();
    if (QPOS == 0) dma_image<100, 150>(lds0 + L_BKV, l16, srd_ws, kvq, hw, 6 * (bi >> 3));
    if (hw == 0) items();
    prog_wait(prog, 1);
    if (QPOS == 1) dma_image<100, 150>(lds0 + L_BKV, l16, srd_ws, kvq, hw, 6 * (bi >> 3));
    if (hw >= 2) {
      const __amdgpu_buffer_rsrc_t r12 = __builtin_amdgcn_make_buffer_rsrc((void*)(hw == 2 ? w1 : w2), 0, D * F * 2, 0x00020000);
      const int base = hw == 2 ? r128 * 122880 + c16 * 192 : r64 * 61440 + c8 * 160, step = hw == 2 ? 4 * 3072 : 4 * 1280;
#pragma unroll
      for (int j = 0; j < 12; ++j) tv[j] = __builtin_amdgcn_raw_buffer_load_b128(r12, vo12, (j < 10 || hw == 3) ? base + j * step : OOB, 0);
      asm volatile("s_waitcnt vmcnt(12)" ::: "memory");
    } else {
      dma_image<32, 68, 0, 2>(lds0 + L_WO, l16, srd_ws, wos, hw, bi >> 2);
      asm volatile("s_waitcnt vmcnt(18)" ::: "memory");
    }
    STAMP(4);
    BAR();
    __builtin_amdgcn_s_setprio(2);
    STAMP(11);
    {
      f32x4 pv[4];
#pragma unroll
      for (int ww = 0; ww < 4; ++ww) pv[ww] = *(LAS f32x4*)((LAS char*)red_off + ww * 8192);
      st128(__builtin_bit_cast(v4u, add4(add4(pv[0], pv[1]), add4(pv[2], pv[3]))), rs, kvp_off, 0);
      PROG(1);
      STAMP(12);
      DRAIN();
#pragma unroll
      for (int j = 0; j < 12; ++j) PIN(tv[j]);
    }
    if (lane == 0) flag_set(fp1w, tag);
    if (hw == 0) {
      STAMP(10);
      __builtin_amdgcn_s_sleep(H1W);
      wait_flag(fp1, 64, tag, lane);
      sig[2] = 1u;
    } else {
      if (H1G) __builtin_amdgcn_s_sleep(H1G);
      prog_wait(prog + 1, 1);
#pragma unroll
      for (int i = 0; i < 12; ++i) {
        if (hw >= 2) st128(tv[i], rs, td[i], 0);
        const int j = hw - 1 + 3 * i;
        if (j < 32) {
          const int pc = 2 * ((j + 4 * (bi >> 2)) & 31), po = (pc < 32 ? pc : pc + 36) * 1024;
          dma2(lds0 + L_WO + po, l16, srd_ws, wos + po);
        }
      }
      STAMP(10);
      sig_wait(sig + 2);
    }
    STAMP(5);
    {
      v4u pv[16];
#pragma unroll
      for (int r = 0; r < 16; ++r) pv[r] = ld128(rs, h16, WS_KVP + (c16 * 16 + r) * 4096);
      SCHED();
      f32x4 v = __builtin_bit_cast(f32x4, pv[0]);
#pragma unroll
      for (int r = 1; r < 16; ++r) v = add4(v, __builtin_bit_cast(f32x4, pv[r]));
      v2u o; o.x = pk2(lo2(v)); o.y = pk2(hi2(v));
      if (ht < 210) *(LAS v2u*)((LAS char*)kvf_off) = o;
      if (ht >= 200 && ht < 210) {
#pragma unroll
        for (int c = 1; c < 4; ++c) *(LAS v2u*)((LAS char*)kvf_off + c * 64) = o;
      }
    }
    DRAIN();
    STAMP(6);
    BAR();
    __builtin_amdgcn_s_setprio(0);
    if (hw >= 2 && lane == 0) flag_set(hw == 2 ? FLAG(5, c16) + r128 : FLAG(11, c8) + r64, tag);
    if (hw == 2) {
      f32x4 s0 = {0.f, 0.f, 0.f, 0.f}, s1 = {0.f, 0.f, 0.f, 0.f};
#pragma unroll
      for (int j = 0; j < 10; ++j) {
        s0 = add4(s0, f32x4{bf_lo(tv[j].x), bf_hi(tv[j].x), bf_lo(tv[j].y), bf_hi(tv[j].y)});
        s1 = add4(s1, f32x4{bf_lo(tv[j].z), bf_hi(tv[j].z), bf_lo(tv[j].w), bf_hi(tv[j].w)});
      }
#pragma unroll
      for (int d = 24; d >= 12; d -= 12) {
        f32x4 u0, u1;
#pragma unroll
        for (int e = 0; e < 4; ++e) { u0[e] = __shfl(s0[e], lane + d); u1[e] = __shfl(s1[e], lane + d); }
        s0 = add4(s0, u0); s1 = add4(s1, u1);
      }
      f32x4 sv;
#pragma unroll
      for (int e = 0; e < 4; ++e) { const float a0 = __shfl(s0[e], lane >> 1), a1 = __shfl(s1[e], lane >> 1); sv[e] = (lane & 1) ? a1 : a0; }
      st128(__builtin_bit_cast(v4u, sv), rs, sel(lane < 24, lane * 16), WS_CS + (c16 * 16 + r128) * 384);
      publish(FLAG(9, c16) + r128, tag, lane);
    }
    if (hw == 1) {
      __builtin_amdgcn_s_sleep(40);
      wait_flag(lane < 32 ? FLAG(11, c8) + lane : FLAG(5, c16) + t, 48, tag, lane);
      sig[3] = 1u;
    }
    sig_wait(sig + 3);
    prog_wait(prog, 2);
    if (hw == 0) {
      wait_flag(FLAG(9, c16) + lane, 16, tag, lane);
      v4u pv[16];
      const int co = sel(lane < 24, l16);
#pragma unroll
      for (int r = 0; r < 16; ++r) pv[r] = ld128(rs, co, WS_CS + (c16 * 16 + r) * 384);
      SCHED();
      f32x4 v = __builtin_bit_cast(f32x4, pv[0]);
#pragma unroll
      for (int r = 1; r < 16; ++r) v = add4(v, __builtin_bit_cast(f32x4, pv[r]));
      if (lane < 24) *(LAS f32x4*)(L + L_CS + lane * 16) = v;
    } else {
      dma_image<70, 120, 0, 3>(lds0 + L_W1, l16, srd_ws, WS_T1 + c16 * 122880, hw - 1, 6 * (bi >> 3));
      prog_wait(prog, 3);
      dma_image<0, 40, 0, 3>(lds0 + L_W1, l16, srd_ws, WS_T1 + c16 * 122880, hw - 1, 5 * (bi >> 3));
      STAMP(7);
      prog_wait(prog, 4);
      dma_image<40, 70, 0, 3>(lds0 + L_W1, l16, srd_ws, WS_T1 + c16 * 122880, hw - 1, 3 * (bi >> 3));
    }
    DRAIN();
    STAMP(8);
    BAR();
    const int w2s = WS_T2 + c8 * 245760;
    prog_wait(prog, 5);
    dma_image<0, 30>(lds0 + L_W2, l16, srd_ws, w2s, hw, bi >> 2);
    STAMP(14);
    prog_wait(prog, 6);
    dma_image<30, W2S>(lds0 + L_W2, l16, srd_ws, w2s, hw, bi >> 2);
    prog_wait(prog, 7);
    if (hw != 0) dma_image<W2S, 110, 0, 3>(lds0 + L_W2, l16, srd_ws, w2s, hw - 1, bi >> 2);
    DRAIN();
    STAMP(9);
    if (hw == 0) wait_flag(fp4, 32, tag, lane);
    STAMP(15);
    BAR_RAW();
    dma_image<110, 150>(lds0 + L_W2, l16, srd_ws, w2s, hw, 2 * (bi >> 2));
    prog_wait(prog, 8);
    if (hw == 0) wait_flag(fp4 + 6 * FLAG_SET / 4, 32, tag, lane);
    else dma_image<150, 198, 0, 3>(lds0 + L_W2 - 153600, l16, srd_ws, w2s, hw - 1, 3 * (bi >> 2));
    asm volatile("s_waitcnt vmcnt(16)" ::: "memory");
    STAMP(17);
    BAR_RAW();
    dma_image<198, 200>(lds0 + L_W2 - 153600, l16, srd_ws, w2s, hw, 0);
    dma_image<200, 240>(lds0 + L_W2 - 153600, l16, srd_ws, w2s, hw, 2 * (bi >> 2));
    asm volatile("s_waitcnt vmcnt(10)" ::: "memory");
    BAR_RAW();
    DRAIN();
    BAR_RAW();
  }

}
}

extern "C" void kernel_launch(void* const* d_in, const int* in_sizes, int n_in, void* d_out, int out_size, void* d_ws,
                              size_t ws_size, hipStream_t stream) {
  const unsigned short* x = (const unsigned short*)d_in[0];
  const unsigned short* wqkv = (const unsigned short*)d_in[1];
  const unsigned short* wo = (const unsigned short*)d_in[2];
  const unsigned short* w1 = (const unsigned short*)d_in[4];
  const unsigned short* b1 = (const unsigned short*)d_in[5];
  const unsigned short* w2 = (const unsigned short*)d_in[6];
  const unsigned short* b2 = (const unsigned short*)d_in[7];
  fused_block<<<256, 512, 0, stream>>>(x, wqkv, wo, w1, b1, w2, b2, (unsigned short*)d_out, (unsigned char*)d_ws);
}
```

```cpp
#include <hip/hip_runtime.h>
#include <hip/hip_bf16.h>

#define STAMPS 0
#define THR 0
#define TOUCH 0
#define CODEPF 1
#define FLAGPF 1
#define OUTAUX 0
#define OUTPF 0
#define S0 6
#define XRPOS 0
#define QPOS 0
#define H1W 8
#define H1G 2
#define W2S 70
#define F6W 0
#define PUBA 8
#define NCDELAY 0
#define HPRIO 0
#define WOLATE 0
#define SLEEP 1

#define GAS __attribute__((address_space(1)))
#define LAS __attribute__((address_space(3)))
#define RLX_AGENT __ATOMIC_RELAXED, __HIP_MEMORY_SCOPE_AGENT
typedef unsigned v4u __attribute__((ext_vector_type(4)));
typedef unsigned v2u __attribute__((ext_vector_type(2)));
typedef float f32x4 __attribute__((ext_vector_type(4)));
typedef float f32x2 __attribute__((ext_vector_type(2)));
typedef __bf16 bf16x8 __attribute__((ext_vector_type(8)));
typedef __bf16 bf16x2 __attribute__((ext_vector_type(2)));
typedef short s16x4 __attribute__((ext_vector_type(4)));
typedef GAS unsigned gu32;

namespace {
constexpr int S = 2048, D = 640, F = 1536;
constexpr float LN_EPS = 1e-5f;
constexpr int AUX_SC1 = 16;
constexpr unsigned kSpinCap = 1u << 17;
constexpr int OOB = 0x7FFFFF00;

constexpr int WS_EPOCH = 0;
constexpr int WS_FLAG = 4096;
constexpr int FLAG_SET = 64 * 256;
constexpr int WS_XN = 1 << 20;
constexpr int WS_O = WS_XN + 128 * 20 * 1024;
constexpr int WS_H = WS_O + 128 * 20 * 1024;
constexpr int WS_H1 = WS_H + 128 * 20 * 1024;
constexpr int WS_KVP = WS_H1 + 128 * 48 * 1024;
constexpr int WS_ST = WS_KVP + 16 * 16 * 4096;
constexpr int WS_DBG = WS_ST + 2048 * 64;
constexpr int WS_TKV = WS_DBG + 256 * 8 * 32 * 8;
constexpr int WS_TO = WS_TKV + 16 * 153600;
constexpr int WS_T1 = WS_TO + 8 * 102400;
constexpr int WS_T2 = WS_T1 + 16 * 122880;
constexpr int WS_CS = WS_T2 + 16 * 122880;
constexpr int WS_BYTES = WS_CS + 16 * 16 * 384;

constexpr int L_BKV = 0, L_BQ = 102400;
constexpr int L_RED = 0, L_KVF = 153600;
constexpr int L_WO = 0, L_W1 = 30720, L_W2 = 0, L_CS = 161792;
constexpr int L_B1 = L_CS + 1152, L_B2 = L_CS + 1536;
constexpr int LDS_BYTES = 163840;

__device__ __forceinline__ f32x4 mfma(v4u a, v4u b, f32x4 c) {
  return __builtin_amdgcn_mfma_f32_16x16x32_bf16(__builtin_bit_cast(bf16x8, a), __builtin_bit_cast(bf16x8, b), c, 0, 0, 0);
}
__device__ __forceinline__ unsigned pk2(f32x2 v) { return __builtin_bit_cast(unsigned, __builtin_convertvector(v, bf16x2)); }
__device__ __forceinline__ unsigned pk2(float lo, float hi) { return pk2(f32x2{lo, hi}); }
__device__ __forceinline__ float bf_lo(unsigned u) { return __builtin_bit_cast(float, u << 16); }
__device__ __forceinline__ float bf_hi(unsigned u) { return __builtin_bit_cast(float, u & 0xFFFF0000u); }
__device__ __forceinline__ f32x2 bf2(unsigned u) { return f32x2{bf_lo(u), bf_hi(u)}; }
__device__ __forceinline__ f32x2 lo2(f32x4 v) { return f32x2{v[0], v[1]}; }
__device__ __forceinline__ f32x2 hi2(f32x4 v) { return f32x2{v[2], v[3]}; }
__device__ __forceinline__ v4u mk_srd(const void* p, unsigned bytes) {
  const unsigned long long a = (unsigned long long)p;
  v4u r;
  r.x = (unsigned)a; r.y = (unsigned)(a >> 32) & 0xFFFFu; r.z = bytes; r.w = 0x00020000u;
  return r;
}
#define DRAIN() asm volatile("s_waitcnt vmcnt(0)" ::: "memory")
#define BAR() asm volatile("s_waitcnt lgkmcnt(0)\n\ts_barrier" ::: "memory")
#define BAR_RAW() asm volatile("s_barrier" ::: "memory")
#define SCHED() __builtin_amdgcn_sched_barrier(0)
#define PIN(x) asm volatile("" : "+v"(x))

__device__ __forceinline__ void dma2(unsigned lds_addr, unsigned voff, v4u srd, unsigned soff) {
  unsigned keep;
  asm volatile("s_mov_b32 %0, m0\n\ts_mov_b32 m0, %1\n\ts_nop 0\n\t"
               "buffer_load_dwordx4 %2, %3, %4 offen sc1 lds\n\t"
               "buffer_load_dwordx4 %2, %3, %4 offen offset:1024 sc1 lds\n\t"
               "s_mov_b32 m0, %0"
               : "=&s"(keep) : "s"(lds_addr), "v"(voff), "s"(srd), "s"(soff) : "memory");
}
template <int LO, int HI, int TH = 0, int NW = 4>
__device__ __forceinline__ void dma_image(unsigned lds_base, unsigned voff, v4u srd, int ws_off, int hw, int rot) {
  constexpr int NP = (HI - LO) / 2;
#pragma unroll 1
  for (int i = hw; i < NP; i += NW) {
    int j = i + rot;
    if (j >= NP) j -= NP;
    dma2(lds_base + LO * 1024 + j * 2048, voff, srd, ws_off + LO * 1024 + j * 2048);
    if (TH) asm volatile("s_waitcnt vmcnt(%0)" :: "i"(TH - 2) : "memory");
  }
}
__device__ __forceinline__ v2u rdtr(LAS char* p) { return __builtin_bit_cast(v2u, __builtin_amdgcn_ds_read_tr16_b64_v4i16((LAS s16x4*)p)); }
struct LBase {
  unsigned a[3];
  __device__ __forceinline__ explicit LBase(unsigned v) { a[0] = v; a[1] = v + 65536; a[2] = v + 131072; PIN(a[0]); PIN(a[1]); PIN(a[2]); }
  __device__ __forceinline__ v2u rd(int off) const { return rdtr((LAS char*)(a[off >> 16]) + (off & 65535)); }
};
template <int MT, int NT, int KS, int PD, bool WA, int M0 = 0, class RB, class GA, class PRE>
__device__ __forceinline__ void kloop(f32x4 (&acc)[MT][NT], RB rb, GA ga, PRE pre) {
  v2u bl[PD + 1][NT], bh[PD + 1][NT];
#pragma unroll
  for (int m = M0; m < MT; ++m)
#pragma unroll
    for (int s = 0; s < NT; ++s) acc[m][s] = f32x4{0.f, 0.f, 0.f, 0.f};
#pragma unroll
  for (int ks = 0; ks < PD; ++ks)
#pragma unroll
    for (int s = 0; s < NT; ++s) { bl[ks][s] = rb(ks, s, 0); bh[ks][s] = rb(ks, s, 1); }
  SCHED();
#pragma unroll
  for (int ks = 0; ks < KS; ++ks) {
    pre(ks);
#pragma unroll
    for (int s = 0; s < NT; ++s)
#pragma unroll
      for (int m = 0; m < MT; ++m) {
        const v2u lo = bl[ks % (PD + 1)][s], hi = bh[ks % (PD + 1)][s];
        const v4u wv = {lo.x, lo.y, hi.x, hi.y};
        acc[m][s] = WA ? mfma(wv, ga(ks, m), acc[m][s]) : mfma(ga(ks, m), wv, acc[m][s]);
        if (ks + PD < KS) {
          if (MT == 1 || m == 0) bl[(ks + PD) % (PD + 1)][s] = rb(ks + PD, s, 0);
          if (MT == 1 || m == 1) bh[(ks + PD) % (PD + 1)][s] = rb(ks + PD, s, 1);
        }
        SCHED();
      }
  }
}
constexpr int ld_hi(int ks, int s0, int W, int KS) {
  const int a = s0 + 2 * (ks + 1), b = ks + 1 + W, c = a < b ? a : b;
  return c < KS ? c : KS;
}
__device__ __forceinline__ v4u ld128(__amdgpu_buffer_rsrc_t rs, int voff, int soff) {
  return __builtin_amdgcn_raw_buffer_load_b128(rs, voff, soff, AUX_SC1);
}
__device__ __forceinline__ v2u ld64(__amdgpu_buffer_rsrc_t rs, int voff, int soff) {
  return __builtin_amdgcn_raw_buffer_load_b64(rs, voff, soff, AUX_SC1);
}
__device__ __forceinline__ void st128(v4u v, __amdgpu_buffer_rsrc_t rs, int voff, int soff) {
  __builtin_amdgcn_raw_buffer_store_b128(v, rs, (int)((unsigned)voff + (unsigned)soff), 0, AUX_SC1);
}
__device__ __forceinline__ int sel(bool c, int a) {
  PIN(a);
  return c ? a : OOB;
}
__device__ __forceinline__ v4u ld_split(__amdgpu_buffer_rsrc_t rs, int lane, int soff) {
  const v2u a = ld64(rs, lane * 8, soff), b = ld64(rs, lane * 8 + 512, soff);
  return v4u{a.x, a.y, b.x, b.y};
}
__device__ __forceinline__ void st64(v2u v, __amdgpu_buffer_rsrc_t rs, int voff, int soff) {
  __builtin_amdgcn_raw_buffer_store_b64(v, rs, voff, soff, AUX_SC1);
}
__device__ __forceinline__ void flag_set(gu32* p, unsigned tag) {
  asm volatile("global_atomic_swap %0, %1, off sc1" :: "v"(p), "v"(tag) : "memory");
}
__device__ __forceinline__ void wait_flag(gu32* p, int n, unsigned tag, int lane) {
#pragma unroll 1
  for (unsigned spins = 0; spins < kSpinCap; ++spins) {
    unsigned v = tag;
    if (lane < n) asm volatile("global_atomic_add %0, %1, %2, off sc0 sc1\n\ts_waitcnt vmcnt(0)" : "=&v"(v) : "v"(p), "v"(0u) : "memory");
    if (__all(v == tag)) break;
    if (SLEEP) __builtin_amdgcn_s_sleep(SLEEP);
  }
  asm volatile("" ::: "memory");
}
__device__ __forceinline__ void sig_wait(volatile LAS unsigned* p) {
#pragma unroll 1
  for (unsigned spins = 0; spins < 64 * kSpinCap; ++spins) {
    if (*p != 0u) break;
    __builtin_amdgcn_s_sleep(1);
  }
  asm volatile("" ::: "memory");
}
__device__ __forceinline__ void prog_wait(volatile LAS v4u* p, unsigned stage) {
#pragma unroll 1
  for (unsigned spins = 0; spins < 64 * kSpinCap; ++spins) {
    const v4u v = *p;
    if (min(min(v.x, v.y), min(v.z, v.w)) >= stage) break;
    __builtin_amdgcn_s_sleep(1);
  }
  asm volatile("" ::: "memory");
}
__device__ __forceinline__ void publish(gu32* flag, unsigned tag, int lane) {
  DRAIN();
  if (lane == 0) flag_set(flag, tag);
}
__device__ __forceinline__ f32x4 add4(f32x4 a, f32x4 b) {
  f32x4 r;
#pragma unroll
  for (int i = 0; i < 4; ++i) { float x; asm("v_add_f32 %0, %1, %2" : "=v"(x) : "v"(a[i]), "v"(b[i])); r[i] = x; }
  return r;
}
__device__ __forceinline__ float xsum32(float x) {
  unsigned u = __builtin_bit_cast(unsigned, x), v = u;
  asm("" : "+v"(v));
  auto r = __builtin_amdgcn_permlane32_swap(u, v, false, false);
  const unsigned a = r[0], b = r[1];
  return __builtin_bit_cast(float, a) + __builtin_bit_cast(float, b);
}
__device__ __forceinline__ float xsum16(float x) {
  unsigned u = __builtin_bit_cast(unsigned, x), v = u;
  asm("" : "+v"(v));
  auto r = __builtin_amdgcn_permlane16_swap(u, v, false, false);
  const unsigned a = r[0], b = r[1];
  return __builtin_bit_cast(float, a) + __builtin_bit_cast(float, b);
}
__device__ __forceinline__ float xsum8(float x) {
  return x + __builtin_bit_cast(float, __builtin_amdgcn_update_dpp(0, __builtin_bit_cast(int, x), 0x128, 0xf, 0xf, false));
}
__device__ __forceinline__ f32x2 phi2(f32x2 t) {
  const f32x2 u = t * 1.4426950408889634f, v = t + 1e-6f;
  const f32x2 e = {__builtin_amdgcn_fmed3f(__builtin_amdgcn_exp2f(u[0]), 0.f, 1.f), __builtin_amdgcn_fmed3f(__builtin_amdgcn_exp2f(u[1]), 0.f, 1.f)};
  return f32x2{fmaxf(v[0], 1e-6f), fmaxf(v[1], 1e-6f)} + e;
}
__device__ __forceinline__ f32x2 gelu2(f32x2 x) {
  const f32x2 u = x * (x * x * -0.1029432397f + -2.302208198f);
  const f32x2 d = f32x2{__builtin_amdgcn_exp2f(u[0]), __builtin_amdgcn_exp2f(u[1])} + 1.f;
  return x * f32x2{__builtin_amdgcn_rcpf(d[0]), __builtin_amdgcn_rcpf(d[1])};
}

#define STAMP(i) do { } while (0)

__global__ void __launch_bounds__(512, 2)
fused_block(const unsigned short* __restrict__ x, const unsigned short* __restrict__ wqkv, unsigned char* ws, const unsigned short* __restrict__ wo,
            const unsigned short* __restrict__ w1, const unsigned short* __restrict__ b1, const unsigned short* __restrict__ w2,
            const unsigned short* __restrict__ b2, unsigned short* __restrict__ out) {
  __shared__ __attribute__((aligned(16))) char smem[LDS_BYTES];
  unsigned long long pc0;
  asm volatile("s_getpc_b64 %0" : "=s"(pc0));
  LAS char* L = (LAS char*)smem;
  const unsigned lds0 = (unsigned)(size_t)L;
  const int tid = threadIdx.x, lane = tid & 63, w = __builtin_amdgcn_readfirstlane(tid >> 6);
  const int b = blockIdx.x, xcd = b & 7, bi = b >> 3;
  const int g = lane >> 4, t = lane & 15;
  const __amdgpu_buffer_rsrc_t rs = __builtin_amdgcn_make_buffer_rsrc((void*)ws, 0, WS_BYTES, 0x00020000);
  gu32* flags = (gu32*)(ws + WS_FLAG);
#define FLAG(set, grp) (flags + ((set) * FLAG_SET + (grp) * 256) / 4)
  asm volatile("" :: "s"(x), "s"(wqkv), "s"(ws), "s"(wo), "s"(w1), "s"(b1), "s"(w2), "s"(b2));
  STAMP(0);
  const int r128 = 4 * (xcd >> 1) + (bi >> 3), c16 = 8 * (xcd & 1) + (bi & 7);
  const int r64 = 8 * (xcd >> 1) + (bi >> 2), c8 = 4 * (xcd & 1) + (bi & 3);

  const __amdgpu_buffer_rsrc_t rx = __builtin_amdgcn_make_buffer_rsrc((void*)x, 0, S * D * 2, 0x00020000);
  const int hw = w - 4, ht = tid - 256;
  volatile LAS unsigned* sig = (volatile LAS unsigned*)(L + L_CS + 1024);
  volatile LAS v4u* prog = (volatile LAS v4u*)(L + L_CS + 1088);
#define PROG(n) do { asm volatile("" ::: "memory"); sig[16 + w] = (n); } while (0)

  const __amdgpu_buffer_rsrc_t rq = __builtin_amdgcn_make_buffer_rsrc((void*)wqkv, 0, D * 3 * D * 2, 0x00020000);
  v4u tv[12];
  unsigned ep;
  const int q10 = lane / 10, c10 = lane % 10;
  if (w < 4) {
#pragma unroll
    for (int j = 0; j < 3; ++j)
      tv[j] = __builtin_amdgcn_raw_buffer_load_b128(rx, (lane & 7) * 1280 + (lane >> 3) * 16, (j < 2 || w < 2) ? 8 * b * 1280 + (w + 4 * j) * 128 : OOB, 0);
    ep = __hip_atomic_load((gu32*)(ws + WS_EPOCH), RLX_AGENT);
  } else if (w == 4) {
    const int vo = sel(lane < 40, q10 * 3840 + (c10 < 5 ? 1280 : 2480) + c10 * 16);
#pragma unroll
    for (int j = 0; j < 10; ++j) tv[j] = __builtin_amdgcn_raw_buffer_load_b128(rq, vo, (40 * r128 + 4 * j) * 3840 + c16 * 80, 0);
  } else if (w == 7) {
    *(LAS v4u*)(L + L_CS + 512 + lane * 16) = v4u{0u, 0u, 0u, 0u};
  }
  BAR();

  if (w < 4) {
  unsigned tag = ep;
  const int grp32 = 4 * r128 + w;
  constexpr int AW = 6;

  {
    const int tok = 8 * b + (lane & 7), cl = lane >> 3;
    const bool has3 = w < 2;
    int xd = ((tok >> 4) * 20 + 2 * w + (cl >> 2)) * 1024 + ((cl & 3) * 16 + (tok & 15)) * 16;
    PIN(xd);
    SCHED();
    f32x2 f[12];
#pragma unroll
    for (int j = 0; j < 3; ++j)
#pragma unroll
      for (int e = 0; e < 4; ++e) f[j * 4 + e] = bf2(tv[j][e]);
    f32x2 s2 = (f[0] + f[1]) + (f[2] + f[3]) + ((f[4] + f[5]) + (f[6] + f[7])) + ((f[8] + f[9]) + (f[10] + f[11]));
    const float mloc = xsum32(xsum16(xsum8(s2[0] + s2[1]))) * (has3 ? 1.f / 192.f : 1.f / 128.f);
    const float m3 = has3 ? mloc : 0.f;
    f32x2 q2 = {0.f, 0.f}, q3 = {0.f, 0.f};
#pragma unroll
    for (int j = 0; j < 12; j += 2) {
      const f32x2 d0 = f[j] - (j < 8 ? mloc : m3), d1 = f[j + 1] - (j < 8 ? mloc : m3);
      q2 += d0 * d0; q3 += d1 * d1;
    }
    q2 += q3;
    const float m2loc = xsum32(xsum16(xsum8(q2[0] + q2[1])));
    if (lane < 8) *(volatile LAS f32x4*)(L + L_CS + 512 + (lane * 4 + w) * 16) = f32x4{mloc, m2loc, 1.f, 1.f};
    STAMP(25);
    f32x4 pr[4];
#pragma unroll 1
    for (unsigned spins = 0; spins < 64 * kSpinCap; ++spins) {
      bool ok = true;
#pragma unroll
      for (int ww = 0; ww < 4; ++ww) {
        pr[ww] = *(volatile LAS f32x4*)(L + L_CS + 512 + ((lane & 7) * 4 + ww) * 16);
        ok = ok && pr[ww][2] == 1.f;
      }
      if (__all(ok)) break;
    }
    const f32x4 p01 = {pr[0][0], pr[0][1], pr[1][0], pr[1][1]}, p23 = {pr[2][0], pr[2][1], pr[3][0], pr[3][1]};
    const float mean = (p01[0] + p01[2]) * 0.3f + (p23[0] + p23[2]) * 0.2f;
    const float e0 = p01[0] - mean, e1 = p01[2] - mean, e2 = p23[0] - mean, e3 = p23[2] - mean;
    const float m2 = (p01[1] + p01[3]) + (p23[1] + p23[3]) + 192.f * (e0 * e0 + e1 * e1) + 128.f * (e2 * e2 + e3 * e3);
    const float rstd = __builtin_amdgcn_rsqf(m2 * (1.f / D) + LN_EPS);
#pragma unroll
    for (int j = 0; j < 3; ++j) {
      v4u o;
#pragma unroll
      for (int e = 0; e < 4; ++e) o[e] = pk2((f[j * 4 + e] - mean) * rstd);
      st128(o, rs, (j < 2 || has3) ? xd : OOB, WS_XN + j * 8192);
    }
    STAMP(26);
    PIN(tag); tag += 1u;
    publish(FLAG(0, b >> 2) + (b & 3) * 4 + w, tag, lane);
    if (w == 0) sig[6] = 1u;
  }

  STAMP(1);

  v4u xr[3];
  {
    wait_flag(FLAG(0, grp32) + lane, 16, tag, lane);
    STAMP(2);
    v4u a[20][2];
    const int abase = WS_XN + 2 * grp32 * 20 * 1024;
#pragma unroll
    for (int ks = 0; ks < S0; ++ks)
#pragma unroll
      for (int m = 0; m < 2; ++m) a[ks][m] = ld128(rs, lane * 16, abase + (m * 20 + ks) * 1024);
    SCHED();
    BAR_RAW();
    STAMP(27);
    {
      f32x4 acc[2][5];
      const LBase bp(lds0 + L_BKV + (lane >> 2) * 160 + (lane & 3) * 8);
      kloop<2, 5, 20, 1, false>(acc, [&](int ks, int s, int h) { return bp.rd(ks * 5120 + h * 2560 + s * 32); },
        [&](int ks, int m) { return a[ks][m]; },
        [&](int ks) {
#pragma unroll
          for (int j = ld_hi(ks - 1, S0, AW, 20); j < ld_hi(ks, S0, AW, 20); ++j)
#pragma unroll
            for (int m = 0; m < 2; ++m) a[j][m] = ld128(rs, lane * 16, abase + (m * 20 + j) * 1024);
          if (ks == 3 || ks == 10) BAR_RAW();
          if (ks == 20 - AW) PROG(1);
        });
      STAMP(3);
      v4u ka[3], vb[3];
#pragma unroll
      for (int s = 0; s < 3; ++s)
#pragma unroll
        for (int m = 0; m < 2; ++m) {
          vb[s][2 * m] = pk2(lo2(acc[m][s + 2])); vb[s][2 * m + 1] = pk2(hi2(acc[m][s + 2]));
        }
#pragma unroll
      for (int e = 0; e < 4; ++e) vb[0][e] = t < 8 ? 0x3F803F80u : vb[0][e];
      const f32x4 z4 = {0.f, 0.f, 0.f, 0.f};
      LAS f32x4* red = (LAS f32x4*)(L + L_RED) + w * 512 + lane;
#pragma unroll
      for (int s = 0; s < 3; ++s) {
#pragma unroll
        for (int m = 0; m < 2; ++m) { ka[s][2 * m] = pk2(phi2(lo2(acc[m][s]))); ka[s][2 * m + 1] = pk2(phi2(hi2(acc[m][s]))); }
#pragma unroll
        for (int sb = 0; sb < 3; ++sb)
          if (s > 0 || sb < 2) red[(3 * s + sb - (s > 0)) * 64] = mfma(ka[s], vb[sb], z4);
      }
      STAMP(28);
      BAR();
    }
    STAMP(4);
    f32x4 qa[2][3];
    {
      unsigned bp0 = lds0 + L_BQ + (lane >> 2) * 64 + (lane & 3) * 8 + (g & 1) * 32;
      unsigned bp1 = lds0 + L_BQ + (lane >> 2) * 64 + (lane & 3) * 8 + 32 - (g & 1) * 32;
      unsigned bp2 = lds0 + L_BQ + 2048 + (lane >> 2) * 16 + (lane & 1) * 8;
      PIN(bp0); PIN(bp1); PIN(bp2);
      kloop<2, 3, 20, 2, true>(qa,
        [&](int ks, int s, int h) {
          return s == 0 ? rdtr((LAS char*)bp0 + ks * 2560 + h * 1024) : s == 1 ? rdtr((LAS char*)bp1 + ks * 2560 + h * 1024) : rdtr((LAS char*)bp2 + ks * 2560 + h * 256);
        },
        [&](int ks, int m) { return a[ks][m]; },
        [&](int ks) {});
    }
    v4u pq0[2], pq1[2];
#pragma unroll
    for (int m = 0; m < 2; ++m) {
      pq0[m].x = pk2(phi2(lo2(qa[m][0]))); pq0[m].y = pk2(phi2(hi2(qa[m][0])));
      pq0[m].z = pk2(phi2(lo2(qa[m][1]))); pq0[m].w = pk2(phi2(hi2(qa[m][1])));
      const unsigned lo = pk2(phi2(lo2(qa[m][2]))), hi = pk2(phi2(hi2(qa[m][2])));
      pq1[m].x = g < 2 ? lo : 0u; pq1[m].y = g < 2 ? hi : 0u; pq1[m].z = 0u; pq1[m].w = 0u;
      PIN(pq0[m]); PIN(pq1[m]);
    }
    STAMP(5);
    BAR_RAW();
#pragma unroll
    for (int j = 0; j < 3; ++j)
      xr[j] = __builtin_amdgcn_raw_buffer_load_b128(rx, sel(j < 2 || g < 2, t * 1280 + g * 16), (4 * r64 + w) * 16 * 1280 + c8 * 160 + j * 64, 0);
    STAMP(17);
    {
      v4u kv[4][2];
#pragma unroll
      for (int es = 3; es >= 0; --es)
#pragma unroll
        for (int ks = 0; ks < 2; ++ks) kv[es][ks] = *(LAS v4u*)(L + L_KVF + (es * 2 + ks) * 1024 + lane * 16);
      SCHED();
      f32x4 o[2][4];
#pragma unroll
      for (int es = 3; es >= 0; --es)
#pragma unroll
        for (int m = 0; m < 2; ++m) o[m][es] = mfma(kv[es][0], pq0[m], f32x4{0.f, 0.f, 0.f, 0.f});
#pragma unroll
      for (int es = 3; es >= 0; --es)
#pragma unroll
        for (int m = 0; m < 2; ++m) o[m][es] = mfma(kv[es][1], pq1[m], o[m][es]);
#pragma unroll
      for (int m = 0; m < 2; ++m) {
        const float i0 = __builtin_amdgcn_rcpf(o[m][3][0] + 1e-6f), i1 = __builtin_amdgcn_rcpf(o[m][3][1] + 1e-6f);
        const float im = g == 0 ? i0 : i1;
        v4u ov;
        ov.x = pk2(lo2(o[m][0]) * i0); ov.y = pk2(hi2(o[m][0]) * i0);
        ov.z = pk2(lo2(o[m][1]) * im); ov.w = pk2(hi2(o[m][1]) * im);
        v2u ol; ol.x = pk2(lo2(o[m][2]) * i1); ol.y = pk2(hi2(o[m][2]) * i1);
        const int tt = 2 * grp32 + m;
        st128(ov, rs, lane * 16, WS_O + (tt * 20 + c16) * 1024);
        st64(ol, rs, g < 2 ? lane * 8 : OOB, WS_O + (tt * 20 + 16) * 1024 + c16 * 256);
      }
      STAMP(18);
      publish(FLAG(2, grp32) + c16, tag, lane);
    }
    STAMP(7);
  }

  {
    const int tt = 4 * r64 + w;
    v4u eye[2];
#pragma unroll
    for (int hf = 0; hf < 2; ++hf) {
      const int e = 16 * hf + t - 8 * g;
#pragma unroll
      for (int d = 0; d < 4; ++d) eye[hf][d] = e == 2 * d ? 0x3F80u : e == 2 * d + 1 ? 0x3F800000u : 0u;
      PIN(eye[hf]);
    }
    f32x4 acc[1][5];
#pragma unroll
    for (int s = 0; s < 5; ++s) { acc[0][s] = mfma(eye[s & 1], xr[s >> 1], f32x4{0.f, 0.f, 0.f, 0.f}); PIN(acc[0][s]); }
    wait_flag(FLAG(2, tt >> 1) + lane, 16, tag, lane);
    STAMP(8);
    if (b == 0 && tid == 0) __hip_atomic_store((gu32*)(ws + WS_EPOCH), tag, RLX_AGENT);
    v4u a[20];
#pragma unroll
    for (int ks = 0; ks < 2 * S0; ++ks) a[ks] = ld128(rs, lane * 16, WS_O + (tt * 20 + ks) * 1024);
    SCHED();
    const LBase bp(lds0 + L_WO + (lane >> 2) * 160 + (lane & 3) * 8);
    kloop<1, 5, 20, 1, true, 1>(acc, [&](int ks, int s, int h) { return bp.rd(ks * 5120 + h * 2560 + s * 32); },
      [&](int ks, int m) { return a[ks]; },
      [&](int ks) {
#pragma unroll
        for (int j = ld_hi(ks - 1, 2 * S0, 2 * AW, 20); j < ld_hi(ks, 2 * S0, 2 * AW, 20); ++j)
          a[j] = j < 16 ? ld128(rs, lane * 16, WS_O + (tt * 20 + j) * 1024) : ld_split(rs, lane, WS_O + (tt * 20 + j) * 1024);
        if (ks == 8) PROG(2);
        if (ks == 14) PROG(3);
      });
    PROG(4);
    STAMP(9);
    v2u hb[5];
    f32x2 hv[10];
#pragma unroll
    for (int s = 0; s < 5; ++s) {
      hb[s].x = pk2(lo2(acc[0][s]));
      hb[s].y = pk2(hi2(acc[0][s]));
      hv[2 * s] = bf2(hb[s].x); hv[2 * s + 1] = bf2(hb[s].y);
    }
    st128(v4u{hb[0].x, hb[0].y, hb[1].x, hb[1].y}, rs, lane * 16, WS_H + (tt * 20 + 2 * c8) * 1024);
    st128(v4u{hb[2].x, hb[2].y, hb[3].x, hb[3].y}, rs, lane * 16, WS_H + (tt * 20 + 2 * c8 + 1) * 1024);
    st64(hb[4], rs, lane * 8, WS_H + (tt * 20 + 16) * 1024 + c8 * 512);
    const f32x2 s2 = ((hv[0] + hv[1]) + (hv[2] + hv[3])) + ((hv[4] + hv[5]) + (hv[6] + hv[7])) + (hv[8] + hv[9]);
    const float mc = xsum32(xsum16(s2[0] + s2[1])) * (1.f / 80.f);
    f32x2 q2 = {0.f, 0.f}, q3 = {0.f, 0.f};
#pragma unroll
    for (int j = 0; j < 10; j += 2) { const f32x2 d0 = hv[j] - mc, d1 = hv[j + 1] - mc; q2 += d0 * d0; q3 += d1 * d1; }
    q2 += q3;
    const float m2 = xsum32(xsum16(q2[0] + q2[1]));
    st64(v2u{__builtin_bit_cast(unsigned, mc), __builtin_bit_cast(unsigned, m2)}, rs, g == 0 ? t * 8 : OOB, WS_ST + (tt * 8 + c8) * 128);
    STAMP(20);
    publish(FLAG(3, tt >> 1) + (tt & 1) * 8 + c8, tag, lane);
    STAMP(10);
  }

  {
    wait_flag(FLAG(3, grp32) + lane, 16, tag, lane);
    STAMP(11);
    v4u stt[2];
#pragma unroll
    for (int m = 0; m < 2; ++m) {
      const v2u s0 = ld64(rs, g * 256 + t * 8, WS_ST + (2 * grp32 + m) * 1024), s1 = ld64(rs, g * 256 + 128 + t * 8, WS_ST + (2 * grp32 + m) * 1024);
      stt[m] = v4u{s0.x, s0.y, s1.x, s1.y};
    }
    v4u a[20][2];
    const int abase = WS_H + 2 * grp32 * 20 * 1024;
#pragma unroll
    for (int ks = 0; ks < S0; ++ks)
#pragma unroll
      for (int m = 0; m < 2; ++m) a[ks][m] = ld128(rs, lane * 16, abase + (m * 20 + ks) * 1024);
    SCHED();
    float nm[2], rr[2];
#pragma unroll
    for (int m = 0; m < 2; ++m) {
      const f32x4 sv = __builtin_bit_cast(f32x4, stt[m]);
      const float mean = xsum32(xsum16(sv[0] + sv[2])) * 0.125f;
      const float m2 = xsum32(xsum16(sv[1] + sv[3] + 80.f * ((sv[0] - mean) * (sv[0] - mean) + (sv[2] - mean) * (sv[2] - mean))));
      const float var = m2 * (1.f / D);
      const float r1 = __builtin_amdgcn_rsqf(var + LN_EPS);
      rr[m] = r1 * __builtin_amdgcn_rsqf(var * r1 * r1 + LN_EPS);
      nm[m] = -mean * rr[m];
      PIN(rr[m]); PIN(nm[m]);
    }
    BAR_RAW();
    STAMP(22);
    const unsigned l64 = lds0 + (lane >> 2) * 64 + (lane & 3) * 8;
    const LBase bp0(l64 + (g & 1) * 32), bp1(l64 + 32 - (g & 1) * 32);
    const int h1base = WS_H1 + 2 * grp32 * 48 * 1024;
    auto finish = [&](const f32x4& c0, const f32x4& c1, int j, int m) {
      v4u o;
#pragma unroll
      for (int s = 0; s < 2; ++s) {
        const f32x4 cs = *(LAS f32x4*)(L + L_CS + ((2 * j + s) * 4 + g) * 16), bb = *(LAS f32x4*)(L + L_B1 + ((2 * j + s) * 4 + g) * 16);
        const f32x4 c = s ? c1 : c0;
        o[2 * s] = pk2(gelu2(lo2(c) * rr[m] + (lo2(cs) * nm[m] + lo2(bb))));
        o[2 * s + 1] = pk2(gelu2(hi2(c) * rr[m] + (hi2(cs) * nm[m] + hi2(bb))));
      }
      st128(o, rs, lane * 16, h1base + (m * 48 + (c16 >> 3) * 24 + (j < 2 ? 2 * (c16 & 7) + j : 16 + (c16 & 7))) * 1024);
    };
    {
      f32x4 acc[2][4];
      kloop<2, 4, 20, 1, true>(acc, [&](int ks, int s, int h) { return ((s & 1) ? bp1 : bp0).rd(L_W1 + (s >> 1) * 40960 + ks * 2048 + h * 1024); },
        [&](int ks, int m) { return a[ks][m]; },
        [&](int ks) {
#pragma unroll
          for (int j = ld_hi(ks - 1, S0, AW, 20); j < ld_hi(ks, S0, AW, 20); ++j)
#pragma unroll
            for (int m = 0; m < 2; ++m)
              a[j][m] = j < 16 ? ld128(rs, lane * 16, abase + (m * 20 + j) * 1024) : ld_split(rs, lane, abase + (m * 20 + j) * 1024);
          if (ks == 20 - AW) PROG(5);
        });
      PROG(6);
      STAMP(12);
#pragma unroll
      for (int j = 0; j < 2; ++j)
#pragma unroll
        for (int m = 0; m < 2; ++m) finish(acc[m][2 * j], acc[m][2 * j + 1], j, m);
      SCHED();
      STAMP(24);
    }
    f32x4 accb[2][2];
    const int kh = w & 1, tt = 4 * r64 + w;
    v4u hr01, hr23;
    v2u hr4;
    kloop<2, 2, 20, 3, true>(accb, [&](int ks, int s, int h) { return (s ? bp1 : bp0).rd(L_W1 + 81920 + ks * 2048 + h * 1024); },
      [&](int ks, int m) { return a[ks][m]; },
      [&](int ks) {
        if (ks == PUBA) { publish(FLAG(4, grp32) + c16, tag, lane); PROG(7); }
        if (ks == PUBA + 2) {
          hr01 = ld128(rs, lane * 16, WS_H + (tt * 20 + 2 * c8) * 1024);
          hr23 = ld128(rs, lane * 16, WS_H + (tt * 20 + 2 * c8 + 1) * 1024);
          hr4 = ld64(rs, lane * 8, WS_H + (tt * 20 + 16) * 1024 + c8 * 512);
        }
      });
    STAMP(13);
    f32x4 acc[2][5];
    {
      const unsigned hrr[10] = {hr01.x, hr01.y, hr01.z, hr01.w, hr23.x, hr23.y, hr23.z, hr23.w, hr4.x, hr4.y};
#pragma unroll
      for (int s = 0; s < 5; ++s) {
        acc[0][s] = *(LAS f32x4*)(L + L_B2 + (s * 4 + g) * 16) + f32x4{bf_lo(hrr[2 * s]), bf_hi(hrr[2 * s]), bf_lo(hrr[2 * s + 1]), bf_hi(hrr[2 * s + 1])};
        PIN(acc[0][s]);
      }
    }
    int oo[3];
    unsigned tw, tr;
    {
      int ln = lane;
      PIN(ln);
      const int qq = ln / 10, cc = ln % 10;
#pragma unroll
      for (int j = 0; j < 3; ++j) { oo[j] = sel(ln < (j < 2 ? 60 : 40), ((tt * 16 + 6 * j + qq) * D + 80 * c8) * 2 + cc * 16); PIN(oo[j]); }
      tw = lds0 + L_W2 + 122880 + w * 3072 + t * 176 + g * 8; tr = lds0 + L_W2 + 122880 + w * 3072 + qq * 176 + cc * 16;
      PIN(tw); PIN(tr);
    }
    BAR_RAW();
    STAMP(14);
    constexpr int WIN = 8;
    v4u ar[WIN][2];
    const int a4[2] = {WS_H1 + (tt * 48 + kh * 24) * 1024, WS_H1 + ((tt ^ 1) * 48 + kh * 24) * 1024};
#pragma unroll
    for (int ks = 0; ks < WIN - 1; ++ks)
#pragma unroll
      for (int m = 0; m < 2; ++m) ar[ks][m] = ld128(rs, lane * 16, a4[m] + ks * 1024);
    SCHED();
#pragma unroll
    for (int m = 0; m < 2; ++m) finish(accb[m][0], accb[m][1], 2, m);
    STAMP(29);
    const LBase bp(lds0 + L_W2 + kh * 5120 + (lane >> 2) * 160 + (lane & 3) * 8);
    kloop<2, 5, 24, 1, true, 1>(acc, [&](int ks, int s, int h) { return bp.rd((ks % 15) * 10240 + h * 2560 + s * 32); },
      [&](int ks, int m) { return ar[ks % WIN][m]; },
      [&](int ks) {
        if (ks == 5) PROG(8);
        if (ks == 9 || ks == 14 || ks == 19) BAR_RAW();
        if (ks == 3) publish(FLAG(10, grp32) + c16, tag, lane);
        if (ks >= 3) {
#pragma unroll
          for (int j = ks == 3 ? WIN - 1 : ld_hi(ks - 1, 1, WIN - 1, 24); j < ld_hi(ks, 1, WIN - 1, 24); ++j)
#pragma unroll
            for (int m = 0; m < 2; ++m) ar[j % WIN][m] = ld128(rs, lane * 16, a4[m] + j * 1024);
        }
      });
    STAMP(15);
    LAS f32x4* xch = (LAS f32x4*)(L + L_W2 + 102400);
#pragma unroll
    for (int s = 0; s < 5; ++s) xch[((w ^ 1) * 5 + s) * 64 + lane] = acc[1][s];
    BAR();
    const __amdgpu_buffer_rsrc_t rout = __builtin_amdgcn_make_buffer_rsrc((void*)out, 0, S * D * 2, 0x00020000);
    f32x4 ot[5];
#pragma unroll
    for (int s = 0; s < 5; ++s) ot[s] = xch[(w * 5 + s) * 64 + lane];
    SCHED();
#pragma unroll
    for (int s = 0; s < 5; ++s) {
      const f32x4 v = acc[0][s] + ot[s];
      *(LAS v2u*)((LAS char*)tw + s * 32) = v2u{pk2(lo2(v)), pk2(hi2(v))};
    }
    asm volatile("" ::: "memory");
    v4u ov[3];
#pragma unroll
    for (int j = 0; j < 3; ++j) ov[j] = *(LAS v4u*)((LAS char*)tr + j * 6 * 176);
#pragma unroll
    for (int j = 0; j < 3; ++j) __builtin_amdgcn_raw_buffer_store_b128(ov[j], rout, oo[j], 0, 0);
    STAMP(16);
  }
  return;
  }

  {
    const v4u srd_ws = mk_srd(ws, WS_BYTES);
    STAMP(16);
    const int q5 = lane / 5, c5 = lane % 5;
    const int f40m = c5 < 4 ? 512 : 128, f40l = c5 < 4 ? q5 * 64 + (c5 ^ (2 * ((q5 >> 2) & 1))) * 16 : 2048 + q5 * 16;
    v2u ub1, ub2;
    unsigned pf0;
    v4u tq[10];
    if (hw == 1) {
      const __amdgpu_buffer_rsrc_t ro = __builtin_amdgcn_make_buffer_rsrc((void*)wo, 0, D * D * 2, 0x00020000);
      const int vq = sel(lane < 40, (8 * (q5 >> 2) + (q5 & 3)) * 3840 + c5 * 16), vo = sel(lane < 40, q10 * 1280 + c10 * 16);
#pragma unroll
      for (int j = 0; j < 5; ++j) {
        const int i8 = 5 * r128 + j, k = 32 * (i8 >> 2) + 16 * (i8 & 1) + 4 * ((i8 >> 1) & 1);
        tq[j] = __builtin_amdgcn_raw_buffer_load_b128(rq, vq, k * 3840 + c16 * 80, 0);
      }
#pragma unroll
      for (int j = 0; j < 5; ++j) tq[5 + j] = __builtin_amdgcn_raw_buffer_load_b128(ro, vo, (20 * r64 + 4 * j) * 1280 + c8 * 160, 0);
    } else if (hw == 2) {
      const GAS unsigned char* cb = (const GAS unsigned char*)(pc0 & ~127ull);
      pf0 = *(const GAS unsigned*)(cb + min(lane * 32 + bi, 346) * 128);
      if (lane < 6) {
        gu32* fp = FLAG(3 * (b >> 6) + (lane >> 1), b & 63) + (lane & 1) * 32;
        asm volatile("global_atomic_add %0, %1, off sc1" :: "v"(fp), "v"(0u) : "memory");
      }
    } else if (hw == 3) {
      const __amdgpu_buffer_rsrc_t rb1 = __builtin_amdgcn_make_buffer_rsrc((void*)b1, 0, F * 2, 0x00020000);
      const __amdgpu_buffer_rsrc_t rb2 = __builtin_amdgcn_make_buffer_rsrc((void*)b2, 0, D * 2, 0x00020000);
      ub1 = __builtin_amdgcn_raw_buffer_load_b64(rb1, lane * 8, c16 * 192, 0);
      ub2 = __builtin_amdgcn_raw_buffer_load_b64(rb2, lane * 8, c8 * 160, 0);
    }
    unsigned tag = __hip_atomic_load((gu32*)(ws + WS_EPOCH), RLX_AGENT);
    if (hw == 0) {
      const int dof = sel(lane < 40, lane * 16);
#pragma unroll
      for (int j = 0; j < 10; ++j) {
        const int k = 40 * r128 + 4 * j, R = (k & ~31) + 16 * ((k >> 2) & 1) + 4 * ((k >> 3) & 3);
        st128(tv[j], rs, dof, WS_TKV + c16 * 153600 + R * 160);
      }
      STAMP(18);
      PIN(tag); tag += 1u;
      publish(FLAG(6, c16) + r128, tag, lane);
      STAMP(13);
    }
    asm volatile("" ::: "memory");
    int red_off, kvf_off, kvp_off;
    auto items = [&]() {
      const int it = ht < 210 ? ht : 209, ie = it / 5, ic = it % 5;
      const int ihead = ie < 40 ? (ie >= 20) : ie - 40;
      const int id = 20 * ihead + 4 * ic, dg = id >> 2;
      const int sa = id >> 4, col = 40 + ie;
      const int tile = 3 * sa + (ie < 40 ? (col >> 4) - 2 : 0) - (sa > 0);
      red_off = lds0 + L_RED + (tile * 64 + (dg & 3) * 16 + (ie < 40 ? (col & 15) : 0)) * 16;
      kvf_off = lds0 + L_KVF + ((ie < 40 ? (ie >> 4) : 3) * 2 + (dg >> 3)) * 1024 + ((dg & 3) * 16 + (ie < 40 ? (ie & 15) : ihead)) * 16 + ((dg >> 2) & 1) * 8;
      kvp_off = WS_KVP + (c16 * 16 + r128) * 4096 + ht * 16;
      PIN(red_off); PIN(kvf_off); PIN(kvp_off);
    };
    if (hw != 0) items();
    int vo12, td[12];
    if (hw >= 2) {
      const int q12 = lane / 12, c12 = lane % 12;
      vo12 = hw == 2 ? sel(lane < 48, q12 * 3072 + c12 * 16) : sel(lane < 40, q10 * 1280 + c10 * 16);
#pragma unroll
      for (int j = 0; j < 12; ++j) {
        const int cc = r128 >> 1, off = 40 * (r128 & 1) + 4 * j;
        const int R = off < 64 ? 64 * cc + off : 32 * (16 + (cc >> 1)) + 16 * (cc & 1) + off - 64;
        const int d1 = WS_T1 + c16 * 122880 + (c12 >> 2) * 40960 + (R + q12) * 64 + ((c12 & 3) ^ (2 * ((R >> 2) & 1))) * 16;
        const int pc = r64 >> 1, ko = 48 * (r64 & 1) + 4 * j, jj = ko >> 5;
        const int d2 = WS_T2 + c8 * 245760 + (2 * (jj < 2 ? 2 * (pc & 7) + jj : 16 + (pc & 7)) + (pc >> 3)) * 5120 + (ko & 31) * 160 + lane * 16;
        td[j] = sel(hw == 2 ? (lane < 48 && j < 10) : lane < 40, hw == 2 ? d1 : d2);
        PIN(td[j]);
      }
      PIN(vo12);
    }
    int l16 = lane * 16, h16 = sel(ht < 210, ht * 16);
    PIN(l16); PIN(h16);
    gu32* fp1 = FLAG(1, c16) + lane;
    gu32* fp78 = lane < 32 ? FLAG(7, c8) + lane : FLAG(8, c16) + t;
    gu32* fp4 = FLAG(4, 2 * r64 + (g & 1)) + t;
    gu32* fp1w = FLAG(1, c16) + r128 * 4 + hw;
    PIN(fp1); PIN(fp78); PIN(fp4); PIN(fp1w);
    ((LAS v4u*)(L + L_KVF))[ht] = v4u{0u, 0u, 0u, 0u};
    ((LAS v4u*)(L + L_KVF))[256 + ht] = v4u{0u, 0u, 0u, 0u};
    asm volatile("" ::: "memory");
    if (hw != 0) {
      if (hw == 1) {
        sig_wait(sig + 6);
#pragma unroll
        for (int j = 0; j < 5; ++j) {
          const int i8 = 5 * r128 + j;
          st128(tq[j], rs, sel(lane < 40, (i8 & 3) * f40m + f40l), WS_TKV + c16 * 153600 + 102400 + (i8 >> 2) * 2560);
        }
#pragma unroll
        for (int j = 0; j < 5; ++j) {
          const int pp = r64 >> 1, e = 20 * (r64 & 1) + 4 * j;
          const int R = e < 32 ? 32 * pp + e : 32 * (16 + (pp >> 2)) + 8 * (pp & 3) + e - 32;
          st128(tq[5 + j], rs, sel(lane < 40, lane * 16), WS_TO + c8 * 102400 + R * 160);
        }
      }
      if (hw == 3) {
        if (lane < 24) *(LAS f32x4*)(L + L_B1 + lane * 16) = f32x4{bf_lo(ub1.x), bf_hi(ub1.x), bf_lo(ub1.y), bf_hi(ub1.y)};
        if (lane < 20) *(LAS f32x4*)(L + L_B2 + lane * 16) = f32x4{bf_lo(ub2.x), bf_hi(ub2.x), bf_lo(ub2.y), bf_hi(ub2.y)};
      }
      if (hw == 2) asm volatile("" :: "v"(pf0));
      PIN(tag); tag += 1u;
      DRAIN();
      if (hw == 1 && lane < 2) flag_set(lane == 0 ? FLAG(8, c16) + r128 : FLAG(7, c8) + r64, tag);
    }
    if (hw == 0) {
      if (F6W) __builtin_amdgcn_s_sleep(F6W);
      wait_flag(FLAG(6, c16) + lane, 16, tag, lane);
      sig[0] = 1u;
    }
    sig_wait(sig);
    STAMP(1);
    const int kvq = WS_TKV + c16 * 153600, wos = WS_TO + c8 * 102400;
    dma_image<0, 24>(lds0 + L_BKV, l16, srd_ws, kvq, hw, 3 * (bi >> 3));
    dma_image<24, 56>(lds0 + L_BKV, l16, srd_ws, kvq, hw, 4 * (bi >> 3));
    asm volatile("s_waitcnt vmcnt(8)" ::: "memory");
    STAMP(2);
    BAR_RAW();
    dma_image<56, 100>(lds0 + L_BKV, l16, srd_ws, kvq, hw, 5 * (bi >> 3));
    asm volatile("s_waitcnt vmcnt(10)" ::: "memory");
    BAR_RAW();
    if (hw == 1) wait_flag(fp78, 48, tag, lane);
    DRAIN();
    STAMP(3);
    BAR_RAW();
    if (QPOS == 0) dma_image<100, 150>(lds0 + L_BKV, l16, srd_ws, kvq, hw, 6 * (bi >> 3));
    if (hw == 0) items();
    prog_wait(prog, 1);
    if (QPOS == 1) dma_image<100, 150>(lds0 + L_BKV, l16, srd_ws, kvq, hw, 6 * (bi >> 3));
    if (hw >= 2) {
      const __amdgpu_buffer_rsrc_t r12 = __builtin_amdgcn_make_buffer_rsrc((void*)(hw == 2 ? w1 : w2), 0, D * F * 2, 0x00020000);
      const int base = hw == 2 ? r128 * 122880 + c16 * 192 : r64 * 61440 + c8 * 160, step = hw == 2 ? 4 * 3072 : 4 * 1280;
#pragma unroll
      for (int j = 0; j < 12; ++j) tv[j] = __builtin_amdgcn_raw_buffer_load_b128(r12, vo12, (j < 10 || hw == 3) ? base + j * step : OOB, 0);
      asm volatile("s_waitcnt vmcnt(12)" ::: "memory");
    } else {
      dma_image<32, 68, 0, 2>(lds0 + L_WO, l16, srd_ws, wos, hw, bi >> 2);
      asm volatile("s_waitcnt vmcnt(18)" ::: "memory");
    }
    STAMP(4);
    BAR();
    __builtin_amdgcn_s_setprio(2);
    STAMP(11);
    {
      f32x4 pv[4];
#pragma unroll
      for (int ww = 0; ww < 4; ++ww) pv[ww] = *(LAS f32x4*)((LAS char*)red_off + ww * 8192);
      st128(__builtin_bit_cast(v4u, add4(add4(pv[0], pv[1]), add4(pv[2], pv[3]))), rs, kvp_off, 0);
      PROG(1);
      STAMP(12);
      DRAIN();
#pragma unroll
      for (int j = 0; j < 12; ++j) PIN(tv[j]);
    }
    if (lane == 0) flag_set(fp1w, tag);
    if (hw == 0) {
      STAMP(10);
      __builtin_amdgcn_s_sleep(H1W);
      wait_flag(fp1, 64, tag, lane);
      sig[2] = 1u;
    } else {
      if (H1G) __builtin_amdgcn_s_sleep(H1G);
      prog_wait(prog + 1, 1);
#pragma unroll
      for (int i = 0; i < 12; ++i) {
        if (hw >= 2) st128(tv[i], rs, td[i], 0);
        const int j = hw - 1 + 3 * i;
        if (j < 32) {
          const int pc = 2 * ((j + 4 * (bi >> 2)) & 31), po = (pc < 32 ? pc : pc + 36) * 1024;
          dma2(lds0 + L_WO + po, l16, srd_ws, wos + po);
        }
      }
      STAMP(10);
      sig_wait(sig + 2);
    }
    STAMP(5);
    {
      v4u pv[16];
#pragma unroll
      for (int r = 0; r < 16; ++r) pv[r] = ld128(rs, h16, WS_KVP + (c16 * 16 + r) * 4096);
      SCHED();
      f32x4 v = __builtin_bit_cast(f32x4, pv[0]);
#pragma unroll
      for (int r = 1; r < 16; ++r) v = add4(v, __builtin_bit_cast(f32x4, pv[r]));
      v2u o; o.x = pk2(lo2(v)); o.y = pk2(hi2(v));
      if (ht < 210) *(LAS v2u*)((LAS char*)kvf_off) = o;
      if (ht >= 200 && ht < 210) {
#pragma unroll
        for (int c = 1; c < 4; ++c) *(LAS v2u*)((LAS char*)kvf_off + c * 64) = o;
      }
    }
    DRAIN();
    STAMP(6);
    BAR();
    __builtin_amdgcn_s_setprio(0);
    if (hw >= 2 && lane == 0) flag_set(hw == 2 ? FLAG(5, c16) + r128 : FLAG(11, c8) + r64, tag);
    if (hw == 2) {
      f32x4 s0 = {0.f, 0.f, 0.f, 0.f}, s1 = {0.f, 0.f, 0.f, 0.f};
#pragma unroll
      for (int j = 0; j < 10; ++j) {
        s0 = add4(s0, f32x4{bf_lo(tv[j].x), bf_hi(tv[j].x), bf_lo(tv[j].y), bf_hi(tv[j].y)});
        s1 = add4(s1, f32x4{bf_lo(tv[j].z), bf_hi(tv[j].z), bf_lo(tv[j].w), bf_hi(tv[j].w)});
      }
#pragma unroll
      for (int d = 24; d >= 12; d -= 12) {
        f32x4 u0, u1;
#pragma unroll
        for (int e = 0; e < 4; ++e) { u0[e] = __shfl(s0[e], lane + d); u1[e] = __shfl(s1[e], lane + d); }
        s0 = add4(s0, u0); s1 = add4(s1, u1);
      }
      f32x4 sv;
#pragma unroll
      for (int e = 0; e < 4; ++e) { const float a0 = __shfl(s0[e], lane >> 1), a1 = __shfl(s1[e], lane >> 1); sv[e] = (lane & 1) ? a1 : a0; }
      st128(__builtin_bit_cast(v4u, sv), rs, sel(lane < 24, lane * 16), WS_CS + (c16 * 16 + r128) * 384);
      publish(FLAG(9, c16) + r128, tag, lane);
    }
    if (hw == 1) {
      __builtin_amdgcn_s_sleep(40);
      wait_flag(lane < 32 ? FLAG(11, c8) + lane : FLAG(5, c16) + t, 48, tag, lane);
      sig[3] = 1u;
    }
    sig_wait(sig + 3);
    prog_wait(prog, 2);
    if (hw == 0) {
      wait_flag(FLAG(9, c16) + lane, 16, tag, lane);
      v4u pv[16];
      const int co = sel(lane < 24, l16);
#pragma unroll
      for (int r = 0; r < 16; ++r) pv[r] = ld128(rs, co, WS_CS + (c16 * 16 + r) * 384);
      SCHED();
      f32x4 v = __builtin_bit_cast(f32x4, pv[0]);
#pragma unroll
      for (int r = 1; r < 16; ++r) v = add4(v, __builtin_bit_cast(f32x4, pv[r]));
      if (lane < 24) *(LAS f32x4*)(L + L_CS + lane * 16) = v;
    } else {
      dma_image<70, 120, 0, 3>(lds0 + L_W1, l16, srd_ws, WS_T1 + c16 * 122880, hw - 1, 6 * (bi >> 3));
      prog_wait(prog, 3);
      dma_image<0, 40, 0, 3>(lds0 + L_W1, l16, srd_ws, WS_T1 + c16 * 122880, hw - 1, 5 * (bi >> 3));
      STAMP(7);
      prog_wait(prog, 4);
      dma_image<40, 70, 0, 3>(lds0 + L_W1, l16, srd_ws, WS_T1 + c16 * 122880, hw - 1, 3 * (bi >> 3));
    }
    DRAIN();
    STAMP(8);
    BAR();
    const int w2s = WS_T2 + c8 * 245760;
    prog_wait(prog, 5);
    dma_image<0, 30>(lds0 + L_W2, l16, srd_ws, w2s, hw, bi >> 2);
    STAMP(14);
    prog_wait(prog, 6);
    dma_image<30, W2S>(lds0 + L_W2, l16, srd_ws, w2s, hw, bi >> 2);
    prog_wait(prog, 7);
    if (hw != 0) dma_image<W2S, 110, 0, 3>(lds0 + L_W2, l16, srd_ws, w2s, hw - 1, bi >> 2);
    DRAIN();
    STAMP(9);
    if (hw == 0) wait_flag(fp4, 32, tag, lane);
    STAMP(15);
    BAR_RAW();
    dma_image<110, 150>(lds0 + L_W2, l16, srd_ws, w2s, hw, 2 * (bi >> 2));
    prog_wait(prog, 8);
    if (hw == 0) wait_flag(fp4 + 6 * FLAG_SET / 4, 32, tag, lane);
    else dma_image<150, 198, 0, 3>(lds0 + L_W2 - 153600, l16, srd_ws, w2s, hw - 1, 3 * (bi >> 2));
    asm volatile("s_waitcnt vmcnt(16)" ::: "memory");
    STAMP(17);
    BAR_RAW();
    dma_image<198, 200>(lds0 + L_W2 - 153600, l16, srd_ws, w2s, hw, 0);
    dma_image<200, 240>(lds0 + L_W2 - 153600, l16, srd_ws, w2s, hw, 2 * (bi >> 2));
    asm volatile("s_waitcnt vmcnt(10)" ::: "memory");
    BAR_RAW();
    DRAIN();
    BAR_RAW();
  }

}
}

extern "C" void kernel_launch(void* const* d_in, const int* in_sizes, int n_in, void* d_out, int out_size, void* d_ws,
                              size_t ws_size, hipStream_t stream) {
  const unsigned short* x = (const unsigned short*)d_in[0];
  const unsigned short* wqkv = (const unsigned short*)d_in[1];
  const unsigned short* wo = (const unsigned short*)d_in[2];
  const unsigned short* w1 = (const unsigned short*)d_in[4];
  const unsigned short* b1 = (const unsigned short*)d_in[5];
  const unsigned short* w2 = (const unsigned short*)d_in[6];
  const unsigned short* b2 = (const unsigned short*)d_in[7];
  fused_block<<<256, 512, 0, stream>>>(x, wqkv, (unsigned char*)d_ws, wo, w1, b1, w2, b2, (unsigned short*)d_out);
}
```

```cpp
#include <hip/hip_runtime.h>
#include <hip/hip_bf16.h>

#define STAMPS 0
#define THR 0
#define TOUCH 0
#define CODEPF 1
#define FLAGPF 1
#define OUTAUX 0
#define OUTPF 0
#define S0 6
#define XRPOS 0
#define QPOS 0
#define H1W 8
#define H1G 2
#define W2S 70
#define F6W 0
#define PUBA 8
#define NCDELAY 0
#define HPRIO 0
#define WOLATE 0
#define SLEEP 1

#define GAS __attribute__((address_space(1)))
#define LAS __attribute__((address_space(3)))
#define RLX_AGENT __ATOMIC_RELAXED, __HIP_MEMORY_SCOPE_AGENT
typedef unsigned v4u __attribute__((ext_vector_type(4)));
typedef unsigned v2u __attribute__((ext_vector_type(2)));
typedef float f32x4 __attribute__((ext_vector_type(4)));
typedef float f32x2 __attribute__((ext_vector_type(2)));
typedef __bf16 bf16x8 __attribute__((ext_vector_type(8)));
typedef __bf16 bf16x2 __attribute__((ext_vector_type(2)));
typedef short s16x4 __attribute__((ext_vector_type(4)));
typedef GAS unsigned gu32;
typedef unsigned long long v8q __attribute__((ext_vector_type(8)));

namespace {
constexpr int S = 2048, D = 640, F = 1536;
constexpr float LN_EPS = 1e-5f;
constexpr int AUX_SC1 = 16;
constexpr unsigned kSpinCap = 1u << 17;
constexpr int OOB = 0x7FFFFF00;

constexpr int WS_EPOCH = 0;
constexpr int WS_FLAG = 4096;
constexpr int FLAG_SET = 64 * 256;
constexpr int WS_XN = 1 << 20;
constexpr int WS_O = WS_XN + 128 * 20 * 1024;
constexpr int WS_H = WS_O + 128 * 20 * 1024;
constexpr int WS_H1 = WS_H + 128 * 20 * 1024;
constexpr int WS_KVP = WS_H1 + 128 * 48 * 1024;
constexpr int WS_ST = WS_KVP + 16 * 16 * 4096;
constexpr int WS_DBG = WS_ST + 2048 * 64;
constexpr int WS_TKV = WS_DBG + 256 * 8 * 32 * 8;
constexpr int WS_TO = WS_TKV + 16 * 153600;
constexpr int WS_T1 = WS_TO + 8 * 102400;
constexpr int WS_T2 = WS_T1 + 16 * 122880;
constexpr int WS_CS = WS_T2 + 16 * 122880;
constexpr int WS_BYTES = WS_CS + 16 * 16 * 384;

constexpr int L_BKV = 0, L_BQ = 102400;
constexpr int L_RED = 0, L_KVF = 153600;
constexpr int L_WO = 0, L_W1 = 30720, L_W2 = 0, L_CS = 161792;
constexpr int L_B1 = L_CS + 1152, L_B2 = L_CS + 1536;
constexpr int LDS_BYTES = 163840;

__device__ __forceinline__ f32x4 mfma(v4u a, v4u b, f32x4 c) {
  return __builtin_amdgcn_mfma_f32_16x16x32_bf16(__builtin_bit_cast(bf16x8, a), __builtin_bit_cast(bf16x8, b), c, 0, 0, 0);
}
__device__ __forceinline__ unsigned pk2(f32x2 v) { return __builtin_bit_cast(unsigned, __builtin_convertvector(v, bf16x2)); }
__device__ __forceinline__ unsigned pk2(float lo, float hi) { return pk2(f32x2{lo, hi}); }
__device__ __forceinline__ float bf_lo(unsigned u) { return __builtin_bit_cast(float, u << 16); }
__device__ __forceinline__ float bf_hi(unsigned u) { return __builtin_bit_cast(float, u & 0xFFFF0000u); }
__device__ __forceinline__ f32x2 bf2(unsigned u) { return f32x2{bf_lo(u), bf_hi(u)}; }
__device__ __forceinline__ f32x2 lo2(f32x4 v) { return f32x2{v[0], v[1]}; }
__device__ __forceinline__ f32x2 hi2(f32x4 v) { return f32x2{v[2], v[3]}; }
__device__ __forceinline__ v4u mk_srd(const void* p, unsigned bytes) {
  const unsigned long long a = (unsigned long long)p;
  v4u r;
  r.x = (unsigned)a; r.y = (unsigned)(a >> 32) & 0xFFFFu; r.z = bytes; r.w = 0x00020000u;
  return r;
}
#define DRAIN() asm volatile("s_waitcnt vmcnt(0)" ::: "memory")
#define BAR() asm volatile("s_waitcnt lgkmcnt(0)\n\ts_barrier" ::: "memory")
#define BAR_RAW() asm volatile("s_barrier" ::: "memory")
#define SCHED() __builtin_amdgcn_sched_barrier(0)
#define PIN(x) asm volatile("" : "+v"(x))

__device__ __forceinline__ void dma2(unsigned lds_addr, unsigned voff, v4u srd, unsigned soff) {
  unsigned keep;
  asm volatile("s_mov_b32 %0, m0\n\ts_mov_b32 m0, %1\n\ts_nop 0\n\t"
               "buffer_load_dwordx4 %2, %3, %4 offen sc1 lds\n\t"
               "buffer_load_dwordx4 %2, %3, %4 offen offset:1024 sc1 lds\n\t"
               "s_mov_b32 m0, %0"
               : "=&s"(keep) : "s"(lds_addr), "v"(voff), "s"(srd), "s"(soff) : "memory");
}
template <int LO, int HI, int TH = 0, int NW = 4>
__device__ __forceinline__ void dma_image(unsigned lds_base, unsigned voff, v4u srd, int ws_off, int hw, int rot) {
  constexpr int NP = (HI - LO) / 2;
#pragma unroll 1
  for (int i = hw; i < NP; i += NW) {
    int j = i + rot;
    if (j >= NP) j -= NP;
    dma2(lds_base + LO * 1024 + j * 2048, voff, srd, ws_off + LO * 1024 + j * 2048);
    if (TH) asm volatile("s_waitcnt vmcnt(%0)" :: "i"(TH - 2) : "memory");
  }
}
__device__ __forceinline__ v2u rdtr(LAS char* p) { return __builtin_bit_cast(v2u, __builtin_amdgcn_ds_read_tr16_b64_v4i16((LAS s16x4*)p)); }
struct LBase {
  unsigned a[3];
  __device__ __forceinline__ explicit LBase(unsigned v) { a[0] = v; a[1] = v + 65536; a[2] = v + 131072; PIN(a[0]); PIN(a[1]); PIN(a[2]); }
  __device__ __forceinline__ v2u rd(int off) const { return rdtr((LAS char*)(a[off >> 16]) + (off & 65535)); }
};
template <int MT, int NT, int KS, int PD, bool WA, int M0 = 0, class RB, class GA, class PRE>
__device__ __forceinline__ void kloop(f32x4 (&acc)[MT][NT], RB rb, GA ga, PRE pre) {
  v2u bl[PD + 1][NT], bh[PD + 1][NT];
#pragma unroll
  for (int m = M0; m < MT; ++m)
#pragma unroll
    for (int s = 0; s < NT; ++s) acc[m][s] = f32x4{0.f, 0.f, 0.f, 0.f};
#pragma unroll
  for (int ks = 0; ks < PD; ++ks)
#pragma unroll
    for (int s = 0; s < NT; ++s) { bl[ks][s] = rb(ks, s, 0); bh[ks][s] = rb(ks, s, 1); }
  SCHED();
#pragma unroll
  for (int ks = 0; ks < KS; ++ks) {
    pre(ks);
#pragma unroll
    for (int s = 0; s < NT; ++s)
#pragma unroll
      for (int m = 0; m < MT; ++m) {
        const v2u lo = bl[ks % (PD + 1)][s], hi = bh[ks % (PD + 1)][s];
        const v4u wv = {lo.x, lo.y, hi.x, hi.y};
        acc[m][s] = WA ? mfma(wv, ga(ks, m), acc[m][s]) : mfma(ga(ks, m), wv, acc[m][s]);
        if (ks + PD < KS) {
          if (MT == 1 || m == 0) bl[(ks + PD) % (PD + 1)][s] = rb(ks + PD, s, 0);
          if (MT == 1 || m == 1) bh[(ks + PD) % (PD + 1)][s] = rb(ks + PD, s, 1);
        }
        SCHED();
      }
  }
}
constexpr int ld_hi(int ks, int s0, int W, int KS) {
  const int a = s0 + 2 * (ks + 1), b = ks + 1 + W, c = a < b ? a : b;
  return c < KS ? c : KS;
}
__device__ __forceinline__ v4u ld128(__amdgpu_buffer_rsrc_t rs, int voff, int soff) {
  return __builtin_amdgcn_raw_buffer_load_b128(rs, voff, soff, AUX_SC1);
}
__device__ __forceinline__ v2u ld64(__amdgpu_buffer_rsrc_t rs, int voff, int soff) {
  return __builtin_amdgcn_raw_buffer_load_b64(rs, voff, soff, AUX_SC1);
}
__device__ __forceinline__ void st128(v4u v, __amdgpu_buffer_rsrc_t rs, int voff, int soff) {
  __builtin_amdgcn_raw_buffer_store_b128(v, rs, (int)((unsigned)voff + (unsigned)soff), 0, AUX_SC1);
}
__device__ __forceinline__ int sel(bool c, int a) {
  PIN(a);
  return c ? a : OOB;
}
__device__ __forceinline__ v4u ld_split(__amdgpu_buffer_rsrc_t rs, int lane, int soff) {
  const v2u a = ld64(rs, lane * 8, soff), b = ld64(rs, lane * 8 + 512, soff);
  return v4u{a.x, a.y, b.x, b.y};
}
__device__ __forceinline__ void st64(v2u v, __amdgpu_buffer_rsrc_t rs, int voff, int soff) {
  __builtin_amdgcn_raw_buffer_store_b64(v, rs, voff, soff, AUX_SC1);
}
__device__ __forceinline__ void flag_set(gu32* p, unsigned tag) {
  asm volatile("global_atomic_swap %0, %1, off sc1" :: "v"(p), "v"(tag) : "memory");
}
__device__ __forceinline__ void wait_flag(gu32* p, int n, unsigned tag, int lane) {
#pragma unroll 1
  for (unsigned spins = 0; spins < kSpinCap; ++spins) {
    unsigned v = tag;
    if (lane < n) asm volatile("global_atomic_add %0, %1, %2, off sc0 sc1\n\ts_waitcnt vmcnt(0)" : "=&v"(v) : "v"(p), "v"(0u) : "memory");
    if (__all(v == tag)) break;
    if (SLEEP) __builtin_amdgcn_s_sleep(SLEEP);
  }
  asm volatile("" ::: "memory");
}
__device__ __forceinline__ void sig_wait(volatile LAS unsigned* p) {
#pragma unroll 1
  for (unsigned spins = 0; spins < 64 * kSpinCap; ++spins) {
    if (*p != 0u) break;
    __builtin_amdgcn_s_sleep(1);
  }
  asm volatile("" ::: "memory");
}
__device__ __forceinline__ void prog_wait(volatile LAS v4u* p, unsigned stage) {
#pragma unroll 1
  for (unsigned spins = 0; spins < 64 * kSpinCap; ++spins) {
    const v4u v = *p;
    if (min(min(v.x, v.y), min(v.z, v.w)) >= stage) break;
    __builtin_amdgcn_s_sleep(1);
  }
  asm volatile("" ::: "memory");
}
__device__ __forceinline__ void publish(gu32* flag, unsigned tag, int lane) {
  DRAIN();
  if (lane == 0) flag_set(flag, tag);
}
__device__ __forceinline__ f32x4 add4(f32x4 a, f32x4 b) {
  f32x4 r;
#pragma unroll
  for (int i = 0; i < 4; ++i) { float x; asm("v_add_f32 %0, %1, %2" : "=v"(x) : "v"(a[i]), "v"(b[i])); r[i] = x; }
  return r;
}
__device__ __forceinline__ float xsum32(float x) {
  unsigned u = __builtin_bit_cast(unsigned, x), v = u;
  asm("" : "+v"(v));
  auto r = __builtin_amdgcn_permlane32_swap(u, v, false, false);
  const unsigned a = r[0], b = r[1];
  return __builtin_bit_cast(float, a) + __builtin_bit_cast(float, b);
}
__device__ __forceinline__ float xsum16(float x) {
  unsigned u = __builtin_bit_cast(unsigned, x), v = u;
  asm("" : "+v"(v));
  auto r = __builtin_amdgcn_permlane16_swap(u, v, false, false);
  const unsigned a = r[0], b = r[1];
  return __builtin_bit_cast(float, a) + __builtin_bit_cast(float, b);
}
__device__ __forceinline__ float xsum8(float x) {
  return x + __builtin_bit_cast(float, __builtin_amdgcn_update_dpp(0, __builtin_bit_cast(int, x), 0x128, 0xf, 0xf, false));
}
__device__ __forceinline__ f32x2 phi2(f32x2 t) {
  const f32x2 u = t * 1.4426950408889634f, v = t + 1e-6f;
  const f32x2 e = {__builtin_amdgcn_fmed3f(__builtin_amdgcn_exp2f(u[0]), 0.f, 1.f), __builtin_amdgcn_fmed3f(__builtin_amdgcn_exp2f(u[1]), 0.f, 1.f)};
  return f32x2{fmaxf(v[0], 1e-6f), fmaxf(v[1], 1e-6f)} + e;
}
__device__ __forceinline__ f32x2 gelu2(f32x2 x) {
  const f32x2 u = x * (x * x * -0.1029432397f + -2.302208198f);
  const f32x2 d = f32x2{__builtin_amdgcn_exp2f(u[0]), __builtin_amdgcn_exp2f(u[1])} + 1.f;
  return x * f32x2{__builtin_amdgcn_rcpf(d[0]), __builtin_amdgcn_rcpf(d[1])};
}

#define STAMP(i) do { } while (0)

__global__ void __launch_bounds__(512, 2)
fused_block(const void* a_x, const void* a_wqkv, void* a_ws, const void* a_wo, const void* a_w1, const void* a_b1, const void* a_w2,
            const void* a_b2, unsigned short* __restrict__ out) {
  __shared__ __attribute__((aligned(16))) char smem[LDS_BYTES];
  v8q ka;
  asm volatile("s_load_dwordx16 %0, %1, 0x0" : "=s"(ka) : "s"(__builtin_amdgcn_kernarg_segment_ptr()));
  SCHED();
  unsigned long long pc0;
  asm volatile("s_getpc_b64 %0" : "=s"(pc0));
  LAS char* L = (LAS char*)smem;
  const unsigned lds0 = (unsigned)(size_t)L;
  const int tid = threadIdx.x, lane = tid & 63, w = __builtin_amdgcn_readfirstlane(tid >> 6);
  const int b = blockIdx.x, xcd = b & 7, bi = b >> 3;
  const int g = lane >> 4, t = lane & 15;
  unsigned pf0;
  if (w == 6)
    pf0 = *(const GAS unsigned*)((const GAS unsigned char*)(pc0 & ~127ull) + min(lane * 32 + bi, 346) * 128);
  asm volatile("" ::: "memory");
  const int r128 = 4 * (xcd >> 1) + (bi >> 3), c16 = 8 * (xcd & 1) + (bi & 7);
  const int r64 = 8 * (xcd >> 1) + (bi >> 2), c8 = 4 * (xcd & 1) + (bi & 3);

  const int hw = w - 4, ht = tid - 256;
  volatile LAS unsigned* sig = (volatile LAS unsigned*)(L + L_CS + 1024);
  volatile LAS v4u* prog = (volatile LAS v4u*)(L + L_CS + 1088);
#define PROG(n) do { asm volatile("" ::: "memory"); sig[16 + w] = (n); } while (0)

  v4u tv[12];
  unsigned ep;
  const int q10 = lane / 10, c10 = lane % 10;
  int vo0 = (lane & 7) * 1280 + (lane >> 3) * 16, vo1 = sel(lane < 40, q10 * 3840 + (c10 < 5 ? 1280 : 2480) + c10 * 16);
  PIN(vo0); PIN(vo1);
  int vo = w < 4 ? vo0 : vo1;
  int s0 = (w < 4 ? b : r128) * (w < 4 ? 8 * 1280 : 40 * 3840) + (w < 4 ? w : c16) * (w < 4 ? 128 : 80);
  int st = w < 4 ? 512 : 4 * 3840, s2 = (w & 6) == 2 ? OOB : s0 + 2 * st;
  PIN(vo);
  asm volatile("s_waitcnt lgkmcnt(0)" : "+s"(ka), "+s"(s0), "+s"(st), "+s"(s2));
  const unsigned short* x = (const unsigned short*)ka[0];
  const unsigned short* wqkv = (const unsigned short*)ka[1];
  unsigned char* ws = (unsigned char*)ka[2];
  const unsigned short* wo = (const unsigned short*)ka[3];
  const unsigned short* w1 = (const unsigned short*)ka[4];
  const unsigned short* b1 = (const unsigned short*)ka[5];
  const unsigned short* w2 = (const unsigned short*)ka[6];
  const unsigned short* b2 = (const unsigned short*)ka[7];
  const __amdgpu_buffer_rsrc_t rs = __builtin_amdgcn_make_buffer_rsrc((void*)ws, 0, WS_BYTES, 0x00020000);
  gu32* flags = (gu32*)(ws + WS_FLAG);
#define FLAG(set, grp) (flags + ((set) * FLAG_SET + (grp) * 256) / 4)
  STAMP(0);
  const __amdgpu_buffer_rsrc_t rx = __builtin_amdgcn_make_buffer_rsrc((void*)x, 0, S * D * 2, 0x00020000);
  const __amdgpu_buffer_rsrc_t rq = __builtin_amdgcn_make_buffer_rsrc((void*)wqkv, 0, D * 3 * D * 2, 0x00020000);
  {
    if (w < 5) {
      const __amdgpu_buffer_rsrc_t rf = __builtin_amdgcn_make_buffer_rsrc((void*)(w < 4 ? x : wqkv), 0, w < 4 ? S * D * 2 : D * 3 * D * 2, 0x00020000);
      tv[0] = __builtin_amdgcn_raw_buffer_load_b128(rf, vo, s0, 0);
      tv[1] = __builtin_amdgcn_raw_buffer_load_b128(rf, vo, s0 + st, 0);
      tv[2] = __builtin_amdgcn_raw_buffer_load_b128(rf, vo, s2, 0);
      ep = __hip_atomic_load((gu32*)(ws + WS_EPOCH), RLX_AGENT);
      if (w == 4) {
#pragma unroll
        for (int j = 3; j < 10; ++j) tv[j] = __builtin_amdgcn_raw_buffer_load_b128(rf, vo, s0 + j * 4 * 3840, 0);
      }
    }
    if (w == 7) *(LAS v4u*)(L + L_CS + 512 + lane * 16) = v4u{0u, 0u, 0u, 0u};
  }
  BAR();

  if (w < 4) {
  unsigned tag = ep;
  const int grp32 = 4 * r128 + w;
  constexpr int AW = 6;

  {
    const int tok = 8 * b + (lane & 7), cl = lane >> 3;
    const bool has3 = w < 2;
    int xd = ((tok >> 4) * 20 + 2 * w + (cl >> 2)) * 1024 + ((cl & 3) * 16 + (tok & 15)) * 16;
    PIN(xd);
    SCHED();
    f32x2 f[12];
#pragma unroll
    for (int j = 0; j < 3; ++j)
#pragma unroll
      for (int e = 0; e < 4; ++e) f[j * 4 + e] = bf2(tv[j][e]);
    f32x2 s2 = (f[0] + f[1]) + (f[2] + f[3]) + ((f[4] + f[5]) + (f[6] + f[7])) + ((f[8] + f[9]) + (f[10] + f[11]));
    const float mloc = xsum32(xsum16(xsum8(s2[0] + s2[1]))) * (has3 ? 1.f / 192.f : 1.f / 128.f);
    const float m3 = has3 ? mloc : 0.f;
    f32x2 q2 = {0.f, 0.f}, q3 = {0.f, 0.f};
#pragma unroll
    for (int j = 0; j < 12; j += 2) {
      const f32x2 d0 = f[j] - (j < 8 ? mloc : m3), d1 = f[j + 1] - (j < 8 ? mloc : m3);
      q2 += d0 * d0; q3 += d1 * d1;
    }
    q2 += q3;
    const float m2loc = xsum32(xsum16(xsum8(q2[0] + q2[1])));
    if (lane < 8) *(volatile LAS f32x4*)(L + L_CS + 512 + (lane * 4 + w) * 16) = f32x4{mloc, m2loc, 1.f, 1.f};
    STAMP(25);
    f32x4 pr[4];
#pragma unroll 1
    for (unsigned spins = 0; spins < 64 * kSpinCap; ++spins) {
      bool ok = true;
#pragma unroll
      for (int ww = 0; ww < 4; ++ww) {
        pr[ww] = *(volatile LAS f32x4*)(L + L_CS + 512 + ((lane & 7) * 4 + ww) * 16);
        ok = ok && pr[ww][2] == 1.f;
      }
      if (__all(ok)) break;
    }
    const f32x4 p01 = {pr[0][0], pr[0][1], pr[1][0], pr[1][1]}, p23 = {pr[2][0], pr[2][1], pr[3][0], pr[3][1]};
    const float mean = (p01[0] + p01[2]) * 0.3f + (p23[0] + p23[2]) * 0.2f;
    const float e0 = p01[0] - mean, e1 = p01[2] - mean, e2 = p23[0] - mean, e3 = p23[2] - mean;
    const float m2 = (p01[1] + p01[3]) + (p23[1] + p23[3]) + 192.f * (e0 * e0 + e1 * e1) + 128.f * (e2 * e2 + e3 * e3);
    const float rstd = __builtin_amdgcn_rsqf(m2 * (1.f / D) + LN_EPS);
#pragma unroll
    for (int j = 0; j < 3; ++j) {
      v4u o;
#pragma unroll
      for (int e = 0; e < 4; ++e) o[e] = pk2((f[j * 4 + e] - mean) * rstd);
      st128(o, rs, (j < 2 || has3) ? xd : OOB, WS_XN + j * 8192);
    }
    STAMP(26);
    PIN(tag); tag += 1u;
    publish(FLAG(0, b >> 2) + (b & 3) * 4 + w, tag, lane);
    if (w == 0) sig[6] = 1u;
  }

  STAMP(1);

  v4u xr[3];
  {
    wait_flag(FLAG(0, grp32) + lane, 16, tag, lane);
    STAMP(2);
    v4u a[20][2];
    const int abase = WS_XN + 2 * grp32 * 20 * 1024;
#pragma unroll
    for (int ks = 0; ks < S0; ++ks)
#pragma unroll
      for (int m = 0; m < 2; ++m) a[ks][m] = ld128(rs, lane * 16, abase + (m * 20 + ks) * 1024);
    SCHED();
    BAR_RAW();
    STAMP(27);
    {
      f32x4 acc[2][5];
      const LBase bp(lds0 + L_BKV + (lane >> 2) * 160 + (lane & 3) * 8);
      kloop<2, 5, 20, 1, false>(acc, [&](int ks, int s, int h) { return bp.rd(ks * 5120 + h * 2560 + s * 32); },
        [&](int ks, int m) { return a[ks][m]; },
        [&](int ks) {
#pragma unroll
          for (int j = ld_hi(ks - 1, S0, AW, 20); j < ld_hi(ks, S0, AW, 20); ++j)
#pragma unroll
            for (int m = 0; m < 2; ++m) a[j][m] = ld128(rs, lane * 16, abase + (m * 20 + j) * 1024);
          if (ks == 3 || ks == 10) BAR_RAW();
          if (ks == 20 - AW) PROG(1);
        });
      STAMP(3);
      v4u ka[3], vb[3];
#pragma unroll
      for (int s = 0; s < 3; ++s)
#pragma unroll
        for (int m = 0; m < 2; ++m) {
          vb[s][2 * m] = pk2(lo2(acc[m][s + 2])); vb[s][2 * m + 1] = pk2(hi2(acc[m][s + 2]));
        }
#pragma unroll
      for (int e = 0; e < 4; ++e) vb[0][e] = t < 8 ? 0x3F803F80u : vb[0][e];
      const f32x4 z4 = {0.f, 0.f, 0.f, 0.f};
      LAS f32x4* red = (LAS f32x4*)(L + L_RED) + w * 512 + lane;
#pragma unroll
      for (int s = 0; s < 3; ++s) {
#pragma unroll
        for (int m = 0; m < 2; ++m) { ka[s][2 * m] = pk2(phi2(lo2(acc[m][s]))); ka[s][2 * m + 1] = pk2(phi2(hi2(acc[m][s]))); }
#pragma unroll
        for (int sb = 0; sb < 3; ++sb)
          if (s > 0 || sb < 2) red[(3 * s + sb - (s > 0)) * 64] = mfma(ka[s], vb[sb], z4);
      }
      STAMP(28);
      BAR();
    }
    STAMP(4);
    f32x4 qa[2][3];
    {
      unsigned bp0 = lds0 + L_BQ + (lane >> 2) * 64 + (lane & 3) * 8 + (g & 1) * 32;
      unsigned bp1 = lds0 + L_BQ + (lane >> 2) * 64 + (lane & 3) * 8 + 32 - (g & 1) * 32;
      unsigned bp2 = lds0 + L_BQ + 2048 + (lane >> 2) * 16 + (lane & 1) * 8;
      PIN(bp0); PIN(bp1); PIN(bp2);
      kloop<2, 3, 20, 2, true>(qa,
        [&](int ks, int s, int h) {
          return s == 0 ? rdtr((LAS char*)bp0 + ks * 2560 + h * 1024) : s == 1 ? rdtr((LAS char*)bp1 + ks * 2560 + h * 1024) : rdtr((LAS char*)bp2 + ks * 2560 + h * 256);
        },
        [&](int ks, int m) { return a[ks][m]; },
        [&](int ks) {});
    }
    v4u pq0[2], pq1[2];
#pragma unroll
    for (int m = 0; m < 2; ++m) {
      pq0[m].x = pk2(phi2(lo2(qa[m][0]))); pq0[m].y = pk2(phi2(hi2(qa[m][0])));
      pq0[m].z = pk2(phi2(lo2(qa[m][1]))); pq0[m].w = pk2(phi2(hi2(qa[m][1])));
      const unsigned lo = pk2(phi2(lo2(qa[m][2]))), hi = pk2(phi2(hi2(qa[m][2])));
      pq1[m].x = g < 2 ? lo : 0u; pq1[m].y = g < 2 ? hi : 0u; pq1[m].z = 0u; pq1[m].w = 0u;
      PIN(pq0[m]); PIN(pq1[m]);
    }
    STAMP(5);
    BAR_RAW();
#pragma unroll
    for (int j = 0; j < 3; ++j)
      xr[j] = __builtin_amdgcn_raw_buffer_load_b128(rx, sel(j < 2 || g < 2, t * 1280 + g * 16), (4 * r64 + w) * 16 * 1280 + c8 * 160 + j * 64, 0);
    STAMP(17);
    {
      v4u kv[4][2];
#pragma unroll
      for (int es = 3; es >= 0; --es)
#pragma unroll
        for (int ks = 0; ks < 2; ++ks) kv[es][ks] = *(LAS v4u*)(L + L_KVF + (es * 2 + ks) * 1024 + lane * 16);
      SCHED();
      f32x4 o[2][4];
#pragma unroll
      for (int es = 3; es >= 0; --es)
#pragma unroll
        for (int m = 0; m < 2; ++m) o[m][es] = mfma(kv[es][0], pq0[m], f32x4{0.f, 0.f, 0.f, 0.f});
#pragma unroll
      for (int es = 3; es >= 0; --es)
#pragma unroll
        for (int m = 0; m < 2; ++m) o[m][es] = mfma(kv[es][1], pq1[m], o[m][es]);
#pragma unroll
      for (int m = 0; m < 2; ++m) {
        const float i0 = __builtin_amdgcn_rcpf(o[m][3][0] + 1e-6f), i1 = __builtin_amdgcn_rcpf(o[m][3][1] + 1e-6f);
        const float im = g == 0 ? i0 : i1;
        v4u ov;
        ov.x = pk2(lo2(o[m][0]) * i0); ov.y = pk2(hi2(o[m][0]) * i0);
        ov.z = pk2(lo2(o[m][1]) * im); ov.w = pk2(hi2(o[m][1]) * im);
        v2u ol; ol.x = pk2(lo2(o[m][2]) * i1); ol.y = pk2(hi2(o[m][2]) * i1);
        const int tt = 2 * grp32 + m;
        st128(ov, rs, lane * 16, WS_O + (tt * 20 + c16) * 1024);
        st64(ol, rs, g < 2 ? lane * 8 : OOB, WS_O + (tt * 20 + 16) * 1024 + c16 * 256);
      }
      STAMP(18);
      publish(FLAG(2, grp32) + c16, tag, lane);
    }
    STAMP(7);
  }

  {
    const int tt = 4 * r64 + w;
    v4u eye[2];
#pragma unroll
    for (int hf = 0; hf < 2; ++hf) {
      const int e = 16 * hf + t - 8 * g;
#pragma unroll
      for (int d = 0; d < 4; ++d) eye[hf][d] = e == 2 * d ? 0x3F80u : e == 2 * d + 1 ? 0x3F800000u : 0u;
      PIN(eye[hf]);
    }
    f32x4 acc[1][5];
#pragma unroll
    for (int s = 0; s < 5; ++s) { acc[0][s] = mfma(eye[s & 1], xr[s >> 1], f32x4{0.f, 0.f, 0.f, 0.f}); PIN(acc[0][s]); }
    wait_flag(FLAG(2, tt >> 1) + lane, 16, tag, lane);
    STAMP(8);
    if (b == 0 && tid == 0) __hip_atomic_store((gu32*)(ws + WS_EPOCH), tag, RLX_AGENT);
    v4u a[20];
#pragma unroll
    for (int ks = 0; ks < 2 * S0; ++ks) a[ks] = ld128(rs, lane * 16, WS_O + (tt * 20 + ks) * 1024);
    SCHED();
    const LBase bp(lds0 + L_WO + (lane >> 2) * 160 + (lane & 3) * 8);
    kloop<1, 5, 20, 1, true, 1>(acc, [&](int ks, int s, int h) { return bp.rd(ks * 5120 + h * 2560 + s * 32); },
      [&](int ks, int m) { return a[ks]; },
      [&](int ks) {
#pragma unroll
        for (int j = ld_hi(ks - 1, 2 * S0, 2 * AW, 20); j < ld_hi(ks, 2 * S0, 2 * AW, 20); ++j)
          a[j] = j < 16 ? ld128(rs, lane * 16, WS_O + (tt * 20 + j) * 1024) : ld_split(rs, lane, WS_O + (tt * 20 + j) * 1024);
        if (ks == 8) PROG(2);
        if (ks == 14) PROG(3);
      });
    PROG(4);
    STAMP(9);
    v2u hb[5];
    f32x2 hv[10];
#pragma unroll
    for (int s = 0; s < 5; ++s) {
      hb[s].x = pk2(lo2(acc[0][s]));
      hb[s].y = pk2(hi2(acc[0][s]));
      hv[2 * s] = bf2(hb[s].x); hv[2 * s + 1] = bf2(hb[s].y);
    }
    st128(v4u{hb[0].x, hb[0].y, hb[1].x, hb[1].y}, rs, lane * 16, WS_H + (tt * 20 + 2 * c8) * 1024);
    st128(v4u{hb[2].x, hb[2].y, hb[3].x, hb[3].y}, rs, lane * 16, WS_H + (tt * 20 + 2 * c8 + 1) * 1024);
    st64(hb[4], rs, lane * 8, WS_H + (tt * 20 + 16) * 1024 + c8 * 512);
    const f32x2 s2 = ((hv[0] + hv[1]) + (hv[2] + hv[3])) + ((hv[4] + hv[5]) + (hv[6] + hv[7])) + (hv[8] + hv[9]);
    const float mc = xsum32(xsum16(s2[0] + s2[1])) * (1.f / 80.f);
    f32x2 q2 = {0.f, 0.f}, q3 = {0.f, 0.f};
#pragma unroll
    for (int j = 0; j < 10; j += 2) { const f32x2 d0 = hv[j] - mc, d1 = hv[j + 1] - mc; q2 += d0 * d0; q3 += d1 * d1; }
    q2 += q3;
    const float m2 = xsum32(xsum16(q2[0] + q2[1]));
    st64(v2u{__builtin_bit_cast(unsigned, mc), __builtin_bit_cast(unsigned, m2)}, rs, g == 0 ? t * 8 : OOB, WS_ST + (tt * 8 + c8) * 128);
    STAMP(20);
    publish(FLAG(3, tt >> 1) + (tt & 1) * 8 + c8, tag, lane);
    STAMP(10);
  }

  {
    wait_flag(FLAG(3, grp32) + lane, 16, tag, lane);
    STAMP(11);
    v4u stt[2];
#pragma unroll
    for (int m = 0; m < 2; ++m) {
      const v2u s0 = ld64(rs, g * 256 + t * 8, WS_ST + (2 * grp32 + m) * 1024), s1 = ld64(rs, g * 256 + 128 + t * 8, WS_ST + (2 * grp32 + m) * 1024);
      stt[m] = v4u{s0.x, s0.y, s1.x, s1.y};
    }
    v4u a[20][2];
    const int abase = WS_H + 2 * grp32 * 20 * 1024;
#pragma unroll
    for (int ks = 0; ks < S0; ++ks)
#pragma unroll
      for (int m = 0; m < 2; ++m) a[ks][m] = ld128(rs, lane * 16, abase + (m * 20 + ks) * 1024);
    SCHED();
    float nm[2], rr[2];
#pragma unroll
    for (int m = 0; m < 2; ++m) {
      const f32x4 sv = __builtin_bit_cast(f32x4, stt[m]);
      const float mean = xsum32(xsum16(sv[0] + sv[2])) * 0.125f;
      const float m2 = xsum32(xsum16(sv[1] + sv[3] + 80.f * ((sv[0] - mean) * (sv[0] - mean) + (sv[2] - mean) * (sv[2] - mean))));
      const float var = m2 * (1.f / D);
      const float r1 = __builtin_amdgcn_rsqf(var + LN_EPS);
      rr[m] = r1 * __builtin_amdgcn_rsqf(var * r1 * r1 + LN_EPS);
      nm[m] = -mean * rr[m];
      PIN(rr[m]); PIN(nm[m]);
    }
    BAR_RAW();
    STAMP(22);
    const unsigned l64 = lds0 + (lane >> 2) * 64 + (lane & 3) * 8;
    const LBase bp0(l64 + (g & 1) * 32), bp1(l64 + 32 - (g & 1) * 32);
    const int h1base = WS_H1 + 2 * grp32 * 48 * 1024;
    auto finish = [&](const f32x4& c0, const f32x4& c1, int j, int m) {
      v4u o;
#pragma unroll
      for (int s = 0; s < 2; ++s) {
        const f32x4 cs = *(LAS f32x4*)(L + L_CS + ((2 * j + s) * 4 + g) * 16), bb = *(LAS f32x4*)(L + L_B1 + ((2 * j + s) * 4 + g) * 16);
        const f32x4 c = s ? c1 : c0;
        o[2 * s] = pk2(gelu2(lo2(c) * rr[m] + (lo2(cs) * nm[m] + lo2(bb))));
        o[2 * s + 1] = pk2(gelu2(hi2(c) * rr[m] + (hi2(cs) * nm[m] + hi2(bb))));
      }
      st128(o, rs, lane * 16, h1base + (m * 48 + (c16 >> 3) * 24 + (j < 2 ? 2 * (c16 & 7) + j : 16 + (c16 & 7))) * 1024);
    };
    {
      f32x4 acc[2][4];
      kloop<2, 4, 20, 1, true>(acc, [&](int ks, int s, int h) { return ((s & 1) ? bp1 : bp0).rd(L_W1 + (s >> 1) * 40960 + ks * 2048 + h * 1024); },
        [&](int ks, int m) { return a[ks][m]; },
        [&](int ks) {
#pragma unroll
          for (int j = ld_hi(ks - 1, S0, AW, 20); j < ld_hi(ks, S0, AW, 20); ++j)
#pragma unroll
            for (int m = 0; m < 2; ++m)
              a[j][m] = j < 16 ? ld128(rs, lane * 16, abase + (m * 20 + j) * 1024) : ld_split(rs, lane, abase + (m * 20 + j) * 1024);
          if (ks == 20 - AW) PROG(5);
        });
      PROG(6);
      STAMP(12);
#pragma unroll
      for (int j = 0; j < 2; ++j)
#pragma unroll
        for (int m = 0; m < 2; ++m) finish(acc[m][2 * j], acc[m][2 * j + 1], j, m);
      SCHED();
      STAMP(24);
    }
    f32x4 accb[2][2];
    const int kh = w & 1, tt = 4 * r64 + w;
    v4u hr01, hr23;
    v2u hr4;
    kloop<2, 2, 20, 3, true>(accb, [&](int ks, int s, int h) { return (s ? bp1 : bp0).rd(L_W1 + 81920 + ks * 2048 + h * 1024); },
      [&](int ks, int m) { return a[ks][m]; },
      [&](int ks) {
        if (ks == PUBA) { publish(FLAG(4, grp32) + c16, tag, lane); PROG(7); }
        if (ks == PUBA + 2) {
          hr01 = ld128(rs, lane * 16, WS_H + (tt * 20 + 2 * c8) * 1024);
          hr23 = ld128(rs, lane * 16, WS_H + (tt * 20 + 2 * c8 + 1) * 1024);
          hr4 = ld64(rs, lane * 8, WS_H + (tt * 20 + 16) * 1024 + c8 * 512);
        }
      });
    STAMP(13);
    f32x4 acc[2][5];
    {
      const unsigned hrr[10] = {hr01.x, hr01.y, hr01.z, hr01.w, hr23.x, hr23.y, hr23.z, hr23.w, hr4.x, hr4.y};
#pragma unroll
      for (int s = 0; s < 5; ++s) {
        acc[0][s] = *(LAS f32x4*)(L + L_B2 + (s * 4 + g) * 16) + f32x4{bf_lo(hrr[2 * s]), bf_hi(hrr[2 * s]), bf_lo(hrr[2 * s + 1]), bf_hi(hrr[2 * s + 1])};
        PIN(acc[0][s]);
      }
    }
    int oo[3];
    unsigned tw, tr;
    {
      int ln = lane;
      PIN(ln);
      const int qq = ln / 10, cc = ln % 10;
#pragma unroll
      for (int j = 0; j < 3; ++j) { oo[j] = sel(ln < (j < 2 ? 60 : 40), ((tt * 16 + 6 * j + qq) * D + 80 * c8) * 2 + cc * 16); PIN(oo[j]); }
      tw = lds0 + L_W2 + 122880 + w * 3072 + t * 176 + g * 8; tr = lds0 + L_W2 + 122880 + w * 3072 + qq * 176 + cc * 16;
      PIN(tw); PIN(tr);
    }
    BAR_RAW();
    STAMP(14);
    constexpr int WIN = 8;
    v4u ar[WIN][2];
    const int a4[2] = {WS_H1 + (tt * 48 + kh * 24) * 1024, WS_H1 + ((tt ^ 1) * 48 + kh * 24) * 1024};
#pragma unroll
    for (int ks = 0; ks < WIN - 1; ++ks)
#pragma unroll
      for (int m = 0; m < 2; ++m) ar[ks][m] = ld128(rs, lane * 16, a4[m] + ks * 1024);
    SCHED();
#pragma unroll
    for (int m = 0; m < 2; ++m) finish(accb[m][0], accb[m][1], 2, m);
    STAMP(29);
    const LBase bp(lds0 + L_W2 + kh * 5120 + (lane >> 2) * 160 + (lane & 3) * 8);
    kloop<2, 5, 24, 1, true, 1>(acc, [&](int ks, int s, int h) { return bp.rd((ks % 15) * 10240 + h * 2560 + s * 32); },
      [&](int ks, int m) { return ar[ks % WIN][m]; },
      [&](int ks) {
        if (ks == 5) PROG(8);
        if (ks == 9 || ks == 14 || ks == 19) BAR_RAW();
        if (ks == 3) publish(FLAG(10, grp32) + c16, tag, lane);
        if (ks >= 3) {
#pragma unroll
          for (int j = ks == 3 ? WIN - 1 : ld_hi(ks - 1, 1, WIN - 1, 24); j < ld_hi(ks, 1, WIN - 1, 24); ++j)
#pragma unroll
            for (int m = 0; m < 2; ++m) ar[j % WIN][m] = ld128(rs, lane * 16, a4[m] + j * 1024);
        }
      });
    STAMP(15);
    LAS f32x4* xch = (LAS f32x4*)(L + L_W2 + 102400);
#pragma unroll
    for (int s = 0; s < 5; ++s) xch[((w ^ 1) * 5 + s) * 64 + lane] = acc[1][s];
    BAR();
    const __amdgpu_buffer_rsrc_t rout = __builtin_amdgcn_make_buffer_rsrc((void*)out, 0, S * D * 2, 0x00020000);
    f32x4 ot[5];
#pragma unroll
    for (int s = 0; s < 5; ++s) ot[s] = xch[(w * 5 + s) * 64 + lane];
    SCHED();
#pragma unroll
    for (int s = 0; s < 5; ++s) {
      const f32x4 v = acc[0][s] + ot[s];
      *(LAS v2u*)((LAS char*)tw + s * 32) = v2u{pk2(lo2(v)), pk2(hi2(v))};
    }
    asm volatile("" ::: "memory");
    v4u ov[3];
#pragma unroll
    for (int j = 0; j < 3; ++j) ov[j] = *(LAS v4u*)((LAS char*)tr + j * 6 * 176);
#pragma unroll
    for (int j = 0; j < 3; ++j) __builtin_amdgcn_raw_buffer_store_b128(ov[j], rout, oo[j], 0, 0);
    STAMP(16);
  }
  return;
  }

  {
    const v4u srd_ws = mk_srd(ws, WS_BYTES);
    STAMP(16);
    const int q5 = lane / 5, c5 = lane % 5;
    const int f40m = c5 < 4 ? 512 : 128, f40l = c5 < 4 ? q5 * 64 + (c5 ^ (2 * ((q5 >> 2) & 1))) * 16 : 2048 + q5 * 16;
    v2u ub1, ub2;
    v4u tq[10];
    if (hw == 1) {
      const __amdgpu_buffer_rsrc_t ro = __builtin_amdgcn_make_buffer_rsrc((void*)wo, 0, D * D * 2, 0x00020000);
      const int vq = sel(lane < 40, (8 * (q5 >> 2) + (q5 & 3)) * 3840 + c5 * 16), vo = sel(lane < 40, q10 * 1280 + c10 * 16);
#pragma unroll
      for (int j = 0; j < 5; ++j) {
        const int i8 = 5 * r128 + j, k = 32 * (i8 >> 2) + 16 * (i8 & 1) + 4 * ((i8 >> 1) & 1);
        tq[j] = __builtin_amdgcn_raw_buffer_load_b128(rq, vq, k * 3840 + c16 * 80, 0);
      }
#pragma unroll
      for (int j = 0; j < 5; ++j) tq[5 + j] = __builtin_amdgcn_raw_buffer_load_b128(ro, vo, (20 * r64 + 4 * j) * 1280 + c8 * 160, 0);
    } else if (hw == 2) {
      if (lane < 6) {
        gu32* fp = FLAG(3 * (b >> 6) + (lane >> 1), b & 63) + (lane & 1) * 32;
        asm volatile("global_atomic_add %0, %1, off sc1" :: "v"(fp), "v"(0u) : "memory");
      }
    } else if (hw == 3) {
      const __amdgpu_buffer_rsrc_t rb1 = __builtin_amdgcn_make_buffer_rsrc((void*)b1, 0, F * 2, 0x00020000);
      const __amdgpu_buffer_rsrc_t rb2 = __builtin_amdgcn_make_buffer_rsrc((void*)b2, 0, D * 2, 0x00020000);
      ub1 = __builtin_amdgcn_raw_buffer_load_b64(rb1, lane * 8, c16 * 192, 0);
      ub2 = __builtin_amdgcn_raw_buffer_load_b64(rb2, lane * 8, c8 * 160, 0);
    }
    unsigned tag = __hip_atomic_load((gu32*)(ws + WS_EPOCH), RLX_AGENT);
    if (hw == 0) {
      const int dof = sel(lane < 40, lane * 16);
#pragma unroll
      for (int j = 0; j < 10; ++j) {
        const int k = 40 * r128 + 4 * j, R = (k & ~31) + 16 * ((k >> 2) & 1) + 4 * ((k >> 3) & 3);
        st128(tv[j], rs, dof, WS_TKV + c16 * 153600 + R * 160);
      }
      STAMP(18);
      PIN(tag); tag += 1u;
      publish(FLAG(6, c16) + r128, tag, lane);
      STAMP(13);
    }
    asm volatile("" ::: "memory");
    int red_off, kvf_off, kvp_off;
    auto items = [&]() {
      const int it = ht < 210 ? ht : 209, ie = it / 5, ic = it % 5;
      const int ihead = ie < 40 ? (ie >= 20) : ie - 40;
      const int id = 20 * ihead + 4 * ic, dg = id >> 2;
      const int sa = id >> 4, col = 40 + ie;
      const int tile = 3 * sa + (ie < 40 ? (col >> 4) - 2 : 0) - (sa > 0);
      red_off = lds0 + L_RED + (tile * 64 + (dg & 3) * 16 + (ie < 40 ? (col & 15) : 0)) * 16;
      kvf_off = lds0 + L_KVF + ((ie < 40 ? (ie >> 4) : 3) * 2 + (dg >> 3)) * 1024 + ((dg & 3) * 16 + (ie < 40 ? (ie & 15) : ihead)) * 16 + ((dg >> 2) & 1) * 8;
      kvp_off = WS_KVP + (c16 * 16 + r128) * 4096 + ht * 16;
      PIN(red_off); PIN(kvf_off); PIN(kvp_off);
    };
    if (hw != 0) items();
    int vo12, td[12];
    if (hw >= 2) {
      const int q12 = lane / 12, c12 = lane % 12;
      vo12 = hw == 2 ? sel(lane < 48, q12 * 3072 + c12 * 16) : sel(lane < 40, q10 * 1280 + c10 * 16);
#pragma unroll
      for (int j = 0; j < 12; ++j) {
        const int cc = r128 >> 1, off = 40 * (r128 & 1) + 4 * j;
        const int R = off < 64 ? 64 * cc + off : 32 * (16 + (cc >> 1)) + 16 * (cc & 1) + off - 64;
        const int d1 = WS_T1 + c16 * 122880 + (c12 >> 2) * 40960 + (R + q12) * 64 + ((c12 & 3) ^ (2 * ((R >> 2) & 1))) * 16;
        const int pc = r64 >> 1, ko = 48 * (r64 & 1) + 4 * j, jj = ko >> 5;
        const int d2 = WS_T2 + c8 * 245760 + (2 * (jj < 2 ? 2 * (pc & 7) + jj : 16 + (pc & 7)) + (pc >> 3)) * 5120 + (ko & 31) * 160 + lane * 16;
        td[j] = sel(hw == 2 ? (lane < 48 && j < 10) : lane < 40, hw == 2 ? d1 : d2);
        PIN(td[j]);
      }
      PIN(vo12);
    }
    int l16 = lane * 16, h16 = sel(ht < 210, ht * 16);
    PIN(l16); PIN(h16);
    gu32* fp1 = FLAG(1, c16) + lane;
    gu32* fp78 = lane < 32 ? FLAG(7, c8) + lane : FLAG(8, c16) + t;
    gu32* fp4 = FLAG(4, 2 * r64 + (g & 1)) + t;
    gu32* fp1w = FLAG(1, c16) + r128 * 4 + hw;
    PIN(fp1); PIN(fp78); PIN(fp4); PIN(fp1w);
    ((LAS v4u*)(L + L_KVF))[ht] = v4u{0u, 0u, 0u, 0u};
    ((LAS v4u*)(L + L_KVF))[256 + ht] = v4u{0u, 0u, 0u, 0u};
    asm volatile("" ::: "memory");
    if (hw != 0) {
      if (hw == 1) {
        sig_wait(sig + 6);
#pragma unroll
        for (int j = 0; j < 5; ++j) {
          const int i8 = 5 * r128 + j;
          st128(tq[j], rs, sel(lane < 40, (i8 & 3) * f40m + f40l), WS_TKV + c16 * 153600 + 102400 + (i8 >> 2) * 2560);
        }
#pragma unroll
        for (int j = 0; j < 5; ++j) {
          const int pp = r64 >> 1, e = 20 * (r64 & 1) + 4 * j;
          const int R = e < 32 ? 32 * pp + e : 32 * (16 + (pp >> 2)) + 8 * (pp & 3) + e - 32;
          st128(tq[5 + j], rs, sel(lane < 40, lane * 16), WS_TO + c8 * 102400 + R * 160);
        }
      }
      if (hw == 3) {
        if (lane < 24) *(LAS f32x4*)(L + L_B1 + lane * 16) = f32x4{bf_lo(ub1.x), bf_hi(ub1.x), bf_lo(ub1.y), bf_hi(ub1.y)};
        if (lane < 20) *(LAS f32x4*)(L + L_B2 + lane * 16) = f32x4{bf_lo(ub2.x), bf_hi(ub2.x), bf_lo(ub2.y), bf_hi(ub2.y)};
      }
      if (hw == 2) asm volatile("" :: "v"(pf0));
      PIN(tag); tag += 1u;
      DRAIN();
      if (hw == 1 && lane < 2) flag_set(lane == 0 ? FLAG(8, c16) + r128 : FLAG(7, c8) + r64, tag);
    }
    if (hw == 0) {
      if (F6W) __builtin_amdgcn_s_sleep(F6W);
      wait_flag(FLAG(6, c16) + lane, 16, tag, lane);
      sig[0] = 1u;
    }
    sig_wait(sig);
    STAMP(1);
    const int kvq = WS_TKV + c16 * 153600, wos = WS_TO + c8 * 102400;
    dma_image<0, 24>(lds0 + L_BKV, l16, srd_ws, kvq, hw, 3 * (bi >> 3));
    dma_image<24, 56>(lds0 + L_BKV, l16, srd_ws, kvq, hw, 4 * (bi >> 3));
    asm volatile("s_waitcnt vmcnt(8)" ::: "memory");
    STAMP(2);
    BAR_RAW();
    dma_image<56, 100>(lds0 + L_BKV, l16, srd_ws, kvq, hw, 5 * (bi >> 3));
    asm volatile("s_waitcnt vmcnt(10)" ::: "memory");
    BAR_RAW();
    if (hw == 1) wait_flag(fp78, 48, tag, lane);
    DRAIN();
    STAMP(3);
    BAR_RAW();
    if (QPOS == 0) dma_image<100, 150>(lds0 + L_BKV, l16, srd_ws, kvq, hw, 6 * (bi >> 3));
    if (hw == 0) items();
    prog_wait(prog, 1);
    if (QPOS == 1) dma_image<100, 150>(lds0 + L_BKV, l16, srd_ws, kvq, hw, 6 * (bi >> 3));
    if (hw >= 2) {
      const __amdgpu_buffer_rsrc_t r12 = __builtin_amdgcn_make_buffer_rsrc((void*)(hw == 2 ? w1 : w2), 0, D * F * 2, 0x00020000);
      const int base = hw == 2 ? r128 * 122880 + c16 * 192 : r64 * 61440 + c8 * 160, step = hw == 2 ? 4 * 3072 : 4 * 1280;
#pragma unroll
      for (int j = 0; j < 12; ++j) tv[j] = __builtin_amdgcn_raw_buffer_load_b128(r12, vo12, (j < 10 || hw == 3) ? base + j * step : OOB, 0);
      asm volatile("s_waitcnt vmcnt(12)" ::: "memory");
    } else {
      dma_image<32, 68, 0, 2>(lds0 + L_WO, l16, srd_ws, wos, hw, bi >> 2);
      asm volatile("s_waitcnt vmcnt(18)" ::: "memory");
    }
    STAMP(4);
    BAR();
    __builtin_amdgcn_s_setprio(2);
    STAMP(11);
    {
      f32x4 pv[4];
#pragma unroll
      for (int ww = 0; ww < 4; ++ww) pv[ww] = *(LAS f32x4*)((LAS char*)red_off + ww * 8192);
      st128(__builtin_bit_cast(v4u, add4(add4(pv[0], pv[1]), add4(pv[2], pv[3]))), rs, kvp_off, 0);
      PROG(1);
      STAMP(12);
      DRAIN();
#pragma unroll
      for (int j = 0; j < 12; ++j) PIN(tv[j]);
    }
    if (lane == 0) flag_set(fp1w, tag);
    if (hw == 0) {
      STAMP(10);
      __builtin_amdgcn_s_sleep(H1W);
      wait_flag(fp1, 64, tag, lane);
      sig[2] = 1u;
    } else {
      if (H1G) __builtin_amdgcn_s_sleep(H1G);
      prog_wait(prog + 1, 1);
#pragma unroll
      for (int i = 0; i < 12; ++i) {
        if (hw >= 2) st128(tv[i], rs, td[i], 0);
        const int j = hw - 1 + 3 * i;
        if (j < 32) {
          const int pc = 2 * ((j + 4 * (bi >> 2)) & 31), po = (pc < 32 ? pc : pc + 36) * 1024;
          dma2(lds0 + L_WO + po, l16, srd_ws, wos + po);
        }
      }
      STAMP(10);
      sig_wait(sig + 2);
    }
    STAMP(5);
    {
      v4u pv[16];
#pragma unroll
      for (int r = 0; r < 16; ++r) pv[r] = ld128(rs, h16, WS_KVP + (c16 * 16 + r) * 4096);
      SCHED();
      f32x4 v = __builtin_bit_cast(f32x4, pv[0]);
#pragma unroll
      for (int r = 1; r < 16; ++r) v = add4(v, __builtin_bit_cast(f32x4, pv[r]));
      v2u o; o.x = pk2(lo2(v)); o.y = pk2(hi2(v));
      if (ht < 210) *(LAS v2u*)((LAS char*)kvf_off) = o;
      if (ht >= 200 && ht < 210) {
#pragma unroll
        for (int c = 1; c < 4; ++c) *(LAS v2u*)((LAS char*)kvf_off + c * 64) = o;
      }
    }
    DRAIN();
    STAMP(6);
    BAR();
    __builtin_amdgcn_s_setprio(0);
    if (hw >= 2 && lane == 0) flag_set(hw == 2 ? FLAG(5, c16) + r128 : FLAG(11, c8) + r64, tag);
    if (hw == 2) {
      f32x4 s0 = {0.f, 0.f, 0.f, 0.f}, s1 = {0.f, 0.f, 0.f, 0.f};
#pragma unroll
      for (int j = 0; j < 10; ++j) {
        s0 = add4(s0, f32x4{bf_lo(tv[j].x), bf_hi(tv[j].x), bf_lo(tv[j].y), bf_hi(tv[j].y)});
        s1 = add4(s1, f32x4{bf_lo(tv[j].z), bf_hi(tv[j].z), bf_lo(tv[j].w), bf_hi(tv[j].w)});
      }
#pragma unroll
      for (int d = 24; d >= 12; d -= 12) {
        f32x4 u0, u1;
#pragma unroll
        for (int e = 0; e < 4; ++e) { u0[e] = __shfl(s0[e], lane + d); u1[e] = __shfl(s1[e], lane + d); }
        s0 = add4(s0, u0); s1 = add4(s1, u1);
      }
      f32x4 sv;
#pragma unroll
      for (int e = 0; e < 4; ++e) { const float a0 = __shfl(s0[e], lane >> 1), a1 = __shfl(s1[e], lane >> 1); sv[e] = (lane & 1) ? a1 : a0; }
      st128(__builtin_bit_cast(v4u, sv), rs, sel(lane < 24, lane * 16), WS_CS + (c16 * 16 + r128) * 384);
      publish(FLAG(9, c16) + r128, tag, lane);
    }
    if (hw == 1) {
      __builtin_amdgcn_s_sleep(40);
      wait_flag(lane < 32 ? FLAG(11, c8) + lane : FLAG(5, c16) + t, 48, tag, lane);
      sig[3] = 1u;
    }
    sig_wait(sig + 3);
    prog_wait(prog, 2);
    if (hw == 0) {
      wait_flag(FLAG(9, c16) + lane, 16, tag, lane);
      v4u pv[16];
      const int co = sel(lane < 24, l16);
#pragma unroll
      for (int r = 0; r < 16; ++r) pv[r] = ld128(rs, co, WS_CS + (c16 * 16 + r) * 384);
      SCHED();
      f32x4 v = __builtin_bit_cast(f32x4, pv[0]);
#pragma unroll
      for (int r = 1; r < 16; ++r) v = add4(v, __builtin_bit_cast(f32x4, pv[r]));
      if (lane < 24) *(LAS f32x4*)(L + L_CS + lane * 16) = v;
    } else {
      dma_image<70, 120, 0, 3>(lds0 + L_W1, l16, srd_ws, WS_T1 + c16 * 122880, hw - 1, 6 * (bi >> 3));
      prog_wait(prog, 3);
      dma_image<0, 40, 0, 3>(lds0 + L_W1, l16, srd_ws, WS_T1 + c16 * 122880, hw - 1, 5 * (bi >> 3));
      STAMP(7);
      prog_wait(prog, 4);
      dma_image<40, 70, 0, 3>(lds0 + L_W1, l16, srd_ws, WS_T1 + c16 * 122880, hw - 1, 3 * (bi >> 3));
    }
    DRAIN();
    STAMP(8);
    BAR();
    const int w2s = WS_T2 + c8 * 245760;
    prog_wait(prog, 5);
    dma_image<0, 30>(lds0 + L_W2, l16, srd_ws, w2s, hw, bi >> 2);
    STAMP(14);
    prog_wait(prog, 6);
    dma_image<30, W2S>(lds0 + L_W2, l16, srd_ws, w2s, hw, bi >> 2);
    prog_wait(prog, 7);
    if (hw != 0) dma_image<W2S, 110, 0, 3>(lds0 + L_W2, l16, srd_ws, w2s, hw - 1, bi >> 2);
    DRAIN();
    STAMP(9);
    if (hw == 0) wait_flag(fp4, 32, tag, lane);
    STAMP(15);
    BAR_RAW();
    dma_image<110, 150>(lds0 + L_W2, l16, srd_ws, w2s, hw, 2 * (bi >> 2));
    prog_wait(prog, 8);
    if (hw == 0) wait_flag(fp4 + 6 * FLAG_SET / 4, 32, tag, lane);
    else dma_image<150, 198, 0, 3>(lds0 + L_W2 - 153600, l16, srd_ws, w2s, hw - 1, 3 * (bi >> 2));
    asm volatile("s_waitcnt vmcnt(16)" ::: "memory");
    STAMP(17);
    BAR_RAW();
    dma_image<198, 200>(lds0 + L_W2 - 153600, l16, srd_ws, w2s, hw, 0);
    dma_image<200, 240>(lds0 + L_W2 - 153600, l16, srd_ws, w2s, hw, 2 * (bi >> 2));
    asm volatile("s_waitcnt vmcnt(10)" ::: "memory");
    BAR_RAW();
    DRAIN();
    BAR_RAW();
  }

}
}

extern "C" void kernel_launch(void* const* d_in, const int* in_sizes, int n_in, void* d_out, int out_size, void* d_ws,
                              size_t ws_size, hipStream_t stream) {
  const unsigned short* x = (const unsigned short*)d_in[0];
  const unsigned short* wqkv = (const unsigned short*)d_in[1];
  const unsigned short* wo = (const unsigned short*)d_in[2];
  const unsigned short* w1 = (const unsigned short*)d_in[4];
  const unsigned short* b1 = (const unsigned short*)d_in[5];
  const unsigned short* w2 = (const unsigned short*)d_in[6];
  const unsigned short* b2 = (const unsigned short*)d_in[7];
  fused_block<<<256, 512, 0, stream>>>(x, wqkv, (unsigned char*)d_ws, wo, w1, b1, w2, b2, (unsigned short*)d_out);
}
```

```cpp
#include <hip/hip_runtime.h>
#include <hip/hip_bf16.h>

#define GAS __attribute__((address_space(1)))
#define LAS __attribute__((address_space(3)))
#define RLX_AGENT __ATOMIC_RELAXED, __HIP_MEMORY_SCOPE_AGENT
typedef unsigned v4u __attribute__((ext_vector_type(4)));
typedef unsigned v2u __attribute__((ext_vector_type(2)));
typedef float f32x4 __attribute__((ext_vector_type(4)));
typedef float f32x2 __attribute__((ext_vector_type(2)));
typedef __bf16 bf16x8 __attribute__((ext_vector_type(8)));
typedef __bf16 bf16x2 __attribute__((ext_vector_type(2)));
typedef short s16x4 __attribute__((ext_vector_type(4)));
typedef GAS unsigned gu32;

namespace {
constexpr int S = 2048, D = 640, F = 1536;
constexpr float LN_EPS = 1e-5f;
constexpr int AUX_SC1 = 16;
constexpr unsigned kSpinCap = 1u << 17;
constexpr int OOB = 0x7FFFFF00;

constexpr int WS_EPOCH = 0;
constexpr int WS_FLAG = 4096;
constexpr int FLAG_SET = 64 * 256;
constexpr int WS_XN = 1 << 20;
constexpr int WS_O = WS_XN + 128 * 20 * 1024;
constexpr int WS_H = WS_O + 128 * 20 * 1024;
constexpr int WS_H1 = WS_H + 128 * 20 * 1024;
constexpr int WS_KVP = WS_H1 + 128 * 48 * 1024;
constexpr int WS_ST = WS_KVP + 16 * 16 * 4096;
constexpr int WS_TKV = WS_ST + 2048 * 64;
constexpr int WS_TO = WS_TKV + 16 * 153600;
constexpr int WS_T1 = WS_TO + 8 * 102400;
constexpr int WS_T2 = WS_T1 + 16 * 122880;
constexpr int WS_CS = WS_T2 + 16 * 122880;
constexpr int WS_BYTES = WS_CS + 16 * 16 * 384;

constexpr int L_BKV = 0, L_BQ = 102400;
constexpr int L_RED = 0, L_KVF = 153600;
constexpr int L_WO = 0, L_W1 = 30720, L_W2 = 0, L_CS = 161792;
constexpr int L_B1 = L_CS + 1152, L_B2 = L_CS + 1536;
constexpr int LDS_BYTES = 163840;

__device__ __forceinline__ f32x4 mfma(v4u a, v4u b, f32x4 c) {
  return __builtin_amdgcn_mfma_f32_16x16x32_bf16(__builtin_bit_cast(bf16x8, a), __builtin_bit_cast(bf16x8, b), c, 0, 0, 0);
}
__device__ __forceinline__ unsigned pk2(f32x2 v) { return __builtin_bit_cast(unsigned, __builtin_convertvector(v, bf16x2)); }
__device__ __forceinline__ float bf_lo(unsigned u) { return __builtin_bit_cast(float, u << 16); }
__device__ __forceinline__ float bf_hi(unsigned u) { return __builtin_bit_cast(float, u & 0xFFFF0000u); }
__device__ __forceinline__ f32x2 bf2(unsigned u) { return f32x2{bf_lo(u), bf_hi(u)}; }
__device__ __forceinline__ f32x2 lo2(f32x4 v) { return f32x2{v[0], v[1]}; }
__device__ __forceinline__ f32x2 hi2(f32x4 v) { return f32x2{v[2], v[3]}; }
__device__ __forceinline__ v4u mk_srd(const void* p, unsigned bytes) {
  const unsigned long long a = (unsigned long long)p;
  v4u r;
  r.x = (unsigned)a; r.y = (unsigned)(a >> 32) & 0xFFFFu; r.z = bytes; r.w = 0x00020000u;
  return r;
}
#define DRAIN() asm volatile("s_waitcnt vmcnt(0)" ::: "memory")
#define BAR() asm volatile("s_waitcnt lgkmcnt(0)\n\ts_barrier" ::: "memory")
#define BAR_RAW() asm volatile("s_barrier" ::: "memory")
#define SCHED() __builtin_amdgcn_sched_barrier(0)
#define PIN(x) asm volatile("" : "+v"(x))

__device__ __forceinline__ void dma2(unsigned lds_addr, unsigned voff, v4u srd, unsigned soff) {
  unsigned keep;
  asm volatile("s_mov_b32 %0, m0\n\ts_mov_b32 m0, %1\n\ts_nop 0\n\t"
               "buffer_load_dwordx4 %2, %3, %4 offen sc1 lds\n\t"
               "buffer_load_dwordx4 %2, %3, %4 offen offset:1024 sc1 lds\n\t"
               "s_mov_b32 m0, %0"
               : "=&s"(keep) : "s"(lds_addr), "v"(voff), "s"(srd), "s"(soff) : "memory");
}
template <int LO, int HI, int NW = 4>
__device__ __forceinline__ void dma_image(unsigned lds_base, unsigned voff, v4u srd, int ws_off, int hw, int rot) {
  constexpr int NP = (HI - LO) / 2;
#pragma unroll 1
  for (int i = hw; i < NP; i += NW) {
    int j = i + rot;
    if (j >= NP) j -= NP;
    dma2(lds_base + LO * 1024 + j * 2048, voff, srd, ws_off + LO * 1024 + j * 2048);
  }
}
__device__ __forceinline__ LAS char* lp(unsigned a) { return (LAS char*)(size_t)a; }
__device__ __forceinline__ v2u rdtr(LAS char* p) { return __builtin_bit_cast(v2u, __builtin_amdgcn_ds_read_tr16_b64_v4i16((LAS s16x4*)p)); }
struct LBase {
  unsigned a[3];
  __device__ __forceinline__ explicit LBase(unsigned v) { a[0] = v; a[1] = v + 65536; a[2] = v + 131072; PIN(a[0]); PIN(a[1]); PIN(a[2]); }
  __device__ __forceinline__ v2u rd(int off) const { return rdtr(lp(a[off >> 16]) + (off & 65535)); }
};
template <int MT, int NT, int KS, int PD, bool WA, int M0 = 0, class RB, class GA, class PRE>
__device__ __forceinline__ void kloop(f32x4 (&acc)[MT][NT], RB rb, GA ga, PRE pre) {
  v2u bl[PD + 1][NT], bh[PD + 1][NT];
#pragma unroll
  for (int m = M0; m < MT; ++m)
#pragma unroll
    for (int s = 0; s < NT; ++s) acc[m][s] = f32x4{0.f, 0.f, 0.f, 0.f};
#pragma unroll
  for (int ks = 0; ks < PD; ++ks)
#pragma unroll
    for (int s = 0; s < NT; ++s) { bl[ks][s] = rb(ks, s, 0); bh[ks][s] = rb(ks, s, 1); }
  SCHED();
#pragma unroll
  for (int ks = 0; ks < KS; ++ks) {
    pre(ks);
#pragma unroll
    for (int s = 0; s < NT; ++s)
#pragma unroll
      for (int m = 0; m < MT; ++m) {
        const v2u lo = bl[ks % (PD + 1)][s], hi = bh[ks % (PD + 1)][s];
        const v4u wv = {lo.x, lo.y, hi.x, hi.y};
        acc[m][s] = WA ? mfma(wv, ga(ks, m), acc[m][s]) : mfma(ga(ks, m), wv, acc[m][s]);
        if (ks + PD < KS) {
          if (MT == 1 || m == 0) bl[(ks + PD) % (PD + 1)][s] = rb(ks + PD, s, 0);
          if (MT == 1 || m == 1) bh[(ks + PD) % (PD + 1)][s] = rb(ks + PD, s, 1);
        }
        SCHED();
      }
  }
}
constexpr int ld_hi(int ks, int s0, int W, int KS) {
  const int a = s0 + 2 * (ks + 1), b = ks + 1 + W, c = a < b ? a : b;
  return c < KS ? c : KS;
}
__device__ __forceinline__ v4u ld128(__amdgpu_buffer_rsrc_t rs, int voff, int soff) {
  return __builtin_amdgcn_raw_buffer_load_b128(rs, voff, soff, AUX_SC1);
}
__device__ __forceinline__ v2u ld64(__amdgpu_buffer_rsrc_t rs, int voff, int soff) {
  return __builtin_amdgcn_raw_buffer_load_b64(rs, voff, soff, AUX_SC1);
}
__device__ __forceinline__ void st128(v4u v, __amdgpu_buffer_rsrc_t rs, int voff, int soff) {
  __builtin_amdgcn_raw_buffer_store_b128(v, rs, (int)((unsigned)voff + (unsigned)soff), 0, AUX_SC1);
}
__device__ __forceinline__ int sel(bool c, int a) {
  PIN(a);
  return c ? a : OOB;
}
__device__ __forceinline__ v4u ld_split(__amdgpu_buffer_rsrc_t rs, int lane, int soff) {
  const v2u a = ld64(rs, lane * 8, soff), b = ld64(rs, lane * 8 + 512, soff);
  return v4u{a.x, a.y, b.x, b.y};
}
__device__ __forceinline__ void st64(v2u v, __amdgpu_buffer_rsrc_t rs, int voff, int soff) {
  __builtin_amdgcn_raw_buffer_store_b64(v, rs, voff, soff, AUX_SC1);
}
__device__ __forceinline__ void flag_set(gu32* p, unsigned tag) {
  asm volatile("global_atomic_swap %0, %1, off sc1" :: "v"(p), "v"(tag) : "memory");
}
__device__ __forceinline__ void wait_flag(gu32* p, int n, unsigned tag, int lane) {
#pragma unroll 1
  for (unsigned spins = 0; spins < kSpinCap; ++spins) {
    unsigned v = tag;
    if (lane < n) asm volatile("global_atomic_add %0, %1, %2, off sc0 sc1\n\ts_waitcnt vmcnt(0)" : "=&v"(v) : "v"(p), "v"(0u) : "memory");
    if (__all(v == tag)) break;
    __builtin_amdgcn_s_sleep(1);
  }
  asm volatile("" ::: "memory");
}
__device__ __forceinline__ void sig_wait(volatile LAS unsigned* p) {
#pragma unroll 1
  for (unsigned spins = 0; spins < 64 * kSpinCap; ++spins) {
    if (*p != 0u) break;
    __builtin_amdgcn_s_sleep(1);
  }
  asm volatile("" ::: "memory");
}
__device__ __forceinline__ void prog_wait(volatile LAS v4u* p, unsigned stage) {
#pragma unroll 1
  for (unsigned spins = 0; spins < 64 * kSpinCap; ++spins) {
    const v4u v = *p;
    if (min(min(v.x, v.y), min(v.z, v.w)) >= stage) break;
    __builtin_amdgcn_s_sleep(1);
  }
  asm volatile("" ::: "memory");
}
__device__ __forceinline__ void publish(gu32* flag, unsigned tag, int lane) {
  DRAIN();
  if (lane == 0) flag_set(flag, tag);
}
__device__ __forceinline__ f32x4 add4(f32x4 a, f32x4 b) {
  f32x4 r;
#pragma unroll
  for (int i = 0; i < 4; ++i) { float x; asm("v_add_f32 %0, %1, %2" : "=v"(x) : "v"(a[i]), "v"(b[i])); r[i] = x; }
  return r;
}
__device__ __forceinline__ float xsum32(float x) {
  unsigned u = __builtin_bit_cast(unsigned, x), v = u;
  asm("" : "+v"(v));
  auto r = __builtin_amdgcn_permlane32_swap(u, v, false, false);
  const unsigned a = r[0], b = r[1];
  return __builtin_bit_cast(float, a) + __builtin_bit_cast(float, b);
}
__device__ __forceinline__ float xsum16(float x) {
  unsigned u = __builtin_bit_cast(unsigned, x), v = u;
  asm("" : "+v"(v));
  auto r = __builtin_amdgcn_permlane16_swap(u, v, false, false);
  const unsigned a = r[0], b = r[1];
  return __builtin_bit_cast(float, a) + __builtin_bit_cast(float, b);
}
__device__ __forceinline__ float xsum8(float x) {
  return x + __builtin_bit_cast(float, __builtin_amdgcn_update_dpp(0, __builtin_bit_cast(int, x), 0x128, 0xf, 0xf, false));
}
__device__ __forceinline__ f32x2 phi2(f32x2 t) {
  const f32x2 u = t * 1.4426950408889634f, v = t + 1e-6f;
  const f32x2 e = {__builtin_amdgcn_fmed3f(__builtin_amdgcn_exp2f(u[0]), 0.f, 1.f), __builtin_amdgcn_fmed3f(__builtin_amdgcn_exp2f(u[1]), 0.f, 1.f)};
  return f32x2{fmaxf(v[0], 1e-6f), fmaxf(v[1], 1e-6f)} + e;
}
__device__ __forceinline__ f32x2 gelu2(f32x2 x) {
  const f32x2 u = x * (x * x * -0.1029432397f + -2.302208198f);
  const f32x2 d = f32x2{__builtin_amdgcn_exp2f(u[0]), __builtin_amdgcn_exp2f(u[1])} + 1.f;
  return x * f32x2{__builtin_amdgcn_rcpf(d[0]), __builtin_amdgcn_rcpf(d[1])};
}

__global__ void __launch_bounds__(512, 2)
fused_block(const unsigned short* __restrict__ x, const unsigned short* __restrict__ wqkv, unsigned char* ws, const unsigned short* __restrict__ wo,
            const unsigned short* __restrict__ w1, const unsigned short* __restrict__ b1, const unsigned short* __restrict__ w2,
            const unsigned short* __restrict__ b2, unsigned short* __restrict__ out) {
  __shared__ __attribute__((aligned(16))) char smem[LDS_BYTES];
  unsigned long long pc0;
  asm volatile("s_getpc_b64 %0" : "=s"(pc0));
  LAS char* L = (LAS char*)smem;
  const unsigned lds0 = (unsigned)(size_t)L;
  const int tid = threadIdx.x, lane = tid & 63, w = __builtin_amdgcn_readfirstlane(tid >> 6);
  const int b = blockIdx.x, xcd = b & 7, bi = b >> 3;
  const int g = lane >> 4, t = lane & 15;
  const __amdgpu_buffer_rsrc_t rs = __builtin_amdgcn_make_buffer_rsrc((void*)ws, 0, WS_BYTES, 0x00020000);
  gu32* flags = (gu32*)(ws + WS_FLAG);
#define FLAG(set, grp) (flags + ((set) * FLAG_SET + (grp) * 256) / 4)
  asm volatile("" :: "s"(x), "s"(wqkv), "s"(ws), "s"(wo), "s"(w1), "s"(b1), "s"(w2), "s"(b2));
  const int r128 = 4 * (xcd >> 1) + (bi >> 3), c16 = 8 * (xcd & 1) + (bi & 7);
  const int r64 = 8 * (xcd >> 1) + (bi >> 2), c8 = 4 * (xcd & 1) + (bi & 3);

  const __amdgpu_buffer_rsrc_t rx = __builtin_amdgcn_make_buffer_rsrc((void*)x, 0, S * D * 2, 0x00020000);
  const int hw = w - 4, ht = tid - 256;
  volatile LAS unsigned* sig = (volatile LAS unsigned*)(L + L_CS + 1024);
  volatile LAS v4u* prog = (volatile LAS v4u*)(L + L_CS + 1088);
#define PROG(n) do { asm volatile("" ::: "memory"); sig[16 + w] = (n); } while (0)

  const __amdgpu_buffer_rsrc_t rq = __builtin_amdgcn_make_buffer_rsrc((void*)wqkv, 0, D * 3 * D * 2, 0x00020000);
  v4u tv[12];
  unsigned ep;
  const int q10 = lane / 10, c10 = lane % 10;
  if (w < 4) {
#pragma unroll
    for (int j = 0; j < 3; ++j)
      tv[j] = __builtin_amdgcn_raw_buffer_load_b128(rx, (lane & 7) * 1280 + (lane >> 3) * 16, (j < 2 || w < 2) ? 8 * b * 1280 + (w + 4 * j) * 128 : OOB, 0);
    ep = __hip_atomic_load((gu32*)(ws + WS_EPOCH), RLX_AGENT);
  } else if (w == 4) {
    const int vo = sel(lane < 40, q10 * 3840 + (c10 < 5 ? 1280 : 2480) + c10 * 16);
#pragma unroll
    for (int j = 0; j < 10; ++j) tv[j] = __builtin_amdgcn_raw_buffer_load_b128(rq, vo, (40 * r128 + 4 * j) * 3840 + c16 * 80, 0);
  } else if (w == 7) {
    *(LAS v4u*)(L + L_CS + 512 + lane * 16) = v4u{0u, 0u, 0u, 0u};
  }
  BAR();

  if (w < 4) {
  unsigned tag = ep;
  const int grp32 = 4 * r128 + w;
  constexpr int AW = 6;

  {
    const int tok = 8 * b + (lane & 7), cl = lane >> 3;
    const bool has3 = w < 2;
    int xd = ((tok >> 4) * 20 + 2 * w + (cl >> 2)) * 1024 + ((cl & 3) * 16 + (tok & 15)) * 16;
    PIN(xd);
    SCHED();
    f32x2 f[12];
#pragma unroll
    for (int j = 0; j < 3; ++j)
#pragma unroll
      for (int e = 0; e < 4; ++e) f[j * 4 + e] = bf2(tv[j][e]);
    f32x2 s2 = (f[0] + f[1]) + (f[2] + f[3]) + ((f[4] + f[5]) + (f[6] + f[7])) + ((f[8] + f[9]) + (f[10] + f[11]));
    const float mloc = xsum32(xsum16(xsum8(s2[0] + s2[1]))) * (has3 ? 1.f / 192.f : 1.f / 128.f);
    const float m3 = has3 ? mloc : 0.f;
    f32x2 q2 = {0.f, 0.f}, q3 = {0.f, 0.f};
#pragma unroll
    for (int j = 0; j < 12; j += 2) {
      const f32x2 d0 = f[j] - (j < 8 ? mloc : m3), d1 = f[j + 1] - (j < 8 ? mloc : m3);
      q2 += d0 * d0; q3 += d1 * d1;
    }
    q2 += q3;
    const float m2loc = xsum32(xsum16(xsum8(q2[0] + q2[1])));
    if (lane < 8) *(volatile LAS f32x4*)(L + L_CS + 512 + (lane * 4 + w) * 16) = f32x4{mloc, m2loc, 1.f, 1.f};
    f32x4 pr[4];
#pragma unroll 1
    for (unsigned spins = 0; spins < 64 * kSpinCap; ++spins) {
      bool ok = true;
#pragma unroll
      for (int ww = 0; ww < 4; ++ww) {
        pr[ww] = *(volatile LAS f32x4*)(L + L_CS + 512 + ((lane & 7) * 4 + ww) * 16);
        ok = ok && pr[ww][2] == 1.f;
      }
      if (__all(ok)) break;
    }
    const f32x4 p01 = {pr[0][0], pr[0][1], pr[1][0], pr[1][1]}, p23 = {pr[2][0], pr[2][1], pr[3][0], pr[3][1]};
    const float mean = (p01[0] + p01[2]) * 0.3f + (p23[0] + p23[2]) * 0.2f;
    const float e0 = p01[0] - mean, e1 = p01[2] - mean, e2 = p23[0] - mean, e3 = p23[2] - mean;
    const float m2 = (p01[1] + p01[3]) + (p23[1] + p23[3]) + 192.f * (e0 * e0 + e1 * e1) + 128.f * (e2 * e2 + e3 * e3);
    const float rstd = __builtin_amdgcn_rsqf(m2 * (1.f / D) + LN_EPS);
#pragma unroll
    for (int j = 0; j < 3; ++j) {
      v4u o;
#pragma unroll
      for (int e = 0; e < 4; ++e) o[e] = pk2((f[j * 4 + e] - mean) * rstd);
      st128(o, rs, (j < 2 || has3) ? xd : OOB, WS_XN + j * 8192);
    }
    PIN(tag); tag += 1u;
    publish(FLAG(0, b >> 2) + (b & 3) * 4 + w, tag, lane);
    if (w == 0) sig[6] = 1u;
  }

  v4u xr[3];
  {
    wait_flag(FLAG(0, grp32) + lane, 16, tag, lane);
    v4u a[20][2];
    const int abase = WS_XN + 2 * grp32 * 20 * 1024;
#pragma unroll
    for (int ks = 0; ks < AW; ++ks)
#pragma unroll
      for (int m = 0; m < 2; ++m) a[ks][m] = ld128(rs, lane * 16, abase + (m * 20 + ks) * 1024);
    SCHED();
    BAR_RAW();
    {
      f32x4 acc[2][5];
      const LBase bp(lds0 + L_BKV + (lane >> 2) * 160 + (lane & 3) * 8);
      kloop<2, 5, 20, 1, false>(acc, [&](int ks, int s, int h) { return bp.rd(ks * 5120 + h * 2560 + s * 32); },
        [&](int ks, int m) { return a[ks][m]; },
        [&](int ks) {
#pragma unroll
          for (int j = ld_hi(ks - 1, AW, AW, 20); j < ld_hi(ks, AW, AW, 20); ++j)
#pragma unroll
            for (int m = 0; m < 2; ++m) a[j][m] = ld128(rs, lane * 16, abase + (m * 20 + j) * 1024);
          if (ks == 3 || ks == 10) BAR_RAW();
          if (ks == 20 - AW) PROG(1);
        });
      v4u ka[3], vb[3];
#pragma unroll
      for (int s = 0; s < 3; ++s)
#pragma unroll
        for (int m = 0; m < 2; ++m) {
          vb[s][2 * m] = pk2(lo2(acc[m][s + 2])); vb[s][2 * m + 1] = pk2(hi2(acc[m][s + 2]));
        }
#pragma unroll
      for (int e = 0; e < 4; ++e) vb[0][e] = t < 8 ? 0x3F803F80u : vb[0][e];
      const f32x4 z4 = {0.f, 0.f, 0.f, 0.f};
      LAS f32x4* red = (LAS f32x4*)(L + L_RED) + w * 512 + lane;
#pragma unroll
      for (int s = 0; s < 3; ++s) {
#pragma unroll
        for (int m = 0; m < 2; ++m) { ka[s][2 * m] = pk2(phi2(lo2(acc[m][s]))); ka[s][2 * m + 1] = pk2(phi2(hi2(acc[m][s]))); }
#pragma unroll
        for (int sb = 0; sb < 3; ++sb)
          if (s > 0 || sb < 2) red[(3 * s + sb - (s > 0)) * 64] = mfma(ka[s], vb[sb], z4);
      }
      BAR();
    }
    f32x4 qa[2][3];
    {
      unsigned bp0 = lds0 + L_BQ + (lane >> 2) * 64 + (lane & 3) * 8 + (g & 1) * 32;
      unsigned bp1 = lds0 + L_BQ + (lane >> 2) * 64 + (lane & 3) * 8 + 32 - (g & 1) * 32;
      unsigned bp2 = lds0 + L_BQ + 2048 + (lane >> 2) * 16 + (lane & 1) * 8;
      PIN(bp0); PIN(bp1); PIN(bp2);
      kloop<2, 3, 20, 2, true>(qa,
        [&](int ks, int s, int h) {
          return s == 0 ? rdtr(lp(bp0) + ks * 2560 + h * 1024) : s == 1 ? rdtr(lp(bp1) + ks * 2560 + h * 1024) : rdtr(lp(bp2) + ks * 2560 + h * 256);
        },
        [&](int ks, int m) { return a[ks][m]; },
        [&](int ks) {});
    }
    v4u pq0[2], pq1[2];
#pragma unroll
    for (int m = 0; m < 2; ++m) {
      pq0[m].x = pk2(phi2(lo2(qa[m][0]))); pq0[m].y = pk2(phi2(hi2(qa[m][0])));
      pq0[m].z = pk2(phi2(lo2(qa[m][1]))); pq0[m].w = pk2(phi2(hi2(qa[m][1])));
      const unsigned lo = pk2(phi2(lo2(qa[m][2]))), hi = pk2(phi2(hi2(qa[m][2])));
      pq1[m].x = g < 2 ? lo : 0u; pq1[m].y = g < 2 ? hi : 0u; pq1[m].z = 0u; pq1[m].w = 0u;
      PIN(pq0[m]); PIN(pq1[m]);
    }
    BAR_RAW();
#pragma unroll
    for (int j = 0; j < 3; ++j)
      xr[j] = __builtin_amdgcn_raw_buffer_load_b128(rx, sel(j < 2 || g < 2, t * 1280 + g * 16), (4 * r64 + w) * 16 * 1280 + c8 * 160 + j * 64, 0);
    {
      v4u kv[4][2];
#pragma unroll
      for (int es = 3; es >= 0; --es)
#pragma unroll
        for (int ks = 0; ks < 2; ++ks) kv[es][ks] = *(LAS v4u*)(L + L_KVF + (es * 2 + ks) * 1024 + lane * 16);
      SCHED();
      f32x4 o[2][4];
#pragma unroll
      for (int es = 3; es >= 0; --es)
#pragma unroll
        for (int m = 0; m < 2; ++m) o[m][es] = mfma(kv[es][0], pq0[m], f32x4{0.f, 0.f, 0.f, 0.f});
#pragma unroll
      for (int es = 3; es >= 0; --es)
#pragma unroll
        for (int m = 0; m < 2; ++m) o[m][es] = mfma(kv[es][1], pq1[m], o[m][es]);
#pragma unroll
      for (int m = 0; m < 2; ++m) {
        const float i0 = __builtin_amdgcn_rcpf(o[m][3][0] + 1e-6f), i1 = __builtin_amdgcn_rcpf(o[m][3][1] + 1e-6f);
        const float im = g == 0 ? i0 : i1;
        v4u ov;
        ov.x = pk2(lo2(o[m][0]) * i0); ov.y = pk2(hi2(o[m][0]) * i0);
        ov.z = pk2(lo2(o[m][1]) * im); ov.w = pk2(hi2(o[m][1]) * im);
        v2u ol; ol.x = pk2(lo2(o[m][2]) * i1); ol.y = pk2(hi2(o[m][2]) * i1);
        const int tt = 2 * grp32 + m;
        st128(ov, rs, lane * 16, WS_O + (tt * 20 + c16) * 1024);
        st64(ol, rs, g < 2 ? lane * 8 : OOB, WS_O + (tt * 20 + 16) * 1024 + c16 * 256);
      }
      publish(FLAG(2, grp32) + c16, tag, lane);
    }
  }

  {
    const int tt = 4 * r64 + w;
    v4u eye[2];
#pragma unroll
    for (int hf = 0; hf < 2; ++hf) {
      const int e = 16 * hf + t - 8 * g;
#pragma unroll
      for (int d = 0; d < 4; ++d) eye[hf][d] = e == 2 * d ? 0x3F80u : e == 2 * d + 1 ? 0x3F800000u : 0u;
      PIN(eye[hf]);
    }
    f32x4 acc[1][5];
#pragma unroll
    for (int s = 0; s < 5; ++s) { acc[0][s] = mfma(eye[s & 1], xr[s >> 1], f32x4{0.f, 0.f, 0.f, 0.f}); PIN(acc[0][s]); }
    wait_flag(FLAG(2, tt >> 1) + lane, 16, tag, lane);
    if (b == 0 && tid == 0) __hip_atomic_store((gu32*)(ws + WS_EPOCH), tag, RLX_AGENT);
    v4u a[20];
#pragma unroll
    for (int ks = 0; ks < 2 * AW; ++ks) a[ks] = ld128(rs, lane * 16, WS_O + (tt * 20 + ks) * 1024);
    SCHED();
    const LBase bp(lds0 + L_WO + (lane >> 2) * 160 + (lane & 3) * 8);
    kloop<1, 5, 20, 1, true, 1>(acc, [&](int ks, int s, int h) { return bp.rd(ks * 5120 + h * 2560 + s * 32); },
      [&](int ks, int m) { return a[ks]; },
      [&](int ks) {
#pragma unroll
        for (int j = ld_hi(ks - 1, 2 * AW, 2 * AW, 20); j < ld_hi(ks, 2 * AW, 2 * AW, 20); ++j)
          a[j] = j < 16 ? ld128(rs, lane * 16, WS_O + (tt * 20 + j) * 1024) : ld_split(rs, lane, WS_O + (tt * 20 + j) * 1024);
        if (ks == 8) PROG(2);
        if (ks == 14) PROG(3);
      });
    PROG(4);
    v2u hb[5];
    f32x2 hv[10];
#pragma unroll
    for (int s = 0; s < 5; ++s) {
      hb[s].x = pk2(lo2(acc[0][s]));
      hb[s].y = pk2(hi2(acc[0][s]));
      hv[2 * s] = bf2(hb[s].x); hv[2 * s + 1] = bf2(hb[s].y);
    }
    st128(v4u{hb[0].x, hb[0].y, hb[1].x, hb[1].y}, rs, lane * 16, WS_H + (tt * 20 + 2 * c8) * 1024);
    st128(v4u{hb[2].x, hb[2].y, hb[3].x, hb[3].y}, rs, lane * 16, WS_H + (tt * 20 + 2 * c8 + 1) * 1024);
    st64(hb[4], rs, lane * 8, WS_H + (tt * 20 + 16) * 1024 + c8 * 512);
    const f32x2 s2 = ((hv[0] + hv[1]) + (hv[2] + hv[3])) + ((hv[4] + hv[5]) + (hv[6] + hv[7])) + (hv[8] + hv[9]);
    const float mc = xsum32(xsum16(s2[0] + s2[1])) * (1.f / 80.f);
    f32x2 q2 = {0.f, 0.f}, q3 = {0.f, 0.f};
#pragma unroll
    for (int j = 0; j < 10; j += 2) { const f32x2 d0 = hv[j] - mc, d1 = hv[j + 1] - mc; q2 += d0 * d0; q3 += d1 * d1; }
    q2 += q3;
    const float m2 = xsum32(xsum16(q2[0] + q2[1]));
    st64(v2u{__builtin_bit_cast(unsigned, mc), __builtin_bit_cast(unsigned, m2)}, rs, g == 0 ? t * 8 : OOB, WS_ST + (tt * 8 + c8) * 128);
    publish(FLAG(3, tt >> 1) + (tt & 1) * 8 + c8, tag, lane);
  }

  {
    wait_flag(FLAG(3, grp32) + lane, 16, tag, lane);
    v4u stt[2];
#pragma unroll
    for (int m = 0; m < 2; ++m) {
      const v2u s0 = ld64(rs, g * 256 + t * 8, WS_ST + (2 * grp32 + m) * 1024), s1 = ld64(rs, g * 256 + 128 + t * 8, WS_ST + (2 * grp32 + m) * 1024);
      stt[m] = v4u{s0.x, s0.y, s1.x, s1.y};
    }
    v4u a[20][2];
    const int abase = WS_H + 2 * grp32 * 20 * 1024;
#pragma unroll
    for (int ks = 0; ks < AW; ++ks)
#pragma unroll
      for (int m = 0; m < 2; ++m) a[ks][m] = ld128(rs, lane * 16, abase + (m * 20 + ks) * 1024);
    SCHED();
    float nm[2], rr[2];
#pragma unroll
    for (int m = 0; m < 2; ++m) {
      const f32x4 sv = __builtin_bit_cast(f32x4, stt[m]);
      const float mean = xsum32(xsum16(sv[0] + sv[2])) * 0.125f;
      const float m2 = xsum32(xsum16(sv[1] + sv[3] + 80.f * ((sv[0] - mean) * (sv[0] - mean) + (sv[2] - mean) * (sv[2] - mean))));
      const float var = m2 * (1.f / D);
      const float r1 = __builtin_amdgcn_rsqf(var + LN_EPS);
      rr[m] = r1 * __builtin_amdgcn_rsqf(var * r1 * r1 + LN_EPS);
      nm[m] = -mean * rr[m];
      PIN(rr[m]); PIN(nm[m]);
    }
    BAR_RAW();
    const unsigned l64 = lds0 + (lane >> 2) * 64 + (lane & 3) * 8;
    const LBase bp0(l64 + (g & 1) * 32), bp1(l64 + 32 - (g & 1) * 32);
    const int h1base = WS_H1 + 2 * grp32 * 48 * 1024;
    auto finish = [&](const f32x4& c0, const f32x4& c1, int j, int m) {
      v4u o;
#pragma unroll
      for (int s = 0; s < 2; ++s) {
        const f32x4 cs = *(LAS f32x4*)(L + L_CS + ((2 * j + s) * 4 + g) * 16), bb = *(LAS f32x4*)(L + L_B1 + ((2 * j + s) * 4 + g) * 16);
        const f32x4 c = s ? c1 : c0;
        o[2 * s] = pk2(gelu2(lo2(c) * rr[m] + (lo2(cs) * nm[m] + lo2(bb))));
        o[2 * s + 1] = pk2(gelu2(hi2(c) * rr[m] + (hi2(cs) * nm[m] + hi2(bb))));
      }
      st128(o, rs, lane * 16, h1base + (m * 48 + (c16 >> 3) * 24 + (j < 2 ? 2 * (c16 & 7) + j : 16 + (c16 & 7))) * 1024);
    };
    {
      f32x4 acc[2][4];
      kloop<2, 4, 20, 1, true>(acc, [&](int ks, int s, int h) { return ((s & 1) ? bp1 : bp0).rd(L_W1 + (s >> 1) * 40960 + ks * 2048 + h * 1024); },
        [&](int ks, int m) { return a[ks][m]; },
        [&](int ks) {
#pragma unroll
          for (int j = ld_hi(ks - 1, AW, AW, 20); j < ld_hi(ks, AW, AW, 20); ++j)
#pragma unroll
            for (int m = 0; m < 2; ++m)
              a[j][m] = j < 16 ? ld128(rs, lane * 16, abase + (m * 20 + j) * 1024) : ld_split(rs, lane, abase + (m * 20 + j) * 1024);
          if (ks == 20 - AW) PROG(5);
        });
      PROG(6);
#pragma unroll
      for (int j = 0; j < 2; ++j)
#pragma unroll
        for (int m = 0; m < 2; ++m) finish(acc[m][2 * j], acc[m][2 * j + 1], j, m);
      SCHED();
    }
    f32x4 accb[2][2];
    const int kh = w & 1, tt = 4 * r64 + w;
    v4u hr01, hr23;
    v2u hr4;
    kloop<2, 2, 20, 3, true>(accb, [&](int ks, int s, int h) { return (s ? bp1 : bp0).rd(L_W1 + 81920 + ks * 2048 + h * 1024); },
      [&](int ks, int m) { return a[ks][m]; },
      [&](int ks) {
        if (ks == 8) { publish(FLAG(4, grp32) + c16, tag, lane); PROG(7); }
        if (ks == 10) {
          hr01 = ld128(rs, lane * 16, WS_H + (tt * 20 + 2 * c8) * 1024);
          hr23 = ld128(rs, lane * 16, WS_H + (tt * 20 + 2 * c8 + 1) * 1024);
          hr4 = ld64(rs, lane * 8, WS_H + (tt * 20 + 16) * 1024 + c8 * 512);
        }
      });
    f32x4 acc[2][5];
    {
      const unsigned hrr[10] = {hr01.x, hr01.y, hr01.z, hr01.w, hr23.x, hr23.y, hr23.z, hr23.w, hr4.x, hr4.y};
#pragma unroll
      for (int s = 0; s < 5; ++s) {
        acc[0][s] = *(LAS f32x4*)(L + L_B2 + (s * 4 + g) * 16) + f32x4{bf_lo(hrr[2 * s]), bf_hi(hrr[2 * s]), bf_lo(hrr[2 * s + 1]), bf_hi(hrr[2 * s + 1])};
        PIN(acc[0][s]);
      }
    }
    int oo[3];
    unsigned tw, tr;
    {
      int ln = lane;
      PIN(ln);
      const int qq = ln / 10, cc = ln % 10;
#pragma unroll
      for (int j = 0; j < 3; ++j) { oo[j] = sel(ln < (j < 2 ? 60 : 40), ((tt * 16 + 6 * j + qq) * D + 80 * c8) * 2 + cc * 16); PIN(oo[j]); }
      tw = lds0 + L_W2 + 122880 + w * 3072 + t * 176 + g * 8; tr = lds0 + L_W2 + 122880 + w * 3072 + qq * 176 + cc * 16;
      PIN(tw); PIN(tr);
    }
    BAR_RAW();
    constexpr int WIN = 8;
    v4u ar[WIN][2];
    const int a4[2] = {WS_H1 + (tt * 48 + kh * 24) * 1024, WS_H1 + ((tt ^ 1) * 48 + kh * 24) * 1024};
#pragma unroll
    for (int ks = 0; ks < WIN - 1; ++ks)
#pragma unroll
      for (int m = 0; m < 2; ++m) ar[ks][m] = ld128(rs, lane * 16, a4[m] + ks * 1024);
    SCHED();
#pragma unroll
    for (int m = 0; m < 2; ++m) finish(accb[m][0], accb[m][1], 2, m);
    const LBase bp(lds0 + L_W2 + kh * 5120 + (lane >> 2) * 160 + (lane & 3) * 8);
    kloop<2, 5, 24, 1, true, 1>(acc, [&](int ks, int s, int h) { return bp.rd((ks % 15) * 10240 + h * 2560 + s * 32); },
      [&](int ks, int m) { return ar[ks % WIN][m]; },
      [&](int ks) {
        if (ks == 5) PROG(8);
        if (ks == 9 || ks == 14 || ks == 19) BAR_RAW();
        if (ks == 3) publish(FLAG(10, grp32) + c16, tag, lane);
        if (ks >= 3) {
#pragma unroll
          for (int j = ks == 3 ? WIN - 1 : ld_hi(ks - 1, 1, WIN - 1, 24); j < ld_hi(ks, 1, WIN - 1, 24); ++j)
#pragma unroll
            for (int m = 0; m < 2; ++m) ar[j % WIN][m] = ld128(rs, lane * 16, a4[m] + j * 1024);
        }
      });
    LAS f32x4* xch = (LAS f32x4*)(L + L_W2 + 102400);
#pragma unroll
    for (int s = 0; s < 5; ++s) xch[((w ^ 1) * 5 + s) * 64 + lane] = acc[1][s];
    BAR();
    const __amdgpu_buffer_rsrc_t rout = __builtin_amdgcn_make_buffer_rsrc((void*)out, 0, S * D * 2, 0x00020000);
    f32x4 ot[5];
#pragma unroll
    for (int s = 0; s < 5; ++s) ot[s] = xch[(w * 5 + s) * 64 + lane];
    SCHED();
#pragma unroll
    for (int s = 0; s < 5; ++s) {
      const f32x4 v = acc[0][s] + ot[s];
      *(LAS v2u*)(lp(tw) + s * 32) = v2u{pk2(lo2(v)), pk2(hi2(v))};
    }
    asm volatile("" ::: "memory");
    v4u ov[3];
#pragma unroll
    for (int j = 0; j < 3; ++j) ov[j] = *(LAS v4u*)(lp(tr) + j * 6 * 176);
#pragma unroll
    for (int j = 0; j < 3; ++j) __builtin_amdgcn_raw_buffer_store_b128(ov[j], rout, oo[j], 0, 0);
  }
  return;
  }

  {
    const v4u srd_ws = mk_srd(ws, WS_BYTES);
    const int q5 = lane / 5, c5 = lane % 5;
    const int f40m = c5 < 4 ? 512 : 128, f40l = c5 < 4 ? q5 * 64 + (c5 ^ (2 * ((q5 >> 2) & 1))) * 16 : 2048 + q5 * 16;
    v2u ub1, ub2;
    unsigned pf0;
    v4u tq[10];
    if (hw == 1) {
      const __amdgpu_buffer_rsrc_t ro = __builtin_amdgcn_make_buffer_rsrc((void*)wo, 0, D * D * 2, 0x00020000);
      const int vq = sel(lane < 40, (8 * (q5 >> 2) + (q5 & 3)) * 3840 + c5 * 16), vo = sel(lane < 40, q10 * 1280 + c10 * 16);
#pragma unroll
      for (int j = 0; j < 5; ++j) {
        const int i8 = 5 * r128 + j, k = 32 * (i8 >> 2) + 16 * (i8 & 1) + 4 * ((i8 >> 1) & 1);
        tq[j] = __builtin_amdgcn_raw_buffer_load_b128(rq, vq, k * 3840 + c16 * 80, 0);
      }
#pragma unroll
      for (int j = 0; j < 5; ++j) tq[5 + j] = __builtin_amdgcn_raw_buffer_load_b128(ro, vo, (20 * r64 + 4 * j) * 1280 + c8 * 160, 0);
    } else if (hw == 2) {
      const GAS unsigned char* cb = (const GAS unsigned char*)(pc0 & ~127ull);
      pf0 = *(const GAS unsigned*)(cb + min(lane * 32 + bi, 346) * 128);
      if (lane < 6) {
        gu32* fp = FLAG(3 * (b >> 6) + (lane >> 1), b & 63) + (lane & 1) * 32;
        asm volatile("global_atomic_add %0, %1, off sc1" :: "v"(fp), "v"(0u) : "memory");
      }
    } else if (hw == 3) {
      const __amdgpu_buffer_rsrc_t rb1 = __builtin_amdgcn_make_buffer_rsrc((void*)b1, 0, F * 2, 0x00020000);
      const __amdgpu_buffer_rsrc_t rb2 = __builtin_amdgcn_make_buffer_rsrc((void*)b2, 0, D * 2, 0x00020000);
      ub1 = __builtin_amdgcn_raw_buffer_load_b64(rb1, lane * 8, c16 * 192, 0);
      ub2 = __builtin_amdgcn_raw_buffer_load_b64(rb2, lane * 8, c8 * 160, 0);
    }
    unsigned tag = __hip_atomic_load((gu32*)(ws + WS_EPOCH), RLX_AGENT);
    if (hw == 0) {
      const int dof = sel(lane < 40, lane * 16);
#pragma unroll
      for (int j = 0; j < 10; ++j) {
        const int k = 40 * r128 + 4 * j, R = (k & ~31) + 16 * ((k >> 2) & 1) + 4 * ((k >> 3) & 3);
        st128(tv[j], rs, dof, WS_TKV + c16 * 153600 + R * 160);
      }
      PIN(tag); tag += 1u;
      publish(FLAG(6, c16) + r128, tag, lane);
    }
    asm volatile("" ::: "memory");
    int red_off, kvf_off, kvp_off;
    auto items = [&]() {
      const int it = ht < 210 ? ht : 209, ie = it / 5, ic = it % 5;
      const int ihead = ie < 40 ? (ie >= 20) : ie - 40;
      const int id = 20 * ihead + 4 * ic, dg = id >> 2;
      const int sa = id >> 4, col = 40 + ie;
      const int tile = 3 * sa + (ie < 40 ? (col >> 4) - 2 : 0) - (sa > 0);
      red_off = lds0 + L_RED + (tile * 64 + (dg & 3) * 16 + (ie < 40 ? (col & 15) : 0)) * 16;
      kvf_off = lds0 + L_KVF + ((ie < 40 ? (ie >> 4) : 3) * 2 + (dg >> 3)) * 1024 + ((dg & 3) * 16 + (ie < 40 ? (ie & 15) : ihead)) * 16 + ((dg >> 2) & 1) * 8;
      kvp_off = WS_KVP + (c16 * 16 + r128) * 4096 + ht * 16;
      PIN(red_off); PIN(kvf_off); PIN(kvp_off);
    };
    if (hw != 0) items();
    int vo12, td[12];
    if (hw >= 2) {
      const int q12 = lane / 12, c12 = lane % 12;
      vo12 = hw == 2 ? sel(lane < 48, q12 * 3072 + c12 * 16) : sel(lane < 40, q10 * 1280 + c10 * 16);
#pragma unroll
      for (int j = 0; j < 12; ++j) {
        const int cc = r128 >> 1, off = 40 * (r128 & 1) + 4 * j;
        const int R = off < 64 ? 64 * cc + off : 32 * (16 + (cc >> 1)) + 16 * (cc & 1) + off - 64;
        const int d1 = WS_T1 + c16 * 122880 + (c12 >> 2) * 40960 + (R + q12) * 64 + ((c12 & 3) ^ (2 * ((R >> 2) & 1))) * 16;
        const int pc = r64 >> 1, ko = 48 * (r64 & 1) + 4 * j, jj = ko >> 5;
        const int d2 = WS_T2 + c8 * 245760 + (2 * (jj < 2 ? 2 * (pc & 7) + jj : 16 + (pc & 7)) + (pc >> 3)) * 5120 + (ko & 31) * 160 + lane * 16;
        td[j] = sel(hw == 2 ? (lane < 48 && j < 10) : lane < 40, hw == 2 ? d1 : d2);
        PIN(td[j]);
      }
      PIN(vo12);
    }
    int l16 = lane * 16, h16 = sel(ht < 210, ht * 16);
    PIN(l16); PIN(h16);
    gu32* fp1 = FLAG(1, c16) + lane;
    gu32* fp78 = lane < 32 ? FLAG(7, c8) + lane : FLAG(8, c16) + t;
    gu32* fp4 = FLAG(4, 2 * r64 + (g & 1)) + t;
    gu32* fp1w = FLAG(1, c16) + r128 * 4 + hw;
    PIN(fp1); PIN(fp78); PIN(fp4); PIN(fp1w);
    ((LAS v4u*)(L + L_KVF))[ht] = v4u{0u, 0u, 0u, 0u};
    ((LAS v4u*)(L + L_KVF))[256 + ht] = v4u{0u, 0u, 0u, 0u};
    asm volatile("" ::: "memory");
    if (hw != 0) {
      if (hw == 1) {
        sig_wait(sig + 6);
#pragma unroll
        for (int j = 0; j < 5; ++j) {
          const int i8 = 5 * r128 + j;
          st128(tq[j], rs, sel(lane < 40, (i8 & 3) * f40m + f40l), WS_TKV + c16 * 153600 + 102400 + (i8 >> 2) * 2560);
        }
#pragma unroll
        for (int j = 0; j < 5; ++j) {
          const int pp = r64 >> 1, e = 20 * (r64 & 1) + 4 * j;
          const int R = e < 32 ? 32 * pp + e : 32 * (16 + (pp >> 2)) + 8 * (pp & 3) + e - 32;
          st128(tq[5 + j], rs, sel(lane < 40, lane * 16), WS_TO + c8 * 102400 + R * 160);
        }
      }
      if (hw == 3) {
        if (lane < 24) *(LAS f32x4*)(L + L_B1 + lane * 16) = f32x4{bf_lo(ub1.x), bf_hi(ub1.x), bf_lo(ub1.y), bf_hi(ub1.y)};
        if (lane < 20) *(LAS f32x4*)(L + L_B2 + lane * 16) = f32x4{bf_lo(ub2.x), bf_hi(ub2.x), bf_lo(ub2.y), bf_hi(ub2.y)};
      }
      if (hw == 2) asm volatile("" :: "v"(pf0));
      PIN(tag); tag += 1u;
      DRAIN();
      if (hw == 1 && lane < 2) flag_set(lane == 0 ? FLAG(8, c16) + r128 : FLAG(7, c8) + r64, tag);
    }
    if (hw == 0) {
      wait_flag(FLAG(6, c16) + lane, 16, tag, lane);
      sig[0] = 1u;
    }
    sig_wait(sig);
    const int kvq = WS_TKV + c16 * 153600, wos = WS_TO + c8 * 102400;
    dma_image<0, 24>(lds0 + L_BKV, l16, srd_ws, kvq, hw, 3 * (bi >> 3));
    dma_image<24, 56>(lds0 + L_BKV, l16, srd_ws, kvq, hw, 4 * (bi >> 3));
    asm volatile("s_waitcnt vmcnt(8)" ::: "memory");
    BAR_RAW();
    dma_image<56, 100>(lds0 + L_BKV, l16, srd_ws, kvq, hw, 5 * (bi >> 3));
    asm volatile("s_waitcnt vmcnt(10)" ::: "memory");
    BAR_RAW();
    if (hw == 1) wait_flag(fp78, 48, tag, lane);
    DRAIN();
    BAR_RAW();
    dma_image<100, 150>(lds0 + L_BKV, l16, srd_ws, kvq, hw, 6 * (bi >> 3));
    if (hw == 0) items();
    prog_wait(prog, 1);
    if (hw >= 2) {
      const __amdgpu_buffer_rsrc_t r12 = __builtin_amdgcn_make_buffer_rsrc((void*)(hw == 2 ? w1 : w2), 0, D * F * 2, 0x00020000);
      const int base = hw == 2 ? r128 * 122880 + c16 * 192 : r64 * 61440 + c8 * 160, step = hw == 2 ? 4 * 3072 : 4 * 1280;
#pragma unroll
      for (int j = 0; j < 12; ++j) tv[j] = __builtin_amdgcn_raw_buffer_load_b128(r12, vo12, (j < 10 || hw == 3) ? base + j * step : OOB, 0);
      asm volatile("s_waitcnt vmcnt(12)" ::: "memory");
    } else {
      dma_image<32, 68, 2>(lds0 + L_WO, l16, srd_ws, wos, hw, bi >> 2);
      asm volatile("s_waitcnt vmcnt(18)" ::: "memory");
    }
    BAR();
    __builtin_amdgcn_s_setprio(2);
    {
      f32x4 pv[4];
#pragma unroll
      for (int ww = 0; ww < 4; ++ww) pv[ww] = *(LAS f32x4*)(lp(red_off) + ww * 8192);
      st128(__builtin_bit_cast(v4u, add4(add4(pv[0], pv[1]), add4(pv[2], pv[3]))), rs, kvp_off, 0);
      PROG(1);
      DRAIN();
#pragma unroll
      for (int j = 0; j < 12; ++j) PIN(tv[j]);
    }
    if (lane == 0) flag_set(fp1w, tag);
    if (hw == 0) {
      __builtin_amdgcn_s_sleep(8);
      wait_flag(fp1, 64, tag, lane);
      sig[2] = 1u;
    } else {
      __builtin_amdgcn_s_sleep(2);
      prog_wait(prog + 1, 1);
#pragma unroll
      for (int i = 0; i < 12; ++i) {
        if (hw >= 2) st128(tv[i], rs, td[i], 0);
        const int j = hw - 1 + 3 * i;
        if (j < 32) {
          const int pc = 2 * ((j + 4 * (bi >> 2)) & 31), po = (pc < 32 ? pc : pc + 36) * 1024;
          dma2(lds0 + L_WO + po, l16, srd_ws, wos + po);
        }
      }
      sig_wait(sig + 2);
    }
    {
      v4u pv[16];
#pragma unroll
      for (int r = 0; r < 16; ++r) pv[r] = ld128(rs, h16, WS_KVP + (c16 * 16 + r) * 4096);
      SCHED();
      f32x4 v = __builtin_bit_cast(f32x4, pv[0]);
#pragma unroll
      for (int r = 1; r < 16; ++r) v = add4(v, __builtin_bit_cast(f32x4, pv[r]));
      v2u o; o.x = pk2(lo2(v)); o.y = pk2(hi2(v));
      if (ht < 210) *(LAS v2u*)(lp(kvf_off)) = o;
      if (ht >= 200 && ht < 210) {
#pragma unroll
        for (int c = 1; c < 4; ++c) *(LAS v2u*)(lp(kvf_off) + c * 64) = o;
      }
    }
    DRAIN();
    BAR();
    __builtin_amdgcn_s_setprio(0);
    if (hw >= 2 && lane == 0) flag_set(hw == 2 ? FLAG(5, c16) + r128 : FLAG(11, c8) + r64, tag);
    if (hw == 2) {
      f32x4 s0 = {0.f, 0.f, 0.f, 0.f}, s1 = {0.f, 0.f, 0.f, 0.f};
#pragma unroll
      for (int j = 0; j < 10; ++j) {
        s0 = add4(s0, f32x4{bf_lo(tv[j].x), bf_hi(tv[j].x), bf_lo(tv[j].y), bf_hi(tv[j].y)});
        s1 = add4(s1, f32x4{bf_lo(tv[j].z), bf_hi(tv[j].z), bf_lo(tv[j].w), bf_hi(tv[j].w)});
      }
#pragma unroll
      for (int d = 24; d >= 12; d -= 12) {
        f32x4 u0, u1;
#pragma unroll
        for (int e = 0; e < 4; ++e) { u0[e] = __shfl(s0[e], lane + d); u1[e] = __shfl(s1[e], lane + d); }
        s0 = add4(s0, u0); s1 = add4(s1, u1);
      }
      f32x4 sv;
#pragma unroll
      for (int e = 0; e < 4; ++e) { const float a0 = __shfl(s0[e], lane >> 1), a1 = __shfl(s1[e], lane >> 1); sv[e] = (lane & 1) ? a1 : a0; }
      st128(__builtin_bit_cast(v4u, sv), rs, sel(lane < 24, lane * 16), WS_CS + (c16 * 16 + r128) * 384);
      publish(FLAG(9, c16) + r128, tag, lane);
    }
    if (hw == 1) {
      __builtin_amdgcn_s_sleep(40);
      wait_flag(lane < 32 ? FLAG(11, c8) + lane : FLAG(5, c16) + t, 48, tag, lane);
      sig[3] = 1u;
    }
    sig_wait(sig + 3);
    prog_wait(prog, 2);
    if (hw == 0) {
      wait_flag(FLAG(9, c16) + lane, 16, tag, lane);
      v4u pv[16];
      const int co = sel(lane < 24, l16);
#pragma unroll
      for (int r = 0; r < 16; ++r) pv[r] = ld128(rs, co, WS_CS + (c16 * 16 + r) * 384);
      SCHED();
      f32x4 v = __builtin_bit_cast(f32x4, pv[0]);
#pragma unroll
      for (int r = 1; r < 16; ++r) v = add4(v, __builtin_bit_cast(f32x4, pv[r]));
      if (lane < 24) *(LAS f32x4*)(L + L_CS + lane * 16) = v;
    } else {
      dma_image<70, 120, 3>(lds0 + L_W1, l16, srd_ws, WS_T1 + c16 * 122880, hw - 1, 6 * (bi >> 3));
      prog_wait(prog, 3);
      dma_image<0, 40, 3>(lds0 + L_W1, l16, srd_ws, WS_T1 + c16 * 122880, hw - 1, 5 * (bi >> 3));
      prog_wait(prog, 4);
      dma_image<40, 70, 3>(lds0 + L_W1, l16, srd_ws, WS_T1 + c16 * 122880, hw - 1, 3 * (bi >> 3));
    }
    DRAIN();
    BAR();
    const int w2s = WS_T2 + c8 * 245760;
    prog_wait(prog, 5);
    dma_image<0, 30>(lds0 + L_W2, l16, srd_ws, w2s, hw, bi >> 2);
    prog_wait(prog, 6);
    dma_image<30, 70>(lds0 + L_W2, l16, srd_ws, w2s, hw, bi >> 2);
    prog_wait(prog, 7);
    if (hw != 0) dma_image<70, 110, 3>(lds0 + L_W2, l16, srd_ws, w2s, hw - 1, bi >> 2);
    DRAIN();
    if (hw == 0) wait_flag(fp4, 32, tag, lane);
    BAR_RAW();
    dma_image<110, 150>(lds0 + L_W2, l16, srd_ws, w2s, hw, 2 * (bi >> 2));
    prog_wait(prog, 8);
    if (hw == 0) wait_flag(fp4 + 6 * FLAG_SET / 4, 32, tag, lane);
    else dma_image<150, 198, 3>(lds0 + L_W2 - 153600, l16, srd_ws, w2s, hw - 1, 3 * (bi >> 2));
    asm volatile("s_waitcnt vmcnt(16)" ::: "memory");
    BAR_RAW();
    dma_image<198, 200>(lds0 + L_W2 - 153600, l16, srd_ws, w2s, hw, 0);
    dma_image<200, 240>(lds0 + L_W2 - 153600, l16, srd_ws, w2s, hw, 2 * (bi >> 2));
    asm volatile("s_waitcnt vmcnt(10)" ::: "memory");
    BAR_RAW();
    DRAIN();
    BAR_RAW();
  }

}
}

extern "C" void kernel_launch(void* const* d_in, const int* in_sizes, int n_in, void* d_out, int out_size, void* d_ws,
                              size_t ws_size, hipStream_t stream) {
  const unsigned short* x = (const unsigned short*)d_in[0];
  const unsigned short* wqkv = (const unsigned short*)d_in[1];
  const unsigned short* wo = (const unsigned short*)d_in[2];
  const unsigned short* w1 = (const unsigned short*)d_in[4];
  const unsigned short* b1 = (const unsigned short*)d_in[5];
  const unsigned short* w2 = (const unsigned short*)d_in[6];
  const unsigned short* b2 = (const unsigned short*)d_in[7];
  fused_block<<<256, 512, 0, stream>>>(x, wqkv, (unsigned char*)d_ws, wo, w1, b1, w2, b2, (unsigned short*)d_out);
}
```

```cpp
#include <hip/hip_runtime.h>
#include <hip/hip_bf16.h>

#define STAMPS 0
#define THR 0
#define TOUCH 0
#define CODEPF 1
#define FLAGPF 1
#define OUTAUX 0
#define OUTPF 0
#define S0 6
#define XRPOS 0
#define QPOS 0
#define H1W 8
#define H1G 2
#define W2S 70
#define F6W 0
#define STW 6
#define PUBA 8
#define NCDELAY 0
#define HPRIO 0
#define WOLATE 0
#define SLEEP 1

#define GAS __attribute__((address_space(1)))
#define LAS __attribute__((address_space(3)))
#define RLX_AGENT __ATOMIC_RELAXED, __HIP_MEMORY_SCOPE_AGENT
typedef unsigned v4u __attribute__((ext_vector_type(4)));
typedef unsigned v2u __attribute__((ext_vector_type(2)));
typedef float f32x4 __attribute__((ext_vector_type(4)));
typedef float f32x2 __attribute__((ext_vector_type(2)));
typedef __bf16 bf16x8 __attribute__((ext_vector_type(8)));
typedef __bf16 bf16x2 __attribute__((ext_vector_type(2)));
typedef short s16x4 __attribute__((ext_vector_type(4)));
typedef GAS unsigned gu32;

namespace {
constexpr int S = 2048, D = 640, F = 1536;
constexpr float LN_EPS = 1e-5f;
constexpr int AUX_SC1 = 16;
constexpr unsigned kSpinCap = 1u << 17;
constexpr int OOB = 0x7FFFFF00;

constexpr int WS_EPOCH = 0;
constexpr int WS_FLAG = 4096;
constexpr int FLAG_SET = 64 * 256;
constexpr int WS_XN = 1 << 20;
constexpr int WS_O = WS_XN + 128 * 20 * 1024;
constexpr int WS_H = WS_O + 128 * 20 * 1024;
constexpr int WS_H1 = WS_H + 128 * 20 * 1024;
constexpr int WS_KVP = WS_H1 + 128 * 48 * 1024;
constexpr int WS_ST = WS_KVP + 16 * 16 * 4096;
constexpr int WS_DBG = WS_ST + 2048 * 64;
constexpr int WS_TKV = WS_DBG + 256 * 8 * 32 * 8;
constexpr int WS_TO = WS_TKV + 16 * 153600;
constexpr int WS_T1 = WS_TO + 8 * 102400;
constexpr int WS_T2 = WS_T1 + 16 * 122880;
constexpr int WS_CS = WS_T2 + 16 * 122880;
constexpr int WS_BYTES = WS_CS + 16 * 16 * 384;

constexpr int L_BKV = 0, L_BQ = 102400;
constexpr int L_RED = 0, L_KVF = 153600;
constexpr int L_WO = 0, L_W1 = 30720, L_W2 = 0, L_CS = 161792;
constexpr int L_B1 = L_CS + 1152, L_B2 = L_CS + 1536;
constexpr int LDS_BYTES = 163840;

__device__ __forceinline__ f32x4 mfma(v4u a, v4u b, f32x4 c) {
  return __builtin_amdgcn_mfma_f32_16x16x32_bf16(__builtin_bit_cast(bf16x8, a), __builtin_bit_cast(bf16x8, b), c, 0, 0, 0);
}
__device__ __forceinline__ unsigned pk2(f32x2 v) { return __builtin_bit_cast(unsigned, __builtin_convertvector(v, bf16x2)); }
__device__ __forceinline__ unsigned pk2(float lo, float hi) { return pk2(f32x2{lo, hi}); }
__device__ __forceinline__ float bf_lo(unsigned u) { return __builtin_bit_cast(float, u << 16); }
__device__ __forceinline__ float bf_hi(unsigned u) { return __builtin_bit_cast(float, u & 0xFFFF0000u); }
__device__ __forceinline__ f32x2 bf2(unsigned u) { return f32x2{bf_lo(u), bf_hi(u)}; }
__device__ __forceinline__ f32x2 lo2(f32x4 v) { return f32x2{v[0], v[1]}; }
__device__ __forceinline__ f32x2 hi2(f32x4 v) { return f32x2{v[2], v[3]}; }
__device__ __forceinline__ v4u mk_srd(const void* p, unsigned bytes) {
  const unsigned long long a = (unsigned long long)p;
  v4u r;
  r.x = (unsigned)a; r.y = (unsigned)(a >> 32) & 0xFFFFu; r.z = bytes; r.w = 0x00020000u;
  return r;
}
#define DRAIN() asm volatile("s_waitcnt vmcnt(0)" ::: "memory")
#define BAR() asm volatile("s_waitcnt lgkmcnt(0)\n\ts_barrier" ::: "memory")
#define BAR_RAW() asm volatile("s_barrier" ::: "memory")
#define SCHED() __builtin_amdgcn_sched_barrier(0)
#define PIN(x) asm volatile("" : "+v"(x))

__device__ __forceinline__ void dma2(unsigned lds_addr, unsigned voff, v4u srd, unsigned soff) {
  unsigned keep;
  asm volatile("s_mov_b32 %0, m0\n\ts_mov_b32 m0, %1\n\ts_nop 0\n\t"
               "buffer_load_dwordx4 %2, %3, %4 offen sc1 lds\n\t"
               "buffer_load_dwordx4 %2, %3, %4 offen offset:1024 sc1 lds\n\t"
               "s_mov_b32 m0, %0"
               : "=&s"(keep) : "s"(lds_addr), "v"(voff), "s"(srd), "s"(soff) : "memory");
}
template <int LO, int HI, int TH = 0, int NW = 4>
__device__ __forceinline__ void dma_image(unsigned lds_base, unsigned voff, v4u srd, int ws_off, int hw, int rot) {
  constexpr int NP = (HI - LO) / 2;
#pragma unroll 1
  for (int i = hw; i < NP; i += NW) {
    int j = i + rot;
    if (j >= NP) j -= NP;
    dma2(lds_base + LO * 1024 + j * 2048, voff, srd, ws_off + LO * 1024 + j * 2048);
    if (TH) asm volatile("s_waitcnt vmcnt(%0)" :: "i"(TH - 2) : "memory");
  }
}
__device__ __forceinline__ v2u rdtr(LAS char* p) { return __builtin_bit_cast(v2u, __builtin_amdgcn_ds_read_tr16_b64_v4i16((LAS s16x4*)p)); }
struct LBase {
  unsigned a[3];
  __device__ __forceinline__ explicit LBase(unsigned v) { a[0] = v; a[1] = v + 65536; a[2] = v + 131072; PIN(a[0]); PIN(a[1]); PIN(a[2]); }
  __device__ __forceinline__ v2u rd(int off) const { return rdtr((LAS char*)(a[off >> 16]) + (off & 65535)); }
};
template <int MT, int NT, int KS, int PD, bool WA, int M0 = 0, class RB, class GA, class PRE>
__device__ __forceinline__ void kloop(f32x4 (&acc)[MT][NT], RB rb, GA ga, PRE pre) {
  v2u bl[PD + 1][NT], bh[PD + 1][NT];
#pragma unroll
  for (int m = M0; m < MT; ++m)
#pragma unroll
    for (int s = 0; s < NT; ++s) acc[m][s] = f32x4{0.f, 0.f, 0.f, 0.f};
#pragma unroll
  for (int ks = 0; ks < PD; ++ks)
#pragma unroll
    for (int s = 0; s < NT; ++s) { bl[ks][s] = rb(ks, s, 0); bh[ks][s] = rb(ks, s, 1); }
  SCHED();
#pragma unroll
  for (int ks = 0; ks < KS; ++ks) {
    pre(ks);
#pragma unroll
    for (int s = 0; s < NT; ++s)
#pragma unroll
      for (int m = 0; m < MT; ++m) {
        const v2u lo = bl[ks % (PD + 1)][s], hi = bh[ks % (PD + 1)][s];
        const v4u wv = {lo.x, lo.y, hi.x, hi.y};
        acc[m][s] = WA ? mfma(wv, ga(ks, m), acc[m][s]) : mfma(ga(ks, m), wv, acc[m][s]);
        if (ks + PD < KS) {
          if (MT == 1 || m == 0) bl[(ks + PD) % (PD + 1)][s] = rb(ks + PD, s, 0);
          if (MT == 1 || m == 1) bh[(ks + PD) % (PD + 1)][s] = rb(ks + PD, s, 1);
        }
        SCHED();
      }
  }
}
constexpr int ld_hi(int ks, int s0, int W, int KS) {
  const int a = s0 + 2 * (ks + 1), b = ks + 1 + W, c = a < b ? a : b;
  return c < KS ? c : KS;
}
__device__ __forceinline__ v4u ld128(__amdgpu_buffer_rsrc_t rs, int voff, int soff) {
  return __builtin_amdgcn_raw_buffer_load_b128(rs, voff, soff, AUX_SC1);
}
__device__ __forceinline__ v2u ld64(__amdgpu_buffer_rsrc_t rs, int voff, int soff) {
  return __builtin_amdgcn_raw_buffer_load_b64(rs, voff, soff, AUX_SC1);
}
__device__ __forceinline__ void st128(v4u v, __amdgpu_buffer_rsrc_t rs, int voff, int soff) {
  __builtin_amdgcn_raw_buffer_store_b128(v, rs, (int)((unsigned)voff + (unsigned)soff), 0, AUX_SC1);
}
__device__ __forceinline__ int sel(bool c, int a) {
  PIN(a);
  return c ? a : OOB;
}
__device__ __forceinline__ v4u ld_split(__amdgpu_buffer_rsrc_t rs, int lane, int soff) {
  const v2u a = ld64(rs, lane * 8, soff), b = ld64(rs, lane * 8 + 512, soff);
  return v4u{a.x, a.y, b.x, b.y};
}
__device__ __forceinline__ void st64(v2u v, __amdgpu_buffer_rsrc_t rs, int voff, int soff) {
  __builtin_amdgcn_raw_buffer_store_b64(v, rs, voff, soff, AUX_SC1);
}
__device__ __forceinline__ void flag_set(gu32* p, unsigned tag) {
  asm volatile("global_atomic_swap %0, %1, off sc1" :: "v"(p), "v"(tag) : "memory");
}
__device__ __forceinline__ void wait_flag(gu32* p, int n, unsigned tag, int lane) {
#pragma unroll 1
  for (unsigned spins = 0; spins < kSpinCap; ++spins) {
    unsigned v = tag;
    if (lane < n) asm volatile("global_atomic_add %0, %1, %2, off sc0 sc1\n\ts_waitcnt vmcnt(0)" : "=&v"(v) : "v"(p), "v"(0u) : "memory");
    if (__all(v == tag)) break;
    if (SLEEP) __builtin_amdgcn_s_sleep(SLEEP);
  }
  asm volatile("" ::: "memory");
}
__device__ __forceinline__ void sig_wait(volatile LAS unsigned* p) {
#pragma unroll 1
  for (unsigned spins = 0; spins < 64 * kSpinCap; ++spins) {
    if (*p != 0u) break;
    __builtin_amdgcn_s_sleep(1);
  }
  asm volatile("" ::: "memory");
}
__device__ __forceinline__ void prog_wait(volatile LAS v4u* p, unsigned stage) {
#pragma unroll 1
  for (unsigned spins = 0; spins < 64 * kSpinCap; ++spins) {
    const v4u v = *p;
    if (min(min(v.x, v.y), min(v.z, v.w)) >= stage) break;
    __builtin_amdgcn_s_sleep(1);
  }
  asm volatile("" ::: "memory");
}
__device__ __forceinline__ void publish(gu32* flag, unsigned tag, int lane) {
  PIN(flag);
  DRAIN();
  if (lane == 0) flag_set(flag, tag);
}
__device__ __forceinline__ f32x4 add4(f32x4 a, f32x4 b) {
  f32x4 r;
#pragma unroll
  for (int i = 0; i < 4; ++i) { float x; asm("v_add_f32 %0, %1, %2" : "=v"(x) : "v"(a[i]), "v"(b[i])); r[i] = x; }
  return r;
}
__device__ __forceinline__ float xsum32(float x) {
  unsigned u = __builtin_bit_cast(unsigned, x), v = u;
  asm("" : "+v"(v));
  auto r = __builtin_amdgcn_permlane32_swap(u, v, false, false);
  const unsigned a = r[0], b = r[1];
  return __builtin_bit_cast(float, a) + __builtin_bit_cast(float, b);
}
__device__ __forceinline__ float xsum16(float x) {
  unsigned u = __builtin_bit_cast(unsigned, x), v = u;
  asm("" : "+v"(v));
  auto r = __builtin_amdgcn_permlane16_swap(u, v, false, false);
  const unsigned a = r[0], b = r[1];
  return __builtin_bit_cast(float, a) + __builtin_bit_cast(float, b);
}
__device__ __forceinline__ float xsum8(float x) {
  return x + __builtin_bit_cast(float, __builtin_amdgcn_update_dpp(0, __builtin_bit_cast(int, x), 0x128, 0xf, 0xf, false));
}
__device__ __forceinline__ f32x2 phi2(f32x2 t) {
  const f32x2 u = t * 1.4426950408889634f, v = t + 1e-6f;
  const f32x2 e = {__builtin_amdgcn_fmed3f(__builtin_amdgcn_exp2f(u[0]), 0.f, 1.f), __builtin_amdgcn_fmed3f(__builtin_amdgcn_exp2f(u[1]), 0.f, 1.f)};
  return f32x2{fmaxf(v[0], 1e-6f), fmaxf(v[1], 1e-6f)} + e;
}
__device__ __forceinline__ f32x2 gelu2(f32x2 x) {
  const f32x2 u = x * (x * x * -0.1029432397f + -2.302208198f);
  const f32x2 d = f32x2{__builtin_amdgcn_exp2f(u[0]), __builtin_amdgcn_exp2f(u[1])} + 1.f;
  return x * f32x2{__builtin_amdgcn_rcpf(d[0]), __builtin_amdgcn_rcpf(d[1])};
}

#define STAMP(i) do { } while (0)

__global__ void __launch_bounds__(512, 2)
fused_block(const unsigned short* __restrict__ x, const unsigned short* __restrict__ wqkv, unsigned char* ws, const unsigned short* __restrict__ wo,
            const unsigned short* __restrict__ w1, const unsigned short* __restrict__ b1, const unsigned short* __restrict__ w2,
            const unsigned short* __restrict__ b2, unsigned short* __restrict__ out) {
  __shared__ __attribute__((aligned(16))) char smem[LDS_BYTES];
  unsigned long long pc0;
  asm volatile("s_getpc_b64 %0" : "=s"(pc0));
  LAS char* L = (LAS char*)smem;
  const unsigned lds0 = (unsigned)(size_t)L;
  const int tid = threadIdx.x, lane = tid & 63, w = __builtin_amdgcn_readfirstlane(tid >> 6);
  const int b = blockIdx.x, xcd = b & 7, bi = b >> 3;
  const int g = lane >> 4, t = lane & 15;
  const __amdgpu_buffer_rsrc_t rs = __builtin_amdgcn_make_buffer_rsrc((void*)ws, 0, WS_BYTES, 0x00020000);
  gu32* flags = (gu32*)(ws + WS_FLAG);
#define FLAG(set, grp) (flags + ((set) * FLAG_SET + (grp) * 256) / 4)
  asm volatile("" :: "s"(x), "s"(wqkv), "s"(ws), "s"(wo), "s"(w1), "s"(b1), "s"(w2), "s"(b2));
  STAMP(0);
  const int r128 = 4 * (xcd >> 1) + (bi >> 3), c16 = 8 * (xcd & 1) + (bi & 7);
  const int r64 = 8 * (xcd >> 1) + (bi >> 2), c8 = 4 * (xcd & 1) + (bi & 3);

  const __amdgpu_buffer_rsrc_t rx = __builtin_amdgcn_make_buffer_rsrc((void*)x, 0, S * D * 2, 0x00020000);
  const int hw = w - 4, ht = tid - 256;
  volatile LAS unsigned* sig = (volatile LAS unsigned*)(L + L_CS + 1024);
  volatile LAS v4u* prog = (volatile LAS v4u*)(L + L_CS + 1088);
#define PROG(n) do { asm volatile("" ::: "memory"); sig[16 + w] = (n); } while (0)

  const __amdgpu_buffer_rsrc_t rq = __builtin_amdgcn_make_buffer_rsrc((void*)wqkv, 0, D * 3 * D * 2, 0x00020000);
  v4u tv[12];
  unsigned ep;
  const int q10 = lane / 10, c10 = lane % 10;
  if (w < 4) {
#pragma unroll
    for (int j = 0; j < 3; ++j)
      tv[j] = __builtin_amdgcn_raw_buffer_load_b128(rx, (lane & 7) * 1280 + (lane >> 3) * 16, (j < 2 || w < 2) ? 8 * b * 1280 + (w + 4 * j) * 128 : OOB, 0);
    ep = __hip_atomic_load((gu32*)(ws + WS_EPOCH), RLX_AGENT);
  } else if (w == 4) {
    const int vo = sel(lane < 40, q10 * 3840 + (c10 < 5 ? 1280 : 2480) + c10 * 16);
#pragma unroll
    for (int j = 0; j < 10; ++j) tv[j] = __builtin_amdgcn_raw_buffer_load_b128(rq, vo, (40 * r128 + 4 * j) * 3840 + c16 * 80, 0);
  } else if (w == 7) {
    *(LAS v4u*)(L + L_CS + 512 + lane * 16) = v4u{0u, 0u, 0u, 0u};
  }
  BAR();

  if (w < 4) {
  unsigned tag = ep;
  const int grp32 = 4 * r128 + w;
  constexpr int AW = 6;

  {
    const int tok = 8 * b + (lane & 7), cl = lane >> 3;
    const bool has3 = w < 2;
    int xd = ((tok >> 4) * 20 + 2 * w + (cl >> 2)) * 1024 + ((cl & 3) * 16 + (tok & 15)) * 16;
    PIN(xd);
    SCHED();
    f32x2 f[12];
#pragma unroll
    for (int j = 0; j < 3; ++j)
#pragma unroll
      for (int e = 0; e < 4; ++e) f[j * 4 + e] = bf2(tv[j][e]);
    f32x2 s2 = (f[0] + f[1]) + (f[2] + f[3]) + ((f[4] + f[5]) + (f[6] + f[7])) + ((f[8] + f[9]) + (f[10] + f[11]));
    const float mloc = xsum32(xsum16(xsum8(s2[0] + s2[1]))) * (has3 ? 1.f / 192.f : 1.f / 128.f);
    const float m3 = has3 ? mloc : 0.f;
    f32x2 q2 = {0.f, 0.f}, q3 = {0.f, 0.f};
#pragma unroll
    for (int j = 0; j < 12; j += 2) {
      const f32x2 d0 = f[j] - (j < 8 ? mloc : m3), d1 = f[j + 1] - (j < 8 ? mloc : m3);
      q2 += d0 * d0; q3 += d1 * d1;
    }
    q2 += q3;
    const float m2loc = xsum32(xsum16(xsum8(q2[0] + q2[1])));
    if (lane < 8) *(volatile LAS f32x4*)(L + L_CS + 512 + (lane * 4 + w) * 16) = f32x4{mloc, m2loc, 1.f, 1.f};
    STAMP(25);
    f32x4 pr[4];
#pragma unroll 1
    for (unsigned spins = 0; spins < 64 * kSpinCap; ++spins) {
      bool ok = true;
#pragma unroll
      for (int ww = 0; ww < 4; ++ww) {
        pr[ww] = *(volatile LAS f32x4*)(L + L_CS + 512 + ((lane & 7) * 4 + ww) * 16);
        ok = ok && pr[ww][2] == 1.f;
      }
      if (__all(ok)) break;
    }
    const f32x4 p01 = {pr[0][0], pr[0][1], pr[1][0], pr[1][1]}, p23 = {pr[2][0], pr[2][1], pr[3][0], pr[3][1]};
    const float mean = (p01[0] + p01[2]) * 0.3f + (p23[0] + p23[2]) * 0.2f;
    const float e0 = p01[0] - mean, e1 = p01[2] - mean, e2 = p23[0] - mean, e3 = p23[2] - mean;
    const float m2 = (p01[1] + p01[3]) + (p23[1] + p23[3]) + 192.f * (e0 * e0 + e1 * e1) + 128.f * (e2 * e2 + e3 * e3);
    const float rstd = __builtin_amdgcn_rsqf(m2 * (1.f / D) + LN_EPS);
#pragma unroll
    for (int j = 0; j < 3; ++j) {
      v4u o;
#pragma unroll
      for (int e = 0; e < 4; ++e) o[e] = pk2((f[j * 4 + e] - mean) * rstd);
      st128(o, rs, (j < 2 || has3) ? xd : OOB, WS_XN + j * 8192);
    }
    STAMP(26);
    PIN(tag); tag += 1u;
    publish(FLAG(0, b >> 2) + (b & 3) * 4 + w, tag, lane);
    if (w == 0) sig[6] = 1u;
  }

  STAMP(1);

  v4u xr[3];
  {
    wait_flag(FLAG(0, grp32) + lane, 16, tag, lane);
    STAMP(2);
    v4u a[20][2];
    const int abase = WS_XN + 2 * grp32 * 20 * 1024;
#pragma unroll
    for (int ks = 0; ks < S0; ++ks)
#pragma unroll
      for (int m = 0; m < 2; ++m) a[ks][m] = ld128(rs, lane * 16, abase + (m * 20 + ks) * 1024);
    SCHED();
    BAR_RAW();
    STAMP(27);
    {
      f32x4 acc[2][5];
      const LBase bp(lds0 + L_BKV + (lane >> 2) * 160 + (lane & 3) * 8);
      kloop<2, 5, 20, 1, false>(acc, [&](int ks, int s, int h) { return bp.rd(ks * 5120 + h * 2560 + s * 32); },
        [&](int ks, int m) { return a[ks][m]; },
        [&](int ks) {
#pragma unroll
          for (int j = ld_hi(ks - 1, S0, AW, 20); j < ld_hi(ks, S0, AW, 20); ++j)
#pragma unroll
            for (int m = 0; m < 2; ++m) a[j][m] = ld128(rs, lane * 16, abase + (m * 20 + j) * 1024);
          if (ks == 3 || ks == 10) BAR_RAW();
          if (ks == 20 - AW) PROG(1);
        });
      STAMP(3);
      v4u ka[3], vb[3];
#pragma unroll
      for (int s = 0; s < 3; ++s)
#pragma unroll
        for (int m = 0; m < 2; ++m) {
          vb[s][2 * m] = pk2(lo2(acc[m][s + 2])); vb[s][2 * m + 1] = pk2(hi2(acc[m][s + 2]));
        }
#pragma unroll
      for (int e = 0; e < 4; ++e) vb[0][e] = t < 8 ? 0x3F803F80u : vb[0][e];
      const f32x4 z4 = {0.f, 0.f, 0.f, 0.f};
      LAS f32x4* red = (LAS f32x4*)(L + L_RED) + w * 512 + lane;
#pragma unroll
      for (int s = 0; s < 3; ++s) {
#pragma unroll
        for (int m = 0; m < 2; ++m) { ka[s][2 * m] = pk2(phi2(lo2(acc[m][s]))); ka[s][2 * m + 1] = pk2(phi2(hi2(acc[m][s]))); }
#pragma unroll
        for (int sb = 0; sb < 3; ++sb)
          if (s > 0 || sb < 2) red[(3 * s + sb - (s > 0)) * 64] = mfma(ka[s], vb[sb], z4);
      }
      STAMP(28);
      BAR();
    }
    STAMP(4);
    f32x4 qa[2][3];
    {
      unsigned bp0 = lds0 + L_BQ + (lane >> 2) * 64 + (lane & 3) * 8 + (g & 1) * 32;
      unsigned bp1 = lds0 + L_BQ + (lane >> 2) * 64 + (lane & 3) * 8 + 32 - (g & 1) * 32;
      unsigned bp2 = lds0 + L_BQ + 2048 + (lane >> 2) * 16 + (lane & 1) * 8;
      PIN(bp0); PIN(bp1); PIN(bp2);
      kloop<2, 3, 20, 2, true>(qa,
        [&](int ks, int s, int h) {
          return s == 0 ? rdtr((LAS char*)bp0 + ks * 2560 + h * 1024) : s == 1 ? rdtr((LAS char*)bp1 + ks * 2560 + h * 1024) : rdtr((LAS char*)bp2 + ks * 2560 + h * 256);
        },
        [&](int ks, int m) { return a[ks][m]; },
        [&](int ks) {});
    }
    v4u pq0[2], pq1[2];
#pragma unroll
    for (int m = 0; m < 2; ++m) {
      pq0[m].x = pk2(phi2(lo2(qa[m][0]))); pq0[m].y = pk2(phi2(hi2(qa[m][0])));
      pq0[m].z = pk2(phi2(lo2(qa[m][1]))); pq0[m].w = pk2(phi2(hi2(qa[m][1])));
      const unsigned lo = pk2(phi2(lo2(qa[m][2]))), hi = pk2(phi2(hi2(qa[m][2])));
      pq1[m].x = g < 2 ? lo : 0u; pq1[m].y = g < 2 ? hi : 0u; pq1[m].z = 0u; pq1[m].w = 0u;
      PIN(pq0[m]); PIN(pq1[m]);
    }
    STAMP(5);
    BAR_RAW();
#pragma unroll
    for (int j = 0; j < 3; ++j)
      xr[j] = __builtin_amdgcn_raw_buffer_load_b128(rx, sel(j < 2 || g < 2, t * 1280 + g * 16), (4 * r64 + w) * 16 * 1280 + c8 * 160 + j * 64, 0);
    STAMP(17);
    {
      v4u kv[4][2];
#pragma unroll
      for (int es = 3; es >= 0; --es)
#pragma unroll
        for (int ks = 0; ks < 2; ++ks) kv[es][ks] = *(LAS v4u*)(L + L_KVF + (es * 2 + ks) * 1024 + lane * 16);
      SCHED();
      f32x4 o[2][4];
#pragma unroll
      for (int es = 3; es >= 0; --es)
#pragma unroll
        for (int m = 0; m < 2; ++m) o[m][es] = mfma(kv[es][0], pq0[m], f32x4{0.f, 0.f, 0.f, 0.f});
#pragma unroll
      for (int es = 3; es >= 0; --es)
#pragma unroll
        for (int m = 0; m < 2; ++m) o[m][es] = mfma(kv[es][1], pq1[m], o[m][es]);
#pragma unroll
      for (int m = 0; m < 2; ++m) {
        const float i0 = __builtin_amdgcn_rcpf(o[m][3][0] + 1e-6f), i1 = __builtin_amdgcn_rcpf(o[m][3][1] + 1e-6f);
        const float im = g == 0 ? i0 : i1;
        v4u ov;
        ov.x = pk2(lo2(o[m][0]) * i0); ov.y = pk2(hi2(o[m][0]) * i0);
        ov.z = pk2(lo2(o[m][1]) * im); ov.w = pk2(hi2(o[m][1]) * im);
        v2u ol; ol.x = pk2(lo2(o[m][2]) * i1); ol.y = pk2(hi2(o[m][2]) * i1);
        const int tt = 2 * grp32 + m;
        st128(ov, rs, lane * 16, WS_O + (tt * 20 + c16) * 1024);
        st64(ol, rs, g < 2 ? lane * 8 : OOB, WS_O + (tt * 20 + 16) * 1024 + c16 * 256);
      }
      STAMP(18);
      publish(FLAG(2, grp32) + c16, tag, lane);
    }
    STAMP(7);
  }

  {
    const int tt = 4 * r64 + w;
    v4u eye[2];
#pragma unroll
    for (int hf = 0; hf < 2; ++hf) {
      const int e = 16 * hf + t - 8 * g;
#pragma unroll
      for (int d = 0; d < 4; ++d) eye[hf][d] = e == 2 * d ? 0x3F80u : e == 2 * d + 1 ? 0x3F800000u : 0u;
      PIN(eye[hf]);
    }
    f32x4 acc[1][5];
#pragma unroll
    for (int s = 0; s < 5; ++s) { acc[0][s] = mfma(eye[s & 1], xr[s >> 1], f32x4{0.f, 0.f, 0.f, 0.f}); PIN(acc[0][s]); }
    wait_flag(FLAG(2, tt >> 1) + lane, 16, tag, lane);
    STAMP(8);
    if (b == 0 && tid == 0) __hip_atomic_store((gu32*)(ws + WS_EPOCH), tag, RLX_AGENT);
    v4u a[20];
#pragma unroll
    for (int ks = 0; ks < 2 * S0; ++ks) a[ks] = ld128(rs, lane * 16, WS_O + (tt * 20 + ks) * 1024);
    SCHED();
    const LBase bp(lds0 + L_WO + (lane >> 2) * 160 + (lane & 3) * 8);
    kloop<1, 5, 20, 1, true, 1>(acc, [&](int ks, int s, int h) { return bp.rd(ks * 5120 + h * 2560 + s * 32); },
      [&](int ks, int m) { return a[ks]; },
      [&](int ks) {
#pragma unroll
        for (int j = ld_hi(ks - 1, 2 * S0, 2 * AW, 20); j < ld_hi(ks, 2 * S0, 2 * AW, 20); ++j)
          a[j] = j < 16 ? ld128(rs, lane * 16, WS_O + (tt * 20 + j) * 1024) : ld_split(rs, lane, WS_O + (tt * 20 + j) * 1024);
        if (ks == 8) PROG(2);
        if (ks == 14) PROG(3);
      });
    PROG(4);
    STAMP(9);
    v2u hb[5];
    f32x2 hv[10];
#pragma unroll
    for (int s = 0; s < 5; ++s) {
      hb[s].x = pk2(lo2(acc[0][s]));
      hb[s].y = pk2(hi2(acc[0][s]));
      hv[2 * s] = bf2(hb[s].x); hv[2 * s + 1] = bf2(hb[s].y);
    }
    st128(v4u{hb[0].x, hb[0].y, hb[1].x, hb[1].y}, rs, lane * 16, WS_H + (tt * 20 + 2 * c8) * 1024);
    st128(v4u{hb[2].x, hb[2].y, hb[3].x, hb[3].y}, rs, lane * 16, WS_H + (tt * 20 + 2 * c8 + 1) * 1024);
    st64(hb[4], rs, lane * 8, WS_H + (tt * 20 + 16) * 1024 + c8 * 512);
    const f32x2 s2 = ((hv[0] + hv[1]) + (hv[2] + hv[3])) + ((hv[4] + hv[5]) + (hv[6] + hv[7])) + (hv[8] + hv[9]);
    const float mc = xsum32(xsum16(s2[0] + s2[1])) * (1.f / 80.f);
    f32x2 q2 = {0.f, 0.f}, q3 = {0.f, 0.f};
#pragma unroll
    for (int j = 0; j < 10; j += 2) { const f32x2 d0 = hv[j] - mc, d1 = hv[j + 1] - mc; q2 += d0 * d0; q3 += d1 * d1; }
    q2 += q3;
    const float m2 = xsum32(xsum16(q2[0] + q2[1]));
    v2u sv = {__builtin_bit_cast(unsigned, mc), __builtin_bit_cast(unsigned, m2)};
    PIN(sv);
    STAMP(20);
    publish(FLAG(3, tt >> 1) + (tt & 1) * 8 + c8, tag, lane);
    st64(sv, rs, g == 0 ? t * 8 : OOB, WS_ST + (tt * 8 + c8) * 128);
    STAMP(10);
  }

  {
    gu32* fst = FLAG(3, 2 * r64 + (w >> 1)) + 32 + (w & 1) * 8 + c8;
    PIN(fst);
    wait_flag(FLAG(3, grp32) + lane, 16, tag, lane);
    DRAIN();
    if (lane == 0) flag_set(fst, tag);
    STAMP(11);
    v4u a[20][2];
    const int abase = WS_H + 2 * grp32 * 20 * 1024;
#pragma unroll
    for (int ks = 0; ks < S0; ++ks)
#pragma unroll
      for (int m = 0; m < 2; ++m) a[ks][m] = ld128(rs, lane * 16, abase + (m * 20 + ks) * 1024);
    SCHED();
    float nm[2], rr[2];
    BAR_RAW();
    STAMP(22);
    const unsigned l64 = lds0 + (lane >> 2) * 64 + (lane & 3) * 8;
    const LBase bp0(l64 + (g & 1) * 32), bp1(l64 + 32 - (g & 1) * 32);
    const int h1base = WS_H1 + 2 * grp32 * 48 * 1024;
    auto finish = [&](const f32x4& c0, const f32x4& c1, int j, int m) {
      v4u o;
#pragma unroll
      for (int s = 0; s < 2; ++s) {
        const f32x4 cs = *(LAS f32x4*)(L + L_CS + ((2 * j + s) * 4 + g) * 16), bb = *(LAS f32x4*)(L + L_B1 + ((2 * j + s) * 4 + g) * 16);
        const f32x4 c = s ? c1 : c0;
        o[2 * s] = pk2(gelu2(lo2(c) * rr[m] + (lo2(cs) * nm[m] + lo2(bb))));
        o[2 * s + 1] = pk2(gelu2(hi2(c) * rr[m] + (hi2(cs) * nm[m] + hi2(bb))));
      }
      st128(o, rs, lane * 16, h1base + (m * 48 + (c16 >> 3) * 24 + (j < 2 ? 2 * (c16 & 7) + j : 16 + (c16 & 7))) * 1024);
    };
    {
      f32x4 acc[2][4];
      kloop<2, 4, 20, 1, true>(acc, [&](int ks, int s, int h) { return ((s & 1) ? bp1 : bp0).rd(L_W1 + (s >> 1) * 40960 + ks * 2048 + h * 1024); },
        [&](int ks, int m) { return a[ks][m]; },
        [&](int ks) {
#pragma unroll
          for (int j = ld_hi(ks - 1, S0, AW, 20); j < ld_hi(ks, S0, AW, 20); ++j)
#pragma unroll
            for (int m = 0; m < 2; ++m)
              a[j][m] = j < 16 ? ld128(rs, lane * 16, abase + (m * 20 + j) * 1024) : ld_split(rs, lane, abase + (m * 20 + j) * 1024);
          if (ks == 20 - AW) PROG(5);
        });
      PROG(6);
      sig_wait(sig + 12 + w);
#pragma unroll
      for (int m = 0; m < 2; ++m) {
        const f32x2 u = *(LAS f32x2*)(L + L_KVF + ((2 * w + m) * 16 + t) * 8);
        rr[m] = u[0]; nm[m] = u[1];
      }
      STAMP(12);
#pragma unroll
      for (int j = 0; j < 2; ++j)
#pragma unroll
        for (int m = 0; m < 2; ++m) finish(acc[m][2 * j], acc[m][2 * j + 1], j, m);
      SCHED();
      STAMP(24);
    }
    f32x4 accb[2][2];
    const int kh = w & 1, tt = 4 * r64 + w;
    v4u hr01, hr23;
    v2u hr4;
    kloop<2, 2, 20, 3, true>(accb, [&](int ks, int s, int h) { return (s ? bp1 : bp0).rd(L_W1 + 81920 + ks * 2048 + h * 1024); },
      [&](int ks, int m) { return a[ks][m]; },
      [&](int ks) {
        if (ks == PUBA) { publish(FLAG(4, grp32) + c16, tag, lane); PROG(7); }
        if (ks == PUBA + 2) {
          hr01 = ld128(rs, lane * 16, WS_H + (tt * 20 + 2 * c8) * 1024);
          hr23 = ld128(rs, lane * 16, WS_H + (tt * 20 + 2 * c8 + 1) * 1024);
          hr4 = ld64(rs, lane * 8, WS_H + (tt * 20 + 16) * 1024 + c8 * 512);
        }
      });
    STAMP(13);
    f32x4 acc[2][5];
    {
      const unsigned hrr[10] = {hr01.x, hr01.y, hr01.z, hr01.w, hr23.x, hr23.y, hr23.z, hr23.w, hr4.x, hr4.y};
#pragma unroll
      for (int s = 0; s < 5; ++s) {
        acc[0][s] = *(LAS f32x4*)(L + L_B2 + (s * 4 + g) * 16) + f32x4{bf_lo(hrr[2 * s]), bf_hi(hrr[2 * s]), bf_lo(hrr[2 * s + 1]), bf_hi(hrr[2 * s + 1])};
        PIN(acc[0][s]);
      }
    }
    int oo[3];
    unsigned tw, tr;
    {
      int ln = lane;
      PIN(ln);
      const int qq = ln / 10, cc = ln % 10;
#pragma unroll
      for (int j = 0; j < 3; ++j) { oo[j] = sel(ln < (j < 2 ? 60 : 40), ((tt * 16 + 6 * j + qq) * D + 80 * c8) * 2 + cc * 16); PIN(oo[j]); }
      tw = lds0 + L_W2 + 122880 + w * 3072 + t * 176 + g * 8; tr = lds0 + L_W2 + 122880 + w * 3072 + qq * 176 + cc * 16;
      PIN(tw); PIN(tr);
    }
    BAR_RAW();
    STAMP(14);
    constexpr int WIN = 8;
    v4u ar[WIN][2];
    const int a4[2] = {WS_H1 + (tt * 48 + kh * 24) * 1024, WS_H1 + ((tt ^ 1) * 48 + kh * 24) * 1024};
#pragma unroll
    for (int ks = 0; ks < WIN - 1; ++ks)
#pragma unroll
      for (int m = 0; m < 2; ++m) ar[ks][m] = ld128(rs, lane * 16, a4[m] + ks * 1024);
    SCHED();
#pragma unroll
    for (int m = 0; m < 2; ++m) finish(accb[m][0], accb[m][1], 2, m);
    STAMP(29);
    const LBase bp(lds0 + L_W2 + kh * 5120 + (lane >> 2) * 160 + (lane & 3) * 8);
    kloop<2, 5, 24, 1, true, 1>(acc, [&](int ks, int s, int h) { return bp.rd((ks % 15) * 10240 + h * 2560 + s * 32); },
      [&](int ks, int m) { return ar[ks % WIN][m]; },
      [&](int ks) {
        if (ks == 5) PROG(8);
        if (ks == 9 || ks == 14 || ks == 19) BAR_RAW();
        if (ks == 3) publish(FLAG(10, grp32) + c16, tag, lane);
        if (ks >= 3) {
#pragma unroll
          for (int j = ks == 3 ? WIN - 1 : ld_hi(ks - 1, 1, WIN - 1, 24); j < ld_hi(ks, 1, WIN - 1, 24); ++j)
#pragma unroll
            for (int m = 0; m < 2; ++m) ar[j % WIN][m] = ld128(rs, lane * 16, a4[m] + j * 1024);
        }
      });
    STAMP(15);
    LAS f32x4* xch = (LAS f32x4*)(L + L_W2 + 102400);
#pragma unroll
    for (int s = 0; s < 5; ++s) xch[((w ^ 1) * 5 + s) * 64 + lane] = acc[1][s];
    BAR();
    const __amdgpu_buffer_rsrc_t rout = __builtin_amdgcn_make_buffer_rsrc((void*)out, 0, S * D * 2, 0x00020000);
    f32x4 ot[5];
#pragma unroll
    for (int s = 0; s < 5; ++s) ot[s] = xch[(w * 5 + s) * 64 + lane];
    SCHED();
#pragma unroll
    for (int s = 0; s < 5; ++s) {
      const f32x4 v = acc[0][s] + ot[s];
      *(LAS v2u*)((LAS char*)tw + s * 32) = v2u{pk2(lo2(v)), pk2(hi2(v))};
    }
    asm volatile("" ::: "memory");
    v4u ov[3];
#pragma unroll
    for (int j = 0; j < 3; ++j) ov[j] = *(LAS v4u*)((LAS char*)tr + j * 6 * 176);
#pragma unroll
    for (int j = 0; j < 3; ++j) __builtin_amdgcn_raw_buffer_store_b128(ov[j], rout, oo[j], 0, 0);
    STAMP(16);
  }
  return;
  }

  {
    const v4u srd_ws = mk_srd(ws, WS_BYTES);
    STAMP(16);
    const int q5 = lane / 5, c5 = lane % 5;
    const int f40m = c5 < 4 ? 512 : 128, f40l = c5 < 4 ? q5 * 64 + (c5 ^ (2 * ((q5 >> 2) & 1))) * 16 : 2048 + q5 * 16;
    v2u ub1, ub2;
    unsigned pf0;
    v4u tq[10];
    if (hw == 1) {
      const __amdgpu_buffer_rsrc_t ro = __builtin_amdgcn_make_buffer_rsrc((void*)wo, 0, D * D * 2, 0x00020000);
      const int vq = sel(lane < 40, (8 * (q5 >> 2) + (q5 & 3)) * 3840 + c5 * 16), vo = sel(lane < 40, q10 * 1280 + c10 * 16);
#pragma unroll
      for (int j = 0; j < 5; ++j) {
        const int i8 = 5 * r128 + j, k = 32 * (i8 >> 2) + 16 * (i8 & 1) + 4 * ((i8 >> 1) & 1);
        tq[j] = __builtin_amdgcn_raw_buffer_load_b128(rq, vq, k * 3840 + c16 * 80, 0);
      }
#pragma unroll
      for (int j = 0; j < 5; ++j) tq[5 + j] = __builtin_amdgcn_raw_buffer_load_b128(ro, vo, (20 * r64 + 4 * j) * 1280 + c8 * 160, 0);
    } else if (hw == 2) {
      const GAS unsigned char* cb = (const GAS unsigned char*)(pc0 & ~127ull);
      pf0 = *(const GAS unsigned*)(cb + min(lane * 32 + bi, 346) * 128);
      if (lane < 6) {
        gu32* fp = FLAG(3 * (b >> 6) + (lane >> 1), b & 63) + (lane & 1) * 32;
        asm volatile("global_atomic_add %0, %1, off sc1" :: "v"(fp), "v"(0u) : "memory");
      }
    } else if (hw == 3) {
      const __amdgpu_buffer_rsrc_t rb1 = __builtin_amdgcn_make_buffer_rsrc((void*)b1, 0, F * 2, 0x00020000);
      const __amdgpu_buffer_rsrc_t rb2 = __builtin_amdgcn_make_buffer_rsrc((void*)b2, 0, D * 2, 0x00020000);
      ub1 = __builtin_amdgcn_raw_buffer_load_b64(rb1, lane * 8, c16 * 192, 0);
      ub2 = __builtin_amdgcn_raw_buffer_load_b64(rb2, lane * 8, c8 * 160, 0);
    }
    unsigned tag = __hip_atomic_load((gu32*)(ws + WS_EPOCH), RLX_AGENT);
    if (hw == 0) {
      const int dof = sel(lane < 40, lane * 16);
#pragma unroll
      for (int j = 0; j < 10; ++j) {
        const int k = 40 * r128 + 4 * j, R = (k & ~31) + 16 * ((k >> 2) & 1) + 4 * ((k >> 3) & 3);
        st128(tv[j], rs, dof, WS_TKV + c16 * 153600 + R * 160);
      }
      STAMP(18);
      PIN(tag); tag += 1u;
      publish(FLAG(6, c16) + r128, tag, lane);
      STAMP(13);
    }
    asm volatile("" ::: "memory");
    int red_off, kvf_off, kvp_off;
    auto items = [&]() {
      const int it = ht < 210 ? ht : 209, ie = it / 5, ic = it % 5;
      const int ihead = ie < 40 ? (ie >= 20) : ie - 40;
      const int id = 20 * ihead + 4 * ic, dg = id >> 2;
      const int sa = id >> 4, col = 40 + ie;
      const int tile = 3 * sa + (ie < 40 ? (col >> 4) - 2 : 0) - (sa > 0);
      red_off = lds0 + L_RED + (tile * 64 + (dg & 3) * 16 + (ie < 40 ? (col & 15) : 0)) * 16;
      kvf_off = lds0 + L_KVF + ((ie < 40 ? (ie >> 4) : 3) * 2 + (dg >> 3)) * 1024 + ((dg & 3) * 16 + (ie < 40 ? (ie & 15) : ihead)) * 16 + ((dg >> 2) & 1) * 8;
      kvp_off = WS_KVP + (c16 * 16 + r128) * 4096 + ht * 16;
      PIN(red_off); PIN(kvf_off); PIN(kvp_off);
    };
    if (hw != 0) items();
    int vo12, td[12];
    if (hw >= 2) {
      const int q12 = lane / 12, c12 = lane % 12;
      vo12 = hw == 2 ? sel(lane < 48, q12 * 3072 + c12 * 16) : sel(lane < 40, q10 * 1280 + c10 * 16);
#pragma unroll
      for (int j = 0; j < 12; ++j) {
        const int cc = r128 >> 1, off = 40 * (r128 & 1) + 4 * j;
        const int R = off < 64 ? 64 * cc + off : 32 * (16 + (cc >> 1)) + 16 * (cc & 1) + off - 64;
        const int d1 = WS_T1 + c16 * 122880 + (c12 >> 2) * 40960 + (R + q12) * 64 + ((c12 & 3) ^ (2 * ((R >> 2) & 1))) * 16;
        const int pc = r64 >> 1, ko = 48 * (r64 & 1) + 4 * j, jj = ko >> 5;
        const int d2 = WS_T2 + c8 * 245760 + (2 * (jj < 2 ? 2 * (pc & 7) + jj : 16 + (pc & 7)) + (pc >> 3)) * 5120 + (ko & 31) * 160 + lane * 16;
        td[j] = sel(hw == 2 ? (lane < 48 && j < 10) : lane < 40, hw == 2 ? d1 : d2);
        PIN(td[j]);
      }
      PIN(vo12);
    }
    int l16 = lane * 16, h16 = sel(ht < 210, ht * 16);
    PIN(l16); PIN(h16);
    gu32* fp1 = FLAG(1, c16) + lane;
    gu32* fp78 = lane < 32 ? FLAG(7, c8) + lane : FLAG(8, c16) + t;
    gu32* fp4 = FLAG(4, 2 * r64 + (g & 1)) + t;
    gu32* fp1w = FLAG(1, c16) + r128 * 4 + hw;
    gu32* fps = FLAG(3, 4 * r128 + hw) + 32 + lane;
    int sto = g * 256 + t * 8;
    PIN(fp1); PIN(fp78); PIN(fp4); PIN(fp1w); PIN(fps); PIN(sto);
    ((LAS v4u*)(L + L_KVF))[ht] = v4u{0u, 0u, 0u, 0u};
    ((LAS v4u*)(L + L_KVF))[256 + ht] = v4u{0u, 0u, 0u, 0u};
    asm volatile("" ::: "memory");
    if (hw != 0) {
      if (hw == 1) {
        sig_wait(sig + 6);
#pragma unroll
        for (int j = 0; j < 5; ++j) {
          const int i8 = 5 * r128 + j;
          st128(tq[j], rs, sel(lane < 40, (i8 & 3) * f40m + f40l), WS_TKV + c16 * 153600 + 102400 + (i8 >> 2) * 2560);
        }
#pragma unroll
        for (int j = 0; j < 5; ++j) {
          const int pp = r64 >> 1, e = 20 * (r64 & 1) + 4 * j;
          const int R = e < 32 ? 32 * pp + e : 32 * (16 + (pp >> 2)) + 8 * (pp & 3) + e - 32;
          st128(tq[5 + j], rs, sel(lane < 40, lane * 16), WS_TO + c8 * 102400 + R * 160);
        }
      }
      if (hw == 3) {
        if (lane < 24) *(LAS f32x4*)(L + L_B1 + lane * 16) = f32x4{bf_lo(ub1.x), bf_hi(ub1.x), bf_lo(ub1.y), bf_hi(ub1.y)};
        if (lane < 20) *(LAS f32x4*)(L + L_B2 + lane * 16) = f32x4{bf_lo(ub2.x), bf_hi(ub2.x), bf_lo(ub2.y), bf_hi(ub2.y)};
      }
      if (hw == 2) asm volatile("" :: "v"(pf0));
      PIN(tag); tag += 1u;
      DRAIN();
      if (hw == 1 && lane < 2) flag_set(lane == 0 ? FLAG(8, c16) + r128 : FLAG(7, c8) + r64, tag);
    }
    if (hw == 0) {
      if (F6W) __builtin_amdgcn_s_sleep(F6W);
      wait_flag(FLAG(6, c16) + lane, 16, tag, lane);
      sig[0] = 1u;
    }
    sig_wait(sig);
    STAMP(1);
    const int kvq = WS_TKV + c16 * 153600, wos = WS_TO + c8 * 102400;
    dma_image<0, 24>(lds0 + L_BKV, l16, srd_ws, kvq, hw, 3 * (bi >> 3));
    dma_image<24, 56>(lds0 + L_BKV, l16, srd_ws, kvq, hw, 4 * (bi >> 3));
    asm volatile("s_waitcnt vmcnt(8)" ::: "memory");
    STAMP(2);
    BAR_RAW();
    dma_image<56, 100>(lds0 + L_BKV, l16, srd_ws, kvq, hw, 5 * (bi >> 3));
    asm volatile("s_waitcnt vmcnt(10)" ::: "memory");
    BAR_RAW();
    if (hw == 1) wait_flag(fp78, 48, tag, lane);
    DRAIN();
    STAMP(3);
    BAR_RAW();
    if (QPOS == 0) dma_image<100, 150>(lds0 + L_BKV, l16, srd_ws, kvq, hw, 6 * (bi >> 3));
    if (hw == 0) items();
    prog_wait(prog, 1);
    if (QPOS == 1) dma_image<100, 150>(lds0 + L_BKV, l16, srd_ws, kvq, hw, 6 * (bi >> 3));
    if (hw >= 2) {
      const __amdgpu_buffer_rsrc_t r12 = __builtin_amdgcn_make_buffer_rsrc((void*)(hw == 2 ? w1 : w2), 0, D * F * 2, 0x00020000);
      const int base = hw == 2 ? r128 * 122880 + c16 * 192 : r64 * 61440 + c8 * 160, step = hw == 2 ? 4 * 3072 : 4 * 1280;
#pragma unroll
      for (int j = 0; j < 12; ++j) tv[j] = __builtin_amdgcn_raw_buffer_load_b128(r12, vo12, (j < 10 || hw == 3) ? base + j * step : OOB, 0);
      asm volatile("s_waitcnt vmcnt(12)" ::: "memory");
    } else {
      dma_image<32, 68, 0, 2>(lds0 + L_WO, l16, srd_ws, wos, hw, bi >> 2);
      asm volatile("s_waitcnt vmcnt(18)" ::: "memory");
    }
    STAMP(4);
    BAR();
    __builtin_amdgcn_s_setprio(2);
    STAMP(11);
    {
      f32x4 pv[4];
#pragma unroll
      for (int ww = 0; ww < 4; ++ww) pv[ww] = *(LAS f32x4*)((LAS char*)red_off + ww * 8192);
      st128(__builtin_bit_cast(v4u, add4(add4(pv[0], pv[1]), add4(pv[2], pv[3]))), rs, kvp_off, 0);
      PROG(1);
      STAMP(12);
      DRAIN();
#pragma unroll
      for (int j = 0; j < 12; ++j) PIN(tv[j]);
    }
    if (lane == 0) flag_set(fp1w, tag);
    if (hw == 0) {
      STAMP(10);
      __builtin_amdgcn_s_sleep(H1W);
      wait_flag(fp1, 64, tag, lane);
      sig[2] = 1u;
    } else {
      if (H1G) __builtin_amdgcn_s_sleep(H1G);
      prog_wait(prog + 1, 1);
#pragma unroll
      for (int i = 0; i < 12; ++i) {
        if (hw >= 2) st128(tv[i], rs, td[i], 0);
        const int j = hw - 1 + 3 * i;
        if (j < 32) {
          const int pc = 2 * ((j + 4 * (bi >> 2)) & 31), po = (pc < 32 ? pc : pc + 36) * 1024;
          dma2(lds0 + L_WO + po, l16, srd_ws, wos + po);
        }
      }
      STAMP(10);
      sig_wait(sig + 2);
    }
    STAMP(5);
    {
      v4u pv[16];
#pragma unroll
      for (int r = 0; r < 16; ++r) pv[r] = ld128(rs, h16, WS_KVP + (c16 * 16 + r) * 4096);
      SCHED();
      f32x4 v = __builtin_bit_cast(f32x4, pv[0]);
#pragma unroll
      for (int r = 1; r < 16; ++r) v = add4(v, __builtin_bit_cast(f32x4, pv[r]));
      v2u o; o.x = pk2(lo2(v)); o.y = pk2(hi2(v));
      if (ht < 210) *(LAS v2u*)((LAS char*)kvf_off) = o;
      if (ht >= 200 && ht < 210) {
#pragma unroll
        for (int c = 1; c < 4; ++c) *(LAS v2u*)((LAS char*)kvf_off + c * 64) = o;
      }
    }
    DRAIN();
    STAMP(6);
    BAR();
    __builtin_amdgcn_s_setprio(0);
    if (hw >= 2 && lane == 0) flag_set(hw == 2 ? FLAG(5, c16) + r128 : FLAG(11, c8) + r64, tag);
    if (hw == 2) {
      f32x4 s0 = {0.f, 0.f, 0.f, 0.f}, s1 = {0.f, 0.f, 0.f, 0.f};
#pragma unroll
      for (int j = 0; j < 10; ++j) {
        s0 = add4(s0, f32x4{bf_lo(tv[j].x), bf_hi(tv[j].x), bf_lo(tv[j].y), bf_hi(tv[j].y)});
        s1 = add4(s1, f32x4{bf_lo(tv[j].z), bf_hi(tv[j].z), bf_lo(tv[j].w), bf_hi(tv[j].w)});
      }
#pragma unroll
      for (int d = 24; d >= 12; d -= 12) {
        f32x4 u0, u1;
#pragma unroll
        for (int e = 0; e < 4; ++e) { u0[e] = __shfl(s0[e], lane + d); u1[e] = __shfl(s1[e], lane + d); }
        s0 = add4(s0, u0); s1 = add4(s1, u1);
      }
      f32x4 sv;
#pragma unroll
      for (int e = 0; e < 4; ++e) { const float a0 = __shfl(s0[e], lane >> 1), a1 = __shfl(s1[e], lane >> 1); sv[e] = (lane & 1) ? a1 : a0; }
      st128(__builtin_bit_cast(v4u, sv), rs, sel(lane < 24, lane * 16), WS_CS + (c16 * 16 + r128) * 384);
      publish(FLAG(9, c16) + r128, tag, lane);
    }
    if (hw == 1) {
      __builtin_amdgcn_s_sleep(40);
      wait_flag(lane < 32 ? FLAG(11, c8) + lane : FLAG(5, c16) + t, 48, tag, lane);
      sig[3] = 1u;
    }
    sig_wait(sig + 3);
    prog_wait(prog, 2);
    if (hw == 0) {
      wait_flag(FLAG(9, c16) + lane, 16, tag, lane);
      v4u pv[16];
      const int co = sel(lane < 24, l16);
#pragma unroll
      for (int r = 0; r < 16; ++r) pv[r] = ld128(rs, co, WS_CS + (c16 * 16 + r) * 384);
      SCHED();
      f32x4 v = __builtin_bit_cast(f32x4, pv[0]);
#pragma unroll
      for (int r = 1; r < 16; ++r) v = add4(v, __builtin_bit_cast(f32x4, pv[r]));
      if (lane < 24) *(LAS f32x4*)(L + L_CS + lane * 16) = v;
    } else {
      dma_image<70, 120, 0, 3>(lds0 + L_W1, l16, srd_ws, WS_T1 + c16 * 122880, hw - 1, 6 * (bi >> 3));
      prog_wait(prog, 3);
      dma_image<0, 40, 0, 3>(lds0 + L_W1, l16, srd_ws, WS_T1 + c16 * 122880, hw - 1, 5 * (bi >> 3));
      STAMP(7);
      prog_wait(prog, 4);
      dma_image<40, 70, 0, 3>(lds0 + L_W1, l16, srd_ws, WS_T1 + c16 * 122880, hw - 1, 3 * (bi >> 3));
    }
    DRAIN();
    STAMP(8);
    BAR();
    {
      __builtin_amdgcn_s_setprio(2);
      __builtin_amdgcn_s_sleep(STW);
      wait_flag(fps, 16, tag, lane);
      v2u s0[2], s1[2];
#pragma unroll
      for (int m = 0; m < 2; ++m) {
        s0[m] = ld64(rs, sto, WS_ST + (8 * r128 + 2 * hw + m) * 1024);
        s1[m] = ld64(rs, sto + 128, WS_ST + (8 * r128 + 2 * hw + m) * 1024);
      }
#pragma unroll
      for (int m = 0; m < 2; ++m) {
        const f32x2 p0 = __builtin_bit_cast(f32x2, s0[m]), p1 = __builtin_bit_cast(f32x2, s1[m]);
        const float a0 = p0[0], q0 = p0[1], a1 = p1[0], q1 = p1[1];
        float mean = xsum32(xsum16(a0 + a1)) * 0.125f;
        PIN(mean);
        float m2 = xsum32(xsum16(q0 + q1 + 80.f * ((a0 - mean) * (a0 - mean) + (a1 - mean) * (a1 - mean))));
        PIN(m2);
        const float var = m2 * (1.f / D);
        const float r1 = __builtin_amdgcn_rsqf(var + LN_EPS);
        const float rr = r1 * __builtin_amdgcn_rsqf(var * r1 * r1 + LN_EPS);
        if (g == 0) *(LAS f32x2*)(L + L_KVF + ((2 * hw + m) * 16 + t) * 8) = f32x2{rr, -mean * rr};
      }
      asm volatile("" ::: "memory");
      sig[12 + hw] = 1u;
      __builtin_amdgcn_s_setprio(0);
      STAMP(20);
    }
    const int w2s = WS_T2 + c8 * 245760;
    prog_wait(prog, 5);
    dma_image<0, 30>(lds0 + L_W2, l16, srd_ws, w2s, hw, bi >> 2);
    STAMP(14);
    prog_wait(prog, 6);
    dma_image<30, W2S>(lds0 + L_W2, l16, srd_ws, w2s, hw, bi >> 2);
    prog_wait(prog, 7);
    if (hw != 0) dma_image<W2S, 110, 0, 3>(lds0 + L_W2, l16, srd_ws, w2s, hw - 1, bi >> 2);
    DRAIN();
    STAMP(9);
    if (hw == 0) wait_flag(fp4, 32, tag, lane);
    STAMP(15);
    BAR_RAW();
    dma_image<110, 150>(lds0 + L_W2, l16, srd_ws, w2s, hw, 2 * (bi >> 2));
    prog_wait(prog, 8);
    if (hw == 0) wait_flag(fp4 + 6 * FLAG_SET / 4, 32, tag, lane);
    else dma_image<150, 198, 0, 3>(lds0 + L_W2 - 153600, l16, srd_ws, w2s, hw - 1, 3 * (bi >> 2));
    asm volatile("s_waitcnt vmcnt(16)" ::: "memory");
    STAMP(17);
    BAR_RAW();
    dma_image<198, 200>(lds0 + L_W2 - 153600, l16, srd_ws, w2s, hw, 0);
    dma_image<200, 240>(lds0 + L_W2 - 153600, l16, srd_ws, w2s, hw, 2 * (bi >> 2));
    asm volatile("s_waitcnt vmcnt(10)" ::: "memory");
    BAR_RAW();
    DRAIN();
    BAR_RAW();
  }

}
}

extern "C" void kernel_launch(void* const* d_in, const int* in_sizes, int n_in, void* d_out, int out_size, void* d_ws,
                              size_t ws_size, hipStream_t stream) {
  const unsigned short* x = (const unsigned short*)d_in[0];
  const unsigned short* wqkv = (const unsigned short*)d_in[1];
  const unsigned short* wo = (const unsigned short*)d_in[2];
  const unsigned short* w1 = (const unsigned short*)d_in[4];
  const unsigned short* b1 = (const unsigned short*)d_in[5];
  const unsigned short* w2 = (const unsigned short*)d_in[6];
  const unsigned short* b2 = (const unsigned short*)d_in[7];
  fused_block<<<256, 512, 0, stream>>>(x, wqkv, (unsigned char*)d_ws, wo, w1, b1, w2, b2, (unsigned short*)d_out);
}
```

```cpp
#include <hip/hip_runtime.h>
#include <hip/hip_bf16.h>

#define GAS __attribute__((address_space(1)))
#define LAS __attribute__((address_space(3)))
#define RLX_AGENT __ATOMIC_RELAXED, __HIP_MEMORY_SCOPE_AGENT
typedef unsigned v4u __attribute__((ext_vector_type(4)));
typedef unsigned v2u __attribute__((ext_vector_type(2)));
typedef float f32x4 __attribute__((ext_vector_type(4)));
typedef float f32x2 __attribute__((ext_vector_type(2)));
typedef __bf16 bf16x8 __attribute__((ext_vector_type(8)));
typedef __bf16 bf16x2 __attribute__((ext_vector_type(2)));
typedef short s16x4 __attribute__((ext_vector_type(4)));
typedef GAS unsigned gu32;

namespace {
constexpr int S = 2048, D = 640, F = 1536;
constexpr float LN_EPS = 1e-5f;
constexpr int AUX_SC1 = 16;
constexpr unsigned kSpinCap = 1u << 17;
constexpr int OOB = 0x7FFFFF00;

constexpr int WS_EPOCH = 0;
constexpr int WS_FLAG = 4096;
constexpr int FLAG_SET = 64 * 256;
constexpr int WS_XN = 1 << 20;
constexpr int WS_O = WS_XN + 128 * 20 * 1024;
constexpr int WS_H = WS_O + 128 * 20 * 1024;
constexpr int WS_H1 = WS_H + 128 * 20 * 1024;
constexpr int WS_KVP = WS_H1 + 128 * 48 * 1024;
constexpr int WS_ST = WS_KVP + 16 * 16 * 4096;
constexpr int WS_TKV = WS_ST + 2048 * 64;
constexpr int WS_TO = WS_TKV + 16 * 153600;
constexpr int WS_T1 = WS_TO + 8 * 102400;
constexpr int WS_T2 = WS_T1 + 16 * 122880;
constexpr int WS_CS = WS_T2 + 16 * 122880;
constexpr int WS_BYTES = WS_CS + 16 * 16 * 384;

constexpr int L_BKV = 0, L_BQ = 102400;
constexpr int L_RED = 0, L_KVF = 153600;
constexpr int L_WO = 0, L_W1 = 30720, L_W2 = 0, L_CS = 161792;
constexpr int L_B1 = L_CS + 1152, L_B2 = L_CS + 1536;
constexpr int LDS_BYTES = 163840;

__device__ __forceinline__ f32x4 mfma(v4u a, v4u b, f32x4 c) {
  return __builtin_amdgcn_mfma_f32_16x16x32_bf16(__builtin_bit_cast(bf16x8, a), __builtin_bit_cast(bf16x8, b), c, 0, 0, 0);
}
__device__ __forceinline__ unsigned pk2(f32x2 v) { return __builtin_bit_cast(unsigned, __builtin_convertvector(v, bf16x2)); }
__device__ __forceinline__ float bf_lo(unsigned u) { return __builtin_bit_cast(float, u << 16); }
__device__ __forceinline__ float bf_hi(unsigned u) { return __builtin_bit_cast(float, u & 0xFFFF0000u); }
__device__ __forceinline__ f32x2 bf2(unsigned u) { return f32x2{bf_lo(u), bf_hi(u)}; }
__device__ __forceinline__ f32x2 lo2(f32x4 v) { return f32x2{v[0], v[1]}; }
__device__ __forceinline__ f32x2 hi2(f32x4 v) { return f32x2{v[2], v[3]}; }
__device__ __forceinline__ v4u mk_srd(const void* p, unsigned bytes) {
  const unsigned long long a = (unsigned long long)p;
  v4u r;
  r.x = (unsigned)a; r.y = (unsigned)(a >> 32) & 0xFFFFu; r.z = bytes; r.w = 0x00020000u;
  return r;
}
#define DRAIN() asm volatile("s_waitcnt vmcnt(0)" ::: "memory")
#define BAR() asm volatile("s_waitcnt lgkmcnt(0)\n\ts_barrier" ::: "memory")
#define BAR_RAW() asm volatile("s_barrier" ::: "memory")
#define SCHED() __builtin_amdgcn_sched_barrier(0)
#define PIN(x) asm volatile("" : "+v"(x))

__device__ __forceinline__ void dma2(unsigned lds_addr, unsigned voff, v4u srd, unsigned soff) {
  unsigned keep;
  asm volatile("s_mov_b32 %0, m0\n\ts_mov_b32 m0, %1\n\ts_nop 0\n\t"
               "buffer_load_dwordx4 %2, %3, %4 offen sc1 lds\n\t"
               "buffer_load_dwordx4 %2, %3, %4 offen offset:1024 sc1 lds\n\t"
               "s_mov_b32 m0, %0"
               : "=&s"(keep) : "s"(lds_addr), "v"(voff), "s"(srd), "s"(soff) : "memory");
}
template <int LO, int HI, int NW = 4>
__device__ __forceinline__ void dma_image(unsigned lds_base, unsigned voff, v4u srd, int ws_off, int hw, int rot) {
  constexpr int NP = (HI - LO) / 2;
#pragma unroll 1
  for (int i = hw; i < NP; i += NW) {
    int j = i + rot;
    if (j >= NP) j -= NP;
    dma2(lds_base + LO * 1024 + j * 2048, voff, srd, ws_off + LO * 1024 + j * 2048);
  }
}
__device__ __forceinline__ LAS char* lp(unsigned a) { return (LAS char*)(size_t)a; }
__device__ __forceinline__ v2u rdtr(LAS char* p) { return __builtin_bit_cast(v2u, __builtin_amdgcn_ds_read_tr16_b64_v4i16((LAS s16x4*)p)); }
struct LBase {
  unsigned a[3];
  __device__ __forceinline__ explicit LBase(unsigned v) { a[0] = v; a[1] = v + 65536; a[2] = v + 131072; PIN(a[0]); PIN(a[1]); PIN(a[2]); }
  __device__ __forceinline__ v2u rd(int off) const { return rdtr(lp(a[off >> 16]) + (off & 65535)); }
};
template <int MT, int NT, int KS, int PD, bool WA, int M0 = 0, class RB, class GA, class PRE>
__device__ __forceinline__ void kloop(f32x4 (&acc)[MT][NT], RB rb, GA ga, PRE pre) {
  v2u bl[PD + 1][NT], bh[PD + 1][NT];
#pragma unroll
  for (int m = M0; m < MT; ++m)
#pragma unroll
    for (int s = 0; s < NT; ++s) acc[m][s] = f32x4{0.f, 0.f, 0.f, 0.f};
#pragma unroll
  for (int ks = 0; ks < PD; ++ks)
#pragma unroll
    for (int s = 0; s < NT; ++s) { bl[ks][s] = rb(ks, s, 0); bh[ks][s] = rb(ks, s, 1); }
  SCHED();
#pragma unroll
  for (int ks = 0; ks < KS; ++ks) {
    pre(ks);
#pragma unroll
    for (int s = 0; s < NT; ++s)
#pragma unroll
      for (int m = 0; m < MT; ++m) {
        const v2u lo = bl[ks % (PD + 1)][s], hi = bh[ks % (PD + 1)][s];
        const v4u wv = {lo.x, lo.y, hi.x, hi.y};
        acc[m][s] = WA ? mfma(wv, ga(ks, m), acc[m][s]) : mfma(ga(ks, m), wv, acc[m][s]);
        if (ks + PD < KS) {
          if (MT == 1 || m == 0) bl[(ks + PD) % (PD + 1)][s] = rb(ks + PD, s, 0);
          if (MT == 1 || m == 1) bh[(ks + PD) % (PD + 1)][s] = rb(ks + PD, s, 1);
        }
        SCHED();
      }
  }
}
constexpr int ld_hi(int ks, int s0, int W, int KS) {
  const int a = s0 + 2 * (ks + 1), b = ks + 1 + W, c = a < b ? a : b;
  return c < KS ? c : KS;
}
__device__ __forceinline__ v4u ld128(__amdgpu_buffer_rsrc_t rs, int voff, int soff) {
  return __builtin_amdgcn_raw_buffer_load_b128(rs, voff, soff, AUX_SC1);
}
__device__ __forceinline__ v2u ld64(__amdgpu_buffer_rsrc_t rs, int voff, int soff) {
  return __builtin_amdgcn_raw_buffer_load_b64(rs, voff, soff, AUX_SC1);
}
__device__ __forceinline__ void st128(v4u v, __amdgpu_buffer_rsrc_t rs, int voff, int soff) {
  __builtin_amdgcn_raw_buffer_store_b128(v, rs, (int)((unsigned)voff + (unsigned)soff), 0, AUX_SC1);
}
__device__ __forceinline__ int sel(bool c, int a) {
  PIN(a);
  return c ? a : OOB;
}
__device__ __forceinline__ v4u ld_split(__amdgpu_buffer_rsrc_t rs, int lane, int soff) {
  const v2u a = ld64(rs, lane * 8, soff), b = ld64(rs, lane * 8 + 512, soff);
  return v4u{a.x, a.y, b.x, b.y};
}
__device__ __forceinline__ void st64(v2u v, __amdgpu_buffer_rsrc_t rs, int voff, int soff) {
  __builtin_amdgcn_raw_buffer_store_b64(v, rs, voff, soff, AUX_SC1);
}
__device__ __forceinline__ void flag_set(gu32* p, unsigned tag) {
  asm volatile("global_atomic_swap %0, %1, off sc1" :: "v"(p), "v"(tag) : "memory");
}
__device__ __forceinline__ void wait_flag(gu32* p, int n, unsigned tag, int lane) {
#pragma unroll 1
  for (unsigned spins = 0; spins < kSpinCap; ++spins) {
    unsigned v = tag;
    if (lane < n) asm volatile("global_atomic_add %0, %1, %2, off sc0 sc1\n\ts_waitcnt vmcnt(0)" : "=&v"(v) : "v"(p), "v"(0u) : "memory");
    if (__all(v == tag)) break;
    __builtin_amdgcn_s_sleep(1);
  }
  asm volatile("" ::: "memory");
}
__device__ __forceinline__ void sig_wait(volatile LAS unsigned* p) {
#pragma unroll 1
  for (unsigned spins = 0; spins < 64 * kSpinCap; ++spins) {
    if (*p != 0u) break;
    __builtin_amdgcn_s_sleep(1);
  }
  asm volatile("" ::: "memory");
}
__device__ __forceinline__ void prog_wait(volatile LAS v4u* p, unsigned stage) {
#pragma unroll 1
  for (unsigned spins = 0; spins < 64 * kSpinCap; ++spins) {
    const v4u v = *p;
    if (min(min(v.x, v.y), min(v.z, v.w)) >= stage) break;
    __builtin_amdgcn_s_sleep(1);
  }
  asm volatile("" ::: "memory");
}
__device__ __forceinline__ void publish(gu32* flag, unsigned tag, int lane) {
  PIN(flag);
  DRAIN();
  if (lane == 0) flag_set(flag, tag);
}
__device__ __forceinline__ f32x4 add4(f32x4 a, f32x4 b) {
  f32x4 r;
#pragma unroll
  for (int i = 0; i < 4; ++i) { float x; asm("v_add_f32 %0, %1, %2" : "=v"(x) : "v"(a[i]), "v"(b[i])); r[i] = x; }
  return r;
}
__device__ __forceinline__ float xsum32(float x) {
  unsigned u = __builtin_bit_cast(unsigned, x), v = u;
  asm("" : "+v"(v));
  auto r = __builtin_amdgcn_permlane32_swap(u, v, false, false);
  const unsigned a = r[0], b = r[1];
  return __builtin_bit_cast(float, a) + __builtin_bit_cast(float, b);
}
__device__ __forceinline__ float xsum16(float x) {
  unsigned u = __builtin_bit_cast(unsigned, x), v = u;
  asm("" : "+v"(v));
  auto r = __builtin_amdgcn_permlane16_swap(u, v, false, false);
  const unsigned a = r[0], b = r[1];
  return __builtin_bit_cast(float, a) + __builtin_bit_cast(float, b);
}
__device__ __forceinline__ float xsum8(float x) {
  return x + __builtin_bit_cast(float, __builtin_amdgcn_update_dpp(0, __builtin_bit_cast(int, x), 0x128, 0xf, 0xf, false));
}
__device__ __forceinline__ f32x2 phi2(f32x2 t) {
  const f32x2 u = t * 1.4426950408889634f, v = t + 1e-6f;
  const f32x2 e = {__builtin_amdgcn_fmed3f(__builtin_amdgcn_exp2f(u[0]), 0.f, 1.f), __builtin_amdgcn_fmed3f(__builtin_amdgcn_exp2f(u[1]), 0.f, 1.f)};
  return f32x2{fmaxf(v[0], 1e-6f), fmaxf(v[1], 1e-6f)} + e;
}
__device__ __forceinline__ f32x2 gelu2(f32x2 x) {
  const f32x2 u = x * (x * x * -0.1029432397f + -2.302208198f);
  const f32x2 d = f32x2{__builtin_amdgcn_exp2f(u[0]), __builtin_amdgcn_exp2f(u[1])} + 1.f;
  return x * f32x2{__builtin_amdgcn_rcpf(d[0]), __builtin_amdgcn_rcpf(d[1])};
}

__global__ void __launch_bounds__(512, 2)
fused_block(const unsigned short* __restrict__ x, const unsigned short* __restrict__ wqkv, unsigned char* ws, const unsigned short* __restrict__ wo,
            const unsigned short* __restrict__ w1, const unsigned short* __restrict__ b1, const unsigned short* __restrict__ w2,
            const unsigned short* __restrict__ b2, unsigned short* __restrict__ out) {
  __shared__ __attribute__((aligned(16))) char smem[LDS_BYTES];
  unsigned long long pc0;
  asm volatile("s_getpc_b64 %0" : "=s"(pc0));
  LAS char* L = (LAS char*)smem;
  const unsigned lds0 = (unsigned)(size_t)L;
  const int tid = threadIdx.x, lane = tid & 63, w = __builtin_amdgcn_readfirstlane(tid >> 6);
  const int b = blockIdx.x, xcd = b & 7, bi = b >> 3;
  const int g = lane >> 4, t = lane & 15;
  const __amdgpu_buffer_rsrc_t rs = __builtin_amdgcn_make_buffer_rsrc((void*)ws, 0, WS_BYTES, 0x00020000);
  gu32* flags = (gu32*)(ws + WS_FLAG);
#define FLAG(set, grp) (flags + ((set) * FLAG_SET + (grp) * 256) / 4)
  asm volatile("" :: "s"(x), "s"(wqkv), "s"(ws), "s"(wo), "s"(w1), "s"(b1), "s"(w2), "s"(b2));
  const int r128 = 4 * (xcd >> 1) + (bi >> 3), c16 = 8 * (xcd & 1) + (bi & 7);
  const int r64 = 8 * (xcd >> 1) + (bi >> 2), c8 = 4 * (xcd & 1) + (bi & 3);

  const __amdgpu_buffer_rsrc_t rx = __builtin_amdgcn_make_buffer_rsrc((void*)x, 0, S * D * 2, 0x00020000);
  const int hw = w - 4, ht = tid - 256;
  volatile LAS unsigned* sig = (volatile LAS unsigned*)(L + L_CS + 1024);
  volatile LAS v4u* prog = (volatile LAS v4u*)(L + L_CS + 1088);
#define PROG(n) do { asm volatile("" ::: "memory"); sig[16 + w] = (n); } while (0)

  const __amdgpu_buffer_rsrc_t rq = __builtin_amdgcn_make_buffer_rsrc((void*)wqkv, 0, D * 3 * D * 2, 0x00020000);
  v4u tv[12];
  unsigned ep;
  const int q10 = lane / 10, c10 = lane % 10;
  if (w < 4) {
#pragma unroll
    for (int j = 0; j < 3; ++j)
      tv[j] = __builtin_amdgcn_raw_buffer_load_b128(rx, (lane & 7) * 1280 + (lane >> 3) * 16, (j < 2 || w < 2) ? 8 * b * 1280 + (w + 4 * j) * 128 : OOB, 0);
    ep = __hip_atomic_load((gu32*)(ws + WS_EPOCH), RLX_AGENT);
  } else if (w == 4) {
    const int vo = sel(lane < 40, q10 * 3840 + (c10 < 5 ? 1280 : 2480) + c10 * 16);
#pragma unroll
    for (int j = 0; j < 10; ++j) tv[j] = __builtin_amdgcn_raw_buffer_load_b128(rq, vo, (40 * r128 + 4 * j) * 3840 + c16 * 80, 0);
  } else if (w == 7) {
    *(LAS v4u*)(L + L_CS + 512 + lane * 16) = v4u{0u, 0u, 0u, 0u};
  }
  BAR();

  if (w < 4) {
  unsigned tag = ep;
  const int grp32 = 4 * r128 + w;
  constexpr int AW = 6;

  {
    const int tok = 8 * b + (lane & 7), cl = lane >> 3;
    const bool has3 = w < 2;
    int xd = ((tok >> 4) * 20 + 2 * w + (cl >> 2)) * 1024 + ((cl & 3) * 16 + (tok & 15)) * 16;
    PIN(xd);
    SCHED();
    f32x2 f[12];
#pragma unroll
    for (int j = 0; j < 3; ++j)
#pragma unroll
      for (int e = 0; e < 4; ++e) f[j * 4 + e] = bf2(tv[j][e]);
    f32x2 s2 = (f[0] + f[1]) + (f[2] + f[3]) + ((f[4] + f[5]) + (f[6] + f[7])) + ((f[8] + f[9]) + (f[10] + f[11]));
    const float mloc = xsum32(xsum16(xsum8(s2[0] + s2[1]))) * (has3 ? 1.f / 192.f : 1.f / 128.f);
    const float m3 = has3 ? mloc : 0.f;
    f32x2 q2 = {0.f, 0.f}, q3 = {0.f, 0.f};
#pragma unroll
    for (int j = 0; j < 12; j += 2) {
      const f32x2 d0 = f[j] - (j < 8 ? mloc : m3), d1 = f[j + 1] - (j < 8 ? mloc : m3);
      q2 += d0 * d0; q3 += d1 * d1;
    }
    q2 += q3;
    const float m2loc = xsum32(xsum16(xsum8(q2[0] + q2[1])));
    if (lane < 8) *(volatile LAS f32x4*)(L + L_CS + 512 + (lane * 4 + w) * 16) = f32x4{mloc, m2loc, 1.f, 1.f};
    f32x4 pr[4];
#pragma unroll 1
    for (unsigned spins = 0; spins < 64 * kSpinCap; ++spins) {
      bool ok = true;
#pragma unroll
      for (int ww = 0; ww < 4; ++ww) {
        pr[ww] = *(volatile LAS f32x4*)(L + L_CS + 512 + ((lane & 7) * 4 + ww) * 16);
        ok = ok && pr[ww][2] == 1.f;
      }
      if (__all(ok)) break;
    }
    const f32x4 p01 = {pr[0][0], pr[0][1], pr[1][0], pr[1][1]}, p23 = {pr[2][0], pr[2][1], pr[3][0], pr[3][1]};
    const float mean = (p01[0] + p01[2]) * 0.3f + (p23[0] + p23[2]) * 0.2f;
    const float e0 = p01[0] - mean, e1 = p01[2] - mean, e2 = p23[0] - mean, e3 = p23[2] - mean;
    const float m2 = (p01[1] + p01[3]) + (p23[1] + p23[3]) + 192.f * (e0 * e0 + e1 * e1) + 128.f * (e2 * e2 + e3 * e3);
    const float rstd = __builtin_amdgcn_rsqf(m2 * (1.f / D) + LN_EPS);
#pragma unroll
    for (int j = 0; j < 3; ++j) {
      v4u o;
#pragma unroll
      for (int e = 0; e < 4; ++e) o[e] = pk2((f[j * 4 + e] - mean) * rstd);
      st128(o, rs, (j < 2 || has3) ? xd : OOB, WS_XN + j * 8192);
    }
    PIN(tag); tag += 1u;
    publish(FLAG(0, b >> 2) + (b & 3) * 4 + w, tag, lane);
    if (w == 0) sig[6] = 1u;
  }

  v4u xr[3];
  {
    wait_flag(FLAG(0, grp32) + lane, 16, tag, lane);
    v4u a[20][2];
    const int abase = WS_XN + 2 * grp32 * 20 * 1024;
#pragma unroll
    for (int ks = 0; ks < AW; ++ks)
#pragma unroll
      for (int m = 0; m < 2; ++m) a[ks][m] = ld128(rs, lane * 16, abase + (m * 20 + ks) * 1024);
    SCHED();
    BAR_RAW();
    {
      f32x4 acc[2][5];
      const LBase bp(lds0 + L_BKV + (lane >> 2) * 160 + (lane & 3) * 8);
      kloop<2, 5, 20, 1, false>(acc, [&](int ks, int s, int h) { return bp.rd(ks * 5120 + h * 2560 + s * 32); },
        [&](int ks, int m) { return a[ks][m]; },
        [&](int ks) {
#pragma unroll
          for (int j = ld_hi(ks - 1, AW, AW, 20); j < ld_hi(ks, AW, AW, 20); ++j)
#pragma unroll
            for (int m = 0; m < 2; ++m) a[j][m] = ld128(rs, lane * 16, abase + (m * 20 + j) * 1024);
          if (ks == 3 || ks == 10) BAR_RAW();
          if (ks == 14) PROG(1);
        });
      v4u ka[3], vb[3];
#pragma unroll
      for (int s = 0; s < 3; ++s)
#pragma unroll
        for (int m = 0; m < 2; ++m) {
          vb[s][2 * m] = pk2(lo2(acc[m][s + 2])); vb[s][2 * m + 1] = pk2(hi2(acc[m][s + 2]));
        }
#pragma unroll
      for (int e = 0; e < 4; ++e) vb[0][e] = t < 8 ? 0x3F803F80u : vb[0][e];
      const f32x4 z4 = {0.f, 0.f, 0.f, 0.f};
      LAS f32x4* red = (LAS f32x4*)(L + L_RED) + w * 512 + lane;
#pragma unroll
      for (int s = 0; s < 3; ++s) {
#pragma unroll
        for (int m = 0; m < 2; ++m) { ka[s][2 * m] = pk2(phi2(lo2(acc[m][s]))); ka[s][2 * m + 1] = pk2(phi2(hi2(acc[m][s]))); }
#pragma unroll
        for (int sb = 0; sb < 3; ++sb)
          if (s > 0 || sb < 2) red[(3 * s + sb - (s > 0)) * 64] = mfma(ka[s], vb[sb], z4);
      }
      BAR();
    }
    f32x4 qa[2][3];
    {
      unsigned bp0 = lds0 + L_BQ + (lane >> 2) * 64 + (lane & 3) * 8 + (g & 1) * 32;
      unsigned bp1 = lds0 + L_BQ + (lane >> 2) * 64 + (lane & 3) * 8 + 32 - (g & 1) * 32;
      unsigned bp2 = lds0 + L_BQ + 2048 + (lane >> 2) * 16 + (lane & 1) * 8;
      PIN(bp0); PIN(bp1); PIN(bp2);
      kloop<2, 3, 20, 2, true>(qa,
        [&](int ks, int s, int h) {
          return s == 0 ? rdtr(lp(bp0) + ks * 2560 + h * 1024) : s == 1 ? rdtr(lp(bp1) + ks * 2560 + h * 1024) : rdtr(lp(bp2) + ks * 2560 + h * 256);
        },
        [&](int ks, int m) { return a[ks][m]; },
        [&](int ks) {});
    }
    v4u pq0[2], pq1[2];
#pragma unroll
    for (int m = 0; m < 2; ++m) {
      pq0[m].x = pk2(phi2(lo2(qa[m][0]))); pq0[m].y = pk2(phi2(hi2(qa[m][0])));
      pq0[m].z = pk2(phi2(lo2(qa[m][1]))); pq0[m].w = pk2(phi2(hi2(qa[m][1])));
      const unsigned lo = pk2(phi2(lo2(qa[m][2]))), hi = pk2(phi2(hi2(qa[m][2])));
      pq1[m].x = g < 2 ? lo : 0u; pq1[m].y = g < 2 ? hi : 0u; pq1[m].z = 0u; pq1[m].w = 0u;
      PIN(pq0[m]); PIN(pq1[m]);
    }
    BAR_RAW();
    {
      v4u kv[4][2];
#pragma unroll
      for (int ks = 0; ks < 2; ++ks)
#pragma unroll
        for (int es = 3; es >= ks; --es) kv[es][ks] = *(LAS v4u*)(L + L_KVF + (es * 2 + ks) * 1024 + lane * 16);
      SCHED();
      f32x4 o[2][4];
#pragma unroll
      for (int es = 3; es >= 0; --es)
#pragma unroll
        for (int m = 0; m < 2; ++m) {
          o[m][es] = mfma(kv[es][0], pq0[m], f32x4{0.f, 0.f, 0.f, 0.f});
          SCHED();
        }
#pragma unroll
      for (int es = 3; es >= 1; --es)
#pragma unroll
        for (int m = 0; m < 2; ++m) {
          o[m][es] = mfma(kv[es][1], pq1[m], o[m][es]);
          SCHED();
        }
#pragma unroll
      for (int m = 0; m < 2; ++m) {
        const float i0 = __builtin_amdgcn_rcpf(o[m][3][0] + 1e-6f), i1 = __builtin_amdgcn_rcpf(o[m][3][1] + 1e-6f);
        const float im = g == 0 ? i0 : i1;
        v4u ov;
        ov.x = pk2(lo2(o[m][0]) * i0); ov.y = pk2(hi2(o[m][0]) * i0);
        ov.z = pk2(lo2(o[m][1]) * im); ov.w = pk2(hi2(o[m][1]) * im);
        v2u ol; ol.x = pk2(lo2(o[m][2]) * i1); ol.y = pk2(hi2(o[m][2]) * i1);
        const int tt = 2 * grp32 + m;
        st128(ov, rs, lane * 16, WS_O + (tt * 20 + c16) * 1024);
        st64(ol, rs, g < 2 ? lane * 8 : OOB, WS_O + (tt * 20 + 16) * 1024 + c16 * 256);
      }
      publish(FLAG(2, grp32) + c16, tag, lane);
    }
  }

  {
    const int tt = 4 * r64 + w;
#pragma unroll
    for (int j = 0; j < 3; ++j)
      xr[j] = __builtin_amdgcn_raw_buffer_load_b128(rx, sel(j < 2 || g < 2, t * 1280 + g * 16), (4 * r64 + w) * 16 * 1280 + c8 * 160 + j * 64, 0);
    v4u eye[2];
#pragma unroll
    for (int hf = 0; hf < 2; ++hf) {
      const int e = 16 * hf + t - 8 * g;
#pragma unroll
      for (int d = 0; d < 4; ++d) eye[hf][d] = e == 2 * d ? 0x3F80u : e == 2 * d + 1 ? 0x3F800000u : 0u;
      PIN(eye[hf]);
    }
    f32x4 acc[1][5];
    wait_flag(FLAG(2, tt >> 1) + lane, 16, tag, lane);
    if (b == 0 && tid == 0) __hip_atomic_store((gu32*)(ws + WS_EPOCH), tag, RLX_AGENT);
    v4u a[20];
#pragma unroll
    for (int ks = 0; ks < 2 * AW; ++ks) a[ks] = ld128(rs, lane * 16, WS_O + (tt * 20 + ks) * 1024);
    SCHED();
#pragma unroll
    for (int s = 0; s < 5; ++s) { acc[0][s] = mfma(eye[s & 1], xr[s >> 1], f32x4{0.f, 0.f, 0.f, 0.f}); PIN(acc[0][s]); }
    const LBase bp(lds0 + L_WO + (lane >> 2) * 160 + (lane & 3) * 8);
    kloop<1, 5, 20, 1, true, 1>(acc, [&](int ks, int s, int h) { return bp.rd(ks * 5120 + h * 2560 + s * 32); },
      [&](int ks, int m) { return a[ks]; },
      [&](int ks) {
#pragma unroll
        for (int j = ld_hi(ks - 1, 2 * AW, 2 * AW, 20); j < ld_hi(ks, 2 * AW, 2 * AW, 20); ++j)
          a[j] = j < 16 ? ld128(rs, lane * 16, WS_O + (tt * 20 + j) * 1024) : ld_split(rs, lane, WS_O + (tt * 20 + j) * 1024);
        if (ks == 8) PROG(2);
        if (ks == 14) PROG(3);
      });
    PROG(4);
    v2u hb[5];
    f32x2 hv[10];
#pragma unroll
    for (int s = 0; s < 5; ++s) {
      hb[s].x = pk2(lo2(acc[0][s]));
      hb[s].y = pk2(hi2(acc[0][s]));
      hv[2 * s] = bf2(hb[s].x); hv[2 * s + 1] = bf2(hb[s].y);
    }
    st128(v4u{hb[0].x, hb[0].y, hb[1].x, hb[1].y}, rs, lane * 16, WS_H + (tt * 20 + 2 * c8) * 1024);
    st128(v4u{hb[2].x, hb[2].y, hb[3].x, hb[3].y}, rs, lane * 16, WS_H + (tt * 20 + 2 * c8 + 1) * 1024);
    st64(hb[4], rs, lane * 8, WS_H + (tt * 20 + 16) * 1024 + c8 * 512);
    const f32x2 s2 = ((hv[0] + hv[1]) + (hv[2] + hv[3])) + ((hv[4] + hv[5]) + (hv[6] + hv[7])) + (hv[8] + hv[9]);
    const float mc = xsum32(xsum16(s2[0] + s2[1])) * (1.f / 80.f);
    f32x2 q2 = {0.f, 0.f}, q3 = {0.f, 0.f};
#pragma unroll
    for (int j = 0; j < 10; j += 2) { const f32x2 d0 = hv[j] - mc, d1 = hv[j + 1] - mc; q2 += d0 * d0; q3 += d1 * d1; }
    q2 += q3;
    const float m2 = xsum32(xsum16(q2[0] + q2[1]));
    v2u sv = {__builtin_bit_cast(unsigned, mc), __builtin_bit_cast(unsigned, m2)};
    PIN(sv);
    publish(FLAG(3, tt >> 1) + (tt & 1) * 8 + c8, tag, lane);
    st64(sv, rs, g == 0 ? t * 8 : OOB, WS_ST + (tt * 8 + c8) * 128);
  }

  {
    gu32* fst = FLAG(3, 2 * r64 + (w >> 1)) + 32 + (w & 1) * 8 + c8;
    PIN(fst);
    wait_flag(FLAG(3, grp32) + lane, 16, tag, lane);
    DRAIN();
    if (lane == 0) flag_set(fst, tag);
    v4u a[20][2];
    const int abase = WS_H + 2 * grp32 * 20 * 1024;
#pragma unroll
    for (int ks = 0; ks < AW; ++ks)
#pragma unroll
      for (int m = 0; m < 2; ++m) a[ks][m] = ld128(rs, lane * 16, abase + (m * 20 + ks) * 1024);
    SCHED();
    float nm[2], rr[2];
    BAR_RAW();
    const unsigned l64 = lds0 + (lane >> 2) * 64 + (lane & 3) * 8;
    const LBase bp0(l64 + (g & 1) * 32), bp1(l64 + 32 - (g & 1) * 32);
    const int h1base = WS_H1 + 2 * grp32 * 48 * 1024;
    auto finish = [&](const f32x4& c0, const f32x4& c1, int j, int m) {
      v4u o;
#pragma unroll
      for (int s = 0; s < 2; ++s) {
        const f32x4 cs = *(LAS f32x4*)(L + L_CS + ((2 * j + s) * 4 + g) * 16), bb = *(LAS f32x4*)(L + L_B1 + ((2 * j + s) * 4 + g) * 16);
        const f32x4 c = s ? c1 : c0;
        o[2 * s] = pk2(gelu2(lo2(c) * rr[m] + (lo2(cs) * nm[m] + lo2(bb))));
        o[2 * s + 1] = pk2(gelu2(hi2(c) * rr[m] + (hi2(cs) * nm[m] + hi2(bb))));
      }
      st128(o, rs, lane * 16, h1base + (m * 48 + (c16 >> 3) * 24 + (j < 2 ? 2 * (c16 & 7) + j : 16 + (c16 & 7))) * 1024);
    };
    {
      f32x4 acc[2][4];
      kloop<2, 4, 20, 1, true>(acc, [&](int ks, int s, int h) { return ((s & 1) ? bp1 : bp0).rd(L_W1 + (s >> 1) * 40960 + ks * 2048 + h * 1024); },
        [&](int ks, int m) { return a[ks][m]; },
        [&](int ks) {
#pragma unroll
          for (int j = ld_hi(ks - 1, AW, AW, 20); j < ld_hi(ks, AW, AW, 20); ++j)
#pragma unroll
            for (int m = 0; m < 2; ++m)
              a[j][m] = j < 16 ? ld128(rs, lane * 16, abase + (m * 20 + j) * 1024) : ld_split(rs, lane, abase + (m * 20 + j) * 1024);
          if (ks == 20 - AW) PROG(5);
        });
      PROG(6);
      sig_wait(sig + 12 + w);
#pragma unroll
      for (int m = 0; m < 2; ++m) {
        const f32x2 u = *(LAS f32x2*)(L + L_KVF + ((2 * w + m) * 16 + t) * 8);
        rr[m] = u[0]; nm[m] = u[1];
      }
#pragma unroll
      for (int j = 0; j < 2; ++j)
#pragma unroll
        for (int m = 0; m < 2; ++m) finish(acc[m][2 * j], acc[m][2 * j + 1], j, m);
      SCHED();
    }
    f32x4 accb[2][2];
    const int kh = w & 1, tt = 4 * r64 + w;
    v4u hr01, hr23;
    v2u hr4;
    kloop<2, 2, 20, 3, true>(accb, [&](int ks, int s, int h) { return (s ? bp1 : bp0).rd(L_W1 + 81920 + ks * 2048 + h * 1024); },
      [&](int ks, int m) { return a[ks][m]; },
      [&](int ks) {
        if (ks == 8) { publish(FLAG(4, grp32) + c16, tag, lane); PROG(7); }
        if (ks == 12) PROG(8);
        if (ks == 10) {
          hr01 = ld128(rs, lane * 16, WS_H + (tt * 20 + 2 * c8) * 1024);
          hr23 = ld128(rs, lane * 16, WS_H + (tt * 20 + 2 * c8 + 1) * 1024);
          hr4 = ld64(rs, lane * 8, WS_H + (tt * 20 + 16) * 1024 + c8 * 512);
        }
      });
    PROG(9);
    f32x4 acc[2][5];
    {
      const unsigned hrr[10] = {hr01.x, hr01.y, hr01.z, hr01.w, hr23.x, hr23.y, hr23.z, hr23.w, hr4.x, hr4.y};
#pragma unroll
      for (int s = 0; s < 5; ++s) {
        acc[0][s] = *(LAS f32x4*)(L + L_B2 + (s * 4 + g) * 16) + f32x4{bf_lo(hrr[2 * s]), bf_hi(hrr[2 * s]), bf_lo(hrr[2 * s + 1]), bf_hi(hrr[2 * s + 1])};
        PIN(acc[0][s]);
      }
    }
    int oo[3];
    unsigned tw, tr;
    {
      int ln = lane;
      PIN(ln);
      const int qq = ln / 10, cc = ln % 10;
#pragma unroll
      for (int j = 0; j < 3; ++j) { oo[j] = sel(ln < (j < 2 ? 60 : 40), ((tt * 16 + 6 * j + qq) * D + 80 * c8) * 2 + cc * 16); PIN(oo[j]); }
      tw = lds0 + L_W2 + 122880 + w * 3072 + t * 176 + g * 8; tr = lds0 + L_W2 + 122880 + w * 3072 + qq * 176 + cc * 16;
      PIN(tw); PIN(tr);
    }
    BAR_RAW();
    constexpr int WIN = 8;
    v4u ar[WIN][2];
    const int a4[2] = {WS_H1 + (tt * 48 + kh * 24) * 1024, WS_H1 + ((tt ^ 1) * 48 + kh * 24) * 1024};
#pragma unroll
    for (int ks = 0; ks < WIN - 1; ++ks)
#pragma unroll
      for (int m = 0; m < 2; ++m) ar[ks][m] = ld128(rs, lane * 16, a4[m] + ks * 1024);
    SCHED();
#pragma unroll
    for (int m = 0; m < 2; ++m) finish(accb[m][0], accb[m][1], 2, m);
    const LBase bp(lds0 + L_W2 + kh * 5120 + (lane >> 2) * 160 + (lane & 3) * 8);
    kloop<2, 5, 24, 1, true, 1>(acc, [&](int ks, int s, int h) { return bp.rd((ks % 15) * 10240 + h * 2560 + s * 32); },
      [&](int ks, int m) { return ar[ks % WIN][m]; },
      [&](int ks) {
        if (ks == 5) PROG(10);
        if (ks == 9 || ks == 14 || ks == 19) BAR_RAW();
        if (ks == 2) publish(FLAG(10, grp32) + c16, tag, lane);
        if (ks >= 2) {
#pragma unroll
          for (int j = ks == 2 ? WIN - 1 : ld_hi(ks - 1, 3, WIN - 1, 24); j < ld_hi(ks, 3, WIN - 1, 24); ++j)
#pragma unroll
            for (int m = 0; m < 2; ++m) ar[j % WIN][m] = ld128(rs, lane * 16, a4[m] + j * 1024);
        }
      });
    LAS f32x4* xch = (LAS f32x4*)(L + L_W2 + 102400);
#pragma unroll
    for (int s = 0; s < 5; ++s) xch[((w ^ 1) * 5 + s) * 64 + lane] = acc[1][s];
    BAR();
    const __amdgpu_buffer_rsrc_t rout = __builtin_amdgcn_make_buffer_rsrc((void*)out, 0, S * D * 2, 0x00020000);
    f32x4 ot[5];
#pragma unroll
    for (int s = 0; s < 5; ++s) ot[s] = xch[(w * 5 + s) * 64 + lane];
    SCHED();
#pragma unroll
    for (int s = 0; s < 5; ++s) {
      const f32x4 v = acc[0][s] + ot[s];
      *(LAS v2u*)(lp(tw) + s * 32) = v2u{pk2(lo2(v)), pk2(hi2(v))};
    }
    asm volatile("" ::: "memory");
    v4u ov[3];
#pragma unroll
    for (int j = 0; j < 3; ++j) ov[j] = *(LAS v4u*)(lp(tr) + j * 6 * 176);
#pragma unroll
    for (int j = 0; j < 3; ++j) __builtin_amdgcn_raw_buffer_store_b128(ov[j], rout, oo[j], 0, 0);
  }
  return;
  }

  {
    const v4u srd_ws = mk_srd(ws, WS_BYTES);
    const int q5 = lane / 5, c5 = lane % 5;
    const int f40m = c5 < 4 ? 512 : 128, f40l = c5 < 4 ? q5 * 64 + (c5 ^ (2 * ((q5 >> 2) & 1))) * 16 : 2048 + q5 * 16;
    v2u ub1, ub2;
    unsigned pf0;
    v4u tq[10];
    if (hw == 1) {
      const __amdgpu_buffer_rsrc_t ro = __builtin_amdgcn_make_buffer_rsrc((void*)wo, 0, D * D * 2, 0x00020000);
      const int vq = sel(lane < 40, (8 * (q5 >> 2) + (q5 & 3)) * 3840 + c5 * 16), vo = sel(lane < 40, q10 * 1280 + c10 * 16);
#pragma unroll
      for (int j = 0; j < 5; ++j) {
        const int i8 = 5 * r128 + j, k = 32 * (i8 >> 2) + 16 * (i8 & 1) + 4 * ((i8 >> 1) & 1);
        tq[j] = __builtin_amdgcn_raw_buffer_load_b128(rq, vq, k * 3840 + c16 * 80, 0);
      }
#pragma unroll
      for (int j = 0; j < 5; ++j) tq[5 + j] = __builtin_amdgcn_raw_buffer_load_b128(ro, vo, (20 * r64 + 4 * j) * 1280 + c8 * 160, 0);
    } else if (hw == 2) {
      const GAS unsigned char* cb = (const GAS unsigned char*)(pc0 & ~127ull);
      pf0 = *(const GAS unsigned*)(cb + min(lane * 32 + bi, 346) * 128);
      if (lane < 6) {
        gu32* fp = FLAG(3 * (b >> 6) + (lane >> 1), b & 63) + (lane & 1) * 32;
        asm volatile("global_atomic_add %0, %1, off sc1" :: "v"(fp), "v"(0u) : "memory");
      }
    } else if (hw == 3) {
      const __amdgpu_buffer_rsrc_t rb1 = __builtin_amdgcn_make_buffer_rsrc((void*)b1, 0, F * 2, 0x00020000);
      const __amdgpu_buffer_rsrc_t rb2 = __builtin_amdgcn_make_buffer_rsrc((void*)b2, 0, D * 2, 0x00020000);
      ub1 = __builtin_amdgcn_raw_buffer_load_b64(rb1, lane * 8, c16 * 192, 0);
      ub2 = __builtin_amdgcn_raw_buffer_load_b64(rb2, lane * 8, c8 * 160, 0);
    }
    unsigned tag = __hip_atomic_load((gu32*)(ws + WS_EPOCH), RLX_AGENT);
    if (hw == 0) {
      const int dof = sel(lane < 40, lane * 16);
#pragma unroll
      for (int j = 0; j < 10; ++j) {
        const int k = 40 * r128 + 4 * j, R = (k & ~31) + 16 * ((k >> 2) & 1) + 4 * ((k >> 3) & 3);
        st128(tv[j], rs, dof, WS_TKV + c16 * 153600 + R * 160);
      }
      PIN(tag); tag += 1u;
      publish(FLAG(6, c16) + r128, tag, lane);
    }
    asm volatile("" ::: "memory");
    int red_off, kvf_off, kvp_off;
    auto items = [&]() {
      const int it = ht < 210 ? ht : 209, ie = it / 5, ic = it % 5;
      const int ihead = ie < 40 ? (ie >= 20) : ie - 40;
      const int id = 20 * ihead + 4 * ic, dg = id >> 2;
      const int sa = id >> 4, col = 40 + ie;
      const int tile = 3 * sa + (ie < 40 ? (col >> 4) - 2 : 0) - (sa > 0);
      red_off = lds0 + L_RED + (tile * 64 + (dg & 3) * 16 + (ie < 40 ? (col & 15) : 0)) * 16;
      kvf_off = lds0 + L_KVF + ((ie < 40 ? (ie >> 4) : 3) * 2 + (dg >> 3)) * 1024 + ((dg & 3) * 16 + (ie < 40 ? (ie & 15) : ihead)) * 16 + ((dg >> 2) & 1) * 8;
      kvp_off = WS_KVP + (c16 * 16 + r128) * 4096 + ht * 16;
      PIN(red_off); PIN(kvf_off); PIN(kvp_off);
    };
    if (hw != 0) items();
    int vo12, td[12];
    if (hw >= 2) {
      const int q12 = lane / 12, c12 = lane % 12;
      vo12 = hw == 2 ? sel(lane < 48, q12 * 3072 + c12 * 16) : sel(lane < 40, q10 * 1280 + c10 * 16);
#pragma unroll
      for (int j = 0; j < 12; ++j) {
        const int cc = r128 >> 1, off = 40 * (r128 & 1) + 4 * j;
        const int R = off < 64 ? 64 * cc + off : 32 * (16 + (cc >> 1)) + 16 * (cc & 1) + off - 64;
        const int d1 = WS_T1 + c16 * 122880 + (c12 >> 2) * 40960 + (R + q12) * 64 + ((c12 & 3) ^ (2 * ((R >> 2) & 1))) * 16;
        const int pc = r64 >> 1, ko = 48 * (r64 & 1) + 4 * j, jj = ko >> 5;
        const int d2 = WS_T2 + c8 * 245760 + (2 * (jj < 2 ? 2 * (pc & 7) + jj : 16 + (pc & 7)) + (pc >> 3)) * 5120 + (ko & 31) * 160 + lane * 16;
        td[j] = sel(hw == 2 ? (lane < 48 && j < 10) : lane < 40, hw == 2 ? d1 : d2);
        PIN(td[j]);
      }
      PIN(vo12);
    }
    int l16 = lane * 16, h16 = sel(ht < 210, ht * 16);
    PIN(l16); PIN(h16);
    gu32* fp1 = FLAG(1, c16) + lane;
    gu32* fp78 = lane < 32 ? FLAG(7, c8) + lane : FLAG(8, c16) + t;
    gu32* fp4 = FLAG(4, 2 * r64 + (g & 1)) + t;
    gu32* fp1w = FLAG(1, c16) + r128 * 4 + hw;
    gu32* fps = FLAG(3, 4 * r128 + hw) + 32 + lane;
    int sto = g * 256 + t * 8;
    PIN(fp1); PIN(fp78); PIN(fp4); PIN(fp1w); PIN(fps); PIN(sto);
    ((LAS v4u*)(L + L_KVF))[ht] = v4u{0u, 0u, 0u, 0u};
    ((LAS v4u*)(L + L_KVF))[256 + ht] = v4u{0u, 0u, 0u, 0u};
    asm volatile("" ::: "memory");
    if (hw != 0) {
      if (hw == 1) {
        sig_wait(sig + 6);
#pragma unroll
        for (int j = 0; j < 5; ++j) {
          const int i8 = 5 * r128 + j;
          st128(tq[j], rs, sel(lane < 40, (i8 & 3) * f40m + f40l), WS_TKV + c16 * 153600 + 102400 + (i8 >> 2) * 2560);
        }
#pragma unroll
        for (int j = 0; j < 5; ++j) {
          const int pp = r64 >> 1, e = 20 * (r64 & 1) + 4 * j;
          const int R = e < 32 ? 32 * pp + e : 32 * (16 + (pp >> 2)) + 8 * (pp & 3) + e - 32;
          st128(tq[5 + j], rs, sel(lane < 40, lane * 16), WS_TO + c8 * 102400 + R * 160);
        }
      }
      if (hw == 3) {
        if (lane < 24) *(LAS f32x4*)(L + L_B1 + lane * 16) = f32x4{bf_lo(ub1.x), bf_hi(ub1.x), bf_lo(ub1.y), bf_hi(ub1.y)};
        if (lane < 20) *(LAS f32x4*)(L + L_B2 + lane * 16) = f32x4{bf_lo(ub2.x), bf_hi(ub2.x), bf_lo(ub2.y), bf_hi(ub2.y)};
      }
      if (hw == 2) asm volatile("" :: "v"(pf0));
      PIN(tag); tag += 1u;
      DRAIN();
      if (hw == 1 && lane < 2) flag_set(lane == 0 ? FLAG(8, c16) + r128 : FLAG(7, c8) + r64, tag);
    }
    if (hw == 0) {
      wait_flag(FLAG(6, c16) + lane, 16, tag, lane);
      sig[0] = 1u;
    }
    sig_wait(sig);
    const int kvq = WS_TKV + c16 * 153600, wos = WS_TO + c8 * 102400;
    dma_image<0, 24>(lds0 + L_BKV, l16, srd_ws, kvq, hw, 3 * (bi >> 3));
    dma_image<24, 56>(lds0 + L_BKV, l16, srd_ws, kvq, hw, 4 * (bi >> 3));
    asm volatile("s_waitcnt vmcnt(8)" ::: "memory");
    BAR_RAW();
    dma_image<56, 100>(lds0 + L_BKV, l16, srd_ws, kvq, hw, 5 * (bi >> 3));
    asm volatile("s_waitcnt vmcnt(10)" ::: "memory");
    BAR_RAW();
    if (hw == 1) wait_flag(fp78, 48, tag, lane);
    DRAIN();
    BAR_RAW();
    dma_image<100, 150>(lds0 + L_BKV, l16, srd_ws, kvq, hw, 6 * (bi >> 3));
    if (hw == 0) items();
    prog_wait(prog, 1);
    if (hw >= 2) {
      const __amdgpu_buffer_rsrc_t r12 = __builtin_amdgcn_make_buffer_rsrc((void*)(hw == 2 ? w1 : w2), 0, D * F * 2, 0x00020000);
      const int base = hw == 2 ? r128 * 122880 + c16 * 192 : r64 * 61440 + c8 * 160, step = hw == 2 ? 4 * 3072 : 4 * 1280;
#pragma unroll
      for (int j = 0; j < 12; ++j) tv[j] = __builtin_amdgcn_raw_buffer_load_b128(r12, vo12, (j < 10 || hw == 3) ? base + j * step : OOB, 0);
      asm volatile("s_waitcnt vmcnt(12)" ::: "memory");
    } else {
      dma_image<32, 68, 2>(lds0 + L_WO, l16, srd_ws, wos, hw, bi >> 2);
      asm volatile("s_waitcnt vmcnt(18)" ::: "memory");
    }
    BAR();
    __builtin_amdgcn_s_setprio(2);
    {
      f32x4 pv[4];
#pragma unroll
      for (int ww = 0; ww < 4; ++ww) pv[ww] = *(LAS f32x4*)(lp(red_off) + ww * 8192);
      st128(__builtin_bit_cast(v4u, add4(add4(pv[0], pv[1]), add4(pv[2], pv[3]))), rs, kvp_off, 0);
      PROG(1);
      DRAIN();
#pragma unroll
      for (int j = 0; j < 12; ++j) PIN(tv[j]);
    }
    if (lane == 0) flag_set(fp1w, tag);
    if (hw == 0) {
      __builtin_amdgcn_s_sleep(8);
      wait_flag(fp1, 64, tag, lane);
      sig[2] = 1u;
    } else {
      __builtin_amdgcn_s_sleep(2);
      prog_wait(prog + 1, 1);
#pragma unroll
      for (int i = 0; i < 12; ++i) {
        if (hw >= 2) st128(tv[i], rs, td[i], 0);
        const int j = hw - 1 + 3 * i;
        if (j < 32) {
          const int pc = 2 * ((j + 4 * (bi >> 2)) & 31), po = (pc < 32 ? pc : pc + 36) * 1024;
          dma2(lds0 + L_WO + po, l16, srd_ws, wos + po);
        }
      }
      sig_wait(sig + 2);
    }
    {
      v4u pv[16];
#pragma unroll
      for (int r = 0; r < 16; ++r) pv[r] = ld128(rs, h16, WS_KVP + (c16 * 16 + r) * 4096);
      SCHED();
      f32x4 v = __builtin_bit_cast(f32x4, pv[0]);
#pragma unroll
      for (int r = 1; r < 16; ++r) v = add4(v, __builtin_bit_cast(f32x4, pv[r]));
      v2u o; o.x = pk2(lo2(v)); o.y = pk2(hi2(v));
      if (ht < 210) *(LAS v2u*)(lp(kvf_off)) = o;
      if (ht >= 200 && ht < 210) {
#pragma unroll
        for (int c = 1; c < 4; ++c) *(LAS v2u*)(lp(kvf_off) + c * 64) = o;
      }
    }
    DRAIN();
    BAR();
    __builtin_amdgcn_s_setprio(0);
    if (hw >= 2 && lane == 0) flag_set(hw == 2 ? FLAG(5, c16) + r128 : FLAG(11, c8) + r64, tag);
    if (hw == 2) {
      f32x4 s0 = {0.f, 0.f, 0.f, 0.f}, s1 = {0.f, 0.f, 0.f, 0.f};
#pragma unroll
      for (int j = 0; j < 10; ++j) {
        s0 = add4(s0, f32x4{bf_lo(tv[j].x), bf_hi(tv[j].x), bf_lo(tv[j].y), bf_hi(tv[j].y)});
        s1 = add4(s1, f32x4{bf_lo(tv[j].z), bf_hi(tv[j].z), bf_lo(tv[j].w), bf_hi(tv[j].w)});
      }
#pragma unroll
      for (int d = 24; d >= 12; d -= 12) {
        f32x4 u0, u1;
#pragma unroll
        for (int e = 0; e < 4; ++e) { u0[e] = __shfl(s0[e], lane + d); u1[e] = __shfl(s1[e], lane + d); }
        s0 = add4(s0, u0); s1 = add4(s1, u1);
      }
      f32x4 sv;
#pragma unroll
      for (int e = 0; e < 4; ++e) { const float a0 = __shfl(s0[e], lane >> 1), a1 = __shfl(s1[e], lane >> 1); sv[e] = (lane & 1) ? a1 : a0; }
      st128(__builtin_bit_cast(v4u, sv), rs, sel(lane < 24, lane * 16), WS_CS + (c16 * 16 + r128) * 384);
      publish(FLAG(9, c16) + r128, tag, lane);
    }
    if (hw == 1) {
      __builtin_amdgcn_s_sleep(40);
      wait_flag(lane < 32 ? FLAG(11, c8) + lane : FLAG(5, c16) + t, 48, tag, lane);
      sig[3] = 1u;
    }
    sig_wait(sig + 3);
    prog_wait(prog, 2);
    if (hw == 0) {
      wait_flag(FLAG(9, c16) + lane, 16, tag, lane);
      v4u pv[16];
      const int co = sel(lane < 24, l16);
#pragma unroll
      for (int r = 0; r < 16; ++r) pv[r] = ld128(rs, co, WS_CS + (c16 * 16 + r) * 384);
      SCHED();
      f32x4 v = __builtin_bit_cast(f32x4, pv[0]);
#pragma unroll
      for (int r = 1; r < 16; ++r) v = add4(v, __builtin_bit_cast(f32x4, pv[r]));
      if (lane < 24) *(LAS f32x4*)(L + L_CS + lane * 16) = v;
    } else {
      dma_image<70, 120, 3>(lds0 + L_W1, l16, srd_ws, WS_T1 + c16 * 122880, hw - 1, 6 * (bi >> 3));
      prog_wait(prog, 3);
      dma_image<0, 40, 3>(lds0 + L_W1, l16, srd_ws, WS_T1 + c16 * 122880, hw - 1, 5 * (bi >> 3));
      prog_wait(prog, 4);
      dma_image<40, 70, 3>(lds0 + L_W1, l16, srd_ws, WS_T1 + c16 * 122880, hw - 1, 3 * (bi >> 3));
    }
    DRAIN();
    BAR();
    {
      __builtin_amdgcn_s_setprio(2);
      __builtin_amdgcn_s_sleep(6);
      wait_flag(fps, 16, tag, lane);
      v2u s0[2], s1[2];
#pragma unroll
      for (int m = 0; m < 2; ++m) {
        s0[m] = ld64(rs, sto, WS_ST + (8 * r128 + 2 * hw + m) * 1024);
        s1[m] = ld64(rs, sto + 128, WS_ST + (8 * r128 + 2 * hw + m) * 1024);
      }
#pragma unroll
      for (int m = 0; m < 2; ++m) {
        const f32x2 p0 = __builtin_bit_cast(f32x2, s0[m]), p1 = __builtin_bit_cast(f32x2, s1[m]);
        const float a0 = p0[0], q0 = p0[1], a1 = p1[0], q1 = p1[1];
        float mean = xsum32(xsum16(a0 + a1)) * 0.125f;
        PIN(mean);
        float m2 = xsum32(xsum16(q0 + q1 + 80.f * ((a0 - mean) * (a0 - mean) + (a1 - mean) * (a1 - mean))));
        PIN(m2);
        const float var = m2 * (1.f / D);
        const float r1 = __builtin_amdgcn_rsqf(var + LN_EPS);
        const float rr = r1 * __builtin_amdgcn_rsqf(var * r1 * r1 + LN_EPS);
        if (g == 0) *(LAS f32x2*)(L + L_KVF + ((2 * hw + m) * 16 + t) * 8) = f32x2{rr, -mean * rr};
      }
      asm volatile("" ::: "memory");
      sig[12 + hw] = 1u;
      __builtin_amdgcn_s_setprio(0);
    }
    const int w2s = WS_T2 + c8 * 245760;
    prog_wait(prog, 5);
    dma_image<0, 30>(lds0 + L_W2, l16, srd_ws, w2s, hw, bi >> 2);
    prog_wait(prog, 6);
    dma_image<30, 70>(lds0 + L_W2, l16, srd_ws, w2s, hw, bi >> 2);
    prog_wait(prog, 7);
    if (hw == 0) {
      DRAIN();
      wait_flag(fp4, 32, tag, lane);
    } else {
      dma_image<70, 110, 3>(lds0 + L_W2, l16, srd_ws, w2s, hw - 1, bi >> 2);
      prog_wait(prog, 8);
      dma_image<110, 134, 3>(lds0 + L_W2, l16, srd_ws, w2s, hw - 1, bi >> 2);
      prog_wait(prog, 9);
      dma_image<134, 146, 3>(lds0 + L_W2, l16, srd_ws, w2s, hw - 1, 0);
      asm volatile("s_waitcnt vmcnt(12)" ::: "memory");
    }
    BAR_RAW();
    dma_image<146, 150>(lds0 + L_W2, l16, srd_ws, w2s, hw, 0);
    prog_wait(prog, 10);
    if (hw == 0) wait_flag(fp4 + 6 * FLAG_SET / 4, 32, tag, lane);
    else dma_image<150, 198, 3>(lds0 + L_W2 - 153600, l16, srd_ws, w2s, hw - 1, 3 * (bi >> 2));
    asm volatile("s_waitcnt vmcnt(16)" ::: "memory");
    BAR_RAW();
    dma_image<198, 200>(lds0 + L_W2 - 153600, l16, srd_ws, w2s, hw, 0);
    dma_image<200, 240>(lds0 + L_W2 - 153600, l16, srd_ws, w2s, hw, 2 * (bi >> 2));
    asm volatile("s_waitcnt vmcnt(10)" ::: "memory");
    BAR_RAW();
    DRAIN();
    BAR_RAW();
  }

}
}

extern "C" void kernel_launch(void* const* d_in, const int* in_sizes, int n_in, void* d_out, int out_size, void* d_ws,
                              size_t ws_size, hipStream_t stream) {
  const unsigned short* x = (const unsigned short*)d_in[0];
  const unsigned short* wqkv = (const unsigned short*)d_in[1];
  const unsigned short* wo = (const unsigned short*)d_in[2];
  const unsigned short* w1 = (const unsigned short*)d_in[4];
  const unsigned short* b1 = (const unsigned short*)d_in[5];
  const unsigned short* w2 = (const unsigned short*)d_in[6];
  const unsigned short* b2 = (const unsigned short*)d_in[7];
  fused_block<<<256, 512, 0, stream>>>(x, wqkv, (unsigned char*)d_ws, wo, w1, b1, w2, b2, (unsigned short*)d_out);
}
```

```cpp
#include <hip/hip_runtime.h>
#include <hip/hip_bf16.h>

#define STAMPS 0
#define THR 0
#define TOUCH 0
#define CODEPF 1
#define FLAGPF 1
#define OUTAUX 0
#define OUTPF 0
#define S0 6
#define XRPOS 0
#define QPOS 0
#define H1W 8
#define H1G 2
#define W2S 70
#define F6W 0
#define STW 6
#define AWD 6
#define PUBB 2
#define P4POS 1
#define P3K 14
#define PUBA 8
#define NCDELAY 0
#define HPRIO 0
#define WOLATE 0
#define SLEEP 1

#define GAS __attribute__((address_space(1)))
#define LAS __attribute__((address_space(3)))
#define RLX_AGENT __ATOMIC_RELAXED, __HIP_MEMORY_SCOPE_AGENT
typedef unsigned v4u __attribute__((ext_vector_type(4)));
typedef unsigned v2u __attribute__((ext_vector_type(2)));
typedef float f32x4 __attribute__((ext_vector_type(4)));
typedef float f32x2 __attribute__((ext_vector_type(2)));
typedef __bf16 bf16x8 __attribute__((ext_vector_type(8)));
typedef __bf16 bf16x2 __attribute__((ext_vector_type(2)));
typedef short s16x4 __attribute__((ext_vector_type(4)));
typedef GAS unsigned gu32;

namespace {
constexpr int S = 2048, D = 640, F = 1536;
constexpr float LN_EPS = 1e-5f;
constexpr int AUX_SC1 = 16;
constexpr unsigned kSpinCap = 1u << 17;
constexpr int OOB = 0x7FFFFF00;

constexpr int WS_EPOCH = 0;
constexpr int WS_FLAG = 4096;
constexpr int FLAG_SET = 64 * 256;
constexpr int WS_XN = 1 << 20;
constexpr int WS_O = WS_XN + 128 * 20 * 1024;
constexpr int WS_H = WS_O + 128 * 20 * 1024;
constexpr int WS_H1 = WS_H + 128 * 20 * 1024;
constexpr int WS_KVP = WS_H1 + 128 * 48 * 1024;
constexpr int WS_ST = WS_KVP + 16 * 16 * 4096;
constexpr int WS_DBG = WS_ST + 2048 * 64;
constexpr int WS_TKV = WS_DBG + 256 * 8 * 32 * 8;
constexpr int WS_TO = WS_TKV + 16 * 153600;
constexpr int WS_T1 = WS_TO + 8 * 102400;
constexpr int WS_T2 = WS_T1 + 16 * 122880;
constexpr int WS_CS = WS_T2 + 16 * 122880;
constexpr int WS_BYTES = WS_CS + 16 * 16 * 384;

constexpr int L_BKV = 0, L_BQ = 102400;
constexpr int L_RED = 0, L_KVF = 153600;
constexpr int L_WO = 0, L_W1 = 30720, L_W2 = 0, L_CS = 161792;
constexpr int L_B1 = L_CS + 1152, L_B2 = L_CS + 1536;
constexpr int LDS_BYTES = 163840;

__device__ __forceinline__ f32x4 mfma(v4u a, v4u b, f32x4 c) {
  return __builtin_amdgcn_mfma_f32_16x16x32_bf16(__builtin_bit_cast(bf16x8, a), __builtin_bit_cast(bf16x8, b), c, 0, 0, 0);
}
__device__ __forceinline__ unsigned pk2(f32x2 v) { return __builtin_bit_cast(unsigned, __builtin_convertvector(v, bf16x2)); }
__device__ __forceinline__ unsigned pk2(float lo, float hi) { return pk2(f32x2{lo, hi}); }
__device__ __forceinline__ float bf_lo(unsigned u) { return __builtin_bit_cast(float, u << 16); }
__device__ __forceinline__ float bf_hi(unsigned u) { return __builtin_bit_cast(float, u & 0xFFFF0000u); }
__device__ __forceinline__ f32x2 bf2(unsigned u) { return f32x2{bf_lo(u), bf_hi(u)}; }
__device__ __forceinline__ f32x2 lo2(f32x4 v) { return f32x2{v[0], v[1]}; }
__device__ __forceinline__ f32x2 hi2(f32x4 v) { return f32x2{v[2], v[3]}; }
__device__ __forceinline__ v4u mk_srd(const void* p, unsigned bytes) {
  const unsigned long long a = (unsigned long long)p;
  v4u r;
  r.x = (unsigned)a; r.y = (unsigned)(a >> 32) & 0xFFFFu; r.z = bytes; r.w = 0x00020000u;
  return r;
}
#define DRAIN() asm volatile("s_waitcnt vmcnt(0)" ::: "memory")
#define BAR() asm volatile("s_waitcnt lgkmcnt(0)\n\ts_barrier" ::: "memory")
#define BAR_RAW() asm volatile("s_barrier" ::: "memory")
#define SCHED() __builtin_amdgcn_sched_barrier(0)
#define PIN(x) asm volatile("" : "+v"(x))

__device__ __forceinline__ void dma2(unsigned lds_addr, unsigned voff, v4u srd, unsigned soff) {
  unsigned keep;
  asm volatile("s_mov_b32 %0, m0\n\ts_mov_b32 m0, %1\n\ts_nop 0\n\t"
               "buffer_load_dwordx4 %2, %3, %4 offen sc1 lds\n\t"
               "buffer_load_dwordx4 %2, %3, %4 offen offset:1024 sc1 lds\n\t"
               "s_mov_b32 m0, %0"
               : "=&s"(keep) : "s"(lds_addr), "v"(voff), "s"(srd), "s"(soff) : "memory");
}
template <int LO, int HI, int TH = 0, int NW = 4>
__device__ __forceinline__ void dma_image(unsigned lds_base, unsigned voff, v4u srd, int ws_off, int hw, int rot) {
  constexpr int NP = (HI - LO) / 2;
#pragma unroll 1
  for (int i = hw; i < NP; i += NW) {
    int j = i + rot;
    if (j >= NP) j -= NP;
    dma2(lds_base + LO * 1024 + j * 2048, voff, srd, ws_off + LO * 1024 + j * 2048);
    if (TH) asm volatile("s_waitcnt vmcnt(%0)" :: "i"(TH - 2) : "memory");
  }
}
__device__ __forceinline__ v2u rdtr(LAS char* p) { return __builtin_bit_cast(v2u, __builtin_amdgcn_ds_read_tr16_b64_v4i16((LAS s16x4*)p)); }
struct LBase {
  unsigned a[3];
  __device__ __forceinline__ explicit LBase(unsigned v) { a[0] = v; a[1] = v + 65536; a[2] = v + 131072; PIN(a[0]); PIN(a[1]); PIN(a[2]); }
  __device__ __forceinline__ v2u rd(int off) const { return rdtr((LAS char*)(a[off >> 16]) + (off & 65535)); }
};
template <int MT, int NT, int KS, int PD, bool WA, int M0 = 0, class RB, class GA, class PRE>
__device__ __forceinline__ void kloop(f32x4 (&acc)[MT][NT], RB rb, GA ga, PRE pre) {
  v2u bl[PD + 1][NT], bh[PD + 1][NT];
#pragma unroll
  for (int m = M0; m < MT; ++m)
#pragma unroll
    for (int s = 0; s < NT; ++s) acc[m][s] = f32x4{0.f, 0.f, 0.f, 0.f};
#pragma unroll
  for (int ks = 0; ks < PD; ++ks)
#pragma unroll
    for (int s = 0; s < NT; ++s) { bl[ks][s] = rb(ks, s, 0); bh[ks][s] = rb(ks, s, 1); }
  SCHED();
#pragma unroll
  for (int ks = 0; ks < KS; ++ks) {
    pre(ks);
#pragma unroll
    for (int s = 0; s < NT; ++s)
#pragma unroll
      for (int m = 0; m < MT; ++m) {
        const v2u lo = bl[ks % (PD + 1)][s], hi = bh[ks % (PD + 1)][s];
        const v4u wv = {lo.x, lo.y, hi.x, hi.y};
        acc[m][s] = WA ? mfma(wv, ga(ks, m), acc[m][s]) : mfma(ga(ks, m), wv, acc[m][s]);
        if (ks + PD < KS) {
          if (MT == 1 || m == 0) bl[(ks + PD) % (PD + 1)][s] = rb(ks + PD, s, 0);
          if (MT == 1 || m == 1) bh[(ks + PD) % (PD + 1)][s] = rb(ks + PD, s, 1);
        }
        SCHED();
      }
  }
}
constexpr int ld_hi(int ks, int s0, int W, int KS) {
  const int a = s0 + 2 * (ks + 1), b = ks + 1 + W, c = a < b ? a : b;
  return c < KS ? c : KS;
}
__device__ __forceinline__ v4u ld128(__amdgpu_buffer_rsrc_t rs, int voff, int soff) {
  return __builtin_amdgcn_raw_buffer_load_b128(rs, voff, soff, AUX_SC1);
}
__device__ __forceinline__ v2u ld64(__amdgpu_buffer_rsrc_t rs, int voff, int soff) {
  return __builtin_amdgcn_raw_buffer_load_b64(rs, voff, soff, AUX_SC1);
}
__device__ __forceinline__ void st128(v4u v, __amdgpu_buffer_rsrc_t rs, int voff, int soff) {
  __builtin_amdgcn_raw_buffer_store_b128(v, rs, (int)((unsigned)voff + (unsigned)soff), 0, AUX_SC1);
}
__device__ __forceinline__ int sel(bool c, int a) {
  PIN(a);
  return c ? a : OOB;
}
__device__ __forceinline__ v4u ld_split(__amdgpu_buffer_rsrc_t rs, int lane, int soff) {
  const v2u a = ld64(rs, lane * 8, soff), b = ld64(rs, lane * 8 + 512, soff);
  return v4u{a.x, a.y, b.x, b.y};
}
__device__ __forceinline__ void st64(v2u v, __amdgpu_buffer_rsrc_t rs, int voff, int soff) {
  __builtin_amdgcn_raw_buffer_store_b64(v, rs, voff, soff, AUX_SC1);
}
__device__ __forceinline__ void flag_set(gu32* p, unsigned tag) {
  asm volatile("global_atomic_swap %0, %1, off sc1" :: "v"(p), "v"(tag) : "memory");
}
__device__ __forceinline__ void wait_flag(gu32* p, int n, unsigned tag, int lane) {
#pragma unroll 1
  for (unsigned spins = 0; spins < kSpinCap; ++spins) {
    unsigned v = tag;
    if (lane < n) asm volatile("global_atomic_add %0, %1, %2, off sc0 sc1\n\ts_waitcnt vmcnt(0)" : "=&v"(v) : "v"(p), "v"(0u) : "memory");
    if (__all(v == tag)) break;
    if (SLEEP) __builtin_amdgcn_s_sleep(SLEEP);
  }
  asm volatile("" ::: "memory");
}
__device__ __forceinline__ void sig_wait(volatile LAS unsigned* p) {
#pragma unroll 1
  for (unsigned spins = 0; spins < 64 * kSpinCap; ++spins) {
    if (*p != 0u) break;
    __builtin_amdgcn_s_sleep(1);
  }
  asm volatile("" ::: "memory");
}
__device__ __forceinline__ void prog_wait(volatile LAS v4u* p, unsigned stage) {
#pragma unroll 1
  for (unsigned spins = 0; spins < 64 * kSpinCap; ++spins) {
    const v4u v = *p;
    if (min(min(v.x, v.y), min(v.z, v.w)) >= stage) break;
    __builtin_amdgcn_s_sleep(1);
  }
  asm volatile("" ::: "memory");
}
__device__ __forceinline__ void publish(gu32* flag, unsigned tag, int lane) {
  PIN(flag);
  DRAIN();
  if (lane == 0) flag_set(flag, tag);
}
__device__ __forceinline__ f32x4 add4(f32x4 a, f32x4 b) {
  f32x4 r;
#pragma unroll
  for (int i = 0; i < 4; ++i) { float x; asm("v_add_f32 %0, %1, %2" : "=v"(x) : "v"(a[i]), "v"(b[i])); r[i] = x; }
  return r;
}
__device__ __forceinline__ float xsum32(float x) {
  unsigned u = __builtin_bit_cast(unsigned, x), v = u;
  asm("" : "+v"(v));
  auto r = __builtin_amdgcn_permlane32_swap(u, v, false, false);
  const unsigned a = r[0], b = r[1];
  return __builtin_bit_cast(float, a) + __builtin_bit_cast(float, b);
}
__device__ __forceinline__ float xsum16(float x) {
  unsigned u = __builtin_bit_cast(unsigned, x), v = u;
  asm("" : "+v"(v));
  auto r = __builtin_amdgcn_permlane16_swap(u, v, false, false);
  const unsigned a = r[0], b = r[1];
  return __builtin_bit_cast(float, a) + __builtin_bit_cast(float, b);
}
__device__ __forceinline__ float xsum8(float x) {
  return x + __builtin_bit_cast(float, __builtin_amdgcn_update_dpp(0, __builtin_bit_cast(int, x), 0x128, 0xf, 0xf, false));
}
__device__ __forceinline__ f32x2 phi2(f32x2 t) {
  const f32x2 u = t * 1.4426950408889634f, v = t + 1e-6f;
  const f32x2 e = {__builtin_amdgcn_fmed3f(__builtin_amdgcn_exp2f(u[0]), 0.f, 1.f), __builtin_amdgcn_fmed3f(__builtin_amdgcn_exp2f(u[1]), 0.f, 1.f)};
  return f32x2{fmaxf(v[0], 1e-6f), fmaxf(v[1], 1e-6f)} + e;
}
__device__ __forceinline__ f32x2 gelu2(f32x2 x) {
  const f32x2 u = x * (x * x * -0.1029432397f + -2.302208198f);
  const f32x2 d = f32x2{__builtin_amdgcn_exp2f(u[0]), __builtin_amdgcn_exp2f(u[1])} + 1.f;
  return x * f32x2{__builtin_amdgcn_rcpf(d[0]), __builtin_amdgcn_rcpf(d[1])};
}

#define STAMP(i) do { } while (0)

__global__ void __launch_bounds__(512, 2)
fused_block(const unsigned short* __restrict__ x, const unsigned short* __restrict__ wqkv, unsigned char* ws, const unsigned short* __restrict__ wo,
            const unsigned short* __restrict__ w1, const unsigned short* __restrict__ b1, const unsigned short* __restrict__ w2,
            const unsigned short* __restrict__ b2, unsigned short* __restrict__ out) {
  __shared__ __attribute__((aligned(16))) char smem[LDS_BYTES];
  unsigned long long pc0;
  asm volatile("s_getpc_b64 %0" : "=s"(pc0));
  LAS char* L = (LAS char*)smem;
  const unsigned lds0 = (unsigned)(size_t)L;
  const int tid = threadIdx.x, lane = tid & 63, w = __builtin_amdgcn_readfirstlane(tid >> 6);
  const int b = blockIdx.x, xcd = b & 7, bi = b >> 3;
  const int g = lane >> 4, t = lane & 15;
  const __amdgpu_buffer_rsrc_t rs = __builtin_amdgcn_make_buffer_rsrc((void*)ws, 0, WS_BYTES, 0x00020000);
  gu32* flags = (gu32*)(ws + WS_FLAG);
#define FLAG(set, grp) (flags + ((set) * FLAG_SET + (grp) * 256) / 4)
  asm volatile("" :: "s"(x), "s"(wqkv), "s"(ws), "s"(wo), "s"(w1), "s"(b1), "s"(w2), "s"(b2));
  STAMP(0);
  const int r128 = 4 * (xcd >> 1) + (bi >> 3), c16 = 8 * (xcd & 1) + (bi & 7);
  const int r64 = 8 * (xcd >> 1) + (bi >> 2), c8 = 4 * (xcd & 1) + (bi & 3);

  const __amdgpu_buffer_rsrc_t rx = __builtin_amdgcn_make_buffer_rsrc((void*)x, 0, S * D * 2, 0x00020000);
  const int hw = w - 4, ht = tid - 256;
  volatile LAS unsigned* sig = (volatile LAS unsigned*)(L + L_CS + 1024);
  volatile LAS v4u* prog = (volatile LAS v4u*)(L + L_CS + 1088);
#define PROG(n) do { asm volatile("" ::: "memory"); sig[16 + w] = (n); } while (0)

  const __amdgpu_buffer_rsrc_t rq = __builtin_amdgcn_make_buffer_rsrc((void*)wqkv, 0, D * 3 * D * 2, 0x00020000);
  v4u tv[12];
  unsigned ep;
  const int q10 = lane / 10, c10 = lane % 10;
  if (w < 4) {
#pragma unroll
    for (int j = 0; j < 3; ++j)
      tv[j] = __builtin_amdgcn_raw_buffer_load_b128(rx, (lane & 7) * 1280 + (lane >> 3) * 16, (j < 2 || w < 2) ? 8 * b * 1280 + (w + 4 * j) * 128 : OOB, 0);
    ep = __hip_atomic_load((gu32*)(ws + WS_EPOCH), RLX_AGENT);
  } else if (w == 4) {
    const int vo = sel(lane < 40, q10 * 3840 + (c10 < 5 ? 1280 : 2480) + c10 * 16);
#pragma unroll
    for (int j = 0; j < 10; ++j) tv[j] = __builtin_amdgcn_raw_buffer_load_b128(rq, vo, (40 * r128 + 4 * j) * 3840 + c16 * 80, 0);
  } else if (w == 7) {
    *(LAS v4u*)(L + L_CS + 512 + lane * 16) = v4u{0u, 0u, 0u, 0u};
  }
  BAR();

  if (w < 4) {
  unsigned tag = ep;
  const int grp32 = 4 * r128 + w;
  constexpr int AW = AWD;

  {
    const int tok = 8 * b + (lane & 7), cl = lane >> 3;
    const bool has3 = w < 2;
    int xd = ((tok >> 4) * 20 + 2 * w + (cl >> 2)) * 1024 + ((cl & 3) * 16 + (tok & 15)) * 16;
    PIN(xd);
    SCHED();
    f32x2 f[12];
#pragma unroll
    for (int j = 0; j < 3; ++j)
#pragma unroll
      for (int e = 0; e < 4; ++e) f[j * 4 + e] = bf2(tv[j][e]);
    f32x2 s2 = (f[0] + f[1]) + (f[2] + f[3]) + ((f[4] + f[5]) + (f[6] + f[7])) + ((f[8] + f[9]) + (f[10] + f[11]));
    const float mloc = xsum32(xsum16(xsum8(s2[0] + s2[1]))) * (has3 ? 1.f / 192.f : 1.f / 128.f);
    const float m3 = has3 ? mloc : 0.f;
    f32x2 q2 = {0.f, 0.f}, q3 = {0.f, 0.f};
#pragma unroll
    for (int j = 0; j < 12; j += 2) {
      const f32x2 d0 = f[j] - (j < 8 ? mloc : m3), d1 = f[j + 1] - (j < 8 ? mloc : m3);
      q2 += d0 * d0; q3 += d1 * d1;
    }
    q2 += q3;
    const float m2loc = xsum32(xsum16(xsum8(q2[0] + q2[1])));
    if (lane < 8) *(volatile LAS f32x4*)(L + L_CS + 512 + (lane * 4 + w) * 16) = f32x4{mloc, m2loc, 1.f, 1.f};
    STAMP(25);
    f32x4 pr[4];
#pragma unroll 1
    for (unsigned spins = 0; spins < 64 * kSpinCap; ++spins) {
      bool ok = true;
#pragma unroll
      for (int ww = 0; ww < 4; ++ww) {
        pr[ww] = *(volatile LAS f32x4*)(L + L_CS + 512 + ((lane & 7) * 4 + ww) * 16);
        ok = ok && pr[ww][2] == 1.f;
      }
      if (__all(ok)) break;
    }
    const f32x4 p01 = {pr[0][0], pr[0][1], pr[1][0], pr[1][1]}, p23 = {pr[2][0], pr[2][1], pr[3][0], pr[3][1]};
    const float mean = (p01[0] + p01[2]) * 0.3f + (p23[0] + p23[2]) * 0.2f;
    const float e0 = p01[0] - mean, e1 = p01[2] - mean, e2 = p23[0] - mean, e3 = p23[2] - mean;
    const float m2 = (p01[1] + p01[3]) + (p23[1] + p23[3]) + 192.f * (e0 * e0 + e1 * e1) + 128.f * (e2 * e2 + e3 * e3);
    const float rstd = __builtin_amdgcn_rsqf(m2 * (1.f / D) + LN_EPS);
#pragma unroll
    for (int j = 0; j < 3; ++j) {
      v4u o;
#pragma unroll
      for (int e = 0; e < 4; ++e) o[e] = pk2((f[j * 4 + e] - mean) * rstd);
      st128(o, rs, (j < 2 || has3) ? xd : OOB, WS_XN + j * 8192);
    }
    STAMP(26);
    PIN(tag); tag += 1u;
    publish(FLAG(0, b >> 2) + (b & 3) * 4 + w, tag, lane);
    if (w == 0) sig[6] = 1u;
  }

  STAMP(1);

  v4u xr[3];
  {
    wait_flag(FLAG(0, grp32) + lane, 16, tag, lane);
    STAMP(2);
    v4u a[20][2];
    const int abase = WS_XN + 2 * grp32 * 20 * 1024;
#pragma unroll
    for (int ks = 0; ks < S0; ++ks)
#pragma unroll
      for (int m = 0; m < 2; ++m) a[ks][m] = ld128(rs, lane * 16, abase + (m * 20 + ks) * 1024);
    SCHED();
    BAR_RAW();
    STAMP(27);
    {
      f32x4 acc[2][5];
      const LBase bp(lds0 + L_BKV + (lane >> 2) * 160 + (lane & 3) * 8);
      kloop<2, 5, 20, 1, false>(acc, [&](int ks, int s, int h) { return bp.rd(ks * 5120 + h * 2560 + s * 32); },
        [&](int ks, int m) { return a[ks][m]; },
        [&](int ks) {
#pragma unroll
          for (int j = ld_hi(ks - 1, S0, AW, 20); j < ld_hi(ks, S0, AW, 20); ++j)
#pragma unroll
            for (int m = 0; m < 2; ++m) a[j][m] = ld128(rs, lane * 16, abase + (m * 20 + j) * 1024);
          if (ks == 3 || ks == 10) BAR_RAW();
          if (ks == 14) PROG(1);
        });
      STAMP(3);
      v4u ka[3], vb[3];
#pragma unroll
      for (int s = 0; s < 3; ++s)
#pragma unroll
        for (int m = 0; m < 2; ++m) {
          vb[s][2 * m] = pk2(lo2(acc[m][s + 2])); vb[s][2 * m + 1] = pk2(hi2(acc[m][s + 2]));
        }
#pragma unroll
      for (int e = 0; e < 4; ++e) vb[0][e] = t < 8 ? 0x3F803F80u : vb[0][e];
      const f32x4 z4 = {0.f, 0.f, 0.f, 0.f};
      LAS f32x4* red = (LAS f32x4*)(L + L_RED) + w * 512 + lane;
#pragma unroll
      for (int s = 0; s < 3; ++s) {
#pragma unroll
        for (int m = 0; m < 2; ++m) { ka[s][2 * m] = pk2(phi2(lo2(acc[m][s]))); ka[s][2 * m + 1] = pk2(phi2(hi2(acc[m][s]))); }
#pragma unroll
        for (int sb = 0; sb < 3; ++sb)
          if (s > 0 || sb < 2) red[(3 * s + sb - (s > 0)) * 64] = mfma(ka[s], vb[sb], z4);
      }
      STAMP(28);
      BAR();
    }
    STAMP(4);
    f32x4 qa[2][3];
    {
      unsigned bp0 = lds0 + L_BQ + (lane >> 2) * 64 + (lane & 3) * 8 + (g & 1) * 32;
      unsigned bp1 = lds0 + L_BQ + (lane >> 2) * 64 + (lane & 3) * 8 + 32 - (g & 1) * 32;
      unsigned bp2 = lds0 + L_BQ + 2048 + (lane >> 2) * 16 + (lane & 1) * 8;
      PIN(bp0); PIN(bp1); PIN(bp2);
      kloop<2, 3, 20, 2, true>(qa,
        [&](int ks, int s, int h) {
          return s == 0 ? rdtr((LAS char*)bp0 + ks * 2560 + h * 1024) : s == 1 ? rdtr((LAS char*)bp1 + ks * 2560 + h * 1024) : rdtr((LAS char*)bp2 + ks * 2560 + h * 256);
        },
        [&](int ks, int m) { return a[ks][m]; },
        [&](int ks) {});
    }
    v4u pq0[2], pq1[2];
#pragma unroll
    for (int m = 0; m < 2; ++m) {
      pq0[m].x = pk2(phi2(lo2(qa[m][0]))); pq0[m].y = pk2(phi2(hi2(qa[m][0])));
      pq0[m].z = pk2(phi2(lo2(qa[m][1]))); pq0[m].w = pk2(phi2(hi2(qa[m][1])));
      const unsigned lo = pk2(phi2(lo2(qa[m][2]))), hi = pk2(phi2(hi2(qa[m][2])));
      pq1[m].x = g < 2 ? lo : 0u; pq1[m].y = g < 2 ? hi : 0u; pq1[m].z = 0u; pq1[m].w = 0u;
      PIN(pq0[m]); PIN(pq1[m]);
    }
    STAMP(5);
    BAR_RAW();
    STAMP(17);
    {
      v4u kv[4][2];
#pragma unroll
      for (int ks = 0; ks < 2; ++ks)
#pragma unroll
        for (int es = 3; es >= ks; --es) kv[es][ks] = *(LAS v4u*)(L + L_KVF + (es * 2 + ks) * 1024 + lane * 16);
      SCHED();
      f32x4 o[2][4];
#pragma unroll
      for (int es = 3; es >= 0; --es)
#pragma unroll
        for (int m = 0; m < 2; ++m) {
          o[m][es] = mfma(kv[es][0], pq0[m], f32x4{0.f, 0.f, 0.f, 0.f});
          SCHED();
        }
#pragma unroll
      for (int es = 3; es >= 1; --es)
#pragma unroll
        for (int m = 0; m < 2; ++m) {
          o[m][es] = mfma(kv[es][1], pq1[m], o[m][es]);
          SCHED();
        }
#pragma unroll
      for (int m = 0; m < 2; ++m) {
        const float i0 = __builtin_amdgcn_rcpf(o[m][3][0] + 1e-6f), i1 = __builtin_amdgcn_rcpf(o[m][3][1] + 1e-6f);
        const float im = g == 0 ? i0 : i1;
        v4u ov;
        ov.x = pk2(lo2(o[m][0]) * i0); ov.y = pk2(hi2(o[m][0]) * i0);
        ov.z = pk2(lo2(o[m][1]) * im); ov.w = pk2(hi2(o[m][1]) * im);
        v2u ol; ol.x = pk2(lo2(o[m][2]) * i1); ol.y = pk2(hi2(o[m][2]) * i1);
        const int tt = 2 * grp32 + m;
        st128(ov, rs, lane * 16, WS_O + (tt * 20 + c16) * 1024);
        st64(ol, rs, g < 2 ? lane * 8 : OOB, WS_O + (tt * 20 + 16) * 1024 + c16 * 256);
      }
      STAMP(18);
      publish(FLAG(2, grp32) + c16, tag, lane);
    }
    STAMP(7);
  }

  {
    const int tt = 4 * r64 + w;
#pragma unroll
    for (int j = 0; j < 3; ++j)
      xr[j] = __builtin_amdgcn_raw_buffer_load_b128(rx, sel(j < 2 || g < 2, t * 1280 + g * 16), (4 * r64 + w) * 16 * 1280 + c8 * 160 + j * 64, 0);
    v4u eye[2];
#pragma unroll
    for (int hf = 0; hf < 2; ++hf) {
      const int e = 16 * hf + t - 8 * g;
#pragma unroll
      for (int d = 0; d < 4; ++d) eye[hf][d] = e == 2 * d ? 0x3F80u : e == 2 * d + 1 ? 0x3F800000u : 0u;
      PIN(eye[hf]);
    }
    f32x4 acc[1][5];
    wait_flag(FLAG(2, tt >> 1) + lane, 16, tag, lane);
    STAMP(8);
    if (b == 0 && tid == 0) __hip_atomic_store((gu32*)(ws + WS_EPOCH), tag, RLX_AGENT);
    v4u a[20];
#pragma unroll
    for (int ks = 0; ks < 2 * S0; ++ks) a[ks] = ld128(rs, lane * 16, WS_O + (tt * 20 + ks) * 1024);
    SCHED();
#pragma unroll
    for (int s = 0; s < 5; ++s) { acc[0][s] = mfma(eye[s & 1], xr[s >> 1], f32x4{0.f, 0.f, 0.f, 0.f}); PIN(acc[0][s]); }
    const LBase bp(lds0 + L_WO + (lane >> 2) * 160 + (lane & 3) * 8);
    kloop<1, 5, 20, 1, true, 1>(acc, [&](int ks, int s, int h) { return bp.rd(ks * 5120 + h * 2560 + s * 32); },
      [&](int ks, int m) { return a[ks]; },
      [&](int ks) {
#pragma unroll
        for (int j = ld_hi(ks - 1, 2 * S0, 2 * AW, 20); j < ld_hi(ks, 2 * S0, 2 * AW, 20); ++j)
          a[j] = j < 16 ? ld128(rs, lane * 16, WS_O + (tt * 20 + j) * 1024) : ld_split(rs, lane, WS_O + (tt * 20 + j) * 1024);
        if (ks == 8) PROG(2);
        if (ks == P3K) PROG(3);
      });
    if (P4POS == 0) PROG(4);
    STAMP(9);
    v2u hb[5];
    f32x2 hv[10];
#pragma unroll
    for (int s = 0; s < 5; ++s) {
      hb[s].x = pk2(lo2(acc[0][s]));
      hb[s].y = pk2(hi2(acc[0][s]));
      hv[2 * s] = bf2(hb[s].x); hv[2 * s + 1] = bf2(hb[s].y);
    }
    st128(v4u{hb[0].x, hb[0].y, hb[1].x, hb[1].y}, rs, lane * 16, WS_H + (tt * 20 + 2 * c8) * 1024);
    st128(v4u{hb[2].x, hb[2].y, hb[3].x, hb[3].y}, rs, lane * 16, WS_H + (tt * 20 + 2 * c8 + 1) * 1024);
    st64(hb[4], rs, lane * 8, WS_H + (tt * 20 + 16) * 1024 + c8 * 512);
    if (P4POS == 1) PROG(4);
    const f32x2 s2 = ((hv[0] + hv[1]) + (hv[2] + hv[3])) + ((hv[4] + hv[5]) + (hv[6] + hv[7])) + (hv[8] + hv[9]);
    const float mc = xsum32(xsum16(s2[0] + s2[1])) * (1.f / 80.f);
    f32x2 q2 = {0.f, 0.f}, q3 = {0.f, 0.f};
#pragma unroll
    for (int j = 0; j < 10; j += 2) { const f32x2 d0 = hv[j] - mc, d1 = hv[j + 1] - mc; q2 += d0 * d0; q3 += d1 * d1; }
    q2 += q3;
    const float m2 = xsum32(xsum16(q2[0] + q2[1]));
    v2u sv = {__builtin_bit_cast(unsigned, mc), __builtin_bit_cast(unsigned, m2)};
    PIN(sv);
    STAMP(20);
    publish(FLAG(3, tt >> 1) + (tt & 1) * 8 + c8, tag, lane);
    if (P4POS == 2) PROG(4);
    st64(sv, rs, g == 0 ? t * 8 : OOB, WS_ST + (tt * 8 + c8) * 128);
    STAMP(10);
  }

  {
    gu32* fst = FLAG(3, 2 * r64 + (w >> 1)) + 32 + (w & 1) * 8 + c8;
    PIN(fst);
    wait_flag(FLAG(3, grp32) + lane, 16, tag, lane);
    DRAIN();
    if (lane == 0) flag_set(fst, tag);
    STAMP(11);
    v4u a[20][2];
    const int abase = WS_H + 2 * grp32 * 20 * 1024;
#pragma unroll
    for (int ks = 0; ks < S0; ++ks)
#pragma unroll
      for (int m = 0; m < 2; ++m) a[ks][m] = ld128(rs, lane * 16, abase + (m * 20 + ks) * 1024);
    SCHED();
    float nm[2], rr[2];
    BAR_RAW();
    STAMP(22);
    const unsigned l64 = lds0 + (lane >> 2) * 64 + (lane & 3) * 8;
    const LBase bp0(l64 + (g & 1) * 32), bp1(l64 + 32 - (g & 1) * 32);
    const int h1base = WS_H1 + 2 * grp32 * 48 * 1024;
    auto finish = [&](const f32x4& c0, const f32x4& c1, int j, int m) {
      v4u o;
#pragma unroll
      for (int s = 0; s < 2; ++s) {
        const f32x4 cs = *(LAS f32x4*)(L + L_CS + ((2 * j + s) * 4 + g) * 16), bb = *(LAS f32x4*)(L + L_B1 + ((2 * j + s) * 4 + g) * 16);
        const f32x4 c = s ? c1 : c0;
        o[2 * s] = pk2(gelu2(lo2(c) * rr[m] + (lo2(cs) * nm[m] + lo2(bb))));
        o[2 * s + 1] = pk2(gelu2(hi2(c) * rr[m] + (hi2(cs) * nm[m] + hi2(bb))));
      }
      st128(o, rs, lane * 16, h1base + (m * 48 + (c16 >> 3) * 24 + (j < 2 ? 2 * (c16 & 7) + j : 16 + (c16 & 7))) * 1024);
    };
    {
      f32x4 acc[2][4];
      kloop<2, 4, 20, 1, true>(acc, [&](int ks, int s, int h) { return ((s & 1) ? bp1 : bp0).rd(L_W1 + (s >> 1) * 40960 + ks * 2048 + h * 1024); },
        [&](int ks, int m) { return a[ks][m]; },
        [&](int ks) {
#pragma unroll
          for (int j = ld_hi(ks - 1, S0, AW, 20); j < ld_hi(ks, S0, AW, 20); ++j)
#pragma unroll
            for (int m = 0; m < 2; ++m)
              a[j][m] = j < 16 ? ld128(rs, lane * 16, abase + (m * 20 + j) * 1024) : ld_split(rs, lane, abase + (m * 20 + j) * 1024);
          if (ks == 20 - AW) PROG(5);
        });
      PROG(6);
      sig_wait(sig + 12 + w);
#pragma unroll
      for (int m = 0; m < 2; ++m) {
        const f32x2 u = *(LAS f32x2*)(L + L_KVF + ((2 * w + m) * 16 + t) * 8);
        rr[m] = u[0]; nm[m] = u[1];
      }
      STAMP(12);
#pragma unroll
      for (int j = 0; j < 2; ++j)
#pragma unroll
        for (int m = 0; m < 2; ++m) finish(acc[m][2 * j], acc[m][2 * j + 1], j, m);
      SCHED();
      STAMP(24);
    }
    f32x4 accb[2][2];
    const int kh = w & 1, tt = 4 * r64 + w;
    v4u hr01, hr23;
    v2u hr4;
    kloop<2, 2, 20, 3, true>(accb, [&](int ks, int s, int h) { return (s ? bp1 : bp0).rd(L_W1 + 81920 + ks * 2048 + h * 1024); },
      [&](int ks, int m) { return a[ks][m]; },
      [&](int ks) {
        if (ks == PUBA) { publish(FLAG(4, grp32) + c16, tag, lane); PROG(7); }
        if (ks == 12) PROG(8);
        if (ks == PUBA + 2) {
          hr01 = ld128(rs, lane * 16, WS_H + (tt * 20 + 2 * c8) * 1024);
          hr23 = ld128(rs, lane * 16, WS_H + (tt * 20 + 2 * c8 + 1) * 1024);
          hr4 = ld64(rs, lane * 8, WS_H + (tt * 20 + 16) * 1024 + c8 * 512);
        }
      });
    PROG(9);
    STAMP(13);
    f32x4 acc[2][5];
    {
      const unsigned hrr[10] = {hr01.x, hr01.y, hr01.z, hr01.w, hr23.x, hr23.y, hr23.z, hr23.w, hr4.x, hr4.y};
#pragma unroll
      for (int s = 0; s < 5; ++s) {
        acc[0][s] = *(LAS f32x4*)(L + L_B2 + (s * 4 + g) * 16) + f32x4{bf_lo(hrr[2 * s]), bf_hi(hrr[2 * s]), bf_lo(hrr[2 * s + 1]), bf_hi(hrr[2 * s + 1])};
        PIN(acc[0][s]);
      }
    }
    int oo[3];
    unsigned tw, tr;
    {
      int ln = lane;
      PIN(ln);
      const int qq = ln / 10, cc = ln % 10;
#pragma unroll
      for (int j = 0; j < 3; ++j) { oo[j] = sel(ln < (j < 2 ? 60 : 40), ((tt * 16 + 6 * j + qq) * D + 80 * c8) * 2 + cc * 16); PIN(oo[j]); }
      tw = lds0 + L_W2 + 122880 + w * 3072 + t * 176 + g * 8; tr = lds0 + L_W2 + 122880 + w * 3072 + qq * 176 + cc * 16;
      PIN(tw); PIN(tr);
    }
    BAR_RAW();
    STAMP(14);
    constexpr int WIN = 8;
    v4u ar[WIN][2];
    const int a4[2] = {WS_H1 + (tt * 48 + kh * 24) * 1024, WS_H1 + ((tt ^ 1) * 48 + kh * 24) * 1024};
#pragma unroll
    for (int ks = 0; ks < WIN - 1; ++ks)
#pragma unroll
      for (int m = 0; m < 2; ++m) ar[ks][m] = ld128(rs, lane * 16, a4[m] + ks * 1024);
    SCHED();
#pragma unroll
    for (int m = 0; m < 2; ++m) finish(accb[m][0], accb[m][1], 2, m);
    STAMP(29);
    const LBase bp(lds0 + L_W2 + kh * 5120 + (lane >> 2) * 160 + (lane & 3) * 8);
    kloop<2, 5, 24, 1, true, 1>(acc, [&](int ks, int s, int h) { return bp.rd((ks % 15) * 10240 + h * 2560 + s * 32); },
      [&](int ks, int m) { return ar[ks % WIN][m]; },
      [&](int ks) {
        if (ks == 5) PROG(10);
        if (ks == 9 || ks == 14 || ks == 19) BAR_RAW();
        if (ks == PUBB) publish(FLAG(10, grp32) + c16, tag, lane);
        if (ks >= PUBB) {
#pragma unroll
          for (int j = ks == PUBB ? WIN - 1 : ld_hi(ks - 1, 7 - 2 * PUBB, WIN - 1, 24); j < ld_hi(ks, 7 - 2 * PUBB, WIN - 1, 24); ++j)
#pragma unroll
            for (int m = 0; m < 2; ++m) ar[j % WIN][m] = ld128(rs, lane * 16, a4[m] + j * 1024);
        }
      });
    STAMP(15);
    LAS f32x4* xch = (LAS f32x4*)(L + L_W2 + 102400);
#pragma unroll
    for (int s = 0; s < 5; ++s) xch[((w ^ 1) * 5 + s) * 64 + lane] = acc[1][s];
    BAR();
    const __amdgpu_buffer_rsrc_t rout = __builtin_amdgcn_make_buffer_rsrc((void*)out, 0, S * D * 2, 0x00020000);
    f32x4 ot[5];
#pragma unroll
    for (int s = 0; s < 5; ++s) ot[s] = xch[(w * 5 + s) * 64 + lane];
    SCHED();
#pragma unroll
    for (int s = 0; s < 5; ++s) {
      const f32x4 v = acc[0][s] + ot[s];
      *(LAS v2u*)((LAS char*)tw + s * 32) = v2u{pk2(lo2(v)), pk2(hi2(v))};
    }
    asm volatile("" ::: "memory");
    v4u ov[3];
#pragma unroll
    for (int j = 0; j < 3; ++j) ov[j] = *(LAS v4u*)((LAS char*)tr + j * 6 * 176);
#pragma unroll
    for (int j = 0; j < 3; ++j) __builtin_amdgcn_raw_buffer_store_b128(ov[j], rout, oo[j], 0, 0);
    STAMP(16);
  }
  return;
  }

  {
    const v4u srd_ws = mk_srd(ws, WS_BYTES);
    STAMP(16);
    const int q5 = lane / 5, c5 = lane % 5;
    const int f40m = c5 < 4 ? 512 : 128, f40l = c5 < 4 ? q5 * 64 + (c5 ^ (2 * ((q5 >> 2) & 1))) * 16 : 2048 + q5 * 16;
    v2u ub1, ub2;
    unsigned pf0;
    v4u tq[10];
    if (hw == 1) {
      const __amdgpu_buffer_rsrc_t ro = __builtin_amdgcn_make_buffer_rsrc((void*)wo, 0, D * D * 2, 0x00020000);
      const int vq = sel(lane < 40, (8 * (q5 >> 2) + (q5 & 3)) * 3840 + c5 * 16), vo = sel(lane < 40, q10 * 1280 + c10 * 16);
#pragma unroll
      for (int j = 0; j < 5; ++j) {
        const int i8 = 5 * r128 + j, k = 32 * (i8 >> 2) + 16 * (i8 & 1) + 4 * ((i8 >> 1) & 1);
        tq[j] = __builtin_amdgcn_raw_buffer_load_b128(rq, vq, k * 3840 + c16 * 80, 0);
      }
#pragma unroll
      for (int j = 0; j < 5; ++j) tq[5 + j] = __builtin_amdgcn_raw_buffer_load_b128(ro, vo, (20 * r64 + 4 * j) * 1280 + c8 * 160, 0);
    } else if (hw == 2) {
      const GAS unsigned char* cb = (const GAS unsigned char*)(pc0 & ~127ull);
      pf0 = *(const GAS unsigned*)(cb + min(lane * 32 + bi, 346) * 128);
      if (lane < 6) {
        gu32* fp = FLAG(3 * (b >> 6) + (lane >> 1), b & 63) + (lane & 1) * 32;
        asm volatile("global_atomic_add %0, %1, off sc1" :: "v"(fp), "v"(0u) : "memory");
      }
    } else if (hw == 3) {
      const __amdgpu_buffer_rsrc_t rb1 = __builtin_amdgcn_make_buffer_rsrc((void*)b1, 0, F * 2, 0x00020000);
      const __amdgpu_buffer_rsrc_t rb2 = __builtin_amdgcn_make_buffer_rsrc((void*)b2, 0, D * 2, 0x00020000);
      ub1 = __builtin_amdgcn_raw_buffer_load_b64(rb1, lane * 8, c16 * 192, 0);
      ub2 = __builtin_amdgcn_raw_buffer_load_b64(rb2, lane * 8, c8 * 160, 0);
    }
    unsigned tag = __hip_atomic_load((gu32*)(ws + WS_EPOCH), RLX_AGENT);
    if (hw == 0) {
      const int dof = sel(lane < 40, lane * 16);
#pragma unroll
      for (int j = 0; j < 10; ++j) {
        const int k = 40 * r128 + 4 * j, R = (k & ~31) + 16 * ((k >> 2) & 1) + 4 * ((k >> 3) & 3);
        st128(tv[j], rs, dof, WS_TKV + c16 * 153600 + R * 160);
      }
      STAMP(18);
      PIN(tag); tag += 1u;
      publish(FLAG(6, c16) + r128, tag, lane);
      STAMP(13);
    }
    asm volatile("" ::: "memory");
    int red_off, kvf_off, kvp_off;
    auto items = [&]() {
      const int it = ht < 210 ? ht : 209, ie = it / 5, ic = it % 5;
      const int ihead = ie < 40 ? (ie >= 20) : ie - 40;
      const int id = 20 * ihead + 4 * ic, dg = id >> 2;
      const int sa = id >> 4, col = 40 + ie;
      const int tile = 3 * sa + (ie < 40 ? (col >> 4) - 2 : 0) - (sa > 0);
      red_off = lds0 + L_RED + (tile * 64 + (dg & 3) * 16 + (ie < 40 ? (col & 15) : 0)) * 16;
      kvf_off = lds0 + L_KVF + ((ie < 40 ? (ie >> 4) : 3) * 2 + (dg >> 3)) * 1024 + ((dg & 3) * 16 + (ie < 40 ? (ie & 15) : ihead)) * 16 + ((dg >> 2) & 1) * 8;
      kvp_off = WS_KVP + (c16 * 16 + r128) * 4096 + ht * 16;
      PIN(red_off); PIN(kvf_off); PIN(kvp_off);
    };
    if (hw != 0) items();
    int vo12, td[12];
    if (hw >= 2) {
      const int q12 = lane / 12, c12 = lane % 12;
      vo12 = hw == 2 ? sel(lane < 48, q12 * 3072 + c12 * 16) : sel(lane < 40, q10 * 1280 + c10 * 16);
#pragma unroll
      for (int j = 0; j < 12; ++j) {
        const int cc = r128 >> 1, off = 40 * (r128 & 1) + 4 * j;
        const int R = off < 64 ? 64 * cc + off : 32 * (16 + (cc >> 1)) + 16 * (cc & 1) + off - 64;
        const int d1 = WS_T1 + c16 * 122880 + (c12 >> 2) * 40960 + (R + q12) * 64 + ((c12 & 3) ^ (2 * ((R >> 2) & 1))) * 16;
        const int pc = r64 >> 1, ko = 48 * (r64 & 1) + 4 * j, jj = ko >> 5;
        const int d2 = WS_T2 + c8 * 245760 + (2 * (jj < 2 ? 2 * (pc & 7) + jj : 16 + (pc & 7)) + (pc >> 3)) * 5120 + (ko & 31) * 160 + lane * 16;
        td[j] = sel(hw == 2 ? (lane < 48 && j < 10) : lane < 40, hw == 2 ? d1 : d2);
        PIN(td[j]);
      }
      PIN(vo12);
    }
    int l16 = lane * 16, h16 = sel(ht < 210, ht * 16);
    PIN(l16); PIN(h16);
    gu32* fp1 = FLAG(1, c16) + lane;
    gu32* fp78 = lane < 32 ? FLAG(7, c8) + lane : FLAG(8, c16) + t;
    gu32* fp4 = FLAG(4, 2 * r64 + (g & 1)) + t;
    gu32* fp1w = FLAG(1, c16) + r128 * 4 + hw;
    gu32* fps = FLAG(3, 4 * r128 + hw) + 32 + lane;
    int sto = g * 256 + t * 8;
    PIN(fp1); PIN(fp78); PIN(fp4); PIN(fp1w); PIN(fps); PIN(sto);
    ((LAS v4u*)(L + L_KVF))[ht] = v4u{0u, 0u, 0u, 0u};
    ((LAS v4u*)(L + L_KVF))[256 + ht] = v4u{0u, 0u, 0u, 0u};
    asm volatile("" ::: "memory");
    if (hw != 0) {
      if (hw == 1) {
        sig_wait(sig + 6);
#pragma unroll
        for (int j = 0; j < 5; ++j) {
          const int i8 = 5 * r128 + j;
          st128(tq[j], rs, sel(lane < 40, (i8 & 3) * f40m + f40l), WS_TKV + c16 * 153600 + 102400 + (i8 >> 2) * 2560);
        }
#pragma unroll
        for (int j = 0; j < 5; ++j) {
          const int pp = r64 >> 1, e = 20 * (r64 & 1) + 4 * j;
          const int R = e < 32 ? 32 * pp + e : 32 * (16 + (pp >> 2)) + 8 * (pp & 3) + e - 32;
          st128(tq[5 + j], rs, sel(lane < 40, lane * 16), WS_TO + c8 * 102400 + R * 160);
        }
      }
      if (hw == 3) {
        if (lane < 24) *(LAS f32x4*)(L + L_B1 + lane * 16) = f32x4{bf_lo(ub1.x), bf_hi(ub1.x), bf_lo(ub1.y), bf_hi(ub1.y)};
        if (lane < 20) *(LAS f32x4*)(L + L_B2 + lane * 16) = f32x4{bf_lo(ub2.x), bf_hi(ub2.x), bf_lo(ub2.y), bf_hi(ub2.y)};
      }
      if (hw == 2) asm volatile("" :: "v"(pf0));
      PIN(tag); tag += 1u;
      DRAIN();
      if (hw == 1 && lane < 2) flag_set(lane == 0 ? FLAG(8, c16) + r128 : FLAG(7, c8) + r64, tag);
    }
    if (hw == 0) {
      if (F6W) __builtin_amdgcn_s_sleep(F6W);
      wait_flag(FLAG(6, c16) + lane, 16, tag, lane);
      sig[0] = 1u;
    }
    sig_wait(sig);
    STAMP(1);
    const int kvq = WS_TKV + c16 * 153600, wos = WS_TO + c8 * 102400;
    dma_image<0, 24>(lds0 + L_BKV, l16, srd_ws, kvq, hw, 3 * (bi >> 3));
    dma_image<24, 56>(lds0 + L_BKV, l16, srd_ws, kvq, hw, 4 * (bi >> 3));
    asm volatile("s_waitcnt vmcnt(8)" ::: "memory");
    STAMP(2);
    BAR_RAW();
    dma_image<56, 100>(lds0 + L_BKV, l16, srd_ws, kvq, hw, 5 * (bi >> 3));
    asm volatile("s_waitcnt vmcnt(10)" ::: "memory");
    BAR_RAW();
    if (hw == 1) wait_flag(fp78, 48, tag, lane);
    DRAIN();
    STAMP(3);
    BAR_RAW();
    if (QPOS == 0) dma_image<100, 150>(lds0 + L_BKV, l16, srd_ws, kvq, hw, 6 * (bi >> 3));
    if (hw == 0) items();
    prog_wait(prog, 1);
    if (QPOS == 1) dma_image<100, 150>(lds0 + L_BKV, l16, srd_ws, kvq, hw, 6 * (bi >> 3));
    if (hw >= 2) {
      const __amdgpu_buffer_rsrc_t r12 = __builtin_amdgcn_make_buffer_rsrc((void*)(hw == 2 ? w1 : w2), 0, D * F * 2, 0x00020000);
      const int base = hw == 2 ? r128 * 122880 + c16 * 192 : r64 * 61440 + c8 * 160, step = hw == 2 ? 4 * 3072 : 4 * 1280;
#pragma unroll
      for (int j = 0; j < 12; ++j) tv[j] = __builtin_amdgcn_raw_buffer_load_b128(r12, vo12, (j < 10 || hw == 3) ? base + j * step : OOB, 0);
      asm volatile("s_waitcnt vmcnt(12)" ::: "memory");
    } else {
      dma_image<32, 68, 0, 2>(lds0 + L_WO, l16, srd_ws, wos, hw, bi >> 2);
      asm volatile("s_waitcnt vmcnt(18)" ::: "memory");
    }
    STAMP(4);
    BAR();
    __builtin_amdgcn_s_setprio(2);
    STAMP(11);
    {
      f32x4 pv[4];
#pragma unroll
      for (int ww = 0; ww < 4; ++ww) pv[ww] = *(LAS f32x4*)((LAS char*)red_off + ww * 8192);
      st128(__builtin_bit_cast(v4u, add4(add4(pv[0], pv[1]), add4(pv[2], pv[3]))), rs, kvp_off, 0);
      PROG(1);
      STAMP(12);
      DRAIN();
#pragma unroll
      for (int j = 0; j < 12; ++j) PIN(tv[j]);
    }
    if (lane == 0) flag_set(fp1w, tag);
    if (hw == 0) {
      STAMP(10);
      __builtin_amdgcn_s_sleep(H1W);
      wait_flag(fp1, 64, tag, lane);
      sig[2] = 1u;
    } else {
      if (H1G) __builtin_amdgcn_s_sleep(H1G);
      prog_wait(prog + 1, 1);
#pragma unroll
      for (int i = 0; i < 12; ++i) {
        if (hw >= 2) st128(tv[i], rs, td[i], 0);
        const int j = hw - 1 + 3 * i;
        if (j < 32) {
          const int pc = 2 * ((j + 4 * (bi >> 2)) & 31), po = (pc < 32 ? pc : pc + 36) * 1024;
          dma2(lds0 + L_WO + po, l16, srd_ws, wos + po);
        }
      }
      STAMP(10);
      sig_wait(sig + 2);
    }
    STAMP(5);
    {
      v4u pv[16];
#pragma unroll
      for (int r = 0; r < 16; ++r) pv[r] = ld128(rs, h16, WS_KVP + (c16 * 16 + r) * 4096);
      SCHED();
      f32x4 v = __builtin_bit_cast(f32x4, pv[0]);
#pragma unroll
      for (int r = 1; r < 16; ++r) v = add4(v, __builtin_bit_cast(f32x4, pv[r]));
      v2u o; o.x = pk2(lo2(v)); o.y = pk2(hi2(v));
      if (ht < 210) *(LAS v2u*)((LAS char*)kvf_off) = o;
      if (ht >= 200 && ht < 210) {
#pragma unroll
        for (int c = 1; c < 4; ++c) *(LAS v2u*)((LAS char*)kvf_off + c * 64) = o;
      }
    }
    DRAIN();
    STAMP(6);
    BAR();
    __builtin_amdgcn_s_setprio(0);
    if (hw >= 2 && lane == 0) flag_set(hw == 2 ? FLAG(5, c16) + r128 : FLAG(11, c8) + r64, tag);
    if (hw == 2) {
      f32x4 s0 = {0.f, 0.f, 0.f, 0.f}, s1 = {0.f, 0.f, 0.f, 0.f};
#pragma unroll
      for (int j = 0; j < 10; ++j) {
        s0 = add4(s0, f32x4{bf_lo(tv[j].x), bf_hi(tv[j].x), bf_lo(tv[j].y), bf_hi(tv[j].y)});
        s1 = add4(s1, f32x4{bf_lo(tv[j].z), bf_hi(tv[j].z), bf_lo(tv[j].w), bf_hi(tv[j].w)});
      }
#pragma unroll
      for (int d = 24; d >= 12; d -= 12) {
        f32x4 u0, u1;
#pragma unroll
        for (int e = 0; e < 4; ++e) { u0[e] = __shfl(s0[e], lane + d); u1[e] = __shfl(s1[e], lane + d); }
        s0 = add4(s0, u0); s1 = add4(s1, u1);
      }
      f32x4 sv;
#pragma unroll
      for (int e = 0; e < 4; ++e) { const float a0 = __shfl(s0[e], lane >> 1), a1 = __shfl(s1[e], lane >> 1); sv[e] = (lane & 1) ? a1 : a0; }
      st128(__builtin_bit_cast(v4u, sv), rs, sel(lane < 24, lane * 16), WS_CS + (c16 * 16 + r128) * 384);
      publish(FLAG(9, c16) + r128, tag, lane);
    }
    if (hw == 1) {
      __builtin_amdgcn_s_sleep(40);
      wait_flag(lane < 32 ? FLAG(11, c8) + lane : FLAG(5, c16) + t, 48, tag, lane);
      sig[3] = 1u;
    }
    sig_wait(sig + 3);
    prog_wait(prog, 2);
    if (hw == 0) {
      wait_flag(FLAG(9, c16) + lane, 16, tag, lane);
      v4u pv[16];
      const int co = sel(lane < 24, l16);
#pragma unroll
      for (int r = 0; r < 16; ++r) pv[r] = ld128(rs, co, WS_CS + (c16 * 16 + r) * 384);
      SCHED();
      f32x4 v = __builtin_bit_cast(f32x4, pv[0]);
#pragma unroll
      for (int r = 1; r < 16; ++r) v = add4(v, __builtin_bit_cast(f32x4, pv[r]));
      if (lane < 24) *(LAS f32x4*)(L + L_CS + lane * 16) = v;
    } else {
      dma_image<70, 120, 0, 3>(lds0 + L_W1, l16, srd_ws, WS_T1 + c16 * 122880, hw - 1, 6 * (bi >> 3));
      prog_wait(prog, 3);
      dma_image<0, 5 * P3K - 30, 0, 3>(lds0 + L_W1, l16, srd_ws, WS_T1 + c16 * 122880, hw - 1, bi >> 3);
      STAMP(7);
      prog_wait(prog, 4);
      dma_image<5 * P3K - 30, 70, 0, 3>(lds0 + L_W1, l16, srd_ws, WS_T1 + c16 * 122880, hw - 1, bi >> 3);
    }
    DRAIN();
    STAMP(8);
    BAR();
    {
      __builtin_amdgcn_s_setprio(2);
      __builtin_amdgcn_s_sleep(STW);
      wait_flag(fps, 16, tag, lane);
      v2u s0[2], s1[2];
#pragma unroll
      for (int m = 0; m < 2; ++m) {
        s0[m] = ld64(rs, sto, WS_ST + (8 * r128 + 2 * hw + m) * 1024);
        s1[m] = ld64(rs, sto + 128, WS_ST + (8 * r128 + 2 * hw + m) * 1024);
      }
#pragma unroll
      for (int m = 0; m < 2; ++m) {
        const f32x2 p0 = __builtin_bit_cast(f32x2, s0[m]), p1 = __builtin_bit_cast(f32x2, s1[m]);
        const float a0 = p0[0], q0 = p0[1], a1 = p1[0], q1 = p1[1];
        float mean = xsum32(xsum16(a0 + a1)) * 0.125f;
        PIN(mean);
        float m2 = xsum32(xsum16(q0 + q1 + 80.f * ((a0 - mean) * (a0 - mean) + (a1 - mean) * (a1 - mean))));
        PIN(m2);
        const float var = m2 * (1.f / D);
        const float r1 = __builtin_amdgcn_rsqf(var + LN_EPS);
        const float rr = r1 * __builtin_amdgcn_rsqf(var * r1 * r1 + LN_EPS);
        if (g == 0) *(LAS f32x2*)(L + L_KVF + ((2 * hw + m) * 16 + t) * 8) = f32x2{rr, -mean * rr};
      }
      asm volatile("" ::: "memory");
      sig[12 + hw] = 1u;
      __builtin_amdgcn_s_setprio(0);
      STAMP(20);
    }
    const int w2s = WS_T2 + c8 * 245760;
    prog_wait(prog, 5);
    dma_image<0, 30>(lds0 + L_W2, l16, srd_ws, w2s, hw, bi >> 2);
    STAMP(14);
    prog_wait(prog, 6);
    dma_image<30, W2S>(lds0 + L_W2, l16, srd_ws, w2s, hw, bi >> 2);
    prog_wait(prog, 7);
    if (hw == 0) {
      DRAIN();
      STAMP(9);
      wait_flag(fp4, 32, tag, lane);
    } else {
      dma_image<W2S, 110, 0, 3>(lds0 + L_W2, l16, srd_ws, w2s, hw - 1, bi >> 2);
      prog_wait(prog, 8);
      dma_image<110, 134, 0, 3>(lds0 + L_W2, l16, srd_ws, w2s, hw - 1, bi >> 2);
      prog_wait(prog, 9);
      dma_image<134, 146, 0, 3>(lds0 + L_W2, l16, srd_ws, w2s, hw - 1, 0);
      asm volatile("s_waitcnt vmcnt(12)" ::: "memory");
      STAMP(9);
    }
    STAMP(15);
    BAR_RAW();
    dma_image<146, 150>(lds0 + L_W2, l16, srd_ws, w2s, hw, 0);
    prog_wait(prog, 10);
    if (hw == 0) wait_flag(fp4 + 6 * FLAG_SET / 4, 32, tag, lane);
    else dma_image<150, 198, 0, 3>(lds0 + L_W2 - 153600, l16, srd_ws, w2s, hw - 1, 3 * (bi >> 2));
    asm volatile("s_waitcnt vmcnt(16)" ::: "memory");
    STAMP(17);
    BAR_RAW();
    dma_image<198, 200>(lds0 + L_W2 - 153600, l16, srd_ws, w2s, hw, 0);
    dma_image<200, 240>(lds0 + L_W2 - 153600, l16, srd_ws, w2s, hw, 2 * (bi >> 2));
    asm volatile("s_waitcnt vmcnt(10)" ::: "memory");
    BAR_RAW();
    DRAIN();
    BAR_RAW();
  }

}
}

extern "C" void kernel_launch(void* const* d_in, const int* in_sizes, int n_in, void* d_out, int out_size, void* d_ws,
                              size_t ws_size, hipStream_t stream) {
  const unsigned short* x = (const unsigned short*)d_in[0];
  const unsigned short* wqkv = (const unsigned short*)d_in[1];
  const unsigned short* wo = (const unsigned short*)d_in[2];
  const unsigned short* w1 = (const unsigned short*)d_in[4];
  const unsigned short* b1 = (const unsigned short*)d_in[5];
  const unsigned short* w2 = (const unsigned short*)d_in[6];
  const unsigned short* b2 = (const unsigned short*)d_in[7];
  fused_block<<<256, 512, 0, stream>>>(x, wqkv, (unsigned char*)d_ws, wo, w1, b1, w2, b2, (unsigned short*)d_out);
}
```

```cpp
#include <hip/hip_runtime.h>
#include <hip/hip_bf16.h>

#define GAS __attribute__((address_space(1)))
#define LAS __attribute__((address_space(3)))
#define RLX_AGENT __ATOMIC_RELAXED, __HIP_MEMORY_SCOPE_AGENT
typedef unsigned v4u __attribute__((ext_vector_type(4)));
typedef unsigned v2u __attribute__((ext_vector_type(2)));
typedef float f32x4 __attribute__((ext_vector_type(4)));
typedef float f32x2 __attribute__((ext_vector_type(2)));
typedef __bf16 bf16x8 __attribute__((ext_vector_type(8)));
typedef __bf16 bf16x2 __attribute__((ext_vector_type(2)));
typedef short s16x4 __attribute__((ext_vector_type(4)));
typedef GAS unsigned gu32;

namespace {
constexpr int S = 2048, D = 640, F = 1536;
constexpr float LN_EPS = 1e-5f;
constexpr int AUX_SC1 = 16;
constexpr unsigned kSpinCap = 1u << 17;
constexpr int OOB = 0x7FFFFF00;

constexpr int WS_EPOCH = 0;
constexpr int WS_FLAG = 4096;
constexpr int FLAG_SET = 64 * 256;
constexpr int WS_XN = 1 << 20;
constexpr int WS_O = WS_XN + 128 * 20 * 1024;
constexpr int WS_H = WS_O + 128 * 20 * 1024;
constexpr int WS_H1 = WS_H + 128 * 20 * 1024;
constexpr int WS_KVP = WS_H1 + 128 * 48 * 1024;
constexpr int WS_ST = WS_KVP + 16 * 16 * 4096;
constexpr int WS_TKV = WS_ST + 2048 * 64;
constexpr int WS_TO = WS_TKV + 16 * 153600;
constexpr int WS_T1 = WS_TO + 8 * 102400;
constexpr int WS_T2 = WS_T1 + 16 * 122880;
constexpr int WS_CS = WS_T2 + 16 * 122880;
constexpr int WS_BYTES = WS_CS + 16 * 16 * 384;

constexpr int L_BKV = 0, L_BQ = 102400;
constexpr int L_RED = 0, L_KVF = 153600;
constexpr int L_WO = 0, L_W1 = 30720, L_W2 = 0, L_CS = 161792;
constexpr int L_B1 = L_CS + 1152, L_B2 = L_CS + 1536;
constexpr int LDS_BYTES = 163840;

__device__ __forceinline__ f32x4 mfma(v4u a, v4u b, f32x4 c) {
  return __builtin_amdgcn_mfma_f32_16x16x32_bf16(__builtin_bit_cast(bf16x8, a), __builtin_bit_cast(bf16x8, b), c, 0, 0, 0);
}
__device__ __forceinline__ unsigned pk2(f32x2 v) { return __builtin_bit_cast(unsigned, __builtin_convertvector(v, bf16x2)); }
__device__ __forceinline__ float bf_lo(unsigned u) { return __builtin_bit_cast(float, u << 16); }
__device__ __forceinline__ float bf_hi(unsigned u) { return __builtin_bit_cast(float, u & 0xFFFF0000u); }
__device__ __forceinline__ f32x2 bf2(unsigned u) { return f32x2{bf_lo(u), bf_hi(u)}; }
__device__ __forceinline__ f32x2 lo2(f32x4 v) { return f32x2{v[0], v[1]}; }
__device__ __forceinline__ f32x2 hi2(f32x4 v) { return f32x2{v[2], v[3]}; }
__device__ __forceinline__ v4u mk_srd(const void* p, unsigned bytes) {
  const unsigned long long a = (unsigned long long)p;
  v4u r;
  r.x = (unsigned)a; r.y = (unsigned)(a >> 32) & 0xFFFFu; r.z = bytes; r.w = 0x00020000u;
  return r;
}
#define DRAIN() asm volatile("s_waitcnt vmcnt(0)" ::: "memory")
#define BAR() asm volatile("s_waitcnt lgkmcnt(0)\n\ts_barrier" ::: "memory")
#define BAR_RAW() asm volatile("s_barrier" ::: "memory")
#define SCHED() __builtin_amdgcn_sched_barrier(0)
#define PIN(x) asm volatile("" : "+v"(x))

__device__ __forceinline__ void dma2(unsigned lds_addr, unsigned voff, v4u srd, unsigned soff) {
  unsigned keep;
  asm volatile("s_mov_b32 %0, m0\n\ts_mov_b32 m0, %1\n\ts_nop 0\n\t"
               "buffer_load_dwordx4 %2, %3, %4 offen sc1 lds\n\t"
               "buffer_load_dwordx4 %2, %3, %4 offen offset:1024 sc1 lds\n\t"
               "s_mov_b32 m0, %0"
               : "=&s"(keep) : "s"(lds_addr), "v"(voff), "s"(srd), "s"(soff) : "memory");
}
template <int LO, int HI, int NW = 4>
__device__ __forceinline__ void dma_image(unsigned lds_base, unsigned voff, v4u srd, int ws_off, int hw, int rot) {
  constexpr int NP = (HI - LO) / 2;
#pragma unroll 1
  for (int i = hw; i < NP; i += NW) {
    int j = i + rot;
    if (j >= NP) j -= NP;
    dma2(lds_base + LO * 1024 + j * 2048, voff, srd, ws_off + LO * 1024 + j * 2048);
  }
}
__device__ __forceinline__ LAS char* lp(unsigned a) { return (LAS char*)(size_t)a; }
__device__ __forceinline__ v2u rdtr(LAS char* p) { return __builtin_bit_cast(v2u, __builtin_amdgcn_ds_read_tr16_b64_v4i16((LAS s16x4*)p)); }
struct LBase {
  unsigned a[3];
  __device__ __forceinline__ explicit LBase(unsigned v) { a[0] = v; a[1] = v + 65536; a[2] = v + 131072; PIN(a[0]); PIN(a[1]); PIN(a[2]); }
  __device__ __forceinline__ v2u rd(int off) const { return rdtr(lp(a[off >> 16]) + (off & 65535)); }
};
template <int MT, int NT, int KS, int PD, bool WA, int M0 = 0, class RB, class GA, class PRE>
__device__ __forceinline__ void kloop(f32x4 (&acc)[MT][NT], RB rb, GA ga, PRE pre) {
  v2u bl[PD + 1][NT], bh[PD + 1][NT];
#pragma unroll
  for (int m = M0; m < MT; ++m)
#pragma unroll
    for (int s = 0; s < NT; ++s) acc[m][s] = f32x4{0.f, 0.f, 0.f, 0.f};
#pragma unroll
  for (int ks = 0; ks < PD; ++ks)
#pragma unroll
    for (int s = 0; s < NT; ++s) { bl[ks][s] = rb(ks, s, 0); bh[ks][s] = rb(ks, s, 1); }
  SCHED();
#pragma unroll
  for (int ks = 0; ks < KS; ++ks) {
    pre(ks);
#pragma unroll
    for (int s = 0; s < NT; ++s)
#pragma unroll
      for (int m = 0; m < MT; ++m) {
        const v2u lo = bl[ks % (PD + 1)][s], hi = bh[ks % (PD + 1)][s];
        const v4u wv = {lo.x, lo.y, hi.x, hi.y};
        acc[m][s] = WA ? mfma(wv, ga(ks, m), acc[m][s]) : mfma(ga(ks, m), wv, acc[m][s]);
        if (ks + PD < KS) {
          if (MT == 1 || m == 0) bl[(ks + PD) % (PD + 1)][s] = rb(ks + PD, s, 0);
          if (MT == 1 || m == 1) bh[(ks + PD) % (PD + 1)][s] = rb(ks + PD, s, 1);
        }
        SCHED();
      }
  }
}
constexpr int ld_hi(int ks, int s0, int W, int KS) {
  const int a = s0 + 2 * (ks + 1), b = ks + 1 + W, c = a < b ? a : b;
  return c < KS ? c : KS;
}
__device__ __forceinline__ v4u ld128(__amdgpu_buffer_rsrc_t rs, int voff, int soff) {
  return __builtin_amdgcn_raw_buffer_load_b128(rs, voff, soff, AUX_SC1);
}
__device__ __forceinline__ v2u ld64(__amdgpu_buffer_rsrc_t rs, int voff, int soff) {
  return __builtin_amdgcn_raw_buffer_load_b64(rs, voff, soff, AUX_SC1);
}
__device__ __forceinline__ void st128(v4u v, __amdgpu_buffer_rsrc_t rs, int voff, int soff) {
  __builtin_amdgcn_raw_buffer_store_b128(v, rs, (int)((unsigned)voff + (unsigned)soff), 0, AUX_SC1);
}
__device__ __forceinline__ int sel(bool c, int a) {
  PIN(a);
  return c ? a : OOB;
}
__device__ __forceinline__ v4u ld_split(__amdgpu_buffer_rsrc_t rs, int lane, int soff) {
  const v2u a = ld64(rs, lane * 8, soff), b = ld64(rs, lane * 8 + 512, soff);
  return v4u{a.x, a.y, b.x, b.y};
}
__device__ __forceinline__ void st64(v2u v, __amdgpu_buffer_rsrc_t rs, int voff, int soff) {
  __builtin_amdgcn_raw_buffer_store_b64(v, rs, voff, soff, AUX_SC1);
}
__device__ __forceinline__ void flag_set(gu32* p, unsigned tag) {
  asm volatile("global_atomic_swap %0, %1, off sc1" :: "v"(p), "v"(tag) : "memory");
}
__device__ __forceinline__ void wait_flag(gu32* p, int n, unsigned tag, int lane) {
#pragma unroll 1
  for (unsigned spins = 0; spins < kSpinCap; ++spins) {
    unsigned v = tag;
    if (lane < n) asm volatile("global_atomic_add %0, %1, %2, off sc0 sc1\n\ts_waitcnt vmcnt(0)" : "=&v"(v) : "v"(p), "v"(0u) : "memory");
    if (__all(v == tag)) break;
    __builtin_amdgcn_s_sleep(1);
  }
  asm volatile("" ::: "memory");
}
__device__ __forceinline__ void sig_wait(volatile LAS unsigned* p) {
#pragma unroll 1
  for (unsigned spins = 0; spins < 64 * kSpinCap; ++spins) {
    if (*p != 0u) break;
    __builtin_amdgcn_s_sleep(1);
  }
  asm volatile("" ::: "memory");
}
__device__ __forceinline__ void prog_wait(volatile LAS v4u* p, unsigned stage) {
#pragma unroll 1
  for (unsigned spins = 0; spins < 64 * kSpinCap; ++spins) {
    const v4u v = *p;
    if (min(min(v.x, v.y), min(v.z, v.w)) >= stage) break;
    __builtin_amdgcn_s_sleep(1);
  }
  asm volatile("" ::: "memory");
}
__device__ __forceinline__ void publish(gu32* flag, unsigned tag, int lane) {
  PIN(flag);
  DRAIN();
  if (lane == 0) flag_set(flag, tag);
}
__device__ __forceinline__ f32x4 add4(f32x4 a, f32x4 b) {
  f32x4 r;
#pragma unroll
  for (int i = 0; i < 4; ++i) { float x; asm("v_add_f32 %0, %1, %2" : "=v"(x) : "v"(a[i]), "v"(b[i])); r[i] = x; }
  return r;
}
__device__ __forceinline__ float xsum32(float x) {
  unsigned u = __builtin_bit_cast(unsigned, x), v = u;
  asm("" : "+v"(v));
  auto r = __builtin_amdgcn_permlane32_swap(u, v, false, false);
  const unsigned a = r[0], b = r[1];
  return __builtin_bit_cast(float, a) + __builtin_bit_cast(float, b);
}
__device__ __forceinline__ float xsum16(float x) {
  unsigned u = __builtin_bit_cast(unsigned, x), v = u;
  asm("" : "+v"(v));
  auto r = __builtin_amdgcn_permlane16_swap(u, v, false, false);
  const unsigned a = r[0], b = r[1];
  return __builtin_bit_cast(float, a) + __builtin_bit_cast(float, b);
}
__device__ __forceinline__ float xsum8(float x) {
  return x + __builtin_bit_cast(float, __builtin_amdgcn_update_dpp(0, __builtin_bit_cast(int, x), 0x128, 0xf, 0xf, false));
}
__device__ __forceinline__ f32x2 phi2(f32x2 t) {
  const f32x2 u = t * 1.4426950408889634f, v = t + 1e-6f;
  const f32x2 e = {__builtin_amdgcn_fmed3f(__builtin_amdgcn_exp2f(u[0]), 0.f, 1.f), __builtin_amdgcn_fmed3f(__builtin_amdgcn_exp2f(u[1]), 0.f, 1.f)};
  return f32x2{fmaxf(v[0], 1e-6f), fmaxf(v[1], 1e-6f)} + e;
}
__device__ __forceinline__ f32x2 gelu2(f32x2 x) {
  const f32x2 u = x * (x * x * -0.1029432397f + -2.302208198f);
  const f32x2 d = f32x2{__builtin_amdgcn_exp2f(u[0]), __builtin_amdgcn_exp2f(u[1])} + 1.f;
  return x * f32x2{__builtin_amdgcn_rcpf(d[0]), __builtin_amdgcn_rcpf(d[1])};
}

__global__ void __launch_bounds__(512, 2)
fused_block(const unsigned short* __restrict__ x, const unsigned short* __restrict__ wqkv, unsigned char* ws, const unsigned short* __restrict__ wo,
            const unsigned short* __restrict__ w1, const unsigned short* __restrict__ b1, const unsigned short* __restrict__ w2,
            const unsigned short* __restrict__ b2, unsigned short* __restrict__ out) {
  __shared__ __attribute__((aligned(16))) char smem[LDS_BYTES];
  unsigned long long pc0;
  asm volatile("s_getpc_b64 %0" : "=s"(pc0));
  LAS char* L = (LAS char*)smem;
  const unsigned lds0 = (unsigned)(size_t)L;
  const int tid = threadIdx.x, lane = tid & 63, w = __builtin_amdgcn_readfirstlane(tid >> 6);
  const int b = blockIdx.x, xcd = b & 7, bi = b >> 3;
  const int g = lane >> 4, t = lane & 15;
  const __amdgpu_buffer_rsrc_t rs = __builtin_amdgcn_make_buffer_rsrc((void*)ws, 0, WS_BYTES, 0x00020000);
  gu32* flags = (gu32*)(ws + WS_FLAG);
#define FLAG(set, grp) (flags + ((set) * FLAG_SET + (grp) * 256) / 4)
  asm volatile("" :: "s"(x), "s"(wqkv), "s"(ws), "s"(wo), "s"(w1), "s"(b1), "s"(w2), "s"(b2));
  const int r128 = 4 * (xcd >> 1) + (bi >> 3), c16 = 8 * (xcd & 1) + (bi & 7);
  const int r64 = 8 * (xcd >> 1) + (bi >> 2), c8 = 4 * (xcd & 1) + (bi & 3);

  const __amdgpu_buffer_rsrc_t rx = __builtin_amdgcn_make_buffer_rsrc((void*)x, 0, S * D * 2, 0x00020000);
  const int hw = w - 4, ht = tid - 256;
  volatile LAS unsigned* sig = (volatile LAS unsigned*)(L + L_CS + 1024);
  volatile LAS v4u* prog = (volatile LAS v4u*)(L + L_CS + 1088);
#define PROG(n) do { asm volatile("" ::: "memory"); sig[16 + w] = (n); } while (0)

  const __amdgpu_buffer_rsrc_t rq = __builtin_amdgcn_make_buffer_rsrc((void*)wqkv, 0, D * 3 * D * 2, 0x00020000);
  v4u tv[12];
  unsigned ep;
  const int q10 = lane / 10, c10 = lane % 10;
  if (w < 4) {
#pragma unroll
    for (int j = 0; j < 3; ++j)
      tv[j] = __builtin_amdgcn_raw_buffer_load_b128(rx, (lane & 7) * 1280 + (lane >> 3) * 16, (j < 2 || w < 2) ? 8 * b * 1280 + (w + 4 * j) * 128 : OOB, 0);
    ep = __hip_atomic_load((gu32*)(ws + WS_EPOCH), RLX_AGENT);
  } else if (w == 4) {
    const int vo = sel(lane < 40, q10 * 3840 + (c10 < 5 ? 1280 : 2480) + c10 * 16);
#pragma unroll
    for (int j = 0; j < 10; ++j) tv[j] = __builtin_amdgcn_raw_buffer_load_b128(rq, vo, (40 * r128 + 4 * j) * 3840 + c16 * 80, 0);
  } else if (w == 7) {
    *(LAS v4u*)(L + L_CS + 512 + lane * 16) = v4u{0u, 0u, 0u, 0u};
  }
  BAR();

  if (w < 4) {
  unsigned tag = ep;
  const int grp32 = 4 * r128 + w;
  constexpr int AW = 6;

  {
    const int tok = 8 * b + (lane & 7), cl = lane >> 3;
    const bool has3 = w < 2;
    int xd = ((tok >> 4) * 20 + 2 * w + (cl >> 2)) * 1024 + ((cl & 3) * 16 + (tok & 15)) * 16;
    PIN(xd);
    SCHED();
    f32x2 f[12];
#pragma unroll
    for (int j = 0; j < 3; ++j)
#pragma unroll
      for (int e = 0; e < 4; ++e) f[j * 4 + e] = bf2(tv[j][e]);
    f32x2 s2 = (f[0] + f[1]) + (f[2] + f[3]) + ((f[4] + f[5]) + (f[6] + f[7])) + ((f[8] + f[9]) + (f[10] + f[11]));
    const float mloc = xsum32(xsum16(xsum8(s2[0] + s2[1]))) * (has3 ? 1.f / 192.f : 1.f / 128.f);
    const float m3 = has3 ? mloc : 0.f;
    f32x2 q2 = {0.f, 0.f}, q3 = {0.f, 0.f};
#pragma unroll
    for (int j = 0; j < 12; j += 2) {
      const f32x2 d0 = f[j] - (j < 8 ? mloc : m3), d1 = f[j + 1] - (j < 8 ? mloc : m3);
      q2 += d0 * d0; q3 += d1 * d1;
    }
    q2 += q3;
    const float m2loc = xsum32(xsum16(xsum8(q2[0] + q2[1])));
    if (lane < 8) *(volatile LAS f32x4*)(L + L_CS + 512 + (lane * 4 + w) * 16) = f32x4{mloc, m2loc, 1.f, 1.f};
    f32x4 pr[4];
#pragma unroll 1
    for (unsigned spins = 0; spins < 64 * kSpinCap; ++spins) {
      bool ok = true;
#pragma unroll
      for (int ww = 0; ww < 4; ++ww) {
        pr[ww] = *(volatile LAS f32x4*)(L + L_CS + 512 + ((lane & 7) * 4 + ww) * 16);
        ok = ok && pr[ww][2] == 1.f;
      }
      if (__all(ok)) break;
    }
    const f32x4 p01 = {pr[0][0], pr[0][1], pr[1][0], pr[1][1]}, p23 = {pr[2][0], pr[2][1], pr[3][0], pr[3][1]};
    const float mean = (p01[0] + p01[2]) * 0.3f + (p23[0] + p23[2]) * 0.2f;
    const float e0 = p01[0] - mean, e1 = p01[2] - mean, e2 = p23[0] - mean, e3 = p23[2] - mean;
    const float m2 = (p01[1] + p01[3]) + (p23[1] + p23[3]) + 192.f * (e0 * e0 + e1 * e1) + 128.f * (e2 * e2 + e3 * e3);
    const float rstd = __builtin_amdgcn_rsqf(m2 * (1.f / D) + LN_EPS);
#pragma unroll
    for (int j = 0; j < 3; ++j) {
      v4u o;
#pragma unroll
      for (int e = 0; e < 4; ++e) o[e] = pk2((f[j * 4 + e] - mean) * rstd);
      st128(o, rs, (j < 2 || has3) ? xd : OOB, WS_XN + j * 8192);
    }
    PIN(tag); tag += 1u;
    publish(FLAG(0, b >> 2) + (b & 3) * 4 + w, tag, lane);
    if (w == 0) sig[6] = 1u;
  }

  v4u xr[3];
  {
    wait_flag(FLAG(0, grp32) + lane, 16, tag, lane);
    v4u a[20][2];
    const int abase = WS_XN + 2 * grp32 * 20 * 1024;
#pragma unroll
    for (int ks = 0; ks < AW; ++ks)
#pragma unroll
      for (int m = 0; m < 2; ++m) a[ks][m] = ld128(rs, lane * 16, abase + (m * 20 + ks) * 1024);
    SCHED();
    BAR_RAW();
    {
      f32x4 acc[2][5];
      const LBase bp(lds0 + L_BKV + (lane >> 2) * 160 + (lane & 3) * 8);
      kloop<2, 5, 20, 1, false>(acc, [&](int ks, int s, int h) { return bp.rd(ks * 5120 + h * 2560 + s * 32); },
        [&](int ks, int m) { return a[ks][m]; },
        [&](int ks) {
#pragma unroll
          for (int j = ld_hi(ks - 1, AW, AW, 20); j < ld_hi(ks, AW, AW, 20); ++j)
#pragma unroll
            for (int m = 0; m < 2; ++m) a[j][m] = ld128(rs, lane * 16, abase + (m * 20 + j) * 1024);
          if (ks == 3 || ks == 10) BAR_RAW();
          if (ks == 14) PROG(1);
        });
      v4u ka[3], vb[3];
#pragma unroll
      for (int s = 0; s < 3; ++s)
#pragma unroll
        for (int m = 0; m < 2; ++m) {
          vb[s][2 * m] = pk2(lo2(acc[m][s + 2])); vb[s][2 * m + 1] = pk2(hi2(acc[m][s + 2]));
        }
#pragma unroll
      for (int e = 0; e < 4; ++e) vb[0][e] = t < 8 ? 0x3F803F80u : vb[0][e];
      const f32x4 z4 = {0.f, 0.f, 0.f, 0.f};
      LAS f32x4* red = (LAS f32x4*)(L + L_RED) + w * 512 + lane;
#pragma unroll
      for (int s = 0; s < 3; ++s) {
#pragma unroll
        for (int m = 0; m < 2; ++m) { ka[s][2 * m] = pk2(phi2(lo2(acc[m][s]))); ka[s][2 * m + 1] = pk2(phi2(hi2(acc[m][s]))); }
#pragma unroll
        for (int sb = 0; sb < 3; ++sb)
          if (s > 0 || sb < 2) red[(3 * s + sb - (s > 0)) * 64] = mfma(ka[s], vb[sb], z4);
      }
      BAR();
    }
    f32x4 qa[2][3];
    {
      unsigned bp0 = lds0 + L_BQ + (lane >> 2) * 64 + (lane & 3) * 8 + (g & 1) * 32;
      unsigned bp1 = lds0 + L_BQ + (lane >> 2) * 64 + (lane & 3) * 8 + 32 - (g & 1) * 32;
      unsigned bp2 = lds0 + L_BQ + 2048 + (lane >> 2) * 16 + (lane & 1) * 8;
      PIN(bp0); PIN(bp1); PIN(bp2);
      kloop<2, 3, 20, 2, true>(qa,
        [&](int ks, int s, int h) {
          return s == 0 ? rdtr(lp(bp0) + ks * 2560 + h * 1024) : s == 1 ? rdtr(lp(bp1) + ks * 2560 + h * 1024) : rdtr(lp(bp2) + ks * 2560 + h * 256);
        },
        [&](int ks, int m) { return a[ks][m]; },
        [&](int ks) {});
    }
    v4u pq0[2], pq1[2];
#pragma unroll
    for (int m = 0; m < 2; ++m) {
      pq0[m].x = pk2(phi2(lo2(qa[m][0]))); pq0[m].y = pk2(phi2(hi2(qa[m][0])));
      pq0[m].z = pk2(phi2(lo2(qa[m][1]))); pq0[m].w = pk2(phi2(hi2(qa[m][1])));
      const unsigned lo = pk2(phi2(lo2(qa[m][2]))), hi = pk2(phi2(hi2(qa[m][2])));
      pq1[m].x = g < 2 ? lo : 0u; pq1[m].y = g < 2 ? hi : 0u; pq1[m].z = 0u; pq1[m].w = 0u;
      PIN(pq0[m]); PIN(pq1[m]);
    }
    BAR_RAW();
    {
      v4u kv[4][2];
#pragma unroll
      for (int ks = 0; ks < 2; ++ks)
#pragma unroll
        for (int es = 3; es >= ks; --es) kv[es][ks] = *(LAS v4u*)(L + L_KVF + (es * 2 + ks) * 1024 + lane * 16);
      SCHED();
      f32x4 o[2][4];
#pragma unroll
      for (int es = 3; es >= 0; --es)
#pragma unroll
        for (int m = 0; m < 2; ++m) {
          o[m][es] = mfma(kv[es][0], pq0[m], f32x4{0.f, 0.f, 0.f, 0.f});
          SCHED();
        }
#pragma unroll
      for (int es = 3; es >= 1; --es)
#pragma unroll
        for (int m = 0; m < 2; ++m) {
          o[m][es] = mfma(kv[es][1], pq1[m], o[m][es]);
          SCHED();
        }
#pragma unroll
      for (int m = 0; m < 2; ++m) {
        const float i0 = __builtin_amdgcn_rcpf(o[m][3][0] + 1e-6f), i1 = __builtin_amdgcn_rcpf(o[m][3][1] + 1e-6f);
        const float im = g == 0 ? i0 : i1;
        v4u ov;
        ov.x = pk2(lo2(o[m][0]) * i0); ov.y = pk2(hi2(o[m][0]) * i0);
        ov.z = pk2(lo2(o[m][1]) * im); ov.w = pk2(hi2(o[m][1]) * im);
        v2u ol; ol.x = pk2(lo2(o[m][2]) * i1); ol.y = pk2(hi2(o[m][2]) * i1);
        const int tt = 2 * grp32 + m;
        st128(ov, rs, lane * 16, WS_O + (tt * 20 + c16) * 1024);
        st64(ol, rs, g < 2 ? lane * 8 : OOB, WS_O + (tt * 20 + 16) * 1024 + c16 * 256);
      }
      publish(FLAG(2, grp32) + c16, tag, lane);
    }
  }

  {
    const int tt = 4 * r64 + w;
#pragma unroll
    for (int j = 0; j < 3; ++j)
      xr[j] = __builtin_amdgcn_raw_buffer_load_b128(rx, sel(j < 2 || g < 2, t * 1280 + g * 16), (4 * r64 + w) * 16 * 1280 + c8 * 160 + j * 64, 0);
    v4u eye[2];
#pragma unroll
    for (int hf = 0; hf < 2; ++hf) {
      const int e = 16 * hf + t - 8 * g;
#pragma unroll
      for (int d = 0; d < 4; ++d) eye[hf][d] = e == 2 * d ? 0x3F80u : e == 2 * d + 1 ? 0x3F800000u : 0u;
      PIN(eye[hf]);
    }
    f32x4 acc[1][5];
    wait_flag(FLAG(2, tt >> 1) + lane, 16, tag, lane);
    if (b == 0 && tid == 0) __hip_atomic_store((gu32*)(ws + WS_EPOCH), tag, RLX_AGENT);
    v4u a[20];
#pragma unroll
    for (int ks = 0; ks < 2 * AW; ++ks) a[ks] = ld128(rs, lane * 16, WS_O + (tt * 20 + ks) * 1024);
    SCHED();
#pragma unroll
    for (int s = 0; s < 5; ++s) { acc[0][s] = mfma(eye[s & 1], xr[s >> 1], f32x4{0.f, 0.f, 0.f, 0.f}); PIN(acc[0][s]); }
    const LBase bp(lds0 + L_WO + (lane >> 2) * 160 + (lane & 3) * 8);
    kloop<1, 5, 20, 1, true, 1>(acc, [&](int ks, int s, int h) { return bp.rd(ks * 5120 + h * 2560 + s * 32); },
      [&](int ks, int m) { return a[ks]; },
      [&](int ks) {
#pragma unroll
        for (int j = ld_hi(ks - 1, 2 * AW, 2 * AW, 20); j < ld_hi(ks, 2 * AW, 2 * AW, 20); ++j)
          a[j] = j < 16 ? ld128(rs, lane * 16, WS_O + (tt * 20 + j) * 1024) : ld_split(rs, lane, WS_O + (tt * 20 + j) * 1024);
        if (ks == 8) PROG(2);
        if (ks == 14) PROG(3);
      });
    v2u hb[5];
    f32x2 hv[10];
#pragma unroll
    for (int s = 0; s < 5; ++s) {
      hb[s].x = pk2(lo2(acc[0][s]));
      hb[s].y = pk2(hi2(acc[0][s]));
      hv[2 * s] = bf2(hb[s].x); hv[2 * s + 1] = bf2(hb[s].y);
    }
    st128(v4u{hb[0].x, hb[0].y, hb[1].x, hb[1].y}, rs, lane * 16, WS_H + (tt * 20 + 2 * c8) * 1024);
    st128(v4u{hb[2].x, hb[2].y, hb[3].x, hb[3].y}, rs, lane * 16, WS_H + (tt * 20 + 2 * c8 + 1) * 1024);
    st64(hb[4], rs, lane * 8, WS_H + (tt * 20 + 16) * 1024 + c8 * 512);
    PROG(4);
    const f32x2 s2 = ((hv[0] + hv[1]) + (hv[2] + hv[3])) + ((hv[4] + hv[5]) + (hv[6] + hv[7])) + (hv[8] + hv[9]);
    const float mc = xsum32(xsum16(s2[0] + s2[1])) * (1.f / 80.f);
    f32x2 q2 = {0.f, 0.f}, q3 = {0.f, 0.f};
#pragma unroll
    for (int j = 0; j < 10; j += 2) { const f32x2 d0 = hv[j] - mc, d1 = hv[j + 1] - mc; q2 += d0 * d0; q3 += d1 * d1; }
    q2 += q3;
    const float m2 = xsum32(xsum16(q2[0] + q2[1]));
    v2u sv = {__builtin_bit_cast(unsigned, mc), __builtin_bit_cast(unsigned, m2)};
    PIN(sv);
    publish(FLAG(3, tt >> 1) + (tt & 1) * 8 + c8, tag, lane);
    st64(sv, rs, g == 0 ? t * 8 : OOB, WS_ST + (tt * 8 + c8) * 128);
  }

  {
    gu32* fst = FLAG(3, 2 * r64 + (w >> 1)) + 32 + (w & 1) * 8 + c8;
    PIN(fst);
    wait_flag(FLAG(3, grp32) + lane, 16, tag, lane);
    DRAIN();
    if (lane == 0) flag_set(fst, tag);
    v4u a[20][2];
    const int abase = WS_H + 2 * grp32 * 20 * 1024;
#pragma unroll
    for (int ks = 0; ks < AW; ++ks)
#pragma unroll
      for (int m = 0; m < 2; ++m) a[ks][m] = ld128(rs, lane * 16, abase + (m * 20 + ks) * 1024);
    SCHED();
    float nm[2], rr[2];
    BAR_RAW();
    const unsigned l64 = lds0 + (lane >> 2) * 64 + (lane & 3) * 8;
    const LBase bp0(l64 + (g & 1) * 32), bp1(l64 + 32 - (g & 1) * 32);
    const int h1base = WS_H1 + 2 * grp32 * 48 * 1024;
    auto finish = [&](const f32x4& c0, const f32x4& c1, int j, int m) {
      v4u o;
#pragma unroll
      for (int s = 0; s < 2; ++s) {
        const f32x4 cs = *(LAS f32x4*)(L + L_CS + ((2 * j + s) * 4 + g) * 16), bb = *(LAS f32x4*)(L + L_B1 + ((2 * j + s) * 4 + g) * 16);
        const f32x4 c = s ? c1 : c0;
        o[2 * s] = pk2(gelu2(lo2(c) * rr[m] + (lo2(cs) * nm[m] + lo2(bb))));
        o[2 * s + 1] = pk2(gelu2(hi2(c) * rr[m] + (hi2(cs) * nm[m] + hi2(bb))));
      }
      st128(o, rs, lane * 16, h1base + (m * 48 + (c16 >> 3) * 24 + (j < 2 ? 2 * (c16 & 7) + j : 16 + (c16 & 7))) * 1024);
    };
    {
      f32x4 acc[2][4];
      kloop<2, 4, 20, 1, true>(acc, [&](int ks, int s, int h) { return ((s & 1) ? bp1 : bp0).rd(L_W1 + (s >> 1) * 40960 + ks * 2048 + h * 1024); },
        [&](int ks, int m) { return a[ks][m]; },
        [&](int ks) {
#pragma unroll
          for (int j = ld_hi(ks - 1, AW, AW, 20); j < ld_hi(ks, AW, AW, 20); ++j)
#pragma unroll
            for (int m = 0; m < 2; ++m)
              a[j][m] = j < 16 ? ld128(rs, lane * 16, abase + (m * 20 + j) * 1024) : ld_split(rs, lane, abase + (m * 20 + j) * 1024);
          if (ks == 20 - AW) PROG(5);
        });
      PROG(6);
      sig_wait(sig + 12 + w);
#pragma unroll
      for (int m = 0; m < 2; ++m) {
        const f32x2 u = *(LAS f32x2*)(L + L_KVF + ((2 * w + m) * 16 + t) * 8);
        rr[m] = u[0]; nm[m] = u[1];
      }
#pragma unroll
      for (int j = 0; j < 2; ++j)
#pragma unroll
        for (int m = 0; m < 2; ++m) finish(acc[m][2 * j], acc[m][2 * j + 1], j, m);
      SCHED();
    }
    f32x4 accb[2][2];
    const int kh = w & 1, tt = 4 * r64 + w;
    v4u hr01, hr23;
    v2u hr4;
    kloop<2, 2, 20, 3, true>(accb, [&](int ks, int s, int h) { return (s ? bp1 : bp0).rd(L_W1 + 81920 + ks * 2048 + h * 1024); },
      [&](int ks, int m) { return a[ks][m]; },
      [&](int ks) {
        if (ks == 8) { publish(FLAG(4, grp32) + c16, tag, lane); PROG(7); }
        if (ks == 12) PROG(8);
        if (ks == 10) {
          hr01 = ld128(rs, lane * 16, WS_H + (tt * 20 + 2 * c8) * 1024);
          hr23 = ld128(rs, lane * 16, WS_H + (tt * 20 + 2 * c8 + 1) * 1024);
          hr4 = ld64(rs, lane * 8, WS_H + (tt * 20 + 16) * 1024 + c8 * 512);
        }
      });
    PROG(9);
    f32x4 acc[2][5];
    {
      const unsigned hrr[10] = {hr01.x, hr01.y, hr01.z, hr01.w, hr23.x, hr23.y, hr23.z, hr23.w, hr4.x, hr4.y};
#pragma unroll
      for (int s = 0; s < 5; ++s) {
        acc[0][s] = *(LAS f32x4*)(L + L_B2 + (s * 4 + g) * 16) + f32x4{bf_lo(hrr[2 * s]), bf_hi(hrr[2 * s]), bf_lo(hrr[2 * s + 1]), bf_hi(hrr[2 * s + 1])};
        PIN(acc[0][s]);
      }
    }
    int oo[3];
    unsigned tw, tr;
    {
      int ln = lane;
      PIN(ln);
      const int qq = ln / 10, cc = ln % 10;
#pragma unroll
      for (int j = 0; j < 3; ++j) { oo[j] = sel(ln < (j < 2 ? 60 : 40), ((tt * 16 + 6 * j + qq) * D + 80 * c8) * 2 + cc * 16); PIN(oo[j]); }
      tw = lds0 + L_W2 + 122880 + w * 3072 + t * 176 + g * 8; tr = lds0 + L_W2 + 122880 + w * 3072 + qq * 176 + cc * 16;
      PIN(tw); PIN(tr);
    }
    BAR_RAW();
    constexpr int WIN = 8;
    v4u ar[WIN][2];
    const int a4[2] = {WS_H1 + (tt * 48 + kh * 24) * 1024, WS_H1 + ((tt ^ 1) * 48 + kh * 24) * 1024};
#pragma unroll
    for (int ks = 0; ks < WIN - 1; ++ks)
#pragma unroll
      for (int m = 0; m < 2; ++m) ar[ks][m] = ld128(rs, lane * 16, a4[m] + ks * 1024);
    SCHED();
#pragma unroll
    for (int m = 0; m < 2; ++m) finish(accb[m][0], accb[m][1], 2, m);
    const LBase bp(lds0 + L_W2 + kh * 5120 + (lane >> 2) * 160 + (lane & 3) * 8);
    kloop<2, 5, 24, 1, true, 1>(acc, [&](int ks, int s, int h) { return bp.rd((ks % 15) * 10240 + h * 2560 + s * 32); },
      [&](int ks, int m) { return ar[ks % WIN][m]; },
      [&](int ks) {
        if (ks == 5) PROG(10);
        if (ks == 9 || ks == 14 || ks == 19) BAR_RAW();
        if (ks == 2) publish(FLAG(10, grp32) + c16, tag, lane);
        if (ks >= 2) {
#pragma unroll
          for (int j = ks == 2 ? WIN - 1 : ld_hi(ks - 1, 3, WIN - 1, 24); j < ld_hi(ks, 3, WIN - 1, 24); ++j)
#pragma unroll
            for (int m = 0; m < 2; ++m) ar[j % WIN][m] = ld128(rs, lane * 16, a4[m] + j * 1024);
        }
      });
    LAS f32x4* xch = (LAS f32x4*)(L + L_W2 + 102400);
#pragma unroll
    for (int s = 0; s < 5; ++s) xch[((w ^ 1) * 5 + s) * 64 + lane] = acc[1][s];
    BAR();
    const __amdgpu_buffer_rsrc_t rout = __builtin_amdgcn_make_buffer_rsrc((void*)out, 0, S * D * 2, 0x00020000);
    f32x4 ot[5];
#pragma unroll
    for (int s = 0; s < 5; ++s) ot[s] = xch[(w * 5 + s) * 64 + lane];
    SCHED();
#pragma unroll
    for (int s = 0; s < 5; ++s) {
      const f32x4 v = acc[0][s] + ot[s];
      *(LAS v2u*)(lp(tw) + s * 32) = v2u{pk2(lo2(v)), pk2(hi2(v))};
    }
    asm volatile("" ::: "memory");
    v4u ov[3];
#pragma unroll
    for (int j = 0; j < 3; ++j) ov[j] = *(LAS v4u*)(lp(tr) + j * 6 * 176);
#pragma unroll
    for (int j = 0; j < 3; ++j) __builtin_amdgcn_raw_buffer_store_b128(ov[j], rout, oo[j], 0, 0);
  }
  return;
  }

  {
    const v4u srd_ws = mk_srd(ws, WS_BYTES);
    const int q5 = lane / 5, c5 = lane % 5;
    const int f40m = c5 < 4 ? 512 : 128, f40l = c5 < 4 ? q5 * 64 + (c5 ^ (2 * ((q5 >> 2) & 1))) * 16 : 2048 + q5 * 16;
    v2u ub1, ub2;
    unsigned pf0;
    v4u tq[10];
    if (hw == 1) {
      const __amdgpu_buffer_rsrc_t ro = __builtin_amdgcn_make_buffer_rsrc((void*)wo, 0, D * D * 2, 0x00020000);
      const int vq = sel(lane < 40, (8 * (q5 >> 2) + (q5 & 3)) * 3840 + c5 * 16), vo = sel(lane < 40, q10 * 1280 + c10 * 16);
#pragma unroll
      for (int j = 0; j < 5; ++j) {
        const int i8 = 5 * r128 + j, k = 32 * (i8 >> 2) + 16 * (i8 & 1) + 4 * ((i8 >> 1) & 1);
        tq[j] = __builtin_amdgcn_raw_buffer_load_b128(rq, vq, k * 3840 + c16 * 80, 0);
      }
#pragma unroll
      for (int j = 0; j < 5; ++j) tq[5 + j] = __builtin_amdgcn_raw_buffer_load_b128(ro, vo, (20 * r64 + 4 * j) * 1280 + c8 * 160, 0);
    } else if (hw == 2) {
      const GAS unsigned char* cb = (const GAS unsigned char*)(pc0 & ~127ull);
      pf0 = *(const GAS unsigned*)(cb + min(lane * 32 + bi, 351) * 128);
      if (lane < 6) {
        gu32* fp = FLAG(3 * (b >> 6) + (lane >> 1), b & 63) + (lane & 1) * 32;
        asm volatile("global_atomic_add %0, %1, off sc1" :: "v"(fp), "v"(0u) : "memory");
      }
    } else if (hw == 3) {
      const __amdgpu_buffer_rsrc_t rb1 = __builtin_amdgcn_make_buffer_rsrc((void*)b1, 0, F * 2, 0x00020000);
      const __amdgpu_buffer_rsrc_t rb2 = __builtin_amdgcn_make_buffer_rsrc((void*)b2, 0, D * 2, 0x00020000);
      ub1 = __builtin_amdgcn_raw_buffer_load_b64(rb1, lane * 8, c16 * 192, 0);
      ub2 = __builtin_amdgcn_raw_buffer_load_b64(rb2, lane * 8, c8 * 160, 0);
    }
    unsigned tag = __hip_atomic_load((gu32*)(ws + WS_EPOCH), RLX_AGENT);
    if (hw == 0) {
      const int dof = sel(lane < 40, lane * 16);
#pragma unroll
      for (int j = 0; j < 10; ++j) {
        const int k = 40 * r128 + 4 * j, R = (k & ~31) + 16 * ((k >> 2) & 1) + 4 * ((k >> 3) & 3);
        st128(tv[j], rs, dof, WS_TKV + c16 * 153600 + R * 160);
      }
      PIN(tag); tag += 1u;
      publish(FLAG(6, c16) + r128, tag, lane);
    }
    asm volatile("" ::: "memory");
    int red_off, kvf_off, kvp_off;
    auto items = [&]() {
      const int it = ht < 210 ? ht : 209, ie = it / 5, ic = it % 5;
      const int ihead = ie < 40 ? (ie >= 20) : ie - 40;
      const int id = 20 * ihead + 4 * ic, dg = id >> 2;
      const int sa = id >> 4, col = 40 + ie;
      const int tile = 3 * sa + (ie < 40 ? (col >> 4) - 2 : 0) - (sa > 0);
      red_off = lds0 + L_RED + (tile * 64 + (dg & 3) * 16 + (ie < 40 ? (col & 15) : 0)) * 16;
      kvf_off = lds0 + L_KVF + ((ie < 40 ? (ie >> 4) : 3) * 2 + (dg >> 3)) * 1024 + ((dg & 3) * 16 + (ie < 40 ? (ie & 15) : ihead)) * 16 + ((dg >> 2) & 1) * 8;
      kvp_off = WS_KVP + (c16 * 16 + r128) * 4096 + ht * 16;
      PIN(red_off); PIN(kvf_off); PIN(kvp_off);
    };
    if (hw != 0) items();
    int vo12, td[12];
    if (hw >= 2) {
      const int q12 = lane / 12, c12 = lane % 12;
      vo12 = hw == 2 ? sel(lane < 48, q12 * 3072 + c12 * 16) : sel(lane < 40, q10 * 1280 + c10 * 16);
#pragma unroll
      for (int j = 0; j < 12; ++j) {
        const int cc = r128 >> 1, off = 40 * (r128 & 1) + 4 * j;
        const int R = off < 64 ? 64 * cc + off : 32 * (16 + (cc >> 1)) + 16 * (cc & 1) + off - 64;
        const int d1 = WS_T1 + c16 * 122880 + (c12 >> 2) * 40960 + (R + q12) * 64 + ((c12 & 3) ^ (2 * ((R >> 2) & 1))) * 16;
        const int pc = r64 >> 1, ko = 48 * (r64 & 1) + 4 * j, jj = ko >> 5;
        const int d2 = WS_T2 + c8 * 245760 + (2 * (jj < 2 ? 2 * (pc & 7) + jj : 16 + (pc & 7)) + (pc >> 3)) * 5120 + (ko & 31) * 160 + lane * 16;
        td[j] = sel(hw == 2 ? (lane < 48 && j < 10) : lane < 40, hw == 2 ? d1 : d2);
        PIN(td[j]);
      }
      PIN(vo12);
    }
    int l16 = lane * 16, h16 = sel(ht < 210, ht * 16);
    PIN(l16); PIN(h16);
    gu32* fp1 = FLAG(1, c16) + lane;
    gu32* fp78 = lane < 32 ? FLAG(7, c8) + lane : FLAG(8, c16) + t;
    gu32* fp4 = FLAG(4, 2 * r64 + (g & 1)) + t;
    gu32* fp1w = FLAG(1, c16) + r128 * 4 + hw;
    gu32* fps = FLAG(3, 4 * r128 + hw) + 32 + lane;
    int sto = g * 256 + t * 8;
    PIN(fp1); PIN(fp78); PIN(fp4); PIN(fp1w); PIN(fps); PIN(sto);
    ((LAS v4u*)(L + L_KVF))[ht] = v4u{0u, 0u, 0u, 0u};
    ((LAS v4u*)(L + L_KVF))[256 + ht] = v4u{0u, 0u, 0u, 0u};
    asm volatile("" ::: "memory");
    if (hw != 0) {
      if (hw == 1) {
        sig_wait(sig + 6);
#pragma unroll
        for (int j = 0; j < 5; ++j) {
          const int i8 = 5 * r128 + j;
          st128(tq[j], rs, sel(lane < 40, (i8 & 3) * f40m + f40l), WS_TKV + c16 * 153600 + 102400 + (i8 >> 2) * 2560);
        }
#pragma unroll
        for (int j = 0; j < 5; ++j) {
          const int pp = r64 >> 1, e = 20 * (r64 & 1) + 4 * j;
          const int R = e < 32 ? 32 * pp + e : 32 * (16 + (pp >> 2)) + 8 * (pp & 3) + e - 32;
          st128(tq[5 + j], rs, sel(lane < 40, lane * 16), WS_TO + c8 * 102400 + R * 160);
        }
      }
      if (hw == 3) {
        if (lane < 24) *(LAS f32x4*)(L + L_B1 + lane * 16) = f32x4{bf_lo(ub1.x), bf_hi(ub1.x), bf_lo(ub1.y), bf_hi(ub1.y)};
        if (lane < 20) *(LAS f32x4*)(L + L_B2 + lane * 16) = f32x4{bf_lo(ub2.x), bf_hi(ub2.x), bf_lo(ub2.y), bf_hi(ub2.y)};
      }
      if (hw == 2) asm volatile("" :: "v"(pf0));
      PIN(tag); tag += 1u;
      DRAIN();
      if (hw == 1 && lane < 2) flag_set(lane == 0 ? FLAG(8, c16) + r128 : FLAG(7, c8) + r64, tag);
    }
    if (hw == 0) {
      wait_flag(FLAG(6, c16) + lane, 16, tag, lane);
      sig[0] = 1u;
    }
    sig_wait(sig);
    const int kvq = WS_TKV + c16 * 153600, wos = WS_TO + c8 * 102400;
    dma_image<0, 24>(lds0 + L_BKV, l16, srd_ws, kvq, hw, 3 * (bi >> 3));
    dma_image<24, 56>(lds0 + L_BKV, l16, srd_ws, kvq, hw, 4 * (bi >> 3));
    asm volatile("s_waitcnt vmcnt(8)" ::: "memory");
    BAR_RAW();
    dma_image<56, 100>(lds0 + L_BKV, l16, srd_ws, kvq, hw, 5 * (bi >> 3));
    asm volatile("s_waitcnt vmcnt(10)" ::: "memory");
    BAR_RAW();
    if (hw == 1) wait_flag(fp78, 48, tag, lane);
    DRAIN();
    BAR_RAW();
    dma_image<100, 150>(lds0 + L_BKV, l16, srd_ws, kvq, hw, 6 * (bi >> 3));
    if (hw == 0) items();
    prog_wait(prog, 1);
    if (hw >= 2) {
      const __amdgpu_buffer_rsrc_t r12 = __builtin_amdgcn_make_buffer_rsrc((void*)(hw == 2 ? w1 : w2), 0, D * F * 2, 0x00020000);
      const int base = hw == 2 ? r128 * 122880 + c16 * 192 : r64 * 61440 + c8 * 160, step = hw == 2 ? 4 * 3072 : 4 * 1280;
#pragma unroll
      for (int j = 0; j < 12; ++j) tv[j] = __builtin_amdgcn_raw_buffer_load_b128(r12, vo12, (j < 10 || hw == 3) ? base + j * step : OOB, 0);
      asm volatile("s_waitcnt vmcnt(12)" ::: "memory");
    } else {
      dma_image<32, 68, 2>(lds0 + L_WO, l16, srd_ws, wos, hw, bi >> 2);
      asm volatile("s_waitcnt vmcnt(18)" ::: "memory");
    }
    BAR();
    __builtin_amdgcn_s_setprio(2);
    {
      f32x4 pv[4];
#pragma unroll
      for (int ww = 0; ww < 4; ++ww) pv[ww] = *(LAS f32x4*)(lp(red_off) + ww * 8192);
      st128(__builtin_bit_cast(v4u, add4(add4(pv[0], pv[1]), add4(pv[2], pv[3]))), rs, kvp_off, 0);
      PROG(1);
      DRAIN();
#pragma unroll
      for (int j = 0; j < 12; ++j) PIN(tv[j]);
    }
    if (lane == 0) flag_set(fp1w, tag);
    if (hw == 0) {
      __builtin_amdgcn_s_sleep(8);
      wait_flag(fp1, 64, tag, lane);
      sig[2] = 1u;
    } else {
      __builtin_amdgcn_s_sleep(2);
      prog_wait(prog + 1, 1);
#pragma unroll
      for (int i = 0; i < 12; ++i) {
        if (hw >= 2) st128(tv[i], rs, td[i], 0);
        const int j = hw - 1 + 3 * i;
        if (j < 32) {
          const int pc = 2 * ((j + 4 * (bi >> 2)) & 31), po = (pc < 32 ? pc : pc + 36) * 1024;
          dma2(lds0 + L_WO + po, l16, srd_ws, wos + po);
        }
      }
      sig_wait(sig + 2);
    }
    {
      v4u pv[16];
#pragma unroll
      for (int r = 0; r < 16; ++r) pv[r] = ld128(rs, h16, WS_KVP + (c16 * 16 + r) * 4096);
      SCHED();
      f32x4 v = __builtin_bit_cast(f32x4, pv[0]);
#pragma unroll
      for (int r = 1; r < 16; ++r) v = add4(v, __builtin_bit_cast(f32x4, pv[r]));
      v2u o; o.x = pk2(lo2(v)); o.y = pk2(hi2(v));
      if (ht < 210) *(LAS v2u*)(lp(kvf_off)) = o;
      if (ht >= 200 && ht < 210) {
#pragma unroll
        for (int c = 1; c < 4; ++c) *(LAS v2u*)(lp(kvf_off) + c * 64) = o;
      }
    }
    DRAIN();
    BAR();
    __builtin_amdgcn_s_setprio(0);
    if (hw >= 2 && lane == 0) flag_set(hw == 2 ? FLAG(5, c16) + r128 : FLAG(11, c8) + r64, tag);
    if (hw == 2) {
      f32x4 s0 = {0.f, 0.f, 0.f, 0.f}, s1 = {0.f, 0.f, 0.f, 0.f};
#pragma unroll
      for (int j = 0; j < 10; ++j) {
        s0 = add4(s0, f32x4{bf_lo(tv[j].x), bf_hi(tv[j].x), bf_lo(tv[j].y), bf_hi(tv[j].y)});
        s1 = add4(s1, f32x4{bf_lo(tv[j].z), bf_hi(tv[j].z), bf_lo(tv[j].w), bf_hi(tv[j].w)});
      }
#pragma unroll
      for (int d = 24; d >= 12; d -= 12) {
        f32x4 u0, u1;
#pragma unroll
        for (int e = 0; e < 4; ++e) { u0[e] = __shfl(s0[e], lane + d); u1[e] = __shfl(s1[e], lane + d); }
        s0 = add4(s0, u0); s1 = add4(s1, u1);
      }
      f32x4 sv;
#pragma unroll
      for (int e = 0; e < 4; ++e) { const float a0 = __shfl(s0[e], lane >> 1), a1 = __shfl(s1[e], lane >> 1); sv[e] = (lane & 1) ? a1 : a0; }
      st128(__builtin_bit_cast(v4u, sv), rs, sel(lane < 24, lane * 16), WS_CS + (c16 * 16 + r128) * 384);
      publish(FLAG(9, c16) + r128, tag, lane);
    }
    if (hw == 1) {
      __builtin_amdgcn_s_sleep(40);
      wait_flag(lane < 32 ? FLAG(11, c8) + lane : FLAG(5, c16) + t, 48, tag, lane);
      sig[3] = 1u;
    }
    sig_wait(sig + 3);
    prog_wait(prog, 2);
    if (hw == 0) {
      wait_flag(FLAG(9, c16) + lane, 16, tag, lane);
      v4u pv[16];
      const int co = sel(lane < 24, l16);
#pragma unroll
      for (int r = 0; r < 16; ++r) pv[r] = ld128(rs, co, WS_CS + (c16 * 16 + r) * 384);
      SCHED();
      f32x4 v = __builtin_bit_cast(f32x4, pv[0]);
#pragma unroll
      for (int r = 1; r < 16; ++r) v = add4(v, __builtin_bit_cast(f32x4, pv[r]));
      if (lane < 24) *(LAS f32x4*)(L + L_CS + lane * 16) = v;
    } else {
      dma_image<70, 120, 3>(lds0 + L_W1, l16, srd_ws, WS_T1 + c16 * 122880, hw - 1, 6 * (bi >> 3));
      prog_wait(prog, 3);
      dma_image<0, 40, 3>(lds0 + L_W1, l16, srd_ws, WS_T1 + c16 * 122880, hw - 1, bi >> 3);
      prog_wait(prog, 4);
      dma_image<40, 70, 3>(lds0 + L_W1, l16, srd_ws, WS_T1 + c16 * 122880, hw - 1, bi >> 3);
    }
    DRAIN();
    BAR();
    {
      __builtin_amdgcn_s_setprio(2);
      __builtin_amdgcn_s_sleep(6);
      wait_flag(fps, 16, tag, lane);
      v2u s0[2], s1[2];
#pragma unroll
      for (int m = 0; m < 2; ++m) {
        s0[m] = ld64(rs, sto, WS_ST + (8 * r128 + 2 * hw + m) * 1024);
        s1[m] = ld64(rs, sto + 128, WS_ST + (8 * r128 + 2 * hw + m) * 1024);
      }
#pragma unroll
      for (int m = 0; m < 2; ++m) {
        const f32x2 p0 = __builtin_bit_cast(f32x2, s0[m]), p1 = __builtin_bit_cast(f32x2, s1[m]);
        const float a0 = p0[0], q0 = p0[1], a1 = p1[0], q1 = p1[1];
        float mean = xsum32(xsum16(a0 + a1)) * 0.125f;
        PIN(mean);
        float m2 = xsum32(xsum16(q0 + q1 + 80.f * ((a0 - mean) * (a0 - mean) + (a1 - mean) * (a1 - mean))));
        PIN(m2);
        const float var = m2 * (1.f / D);
        const float r1 = __builtin_amdgcn_rsqf(var + LN_EPS);
        const float rr = r1 * __builtin_amdgcn_rsqf(var * r1 * r1 + LN_EPS);
        if (g == 0) *(LAS f32x2*)(L + L_KVF + ((2 * hw + m) * 16 + t) * 8) = f32x2{rr, -mean * rr};
      }
      asm volatile("" ::: "memory");
      sig[12 + hw] = 1u;
      __builtin_amdgcn_s_setprio(0);
    }
    const int w2s = WS_T2 + c8 * 245760;
    prog_wait(prog, 5);
    dma_image<0, 30>(lds0 + L_W2, l16, srd_ws, w2s, hw, bi >> 2);
    prog_wait(prog, 6);
    dma_image<30, 70>(lds0 + L_W2, l16, srd_ws, w2s, hw, bi >> 2);
    prog_wait(prog, 7);
    if (hw == 0) {
      DRAIN();
      wait_flag(fp4, 32, tag, lane);
    } else {
      dma_image<70, 110, 3>(lds0 + L_W2, l16, srd_ws, w2s, hw - 1, bi >> 2);
      prog_wait(prog, 8);
      dma_image<110, 134, 3>(lds0 + L_W2, l16, srd_ws, w2s, hw - 1, bi >> 2);
      prog_wait(prog, 9);
      dma_image<134, 146, 3>(lds0 + L_W2, l16, srd_ws, w2s, hw - 1, 0);
      asm volatile("s_waitcnt vmcnt(12)" ::: "memory");
    }
    BAR_RAW();
    dma_image<146, 150>(lds0 + L_W2, l16, srd_ws, w2s, hw, 0);
    prog_wait(prog, 10);
    if (hw == 0) wait_flag(fp4 + 6 * FLAG_SET / 4, 32, tag, lane);
    else dma_image<150, 198, 3>(lds0 + L_W2 - 153600, l16, srd_ws, w2s, hw - 1, 3 * (bi >> 2));
    asm volatile("s_waitcnt vmcnt(16)" ::: "memory");
    BAR_RAW();
    dma_image<198, 200>(lds0 + L_W2 - 153600, l16, srd_ws, w2s, hw, 0);
    dma_image<200, 240>(lds0 + L_W2 - 153600, l16, srd_ws, w2s, hw, 2 * (bi >> 2));
    asm volatile("s_waitcnt vmcnt(10)" ::: "memory");
    BAR_RAW();
    DRAIN();
    BAR_RAW();
  }

}
}

extern "C" void kernel_launch(void* const* d_in, const int* in_sizes, int n_in, void* d_out, int out_size, void* d_ws,
                              size_t ws_size, hipStream_t stream) {
  const unsigned short* x = (const unsigned short*)d_in[0];
  const unsigned short* wqkv = (const unsigned short*)d_in[1];
  const unsigned short* wo = (const unsigned short*)d_in[2];
  const unsigned short* w1 = (const unsigned short*)d_in[4];
  const unsigned short* b1 = (const unsigned short*)d_in[5];
  const unsigned short* w2 = (const unsigned short*)d_in[6];
  const unsigned short* b2 = (const unsigned short*)d_in[7];
  fused_block<<<256, 512, 0, stream>>>(x, wqkv, (unsigned char*)d_ws, wo, w1, b1, w2, b2, (unsigned short*)d_out);
}
```
